```python
import torch

def _softmax(x, axis=-1):
    return torch.softmax(x, dim=axis)
def _gelu(x):
    return torch.nn.functional.gelu(x, approximate="tanh")
def _silu(x):
    return torch.nn.functional.silu(x)
def _sigmoid(x):
    return torch.sigmoid(x)
def _layernorm(x, axis=-1, eps=1e-5):
    m = x.mean(dim=axis, keepdim=True)
    v = x.var(dim=axis, keepdim=True, unbiased=False)
    return (x - m) / torch.sqrt(v + eps)
def _scatter_add(tgt, idx, src, axis=0):
    return tgt.clone().index_add_(axis, idx, src)
def _segment_sum(src, seg, n):
    out = torch.zeros((n,) + src.shape[1:], dtype=src.dtype)
    return out.index_add_(0, seg, src)
def _pad(x, pw):
    flat = tuple(v for pair in reversed(pw) for v in pair)
    return torch.nn.functional.pad(x, flat)

kernel_name = 'parallel_sum__sliding_block__mixer_token_mlp__prompt'

def setup_inputs():

    _w = (lambda a: (lambda t: t.to(torch.bfloat16) if t.is_floating_point() else t)(torch.as_tensor(a)))
    def _A_setup():
        B, S, D = 1, 2048, 1024
        return {
            "x": _w((lambda *s: torch.randn(*s, dtype=torch.bfloat16) * 0.05)(B, S, D)),
            "wqkv": _w((lambda *s: torch.randn(*s, dtype=torch.bfloat16) * 0.05)(D, 3 * D)),
            "wo": _w((lambda *s: torch.randn(*s, dtype=torch.bfloat16) * 0.05)(D, D)),
        }
    _A_in = _A_setup()

    def _B_setup():
        B, S, D, F = 1, 2048, 1024, 3584
        return {
            "x": _w((lambda *s: torch.randn(*s, dtype=torch.bfloat16) * 0.05)(B, S, D)),
            "w1": _w((lambda *s: torch.randn(*s, dtype=torch.bfloat16) * 3.2)(S, F) * (S ** -0.5)),
            "b1": _w((lambda *s: torch.randn(*s, dtype=torch.bfloat16) * 3.2)(F) * 0.02),
            "w2": _w((lambda *s: torch.randn(*s, dtype=torch.bfloat16) * 0.4)(F, S) * (F ** -0.5)),
            "b2": _w((lambda *s: torch.randn(*s, dtype=torch.bfloat16) * 0.4)(S) * 0.02),
        }
    _B_in = _B_setup()

    return {
        "A_x": _A_in["x"],
        "A_wqkv": _A_in["wqkv"],
        "A_wo": _A_in["wo"],
        "B_x": _B_in["x"],
        "B_w1": _B_in["w1"],
        "B_b1": _B_in["b1"],
        "B_w2": _B_in["w2"],
        "B_b2": _B_in["b2"],
    }

def reference(A_x, A_wqkv, A_wo, B_x, B_w1, B_b1, B_w2, B_b2):
    def _A_ref(x, wqkv, wo):
        B, S, D = x.shape
        H = 32; Dh = D // H; W = 256
        qkv = (lambda x, *d: x.permute(*d))((x @ wqkv).reshape(B, S, 3, H, Dh), 2, 0, 3, 1, 4)
        q, k, v = qkv[0], qkv[1], qkv[2]
        scores = torch.einsum("bhqd,bhkd->bhqk", q, k) * (Dh ** -0.5)
        idx = torch.arange(S)
        mask = (idx[:, None] - idx[None, :] >= 0) & (idx[:, None] - idx[None, :] < W)
        scores = torch.where(mask[None, None], scores, -1e9)
        p = _softmax(scores, axis=-1)
        o = torch.einsum("bhqk,bhkd->bhqd", p, v)
        return (lambda x, *d: x.permute(*d))(o, 0, 2, 1, 3).reshape(B, S, D) @ wo

    def _B_ref(x, w1, b1, w2, b2):
        B, S, D = x.shape
        h = _layernorm(x, axis=-1)
        u = torch.einsum("bsd,sf->bdf", h, w1) + b1
        u = _gelu(u)
        y = torch.einsum("bdf,fs->bds", u, w2) + b2
        return (lambda x, *d: x.permute(*d))(y, 0, 2, 1)

    x = A_x
    xn = _layernorm(x, axis=-1)
    return x + _A_ref(xn, A_wqkv, A_wo) + _B_ref(xn, B_w1, B_b1, B_w2, B_b2)
```

```cpp
#include <hip/hip_runtime.h>
#include <hip/hip_bf16.h>
#include <stdint.h>

#define LAS __attribute__((address_space(3)))
#define GAS __attribute__((address_space(1)))
typedef __bf16 bf16x8 __attribute__((ext_vector_type(8)));
typedef __bf16 bf16x4 __attribute__((ext_vector_type(4)));
typedef __bf16 bf16x2 __attribute__((ext_vector_type(2)));
typedef float f32x2 __attribute__((ext_vector_type(2)));
typedef float f32x4 __attribute__((ext_vector_type(4)));
typedef float f32x16 __attribute__((ext_vector_type(16)));
typedef unsigned u32x4 __attribute__((ext_vector_type(4)));
typedef unsigned u32x2 __attribute__((ext_vector_type(2)));
typedef unsigned short u16;

constexpr int S_ = 2048, D_ = 1024, F_ = 3584, NH = 32;

constexpr size_t WS_FLAGS = 0;
constexpr size_t WS_XN    = 1 << 16;
constexpr size_t WS_H     = WS_XN  + (size_t)S_ * D_ * 2;
constexpr size_t WS_QKV   = WS_H   + (size_t)S_ * D_ * 2;
constexpr size_t WS_U     = WS_QKV + (size_t)S_ * 3 * D_ * 2;
constexpr size_t WS_O     = WS_U   + (size_t)D_ * F_ * 2;
constexpr size_t WS_PART  = WS_O   + (size_t)S_ * D_ * 2;

#define WAIT_VM(n) asm volatile("s_waitcnt vmcnt(%0)" ::"n"(n) : "memory")
#define WAIT_LGKM0() asm volatile("s_waitcnt lgkmcnt(0)" ::: "memory")
#define BARRIER() do { asm volatile("" ::: "memory"); __builtin_amdgcn_s_barrier(); asm volatile("" ::: "memory"); } while (0)

__device__ __forceinline__ float bf2f(u16 v) { return __uint_as_float((unsigned)v << 16); }
__device__ __forceinline__ unsigned pack2(float lo, float hi) {
    f32x2 v = {lo, hi};
    bf16x2 b = __builtin_convertvector(v, bf16x2);
    return __builtin_bit_cast(unsigned, b);
}
__device__ __forceinline__ float rbf(float x) {
    return __uint_as_float(pack2(x, 0.f) << 16);
}
__device__ __forceinline__ float wave_sum(float v) {
#pragma unroll
    for (int o = 32; o >= 1; o >>= 1) v += __shfl_xor(v, o);
    return v;
}

__global__ __launch_bounds__(256) void k_ln(const u16* __restrict__ x, u16* __restrict__ xn, u16* __restrict__ h, unsigned* __restrict__ flags) {
    const int lane = threadIdx.x & 63, row = blockIdx.x * 4 + (threadIdx.x >> 6);
    if (blockIdx.x < 64) flags[blockIdx.x * 256 + threadIdx.x] = 0u;
    const u32x4* px = (const u32x4*)(x + (size_t)row * D_);
    u32x4 a = px[lane], b = px[64 + lane];
    float v[16];
#pragma unroll
    for (int i = 0; i < 4; ++i) {
        v[2 * i] = __uint_as_float(a[i] << 16); v[2 * i + 1] = __uint_as_float(a[i] & 0xffff0000u);
        v[8 + 2 * i] = __uint_as_float(b[i] << 16); v[8 + 2 * i + 1] = __uint_as_float(b[i] & 0xffff0000u);
    }
#pragma unroll
    for (int pass = 0; pass < 2; ++pass) {
        float s = 0.f;
#pragma unroll
        for (int i = 0; i < 16; ++i) s += v[i];
        const float mean = wave_sum(s) * (1.0f / D_);
        float q = 0.f;
#pragma unroll
        for (int i = 0; i < 16; ++i) { const float d = v[i] - mean; q += d * d; }
        const float var = wave_sum(q) * (1.0f / D_);
        const float mb = rbf(mean);
        const float sd = rbf(__builtin_sqrtf(rbf(rbf(var) + 1e-5f)));
        const float rs = 1.0f / sd;
        u32x4 oa, ob;
#pragma unroll
        for (int i = 0; i < 8; ++i) {
            const float d0 = rbf(v[2 * i] - mb), d1 = rbf(v[2 * i + 1] - mb);
            const unsigned p = pack2(d0 / sd, d1 / sd);
            if (i < 4) oa[i] = p; else ob[i - 4] = p;
            v[2 * i] = __uint_as_float(p << 16); v[2 * i + 1] = __uint_as_float(p & 0xffff0000u);
        }
        (void)rs;
        u32x4* po = (u32x4*)((pass == 0 ? xn : h) + (size_t)row * D_);
        po[lane] = oa; po[64 + lane] = ob;
    }
}

template <int X> __device__ __forceinline__ int tr_swz(int row) {
    if (X == 224) return ((row >> 3) & 1) << 1;
    if (X == 192) return (((row >> 3) & 1) << 1) | (((row >> 1) & 1) << 2);
    return (((row >> 3) & 1) << 1) | ((row & 3) << 2);
}
template <bool TR, int X> struct Op {
    static constexpr int BYTES = X * 128;
    static constexpr int NI = X / 8;
    static constexpr int NIW = NI / 4;
    __device__ static __forceinline__ unsigned src_off(int i, int lane, unsigned ld) {
        const int L = i * 64 + lane;
        if (TR) { constexpr int CPR = X / 8; const int r = L / CPR, ph = L % CPR; return (unsigned)r * ld + (unsigned)((ph ^ tr_swz<X>(r)) * 16); }
        else { const int r = L >> 3, ph = L & 7; return (unsigned)r * ld + (unsigned)((ph ^ ((r >> 1) & 7)) * 16); }
    }
};

__device__ __forceinline__ void glds16(const char* g, LAS char* l) {
    __builtin_amdgcn_global_load_lds((const GAS unsigned*)g, (LAS unsigned*)l, 16, 0, 0);
}

template <bool TR, int X, int NF> struct Frag {
    unsigned o[TR ? NF : 2];
    __device__ __forceinline__ void init(int lane, int x0) {
        if (TR) {
            const int g = lane >> 4, q = (lane & 15) >> 2, p = lane & 3, row = 8 * g + q;
#pragma unroll
            for (int n = 0; n < NF; ++n) {
                const int ch = (x0 / 8 + 2 * n + (p >> 1)) ^ tr_swz<X>(row);
                o[n] = (unsigned)(row * (2 * X) + ch * 16 + 8 * (p & 1));
            }
        } else {
            const int r = lane & 15, g = lane >> 4;
#pragma unroll
            for (int kk = 0; kk < 2; ++kk) o[kk] = (unsigned)((x0 + r) * 128 + (((kk * 4 + g) ^ ((r >> 1) & 7)) * 16));
        }
    }
    __device__ __forceinline__ bf16x8 read(LAS char* base, int n, int kk) const {
        if (TR) {
            LAS char* p = base + o[n] + kk * 32 * (2 * X);
            bf16x4 lo = __builtin_amdgcn_ds_read_tr16_b64_v4bf16((LAS bf16x4*)p);
            bf16x4 hi = __builtin_amdgcn_ds_read_tr16_b64_v4bf16((LAS bf16x4*)(p + 4 * (2 * X)));
            return __builtin_shufflevector(lo, hi, 0, 1, 2, 3, 4, 5, 6, 7);
        } else {
            return *(LAS bf16x8*)(base + o[kk] + n * 2048);
        }
    }
};

template <int FM, int FN> struct Regs { bf16x8 m[FM], n[FN]; };

template <bool TM, int BM, bool TN, int BN>
struct Consumer {
    static constexpr int FM = BM / 32, FN = BN / 32 + ((BN / 16) & 1 ? 0 : 0);
    static constexpr int FNW = BN / 32;
};

#define MFMA16(a, b, c) __builtin_amdgcn_mfma_f32_16x16x32_bf16(a, b, c, 0, 0, 0)

template <int FM, int FN, class FragM, class FragN>
__device__ __forceinline__ void read_frags(Regs<FM, FN>& R, const FragM& fm, const FragN& fn, LAS char* sm, LAS char* sn, int kk) {
#pragma unroll
    for (int i = 0; i < FM; ++i) R.m[i] = fm.read(sm, i, kk);
#pragma unroll
    for (int j = 0; j < FN; ++j) R.n[j] = fn.read(sn, j, kk);
}
template <int FM, int FN>
__device__ __forceinline__ void mma(f32x4 (&acc)[FM][FN], const Regs<FM, FN>& R) {
#pragma unroll
    for (int j = 0; j < FN; ++j)
#pragma unroll
        for (int i = 0; i < FM; ++i) acc[i][j] = MFMA16(R.n[j], R.m[i], acc[i][j]);
}
template <int NDS, int NMF> __device__ __forceinline__ void interleave() {
#pragma unroll
    for (int i = 0; i < NMF; ++i) {
        __builtin_amdgcn_sched_group_barrier(0x8, 1, 0);
        if (i < NDS) __builtin_amdgcn_sched_group_barrier(0x100, 1, 0);
    }
}

template <bool TM, int BM, bool TN, int BN, int NS, int SB, int FM, int FN>
__device__ __forceinline__ void consume(LAS char* lds, int& slot, int nk, f32x4 (&acc)[FM][FN], int lane, int wm, int wn) {
    Frag<TM, BM, FM> fm; fm.init(lane, wm * (FM * 16));
    Frag<TN, BN, FN> fn; fn.init(lane, wn * (FN * 16));
    constexpr int NDS = FM * (TM ? 2 : 1) + FN * (TN ? 2 : 1);
    Regs<FM, FN> R0, R1;
    LAS char* cur = lds + slot * SB;
    read_frags(R0, fm, fn, cur, cur + BM * 128, 0);
    for (int t = 0; t < nk; ++t) {
        read_frags(R1, fm, fn, cur, cur + BM * 128, 1);
        mma(acc, R0);
        interleave<NDS, FM * FN>();
        __builtin_amdgcn_sched_barrier(0);
        WAIT_LGKM0();
        BARRIER();
        __builtin_amdgcn_sched_barrier(0);
        slot = (slot + 1 == NS) ? 0 : slot + 1;
        cur = lds + slot * SB;
        if (t + 1 < nk) {
            read_frags(R0, fm, fn, cur, cur + BM * 128, 0);
            mma(acc, R1);
            interleave<NDS, FM * FN>();
        } else {
            mma(acc, R1);
        }
        __builtin_amdgcn_sched_barrier(0);
    }
}

template <bool TM, int BM, bool TN, int BN>
struct Loader {
    using OM = Op<TM, BM>; using ON = Op<TN, BN>;
    static constexpr int NPW = OM::NIW + ON::NIW;
    unsigned om[OM::NIW], on[ON::NIW];
    __device__ __forceinline__ void init(int lw, int lane, unsigned ldm, unsigned ldn) {
#pragma unroll
        for (int i = 0; i < OM::NIW; ++i) om[i] = OM::src_off(lw + 4 * i, lane, ldm);
#pragma unroll
        for (int i = 0; i < ON::NIW; ++i) on[i] = ON::src_off(lw + 4 * i, lane, ldn);
    }
    __device__ __forceinline__ void issue(const char* gm, const char* gn, LAS char* dst, int lw) const {
#pragma unroll
        for (int i = 0; i < OM::NIW; ++i) glds16(gm + om[i], dst + (lw + 4 * i) * 1024);
#pragma unroll
        for (int i = 0; i < ON::NIW; ++i) glds16(gn + on[i], dst + OM::BYTES + (lw + 4 * i) * 1024);
    }
};

__device__ __forceinline__ float gelu_tanh(float u) {
    const float c1 = -2.0f * 0.7978845608028654f * 1.4426950408889634f, c2 = c1 * 0.044715f;
    const float e = __builtin_amdgcn_exp2f(u * __builtin_fmaf(u * u, c2, c1));
    return u * __builtin_amdgcn_rcpf(1.0f + e);
}
__device__ __forceinline__ u32x4 pair16(unsigned x0, unsigned x1, unsigned y0, unsigned y1) {
    auto a = __builtin_amdgcn_permlane16_swap(x0, y0, false, false);
    auto b = __builtin_amdgcn_permlane16_swap(x1, y1, false, false);
    return (u32x4){a[0], b[0], a[1], b[1]};
}

constexpr int K2_SB_M = (128 + 224) * 128, K2_SB_Q = (128 + 192) * 128;
constexpr int K2_LDS = 3 * K2_SB_M;

__global__ __launch_bounds__(512, 2) void k_gemm1(const u16* __restrict__ xn, const u16* __restrict__ h, const u16* __restrict__ wqkv,
                                                  const u16* __restrict__ w1, const u16* __restrict__ b1, u16* __restrict__ qkv, u16* __restrict__ U) {
    extern __shared__ __attribute__((aligned(16))) char smem[];
    LAS char* lds = (LAS char*)smem;
    const int tid = threadIdx.x, lane = tid & 63, wid = __builtin_amdgcn_readfirstlane(tid >> 6);
    const int grp = blockIdx.x & 7, idx = blockIdx.x >> 3;
    if (grp < 4) {
        constexpr int NK = S_ / 64, SB = K2_SB_M;
        const int tm = idx >> 2, tn = grp * 4 + (idx & 3);
        if (wid >= 4) {
            const int lw = wid - 4;
            Loader<true, 128, true, 224> L; L.init(lw, lane, D_ * 2, F_ * 2);
            const char* gm = (const char*)h + tm * 256; const char* gn = (const char*)w1 + tn * 448;
            constexpr size_t sm_ = (size_t)64 * D_ * 2, sn_ = (size_t)64 * F_ * 2;
            constexpr int N = L.NPW;
#pragma unroll
            for (int s = 0; s < 3; ++s) L.issue(gm + s * sm_, gn + s * sn_, lds + s * SB, lw);
            WAIT_VM(2 * N); BARRIER();
            int slot = 0;
            for (int t = 0; t < NK; ++t) {
                if (t + 2 < NK) WAIT_VM(N); else WAIT_VM(0);
                BARRIER();
                if (t + 3 < NK) L.issue(gm + (t + 3) * sm_, gn + (t + 3) * sn_, lds + slot * SB, lw);
                slot = (slot == 2) ? 0 : slot + 1;
            }
        } else {
            const int wm = wid >> 1, wn = wid & 1;
            f32x4 acc[4][7] = {};
            int slot = 0;
            BARRIER();
            consume<true, 128, true, 224, 3, SB, 4, 7>(lds, slot, NK, acc, lane, wm, wn);
            const int fr = lane & 15, fq = lane >> 4;
            const int f0 = tn * 224 + wn * 112, d0 = tm * 128 + wm * 64;
            unsigned pk[4][7][2];
#pragma unroll
            for (int j = 0; j < 7; ++j) {
                const u32x2 bb = *(const u32x2*)(b1 + f0 + j * 16 + fq * 4);
                const float bv0 = __uint_as_float(bb[0] << 16), bv1 = __uint_as_float(bb[0] & 0xffff0000u), bv2 = __uint_as_float(bb[1] << 16), bv3 = __uint_as_float(bb[1] & 0xffff0000u);
#pragma unroll
                for (int i = 0; i < 4; ++i) {
                    pk[i][j][0] = pack2(gelu_tanh(acc[i][j][0] + bv0), gelu_tanh(acc[i][j][1] + bv1));
                    pk[i][j][1] = pack2(gelu_tanh(acc[i][j][2] + bv2), gelu_tanh(acc[i][j][3] + bv3));
                }
            }
#pragma unroll
            for (int i = 0; i < 4; ++i) {
                u16* rowp = U + (size_t)(d0 + i * 16 + fr) * F_ + f0 + (fq & 1) * 16 + (fq >> 1) * 8;
#pragma unroll
                for (int jp = 0; jp < 3; ++jp)
                    *(u32x4*)(rowp + jp * 32) = pair16(pk[i][2 * jp][0], pk[i][2 * jp][1], pk[i][2 * jp + 1][0], pk[i][2 * jp + 1][1]);
            }
#pragma unroll
            for (int ip = 0; ip < 2; ++ip) {
                u16* rowp = U + (size_t)(d0 + (2 * ip + (fq & 1)) * 16 + fr) * F_ + f0 + 96 + (fq >> 1) * 8;
                *(u32x4*)rowp = pair16(pk[2 * ip][6][0], pk[2 * ip][6][1], pk[2 * ip + 1][6][0], pk[2 * ip + 1][6][1]);
            }
        }
    } else {
        constexpr int NK = D_ / 64, SB = K2_SB_Q;
        const int tn = (grp - 4) * 4 + (idx & 3), tm0 = idx >> 2;
        if (wid >= 4) {
            const int lw = wid - 4;
            Loader<false, 128, true, 192> L; L.init(lw, lane, D_ * 2, 3 * D_ * 2);
            const char* gm = (const char*)xn + (size_t)tm0 * 128 * D_ * 2; const char* gn = (const char*)wqkv + tn * 384;
            constexpr size_t sn_ = (size_t)64 * 3 * D_ * 2, tile2 = (size_t)1024 * D_ * 2;
            constexpr int N = L.NPW;
#pragma unroll
            for (int s = 0; s < 3; ++s) L.issue(gm + s * 128, gn + s * sn_, lds + s * SB, lw);
            WAIT_VM(2 * N); BARRIER();
            int slot = 0;
            for (int t = 0; t < 2 * NK; ++t) {
                if (t + 2 < 2 * NK) WAIT_VM(N); else WAIT_VM(0);
                BARRIER();
                if (t + 3 < 2 * NK) { const int u = t + 3, kt = u & (NK - 1); L.issue(gm + (u >= NK ? tile2 : 0) + kt * 128, gn + kt * sn_, lds + slot * SB, lw); }
                slot = (slot == 2) ? 0 : slot + 1;
            }
        } else {
            const int wm = wid >> 1, wn = wid & 1;
            const int fr = lane & 15, fq = lane >> 4;
            int slot = 0;
            BARRIER();
            for (int rnd = 0; rnd < 2; ++rnd) {
                f32x4 acc[4][6] = {};
                consume<false, 128, true, 192, 3, SB, 4, 6>(lds, slot, NK, acc, lane, wm, wn);
                const int gh0 = tn * 6 + wn * 3, s0 = (tm0 + rnd * 8) * 128 + wm * 64;
#pragma unroll
                for (int jp = 0; jp < 3; ++jp) {
                    const int gh = gh0 + jp;
                    const float sc = gh < NH ? 0.17677669529663687f * 1.4426950408889634f : 1.0f;
#pragma unroll
                    for (int i = 0; i < 4; ++i) {
                        const f32x4 a = acc[i][2 * jp] * sc, b = acc[i][2 * jp + 1] * sc;
                        u16* p = qkv + ((size_t)gh * S_ + s0 + i * 16 + fr) * 32 + (fq & 1) * 16 + (fq >> 1) * 8;
                        *(u32x4*)p = pair16(pack2(a[0], a[1]), pack2(a[2], a[3]), pack2(b[0], b[1]), pack2(b[2], b[3]));
                    }
                }
            }
        }
    }
}

#define MFMA32(a, b, c) __builtin_amdgcn_mfma_f32_32x32x16_bf16(a, b, c, 0, 0, 0)
__device__ __forceinline__ void attn_body(LAS char* lds, const u16* qkv, u16* o, int hd, int qb, int wid, int lane) {
    const int q0 = qb * 256;
    const char* Kg = (const char*)(qkv + (size_t)(NH + hd) * S_ * 32);
    const char* Vg = (const char*)(qkv + (size_t)(2 * NH + hd) * S_ * 32);
    const u16* Qg = qkv + (size_t)hd * S_ * 32;
#pragma unroll
    for (int i = 0; i < 4; ++i) {
        const int L = (wid * 4 + i) * 64 + lane, r = L >> 2, c = L & 3;
        int key = q0 - 256 + r; key = key < 0 ? 0 : key;
        glds16(Kg + key * 64 + ((c ^ ((r >> 2) & 3)) * 16), lds + (wid * 4 + i) * 1024);
        glds16(Vg + key * 64 + c * 16, lds + 32768 + (wid * 4 + i) * 1024);
    }
    const int r32 = lane & 31, hh = lane >> 5;
    const bf16x8 qf0 = *(const bf16x8*)(Qg + (size_t)(q0 + wid * 32 + r32) * 32 + hh * 8);
    const bf16x8 qf1 = *(const bf16x8*)(Qg + (size_t)(q0 + wid * 32 + r32) * 32 + 16 + hh * 8);
    WAIT_VM(0);
    BARRIER();
    f32x16 sc[9];
    const int sw = (r32 >> 2) & 3;
    LAS char* kp = lds + (wid * 32 + r32) * 64;
#pragma unroll
    for (int j = 0; j < 9; ++j) {
        const bf16x8 k0 = *(LAS bf16x8*)(kp + j * 2048 + ((hh ^ sw) * 16));
        const bf16x8 k1 = *(LAS bf16x8*)(kp + j * 2048 + (((2 + hh) ^ sw) * 16));
        f32x16 z = {};
        z = MFMA32(k0, qf0, z);
        sc[j] = MFMA32(k1, qf1, z);
    }
    const float NEG = -INFINITY;
#pragma unroll
    for (int r = 0; r < 16; ++r) {
        const int kr = (r & 3) + 8 * (r >> 2) + 4 * hh;
        if (kr <= r32) sc[0][r] = NEG;
        if (kr > r32) sc[8][r] = NEG;
    }
    if (qb == 0) {
#pragma unroll
        for (int j = 0; j < 8; ++j)
            if (wid + j < 8) {
#pragma unroll
                for (int r = 0; r < 16; ++r) sc[j][r] = NEG;
            }
    }
    float m = NEG;
#pragma unroll
    for (int j = 0; j < 9; ++j)
#pragma unroll
        for (int r = 0; r < 16; r += 2) m = __builtin_fmaxf(__builtin_fmaxf(m, sc[j][r]), sc[j][r + 1]);
    {
        auto rr = __builtin_amdgcn_permlane32_swap(__float_as_uint(m), __float_as_uint(m), false, false);
        m = __builtin_fmaxf(__uint_as_float(rr[0]), __uint_as_float(rr[1]));
    }
    float l = 0.f;
    f32x16 oacc = {};
    LAS char* vp = lds + 32768 + (wid * 32 + 4 * hh + ((lane & 15) >> 2)) * 64 + ((lane >> 4) & 1) * 32 + (lane & 3) * 8;
#pragma unroll
    for (int j = 0; j < 9; ++j) {
        unsigned pw[8];
#pragma unroll
        for (int r = 0; r < 16; r += 2) {
            const float p0 = __builtin_amdgcn_exp2f(sc[j][r] - m), p1 = __builtin_amdgcn_exp2f(sc[j][r + 1] - m);
            l += p0 + p1;
            pw[r >> 1] = pack2(p0, p1);
        }
#pragma unroll
        for (int s = 0; s < 2; ++s) {
            const bf16x4 lo = __builtin_amdgcn_ds_read_tr16_b64_v4bf16((LAS bf16x4*)(vp + j * 2048 + s * 1024));
            const bf16x4 hi = __builtin_amdgcn_ds_read_tr16_b64_v4bf16((LAS bf16x4*)(vp + j * 2048 + s * 1024 + 512));
            const bf16x8 vf = __builtin_shufflevector(lo, hi, 0, 1, 2, 3, 4, 5, 6, 7);
            const u32x4 pp = {pw[4 * s], pw[4 * s + 1], pw[4 * s + 2], pw[4 * s + 3]};
            oacc = MFMA32(vf, __builtin_bit_cast(bf16x8, pp), oacc);
        }
    }
    {
        auto rr = __builtin_amdgcn_permlane32_swap(__float_as_uint(l), __float_as_uint(l), false, false);
        l = __uint_as_float(rr[0]) + __uint_as_float(rr[1]);
    }
    const float inv = 1.0f / l;
    unsigned w[8];
#pragma unroll
    for (int g = 0; g < 4; ++g) { w[2 * g] = pack2(oacc[4 * g] * inv, oacc[4 * g + 1] * inv); w[2 * g + 1] = pack2(oacc[4 * g + 2] * inv, oacc[4 * g + 3] * inv); }
    u16* op = o + (size_t)(q0 + wid * 32 + r32) * D_ + hd * 32 + hh * 8;
#pragma unroll
    for (int gp = 0; gp < 2; ++gp) {
        auto a = __builtin_amdgcn_permlane32_swap(w[4 * gp], w[4 * gp + 2], false, false);
        auto b = __builtin_amdgcn_permlane32_swap(w[4 * gp + 1], w[4 * gp + 3], false, false);
        *(u32x4*)(op + gp * 16) = (u32x4){a[0], b[0], a[1], b[1]};
    }
}

__global__ __launch_bounds__(512, 2) void k_attn(const u16* __restrict__ qkv, u16* __restrict__ o) {
    extern __shared__ __attribute__((aligned(16))) char smem[];
    const int tid = threadIdx.x, lane = tid & 63, wid = __builtin_amdgcn_readfirstlane(tid >> 6);
    attn_body((LAS char*)smem, qkv, o, blockIdx.x >> 3, blockIdx.x & 7, wid, lane);
}

constexpr int K4_SB = 256 * 128, K4_NS = 4, K4_LDS = K4_NS * K4_SB;
constexpr int KA = 34, KB1 = F_ / 64 - KA, KB2 = D_ / 64;

__global__ __launch_bounds__(512, 2) void k_gemm2(const u16* __restrict__ x, const u16* __restrict__ w2, const u16* __restrict__ U, const u16* __restrict__ o,
                                                  const u16* __restrict__ wo, const u16* __restrict__ b2, float* part, unsigned* flags, u16* __restrict__ out) {
    extern __shared__ __attribute__((aligned(16))) char smem[];
    LAS char* lds = (LAS char*)smem;
    const int tid = threadIdx.x, lane = tid & 63, wid = __builtin_amdgcn_readfirstlane(tid >> 6);
    const int grp = blockIdx.x & 7, idx = blockIdx.x >> 3;
    const bool partB = grp >= 4;
    const int tm = (grp & 3) * 4 + (idx >> 3), tn = idx & 7, tile = tm * 8 + tn;
    constexpr int SB = K4_SB, NS = K4_NS;
    const int nk1 = partB ? KB1 : KA, k0 = partB ? KA : 0, nk = partB ? KB1 + KB2 : KA;
    if (wid >= 4) {
        const int lw = wid - 4;
        Loader<true, 128, false, 128> L; L.init(lw, lane, S_ * 2, F_ * 2);
        const char* gm = (const char*)w2 + (size_t)k0 * 64 * S_ * 2 + tm * 256; const char* gn = (const char*)U + (size_t)tn * 128 * F_ * 2 + k0 * 128;
        unsigned stepm = 64 * S_ * 2, stepn = 128;
        constexpr int N = L.NPW;
        auto issue = [&](int u, int slot) {
            if (u == nk1) {
                gm = (const char*)o + (size_t)tm * 128 * D_ * 2; gn = (const char*)wo + tn * 256; stepm = 128; stepn = 64 * D_ * 2;
#pragma unroll
                for (int i = 0; i < 4; ++i) { L.om[i] = Op<false, 128>::src_off(lw + 4 * i, lane, D_ * 2); L.on[i] = Op<true, 128>::src_off(lw + 4 * i, lane, D_ * 2); }
            }
            L.issue(gm, gn, lds + slot * SB, lw);
            gm += stepm; gn += stepn;
        };
#pragma unroll
        for (int s = 0; s < NS; ++s) issue(s, s);
        WAIT_VM((NS - 1) * N); BARRIER();
        int slot = 0;
        for (int t = 0; t < nk; ++t) {
            const int rem = nk - (t + 2);
            if (rem >= NS - 2) WAIT_VM((NS - 2) * N); else if (rem == 1) WAIT_VM(N); else WAIT_VM(0);
            BARRIER();
            if (t + NS < nk) issue(t + NS, slot);
            slot = (slot + 1 == NS) ? 0 : slot + 1;
        }
        if (partB && lw == 0) {
            GAS unsigned* f = (GAS unsigned*)flags + tile * 64;
            unsigned spins = 0;
            while ((unsigned)__builtin_amdgcn_readfirstlane(__hip_atomic_load(f, __ATOMIC_RELAXED, __HIP_MEMORY_SCOPE_AGENT)) < 4u) {
                __builtin_amdgcn_s_sleep(1);
                if (++spins > (1u << 22)) break;
            }
            __builtin_amdgcn_fence(__ATOMIC_ACQUIRE, "agent");
            WAIT_VM(0);
        }
        if (partB) BARRIER();
    } else {
        const int wm = wid >> 1, wn = wid & 1;
        const int fr = lane & 15, fq = lane >> 4;
        f32x4 acc[4][4] = {};
        int slot = 0;
        BARRIER();
        consume<true, 128, false, 128, NS, SB, 4, 4>(lds, slot, nk1, acc, lane, wm, wn);
        float* pt = part + (size_t)tile * 16384 + wid * 4096 + lane * 4;
        if (!partB) {
            const __amdgpu_buffer_rsrc_t rs = __builtin_amdgcn_make_buffer_rsrc(part + (size_t)tile * 16384, 0, 65536, 0x00020000);
#pragma unroll
            for (int i = 0; i < 4; ++i)
#pragma unroll
                for (int j = 0; j < 4; ++j)
                    __builtin_amdgcn_raw_buffer_store_b128(__builtin_bit_cast(u32x4, acc[i][j]), rs, (wid * 4096 + (i * 4 + j) * 256 + lane * 4) * 4, 0, 16);
            WAIT_VM(0);
            if (lane == 0) __hip_atomic_fetch_add((GAS unsigned*)flags + tile * 64, 1u, __ATOMIC_RELAXED, __HIP_MEMORY_SCOPE_AGENT);
        } else {
            consume<false, 128, true, 128, NS, SB, 4, 4>(lds, slot, KB2, acc, lane, wm, wn);
            BARRIER();
            const int s0 = tm * 128 + wm * 64, d0 = tn * 128 + wn * 64;
#pragma unroll
            for (int i = 0; i < 4; ++i) {
                const int srow = s0 + i * 16 + fr;
                const float bias = bf2f(b2[srow]);
#pragma unroll
                for (int j = 0; j < 4; ++j) acc[i][j] += *(const f32x4*)(pt + (i * 4 + j) * 256) + bias;
#pragma unroll
                for (int jp = 0; jp < 2; ++jp) {
                    f32x4 lo, hi;
#pragma unroll
                    for (int e = 0; e < 4; ++e) {
                        auto r = __builtin_amdgcn_permlane16_swap(__float_as_uint(acc[i][2 * jp][e]), __float_as_uint(acc[i][2 * jp + 1][e]), false, false);
                        lo[e] = __uint_as_float(r[0]); hi[e] = __uint_as_float(r[1]);
                    }
                    const size_t off = (size_t)srow * D_ + d0 + jp * 32 + (fq & 1) * 16 + (fq >> 1) * 8;
                    const u32x4 xv = *(const u32x4*)(x + off);
                    u32x4 w;
                    w[0] = pack2(lo[0] + __uint_as_float(xv[0] << 16), lo[1] + __uint_as_float(xv[0] & 0xffff0000u));
                    w[1] = pack2(lo[2] + __uint_as_float(xv[1] << 16), lo[3] + __uint_as_float(xv[1] & 0xffff0000u));
                    w[2] = pack2(hi[0] + __uint_as_float(xv[2] << 16), hi[1] + __uint_as_float(xv[2] & 0xffff0000u));
                    w[3] = pack2(hi[2] + __uint_as_float(xv[3] << 16), hi[3] + __uint_as_float(xv[3] & 0xffff0000u));
                    *(u32x4*)(out + off) = w;
                }
            }
        }
    }
}

extern "C" void kernel_launch(void* const* d_in, const int* in_sizes, int n_in, void* d_out, int out_size, void* d_ws, size_t ws_size, hipStream_t stream) {
    const u16* x = (const u16*)d_in[0];
    const u16* wqkv = (const u16*)d_in[1];
    const u16* wo = (const u16*)d_in[2];
    const u16* w1 = (const u16*)d_in[4];
    const u16* b1 = (const u16*)d_in[5];
    const u16* w2 = (const u16*)d_in[6];
    const u16* b2 = (const u16*)d_in[7];
    char* ws = (char*)d_ws;
    unsigned* flags = (unsigned*)(ws + WS_FLAGS);
    u16* xn = (u16*)(ws + WS_XN); u16* h = (u16*)(ws + WS_H); u16* qkv = (u16*)(ws + WS_QKV); u16* U = (u16*)(ws + WS_U); u16* o = (u16*)(ws + WS_O);
    float* part = (float*)(ws + WS_PART);
    (void)hipFuncSetAttribute((const void*)k_gemm1, hipFuncAttributeMaxDynamicSharedMemorySize, K2_LDS);
    (void)hipFuncSetAttribute((const void*)k_gemm2, hipFuncAttributeMaxDynamicSharedMemorySize, K4_LDS);
    (void)hipFuncSetAttribute((const void*)k_attn, hipFuncAttributeMaxDynamicSharedMemorySize, 65536);
    k_ln<<<S_ / 4, 256, 0, stream>>>(x, xn, h, flags);
    k_gemm1<<<256, 512, K2_LDS, stream>>>(xn, h, wqkv, w1, b1, qkv, U);
    k_attn<<<256, 512, 65536, stream>>>(qkv, o);
    k_gemm2<<<256, 512, K4_LDS, stream>>>(x, w2, U, o, wo, b2, part, flags, (u16*)d_out);
}
```

```cpp
#include <hip/hip_runtime.h>
#include <hip/hip_bf16.h>
#include <stdint.h>

#define LAS __attribute__((address_space(3)))
#define GAS __attribute__((address_space(1)))
typedef __bf16 bf16x8 __attribute__((ext_vector_type(8)));
typedef __bf16 bf16x4 __attribute__((ext_vector_type(4)));
typedef __bf16 bf16x2 __attribute__((ext_vector_type(2)));
typedef float f32x2 __attribute__((ext_vector_type(2)));
typedef float f32x4 __attribute__((ext_vector_type(4)));
typedef float f32x16 __attribute__((ext_vector_type(16)));
typedef unsigned u32x4 __attribute__((ext_vector_type(4)));
typedef unsigned u32x2 __attribute__((ext_vector_type(2)));
typedef unsigned short u16;
typedef unsigned long long u64;

constexpr int S_ = 2048, D_ = 1024, F_ = 3584, NH = 32;

constexpr size_t WS_FLAGS = 0;
constexpr size_t WS_STAT  = 1 << 16;
constexpr size_t WS_CS    = WS_STAT + (size_t)S_ * 8;
constexpr size_t WS_H     = 1 << 17;
constexpr size_t WS_QKV   = WS_H   + (size_t)S_ * D_ * 2;
constexpr size_t WS_U     = WS_QKV + (size_t)S_ * 3 * D_ * 2;
constexpr size_t WS_O     = WS_U   + (size_t)D_ * F_ * 2;
constexpr size_t WS_PART  = WS_O   + (size_t)S_ * D_ * 2;
constexpr int FL_E = 0;
constexpr int FL_L = 32;
constexpr int FL_C = 5 * 32;
constexpr int FL_U = 8 * 32;
constexpr int FL_Q = 16 * 32;
constexpr int FL_O = 32 * 32;
constexpr int FL_P = 40 * 32;

constexpr size_t WS_DBG = 200u << 20;
#define STAMPL(kid, i) do {} while (0)
#define STAMP(kid, i) do {} while (0)
#define WAIT_VM(n) asm volatile("s_waitcnt vmcnt(%0)" ::"n"(n) : "memory")
#define WAIT_LGKM0() __builtin_amdgcn_s_waitcnt(0xC07F)
#define CFENCE() asm volatile("" ::: "memory")
#define BARRIER() do { CFENCE(); __builtin_amdgcn_s_barrier(); CFENCE(); } while (0)
#define SCHED0() __builtin_amdgcn_sched_barrier(0)
#define RLX_AGENT __ATOMIC_RELAXED, __HIP_MEMORY_SCOPE_AGENT
constexpr int SC1 = 16;

__device__ __forceinline__ float bf2f(u16 v) { return __uint_as_float((unsigned)v << 16); }
__device__ __forceinline__ float lo16(unsigned v) { return __uint_as_float(v << 16); }
__device__ __forceinline__ float hi16(unsigned v) { return __uint_as_float(v & 0xffff0000u); }
__device__ __forceinline__ unsigned pack2(float lo, float hi) {
    f32x2 v = {lo, hi};
    bf16x2 b = __builtin_convertvector(v, bf16x2);
    return __builtin_bit_cast(unsigned, b);
}
__device__ __forceinline__ float rbf(float x) { return __uint_as_float(pack2(x, 0.f) << 16); }
template <int CTRL, int RMASK> __device__ __forceinline__ float dpp_add(float v) {
    return v + __builtin_bit_cast(float, __builtin_amdgcn_update_dpp(0, __builtin_bit_cast(int, v), CTRL, RMASK, 0xf, false));
}
__device__ __forceinline__ float wave_sum(float v) {
    v = dpp_add<0x111, 0xf>(v); v = dpp_add<0x112, 0xf>(v); v = dpp_add<0x114, 0xf>(v); v = dpp_add<0x118, 0xf>(v);
    v = dpp_add<0x142, 0xa>(v); v = dpp_add<0x143, 0xc>(v);
    return __builtin_bit_cast(float, __builtin_amdgcn_readlane(__builtin_bit_cast(int, v), 63));
}
__device__ __forceinline__ __amdgpu_buffer_rsrc_t rsrc(const void* p, unsigned bytes) { return __builtin_amdgcn_make_buffer_rsrc((void*)p, 0, bytes, 0x00020000); }

enum { ROW = 0, TR = 1, FRAG = 2 };
constexpr int PIECE = 1056, BLK = 8 * PIECE;
template <int T, int X> struct Op {
    static constexpr int BYTES = X * 132;
    __device__ static __forceinline__ unsigned src_off(int i, int lane, unsigned ld, unsigned cb) {
        const int blk = i >> 3, p = i & 7, pos = lane >> 3;
        if (T == TR) {
            if (X == 224 && blk == 3) {
                const int ps = lane >> 2, g = (p & 1) | ((ps >> 3) << 1), r = 32 * (p >> 1) + 8 * g + 4 * ((ps >> 2) & 1) + (ps & 3);
                return (unsigned)r * ld + cb + (unsigned)((lane & 3) * 16);
            }
            const int g = (p >> 2) | (pos & 2), r = 32 * (pos >> 2) + 8 * g + 4 * (pos & 1) + (p & 3);
            return (unsigned)r * ld + cb + (unsigned)((lane & 7) * 16);
        } else if (T == ROW) {
            const int j = ((pos >> 1) & 1) | ((pos & 1) << 1) | (pos & 4);
            return (unsigned)(blk * 64 + j * 8 + p) * ld + (unsigned)((lane & 7) * 16);
        } else {
            return (unsigned)blk * ld + (unsigned)(p * 1024 + lane * 16);
        }
    }
};
template <int AUX> __device__ __forceinline__ void glds16(const char* g, LAS char* l) { __builtin_amdgcn_global_load_lds((const GAS unsigned*)g, (LAS unsigned*)l, 16, 0, AUX); }
__device__ __forceinline__ void glds16_hidden(const char* g, LAS char* l) {
    unsigned keep;
    asm volatile("s_mov_b32 %0, m0\n\ts_mov_b32 m0, %2\n\ts_nop 0\n\tglobal_load_lds_dwordx4 %1, off sc1\n\ts_mov_b32 m0, %0"
                 : "=&s"(keep) : "v"(g), "s"(__builtin_amdgcn_readfirstlane((unsigned)(uintptr_t)l)) : "memory");
}
__device__ __forceinline__ void glds4(const char* g, LAS char* l) { __builtin_amdgcn_global_load_lds((const GAS unsigned*)g, (LAS unsigned*)l, 4, 0, 0); }

template <int T, int X> struct Addr {
    unsigned a, b;
    static constexpr int NV = (X == 224 || X == 192) ? 2 : 1;
    __device__ __forceinline__ void init(unsigned base, int lane, int w) {
        const int g = lane >> 4;
        b = 0;
        if (T == TR) {
            const int q = (lane & 15) >> 2, p = lane & 3;
            a = base + (unsigned)((q | ((g & 1) << 2)) * PIECE + (g >> 1) * 256 + p * 8 + (X == 128 ? w * BLK : w * 64));
            b = base + (unsigned)(3 * BLK + (g & 1) * PIECE + (g >> 1) * 512 + q * 64 + w * 32 + p * 8);
        } else if (T == ROW) {
            const int r = lane & 15;
            a = base + (unsigned)(w * BLK + (r & 7) * PIECE + (r >> 3) * 256 + g * 16);
        } else {
            a = base + (unsigned)(w * 4 * PIECE + lane * 16);
        }
    }
    __device__ __forceinline__ void step(unsigned d) { a += d; if (NV == 2) b += d; }
    __device__ __forceinline__ bf16x8 read(int n, int kk) const {
        if (T == TR) {
            LAS char* p; int t;
            if (X == 224 && n == 6) { p = (LAS char*)(uintptr_t)b + kk * 2 * PIECE; t = 256; }
            else if (X == 192) { p = (LAS char*)(uintptr_t)((n & 1) ? b : a) + kk * 512 + (n >> 1) * BLK; t = 128; }
            else { p = (LAS char*)(uintptr_t)a + kk * 512 + (X == 128 ? n * 32 : (n >> 1) * BLK + (n & 1) * 32); t = 128; }
            const bf16x4 lo = __builtin_amdgcn_ds_read_tr16_b64_v4bf16((LAS bf16x4*)p);
            const bf16x4 hi = __builtin_amdgcn_ds_read_tr16_b64_v4bf16((LAS bf16x4*)(p + t));
            return __builtin_shufflevector(lo, hi, 0, 1, 2, 3, 4, 5, 6, 7);
        } else if (T == ROW) {
            return *(LAS bf16x8*)((LAS char*)(uintptr_t)a + (n & 1) * 128 + (n >> 1) * 512 + kk * 64);
        } else {
            return *(LAS bf16x8*)((LAS char*)(uintptr_t)a + kk * BLK + n * PIECE);
        }
    }
};
template <int FM, int FN> struct Regs { bf16x8 m[FM], n[FN]; };
#define MFMA16(a, b, c) __builtin_amdgcn_mfma_f32_16x16x32_bf16(a, b, c, 0, 0, 0)

template <int TM, int BM, int TN, int BN, int FM, int FN> struct Reader {
    Addr<TM, BM> m; Addr<TN, BN> n;
    static constexpr int NDS = FM * (TM == TR ? 2 : 1) + FN * (TN == TR ? 2 : 1), NV = Addr<TM, BM>::NV + Addr<TN, BN>::NV;
    __device__ __forceinline__ void init(unsigned slot_base, int lane, int wm, int wn) { m.init(slot_base, lane, wm); n.init(slot_base + BM * 132, lane, wn); }
    __device__ __forceinline__ void read(Regs<FM, FN>& R, int kk) const {
#pragma unroll
        for (int i = 0; i < FM; ++i) R.m[i] = m.read(i, kk);
#pragma unroll
        for (int j = 0; j < FN; ++j) R.n[j] = n.read(j, kk);
    }
    __device__ __forceinline__ void step(unsigned d) { m.step(d); n.step(d); }
};
template <int AM, int FM, int FN> __device__ __forceinline__ void mma(f32x4 (&acc)[AM][FN], const Regs<FM, FN>& R) {
    const u32x4 one = {0x3f803f80u, 0x3f803f80u, 0x3f803f80u, 0x3f803f80u};
#pragma unroll
    for (int j = 0; j < FN; ++j) {
#pragma unroll
        for (int i = 0; i < FM; ++i) acc[i][j] = MFMA16(R.n[j], R.m[i], acc[i][j]);
        if (AM > FM && !(j & 1)) acc[AM - 1][j] = MFMA16(R.n[j], __builtin_bit_cast(bf16x8, one), acc[AM - 1][j]);
    }
}
template <int NDS, int NV, int NMF> __device__ __forceinline__ void interleave() {
#pragma unroll
    for (int i = 0; i < NMF; ++i) {
        __builtin_amdgcn_sched_group_barrier(0x8, 1, 0);
        if (i < NDS) __builtin_amdgcn_sched_group_barrier(0x80, 1, 0);
        if (i >= NMF - NV) __builtin_amdgcn_sched_group_barrier(0x2, 1, 0);
    }
}
template <int NS, int SB, class RD, class RDN, int AM, int FM, int FN>
__device__ __forceinline__ void stage_step(int& slot, RD& rd, RDN& rn, Regs<FM, FN>& R0, f32x4 (&acc)[AM][FN]) {
    constexpr int NMF = FM * FN + (AM - FM) * (FN / 2);
    Regs<FM, FN> R1;
    const unsigned d = (slot + 1 == NS) ? (unsigned)(-(NS - 1) * SB) : (unsigned)SB;
    slot = (slot + 1 == NS) ? 0 : slot + 1;
    rd.read(R1, 1);
    rn.step(d);
    mma(acc, R0);
    interleave<RD::NDS, RDN::NV, NMF>();
    SCHED0();
    WAIT_LGKM0();
    BARRIER();
    SCHED0();
    rn.read(R0, 0);
    mma(acc, R1);
    interleave<RDN::NDS, 0, NMF>();
    SCHED0();
}

template <int NM, int NN> struct Loader {
    static constexpr int NPW = NM + NN;
    unsigned om[NM], on[NN];
    template <int T> __device__ __forceinline__ void init_m(int lw, int lane, unsigned ld) {
#pragma unroll
        for (int i = 0; i < NM; ++i) om[i] = Op<T, NM * 32>::src_off(lw + 4 * i, lane, ld, (i >> 1) * 128);
    }
    template <int T> __device__ __forceinline__ void init_n(int lw, int lane, unsigned ld, unsigned c0 = 0, unsigned c1 = 128, unsigned c2 = 256, unsigned c3 = 384) {
#pragma unroll
        for (int i = 0; i < NN; ++i) on[i] = Op<T, NN * 32>::src_off(lw + 4 * i, lane, ld, (i >> 1) == 0 ? c0 : (i >> 1) == 1 ? c1 : (i >> 1) == 2 ? c2 : c3);
    }
    template <int AUX> __device__ __forceinline__ void issue_m(const char* gm, LAS char* dst, int lw) const {
#pragma unroll
        for (int i = 0; i < NM; ++i) glds16<AUX>(gm + om[i], dst + (lw + 4 * i) * PIECE);
    }
    template <int AUX> __device__ __forceinline__ void issue_n(const char* gn, LAS char* dst, int lw) const {
#pragma unroll
        for (int i = 0; i < NN; ++i) glds16<AUX>(gn + on[i], dst + NM * 32 * 132 + (lw + 4 * i) * PIECE);
    }
};
#define LEAD 3
__device__ __forceinline__ int korder(int p, int c, int nk) {
    const int r = (p - c) & 3, p0 = p - r;
    if (r > LEAD || p0 < 0 || p0 + LEAD >= nk) return p;
    return r == 0 ? p + LEAD : p - 1;
}
__device__ __forceinline__ f32x2 gelu2(f32x2 u) {
    const float c1 = -2.0f * 0.7978845608028654f * 1.4426950408889634f, c2 = c1 * 0.044715f;
    const f32x2 z = u * ((u * u) * c2 + c1);
    f32x2 e; e.x = __builtin_amdgcn_exp2f(z.x); e.y = __builtin_amdgcn_exp2f(z.y);
    e = e + 1.0f;
    f32x2 r; r.x = __builtin_amdgcn_rcpf(e.x); r.y = __builtin_amdgcn_rcpf(e.y);
    return u * r;
}
__device__ __forceinline__ u32x4 pair16(unsigned x0, unsigned x1, unsigned y0, unsigned y1) {
    auto a = __builtin_amdgcn_permlane16_swap(x0, y0, false, false);
    auto b = __builtin_amdgcn_permlane16_swap(x1, y1, false, false);
    return (u32x4){a[0], b[0], a[1], b[1]};
}
__device__ __forceinline__ bool flags_set(u64* flags, int idx, u64 tag) {
    const u64 v = __hip_atomic_load((GAS u64*)flags + (idx < 0 ? 0 : idx), RLX_AGENT);
    const bool ok = __builtin_amdgcn_ballot_w64(idx >= 0 && v - 1ull != tag) == 0ull;
    CFENCE();
    return ok;
}
__device__ __forceinline__ void poll_flags(u64* flags, int idx, u64 tag) {
#pragma unroll 1
    for (unsigned spins = 0; spins < (1u << 20); ++spins) {
        if (flags_set(flags, idx, tag)) break;
        __builtin_amdgcn_s_sleep(2);
    }
}
__device__ __forceinline__ void set_flag(u64* flags, int idx, u64 tag) {
    asm volatile("" : "+v"(tag));
    __hip_atomic_store((GAS u64*)flags + idx, tag + 1ull, RLX_AGENT);
}

struct Params {
    const u16 *x, *wqkv, *wo, *w1, *b1, *w2, *b2;
    u16* out;
    char* ws;
    __device__ __forceinline__ u16* h() const { return (u16*)(ws + WS_H); }
    __device__ __forceinline__ u16* qkv() const { return (u16*)(ws + WS_QKV); }
    __device__ __forceinline__ u16* U() const { return (u16*)(ws + WS_U); }
    __device__ __forceinline__ u16* o() const { return (u16*)(ws + WS_O); }
    __device__ __forceinline__ float* stat() const { return (float*)(ws + WS_STAT); }
    __device__ __forceinline__ float* cs() const { return (float*)(ws + WS_CS); }
    __device__ __forceinline__ float* part() const { return (float*)(ws + WS_PART); }
    __device__ __forceinline__ u64* flags() const { return (u64*)(ws + WS_FLAGS); }
};

constexpr int SB_M = (128 + 224) * 132, SB_Q = (128 + 192) * 132, SB_F = 256 * 132, NS_F = 4;
constexpr int LDS_QX = 3 * SB_Q;
constexpr int LDS_AQ = LDS_QX + 5120;
constexpr int LDS_SINK = LDS_AQ + 16384, LDS_CTL = LDS_SINK + 2048 + 256, LDS_BYTES = LDS_CTL + 32;
static_assert(LDS_SINK >= 3 * SB_M + 8192 && LDS_BYTES <= 160 * 1024, "LDS map");
#define KA 20
#define PF2 1
#define WM_HALF 0
constexpr int KW = 8, KB1 = F_ / 64 - KA;

__device__ __forceinline__ void prefetch_next(const Params& P, LAS char* lds, int batch, int lw, int lane) {
    const int piece = batch * 4 + lw;
    const char* src = piece < 7 ? (const char*)P.w2 + ((size_t)piece * 256 + blockIdx.x) * 8192 : (const char*)P.wo + (size_t)blockIdx.x * 8192;
    glds4(src + lane * 128, lds + LDS_SINK + 2048);
}
__device__ __forceinline__ int qkv_head(int tn, int blk) { const int b = tn * 3 + blk, r = b / 24, w = (b % 24) / 8, p = b % 8; return w * NH + r * 16 + p * 2; }
__device__ __forceinline__ int qkv_tile(int which, int hd) { return ((hd >> 4) * 24 + which * 8 + ((hd & 15) >> 1)) / 3; }

__device__ __forceinline__ f32x2 ln_row(const u32x4 a, const u32x4 b, u32x4& oa, u32x4& ob) {
    f32x2 v[8];
#pragma unroll
    for (int i = 0; i < 4; ++i) { v[i] = (f32x2){lo16(a[i]), hi16(a[i])}; v[4 + i] = (f32x2){lo16(b[i]), hi16(b[i])}; }
    const f32x2 s2 = ((v[0] + v[1]) + (v[2] + v[3])) + ((v[4] + v[5]) + (v[6] + v[7]));
    const float mean = wave_sum(s2.x + s2.y) * (1.0f / D_);
    f32x2 q2 = {0.f, 0.f};
#pragma unroll
    for (int i = 0; i < 8; ++i) { const f32x2 d = v[i] - mean; q2 += d * d; }
    const float var = wave_sum(q2.x + q2.y) * (1.0f / D_);
    const float mb = rbf(mean), rs = __builtin_amdgcn_rcpf(rbf(__builtin_amdgcn_sqrtf(rbf(rbf(var) + 1e-5f))));
    const float mb2 = rbf((mean - mb) * rs), rs2 = __builtin_amdgcn_rcpf(rbf(__builtin_amdgcn_sqrtf(rbf(rbf(var * rs * rs) + 1e-5f))));
    const float g = rs * rs2, c = -mb2 * rs2;
#pragma unroll
    for (int i = 0; i < 8; ++i) {
        const f32x2 t = (v[i] - mb) * g + c;
        if (i < 4) oa[i] = pack2(t.x, t.y); else ob[i - 4] = pack2(t.x, t.y);
    }
    return (f32x2){mb, rs};
}

#define MFMA32(a, b, c) __builtin_amdgcn_mfma_f32_32x32x16_bf16(a, b, c, 0, 0, 0)
__device__ __forceinline__ int attn_dep(int hd, int qb, int lane) {
    if (lane >= 12) return -1;
    const int which = lane >> 2, st = 2 * qb - 2 + (lane & 3);
    if (st < 0 || (which == 0 && (lane & 3) < 2)) return -1;
    return FL_Q + st * 32 + qkv_tile(which, hd);
}
template <int NW, int WHICH> __device__ __forceinline__ void attn_load(const Params& P, LAS char* dst, int hd, int qb, int w, int lane) {
    const char* g = (const char*)(P.qkv() + (size_t)(WHICH * NH + hd) * S_ * 32) + (WHICH == 2 ? lane & 3 : (lane & 3) ^ ((lane >> 4) & 3)) * 16;
#pragma unroll
    for (int i = 0; i < (WHICH == 0 ? 16 : 32) / NW; ++i) {
        const int pc = i * NW + w;
        int row = qb * 256 - (WHICH == 0 ? 0 : 256) + pc * 16 + (lane >> 2); row = row < 0 ? 0 : row;
        glds16_hidden(g + row * 64, dst + pc * 1024);
    }
}
__device__ __forceinline__ float attn_scores(LAS char* buf, LAS char* qbuf, int qb, int wid, int lane, unsigned up, f32x16 (&sc)[8]) {
    const int r32 = lane & 31, hh = lane >> 5, sw = (r32 >> 2) & 3;
    LAS char* k0p = buf + (wid * 32 + r32) * 64 + ((hh ^ sw) * 16);
    LAS char* k1p = buf + (wid * 32 + r32) * 64 + (((2 + hh) ^ sw) * 16);
    const bf16x8 qf0 = *(LAS bf16x8*)(qbuf + (wid * 32 + r32) * 64 + ((hh ^ sw) * 16));
    const bf16x8 qf1 = *(LAS bf16x8*)(qbuf + (wid * 32 + r32) * 64 + (((2 + hh) ^ sw) * 16));
    bf16x8 ka[9], kb[9];
#pragma unroll
    for (int j = 0; j < 9; ++j) { const int jj = j == 0 ? 8 : j - 1; ka[j] = *(LAS bf16x8*)(k0p + jj * 2048); kb[j] = *(LAS bf16x8*)(k1p + jj * 2048); }
    const float NEG = -INFINITY;
    const f32x16 zero = {};
    f32x16 s8 = MFMA32(kb[0], qf1, MFMA32(ka[0], qf0, zero));
#pragma unroll
    for (int j = 0; j < 8; ++j) sc[j] = MFMA32(kb[j + 1], qf1, MFMA32(ka[j + 1], qf0, zero));
    if (qb == 0) {
#pragma unroll
        for (int j = 0; j < 8; ++j)
            if (wid + j < 8) {
#pragma unroll
                for (int r = 0; r < 16; ++r) sc[j][r] = NEG;
            }
    }
#pragma unroll
    for (int r = 0; r < 16; ++r) {
        const unsigned mk = (unsigned)__builtin_amdgcn_sbfe((int)up, (r & 3) + 8 * (r >> 2), 1);
        sc[0][r] = __uint_as_float((__float_as_uint(sc[0][r]) & mk) | (__float_as_uint(s8[r]) & ~mk));
    }
    float m0 = NEG, m1 = NEG;
#pragma unroll
    for (int j = 0; j < 8; ++j)
#pragma unroll
        for (int r = 0; r < 16; r += 4) {
            m0 = __builtin_fmaxf(__builtin_fmaxf(m0, sc[j][r]), sc[j][r + 1]);
            m1 = __builtin_fmaxf(__builtin_fmaxf(m1, sc[j][r + 2]), sc[j][r + 3]);
        }
    const float m = __builtin_fmaxf(m0, m1);
    auto rr = __builtin_amdgcn_permlane32_swap(__float_as_uint(m), __float_as_uint(m), false, false);
    return __builtin_fmaxf(__uint_as_float(rr[0]), __uint_as_float(rr[1]));
}
__device__ __forceinline__ void pv_mma(LAS char* vp, int j, const unsigned (&pw)[8], f32x16& oacc) {
#pragma unroll
    for (int s = 0; s < 2; ++s) {
        const bf16x4 lo = __builtin_amdgcn_ds_read_tr16_b64_v4bf16((LAS bf16x4*)(vp + j * 2048 + s * 1024));
        const bf16x4 hi = __builtin_amdgcn_ds_read_tr16_b64_v4bf16((LAS bf16x4*)(vp + j * 2048 + s * 1024 + 512));
        const bf16x8 vf = __builtin_shufflevector(lo, hi, 0, 1, 2, 3, 4, 5, 6, 7);
        const u32x4 pp = {pw[4 * s], pw[4 * s + 1], pw[4 * s + 2], pw[4 * s + 3]};
        oacc = MFMA32(vf, __builtin_bit_cast(bf16x8, pp), oacc);
    }
}
template <int J0, int J1> __device__ __forceinline__ void attn_pv(LAS char* buf, int wid, int lane, unsigned up, f32x16 (&sc)[8], float m, f32x2& l, f32x16& oacc) {
    const int hh = lane >> 5;
    LAS char* vp = buf + (wid * 32 + 4 * hh + ((lane & 15) >> 2)) * 64 + ((lane >> 4) & 1) * 32 + (lane & 3) * 8;
#pragma unroll
    for (int j = J0; j < J1; ++j) {
        unsigned pw[8];
#pragma unroll
        for (int r = 0; r < 16; r += 2) {
            const f32x2 d = (f32x2){sc[j][r], sc[j][r + 1]} - m;
            const f32x2 p = {__builtin_amdgcn_exp2f(d.x), __builtin_amdgcn_exp2f(d.y)};
            l += p;
            pw[r >> 1] = pack2(p.x, p.y);
        }
        if (j == 0) {
            unsigned p8[8];
#pragma unroll
            for (int i = 0; i < 8; ++i) {
                const int kr = ((2 * i) & 3) + 8 * (i >> 1);
                const unsigned mk = ((unsigned)__builtin_amdgcn_sbfe((int)up, kr, 1) & 0xffffu) | ((unsigned)__builtin_amdgcn_sbfe((int)up, kr + 1, 1) & 0xffff0000u);
                p8[i] = pw[i] & ~mk; pw[i] &= mk;
            }
            pv_mma(vp, 8, p8, oacc);
        }
        pv_mma(vp, j, pw, oacc);
    }
}
__device__ __forceinline__ void attn_out(const Params& P, int hd, int qb, int wid, int lane, f32x2 l2, const f32x16& oacc) {
    const int r32 = lane & 31, hh = lane >> 5;
    float l = l2.x + l2.y;
    {
        auto rr = __builtin_amdgcn_permlane32_swap(__float_as_uint(l), __float_as_uint(l), false, false);
        l = __uint_as_float(rr[0]) + __uint_as_float(rr[1]);
    }
    const float inv = 1.0f / l;
    unsigned w[8];
#pragma unroll
    for (int g = 0; g < 4; ++g) { w[2 * g] = pack2(oacc[4 * g] * inv, oacc[4 * g + 1] * inv); w[2 * g + 1] = pack2(oacc[4 * g + 2] * inv, oacc[4 * g + 3] * inv); }
    const __amdgpu_buffer_rsrc_t rs = rsrc(P.o(), (unsigned)S_ * D_ * 2);
    const unsigned off = (unsigned)((hd * (S_ / 16) + qb * 16 + wid * 2 + (r32 >> 4)) * 1024 + (hh * 16 + (r32 & 15)) * 16);
#pragma unroll
    for (int gp = 0; gp < 2; ++gp) {
        auto a = __builtin_amdgcn_permlane32_swap(w[4 * gp], w[4 * gp + 2], false, false);
        auto b = __builtin_amdgcn_permlane32_swap(w[4 * gp + 1], w[4 * gp + 3], false, false);
        __builtin_amdgcn_raw_buffer_store_b128((u32x4){a[0], b[0], a[1], b[1]}, rs, off + gp * 512, 0, SC1);
    }
}

__global__ __launch_bounds__(512, 2) void k_main(const Params P) {
    extern __shared__ __attribute__((aligned(16))) char smem[];
    LAS char* lds = (LAS char*)smem;
    const int tid = threadIdx.x, lane = tid & 63, wid = __builtin_amdgcn_readfirstlane(tid >> 6);
    const int grp = blockIdx.x & 7, idx = blockIdx.x >> 3;
    const bool loader = wid >= 4;
    const int lw = wid - 4, wm = wid >> 1, wn = wid & 1;
    const unsigned ring0 = (unsigned)(uintptr_t)lds;
    const int pf_at = 4 + (idx % 12);
    const int i16 = idx & 15;
    const bool isM = idx < 16;
    const int fm = (grp >> 1) * 4 + (i16 >> 2), fn = (grp & 1) * 4 + (i16 & 3);
    const int um = (grp & 1) * 4 + (i16 >> 2), un = (grp >> 1) * 4 + (i16 & 3);
    const int hd = grp * 2 + (i16 >> 3), qb = i16 & 7;
    STAMP(1, 0);
    const u64 tag = __hip_atomic_load((GAS u64*)P.flags() + FL_E, RLX_AGENT);

    if (tid < 8) *(LAS unsigned*)(lds + LDS_CTL + tid * 4) = 0u;
    if (isM) {
        constexpr int NK = S_ / 64, SB = SB_M;
        const int tm = um, tn = un, mi = blockIdx.x;
        const char* gm = (const char*)P.h() + tm * 256; const char* gn = (const char*)P.w1 + tn * 448;
        const int cls = (tm + tn) & 3;
        Loader<4, 7> L;
        {
            const int row = mi * 16 + wid * 2;
            const u32x4* px = (const u32x4*)(P.x + (size_t)row * D_);
            u32x4 a0 = px[lane], b0 = px[64 + lane], a1 = px[128 + lane], b1 = px[192 + lane];
            SCHED0();
            if (loader) {
                const unsigned c0 = (tn & 1) * 64;
                L.init_m<TR>(lw, lane, D_ * 2);
                L.init_n<TR>(lw, lane, F_ * 2, c0, c0 + 128, c0 + 256, (tn & 1) ? 0 : 384);
                asm volatile("" : "+v"(a0), "+v"(b0), "+v"(a1), "+v"(b1));
                L.issue_n<0>(gn + (size_t)korder(0, cls, NK) * (64 * F_ * 2), lds, lw);
            }
            STAMPL(0, 0);
            const __amdgpu_buffer_rsrc_t rh = rsrc(P.h(), (unsigned)S_ * D_ * 2);
            u32x4 oa0, ob0, oa1, ob1;
            const f32x2 s0 = ln_row(a0, b0, oa0, ob0), s1 = ln_row(a1, b1, oa1, ob1);
            __builtin_amdgcn_raw_buffer_store_b128(oa0, rh, (unsigned)(row * 2048 + lane * 16), 0, SC1);
            __builtin_amdgcn_raw_buffer_store_b128(ob0, rh, (unsigned)(row * 2048 + 1024 + lane * 16), 0, SC1);
            __builtin_amdgcn_raw_buffer_store_b128(oa1, rh, (unsigned)(row * 2048 + 2048 + lane * 16), 0, SC1);
            __builtin_amdgcn_raw_buffer_store_b128(ob1, rh, (unsigned)(row * 2048 + 3072 + lane * 16), 0, SC1);
            if (lane == 0) __builtin_amdgcn_raw_buffer_store_b128(__builtin_bit_cast(u32x4, (f32x4){s0.x, s0.y, s1.x, s1.y}), rsrc(P.stat(), S_ * 8), (unsigned)(row * 8), 0, SC1);
            SCHED0();
            STAMPL(0, 1);
        }
        if (loader) {
            WAIT_VM(0);
            BARRIER();
            STAMPL(0, 2);
            if (tid == 256 + 64) set_flag(P.flags(), FL_L + mi, tag);
#pragma unroll
            for (int u = 1; u < 3; ++u) L.issue_n<0>(gn + (size_t)korder(u, cls, NK) * (64 * F_ * 2), lds + u * SB, lw);
            if (lw == 0) { poll_flags(P.flags(), FL_L + lane, tag); poll_flags(P.flags(), FL_L + 64 + lane, tag); }
            STAMPL(0, 3);
            BARRIER();
#pragma unroll
            for (int u = 0; u < 3; ++u) L.issue_m<SC1>(gm + (size_t)korder(u, cls, NK) * (64 * D_ * 2), lds + u * SB, lw);
            WAIT_VM(8); BARRIER();
            int slot = 0, t = 0;
            auto issue = [&]() {
                const int k = korder(t + 3, cls, NK);
                L.issue_m<SC1>(gm + (size_t)k * (64 * D_ * 2), lds + slot * SB, lw);
                L.issue_n<0>(gn + (size_t)k * (64 * F_ * 2), lds + slot * SB, lw);
                slot = (slot == 2) ? 0 : slot + 1;
            };
            WAIT_VM(4); BARRIER(); issue(); ++t;
#pragma unroll 1
            for (int seg = 0; seg < 3; ++seg) {
                const int end = seg == 0 ? pf_at : seg == 1 ? pf_at + 12 : NK - 3;
#pragma unroll 1
                for (; t < end; ++t) { WAIT_VM(L.NPW); BARRIER(); issue(); }
                if (PF2 && seg < 2) prefetch_next(P, lds, seg, lw, lane);
            }
            WAIT_VM(L.NPW); BARRIER();
            WAIT_VM(0); BARRIER();
            BARRIER();
            __builtin_amdgcn_s_waitcnt(0x0F70);
        } else {
            const int fr = lane & 15, fq = lane >> 4;
            const int fb = tn * 224 + ((tn & 1) ? 32 : 0) + wn * 32, fh = tn * 224 + ((tn & 1) ? 0 : 192) + wn * 16;
            WAIT_VM(0);
            WAIT_LGKM0();
            BARRIER();
            f32x4 acc[4][7];
#pragma unroll
            for (int j = 0; j < 7; ++j) {
                const u32x2 bb = *(const u32x2*)(P.b1 + (j < 6 ? fb + (j >> 1) * 64 + (j & 1) * 16 : fh) + fq * 4);
                const f32x4 bv = {lo16(bb[0]), hi16(bb[0]), lo16(bb[1]), hi16(bb[1])};
#pragma unroll
                for (int i = 0; i < 4; ++i) acc[i][j] = bv;
            }
            Reader<TR, 128, TR, 224, 4, 7> rd; rd.init(ring0, lane, wm, wn);
            Regs<4, 7> R0;
            int slot = 0;
            BARRIER();
            __builtin_amdgcn_s_waitcnt(0x0070);
            BARRIER();
            STAMP(1, 1);
            rd.read(R0, 0);
            for (int t = 0; t < NK / 4; ++t) {
                stage_step<3, SB>(slot, rd, rd, R0, acc); stage_step<3, SB>(slot, rd, rd, R0, acc);
                stage_step<3, SB>(slot, rd, rd, R0, acc); stage_step<3, SB>(slot, rd, rd, R0, acc);
            }
            STAMP(1, 2);
            const __amdgpu_buffer_rsrc_t rs = rsrc(P.U(), (unsigned)D_ * F_ * 2);
            const unsigned df = (unsigned)(tm * 8 + wm * 4);
            const unsigned ob = (unsigned)((fb >> 5) * 64 + df) * 1024u + (unsigned)((((fq & 1) * 2 + (fq >> 1)) * 16 + fr) * 16);
            const unsigned oh = (unsigned)((fh >> 5) * 64 + df + (fq & 1)) * 1024u + (unsigned)(((wn * 2 + (fq >> 1)) * 16 + fr) * 16);
            unsigned pk[4][7][2];
#pragma unroll
            for (int i = 0; i < 4; ++i) {
#pragma unroll
                for (int j = 0; j < 7; ++j) {
                    const f32x2 a = gelu2((f32x2){acc[i][j][0], acc[i][j][1]}), b = gelu2((f32x2){acc[i][j][2], acc[i][j][3]});
                    pk[i][j][0] = pack2(a.x, a.y); pk[i][j][1] = pack2(b.x, b.y);
                }
#pragma unroll
                for (int jp = 0; jp < 3; ++jp)
                    __builtin_amdgcn_raw_buffer_store_b128(pair16(pk[i][2 * jp][0], pk[i][2 * jp][1], pk[i][2 * jp + 1][0], pk[i][2 * jp + 1][1]), rs, ob + i * 1024 + jp * 2 * 65536, 0, SC1);
                if (i & 1)
                    __builtin_amdgcn_raw_buffer_store_b128(pair16(pk[i - 1][6][0], pk[i - 1][6][1], pk[i][6][0], pk[i][6][1]), rs, oh + (i - 1) * 1024, 0, SC1);
                SCHED0();
            }
            STAMP(1, 3);
        }
    } else {
        constexpr int NK = D_ / 64, SB = SB_Q;
        const int tm = fm;
        if (loader) {
            Loader<4, 6> L; L.init_m<ROW>(lw, lane, D_ * 2);
            L.init_n<TR>(lw, lane, 3 * D_ * 2, qkv_head(fn, 0) * 64, qkv_head(fn, 1) * 64, qkv_head(fn, 2) * 64);
            const char* gm = (const char*)P.x + (size_t)tm * 128 * D_ * 2; const char* gn = (const char*)P.wqkv;
            int slot = 0;
            auto issue = [&](int u) {
                const int k = korder(u & (NK - 1), (tm + fn) & 3, NK);
                L.issue_m<0>(gm + k * 128, lds + slot * SB, lw);
                L.issue_n<0>(gn + (size_t)k * (64 * 3 * D_ * 2) + (u >= NK ? 16 * 64 : 0), lds + slot * SB, lw);
                slot = (slot == 2) ? 0 : slot + 1;
            };
            issue(0); issue(1);
            WAIT_VM(L.NPW); BARRIER();
            issue(2);
            int t = 0;
#pragma unroll 1
            for (int seg = 0; seg < 4; ++seg) {
                const int end = seg == 0 ? 12 : seg == 1 ? 13 + (idx & 7) : seg == 2 ? 21 + (idx & 7) : 2 * NK - 3;
#pragma unroll 1
                for (; t < end; ++t) { WAIT_VM(L.NPW); BARRIER(); issue(t + 3); }
                if (seg == 0) {
                    if (lw == 0) {
                        poll_flags(P.flags(), lane < 8 ? FL_L + tm * 8 + lane : -1, tag);
                        glds16<SC1>((const char*)P.stat() + tm * 1024 + lane * 16, lds + LDS_QX);
                    }
                } else if (PF2 && seg < 3) prefetch_next(P, lds, seg - 1, lw, lane);
            }
            poll_flags(P.flags(), attn_dep(hd, qb, lane), tag);
            WAIT_VM(0);
            BARRIER();
            attn_load<4, 0>(P, lds + LDS_AQ, hd, qb, lw, lane);
            attn_load<4, 1>(P, lds + 2 * SB, hd, qb, lw, lane);
            BARRIER();
            attn_load<4, 2>(P, lds, hd, qb, lw, lane);
            BARRIER();
            WAIT_VM(0);
            __builtin_amdgcn_s_waitcnt(0x0F70);
        } else {
            const int fr = lane & 15, fq = lane >> 4;
            Reader<ROW, 128, TR, 192, 4, 6> rd; rd.init(ring0, lane, wm, wn);
            rd.n.b = rd.n.a + 32 - wm * 32; rd.n.a += wm * 32;
            Regs<4, 6> R0;
            int slot = 0;
            __builtin_amdgcn_s_waitcnt(0x0070);
            BARRIER();
            STAMP(1, 1);
            rd.read(R0, 0);
            const __amdgpu_buffer_rsrc_t rs = rsrc(P.qkv(), (unsigned)S_ * 3 * D_ * 2);
#pragma unroll 1
            for (int rnd = 0; rnd < 2; ++rnd) {
                f32x4 acc[5][6] = {};
                for (int t = 0; t < NK / 4; ++t) {
                    if (rnd == 1 && t == 1) WAIT_VM(0);
                    if (rnd == 1 && t == 2 && tid == 0) set_flag(P.flags(), FL_Q + tm * 32 + fn, tag);
                    stage_step<3, SB>(slot, rd, rd, R0, acc); stage_step<3, SB>(slot, rd, rd, R0, acc);
                    stage_step<3, SB>(slot, rd, rd, R0, acc); stage_step<3, SB>(slot, rd, rd, R0, acc);
                }
                STAMP(1, 2);
                LAS char* ex = lds + LDS_QX + 1024 + rnd * 1024;
                if (fr == 0) {
#pragma unroll
                    for (int jp = 0; jp < 3; ++jp) *(LAS f32x4*)(ex + wid * 256 + jp * 64 + fq * 16) = acc[4][2 * jp];
                }
                WAIT_LGKM0();
                CFENCE();
                __hip_atomic_store((LAS unsigned*)(lds + LDS_CTL + wid * 4), (unsigned)(rnd + 1), __ATOMIC_RELAXED, __HIP_MEMORY_SCOPE_WORKGROUP);
                const int s0 = tm * 128 + wm * 64;
                f32x2 st[4];
#pragma unroll
                for (int i = 0; i < 4; ++i) st[i] = *(LAS f32x2*)(lds + LDS_QX + (wm * 64 + i * 16 + fr) * 8);
                while (__builtin_amdgcn_readfirstlane(__hip_atomic_load((LAS unsigned*)(lds + LDS_CTL + (wid ^ 2) * 4), __ATOMIC_RELAXED, __HIP_MEMORY_SCOPE_WORKGROUP)) <= rnd) {}
                CFENCE();
#pragma unroll
                for (int jp = 0; jp < 3; ++jp) {
                    const int gh = qkv_head(fn, jp) + rnd * 16 + wn;
                    const float sc = gh < NH ? 0.17677669529663687f * 1.4426950408889634f : 1.0f;
                    const f32x4 ca = acc[4][2 * jp], cb = *(LAS f32x4*)(ex + (wid ^ 2) * 256 + jp * 64 + fq * 16);
#pragma unroll
                    for (int i = 0; i < 4; ++i) {
                        const float r = st[i].y * sc, mr = -st[i].x * r;
                        const f32x4 a = acc[i][2 * jp] * r + ca * mr, b = acc[i][2 * jp + 1] * r + cb * mr;
                        __builtin_amdgcn_raw_buffer_store_b128(pair16(pack2(a[0], a[1]), pack2(a[2], a[3]), pack2(b[0], b[1]), pack2(b[2], b[3])), rs,
                                                               (unsigned)(((gh * S_ + s0 + i * 16 + fr) * 32 + ((fq & 1) ^ wm) * 16 + (fq >> 1) * 8) * 2), 0, SC1);
                    }
                }
            }
            STAMP(1, 3);
        }
        WAIT_LGKM0();
        BARRIER();
#pragma unroll 1
        for (int it = 0; it < 2; ++it) {
            LAS char* kbuf = lds + (it ? SB : 2 * SB);
            LAS char* buf = lds + (it ? 2 * SB : 0);
            STAMP(2, 2 * it);
            f32x16 sc[8];
            int ln = lane;
            asm volatile("" : "+v"(ln));
            const int dg = (ln & 31) - 4 * (ln >> 5);
            const unsigned up = dg < 0 ? ~0u : ~1u << dg;
            const float m = attn_scores(kbuf, lds + LDS_AQ, qb, wid, ln, up, sc);
            WAIT_VM(0);
            BARRIER();
            if (tid == 0) set_flag(P.flags(), it == 0 ? FL_Q + tm * 32 + fn + 8 : FL_O + qb * 32 + hd, tag);
            STAMP(2, 2 * it + 1);
            f32x2 l = {0.f, 0.f};
            f32x16 oacc = {};
            attn_pv<0, 4>(buf, wid, ln, up, sc, m, l, oacc);
            if (it == 0) {
                if (wid == 4) poll_flags(P.flags(), attn_dep(hd + 16, qb, ln), tag);
                BARRIER();
                attn_load<8, 0>(P, lds + LDS_AQ, hd + 16, qb, wid, ln);
                attn_load<8, 1>(P, lds + SB, hd + 16, qb, wid, ln);
                attn_load<8, 2>(P, lds + 2 * SB, hd + 16, qb, wid, ln);
            }
            attn_pv<4, 8>(buf, wid, ln, up, sc, m, l, oacc);
            attn_out(P, hd + 16 * it, qb, wid, ln, l, oacc);
            if (it == 0) WAIT_VM(2); else WAIT_VM(0);
            WAIT_LGKM0();
            BARRIER();
        }
        if (tid == 0) set_flag(P.flags(), FL_O + qb * 32 + hd + 16, tag);
        STAMP(2, 4);
    }

    {
        constexpr int SB = SB_F, NS = NS_F;
        const bool fin = isM;
        const int tm = fm, tn = fn, tile = tm * 8 + tn, cls = (tm + tn) & 3;
        const int wh = isM ? WM_HALF : 1 - WM_HALF;
        const int nf = fin ? KB1 : KA, k0 = fin ? KA : 0, nk = KW + nf;
        if (loader) {
            Loader<4, 4> L; L.init_m<FRAG>(lw, lane, (S_ / 16) * 1024); L.init_n<TR>(lw, lane, D_ * 2);
            const char* go = (const char*)P.o() + (size_t)wh * KW * (2 * (S_ / 16) * 1024) + tm * 8192;
            const char* gw = (const char*)P.wo + (size_t)wh * KW * (64 * D_ * 2) + tn * 256;
            const char* gm = (const char*)P.w2 + (size_t)k0 * 64 * S_ * 2 + tm * 256; const char* gn = (const char*)P.U() + (size_t)k0 * 2 * 65536 + tn * 8192;
#pragma unroll
            for (int u = 0; u < NS; ++u) L.issue_n<0>(gw + (size_t)korder(u, cls, KW) * (64 * D_ * 2), lds + u * SB, lw);
            poll_flags(P.flags(), lane < 16 ? FL_O + (tm >> 1) * 32 + wh * 16 + lane : -1, tag);
            if (lw == 0) asm volatile("ds_write_b32 %0, %1" ::"v"(ring0 + LDS_CTL + 16), "v"(1u) : "memory");
#pragma unroll
            for (int u = 0; u < NS; ++u) L.issue_m<SC1>(go + (size_t)korder(u, cls, KW) * (2 * (S_ / 16) * 1024), lds + u * SB, lw);
            WAIT_VM(12); BARRIER();
            const int ulo = (k0 * 64) / 224, uhi = ((k0 + nf) * 64 - 1) / 224;
            const int udep = (lane >= ulo && lane <= uhi) ? FL_U + tn * 32 + lane : -1;
#pragma unroll
            for (int t = 0; t < KW - NS; ++t) {
                if (t == 0) WAIT_VM(8); else if (t == 1) WAIT_VM(12); else WAIT_VM(16);
                BARRIER();
                const int k = korder(t + NS, cls, KW);
                L.issue_m<SC1>(go + (size_t)k * (2 * (S_ / 16) * 1024), lds + t * SB, lw);
                L.issue_n<0>(gw + (size_t)k * (64 * D_ * 2), lds + t * SB, lw);
            }
            L.init_m<TR>(lw, lane, S_ * 2); L.init_n<FRAG>(lw, lane, 65536);
            int nxt = KW;
            auto issue = [&]() {
                const int k = korder(nxt - KW, cls, nf);
                LAS char* dst = lds + (nxt & (NS - 1)) * SB;
                L.issue_m<0>(gm + (size_t)k * (64 * S_ * 2), dst, lw);
                L.issue_n<SC1>(gn + (size_t)k * (2 * 65536), dst, lw);
                ++nxt;
            };
            bool uready = false;
#pragma unroll 1
            for (int t = KW - NS; t < KW; ++t) {
                if (t == KW - 1 && !uready) { poll_flags(P.flags(), udep, tag); uready = true; issue(); issue(); issue(); }
                if (nxt - (t + 2) >= 2) WAIT_VM(16); else WAIT_VM(0);
                BARRIER();
                if (!uready) uready = flags_set(P.flags(), udep, tag);
                if (uready) while (nxt <= t + NS) issue();
            }
#pragma unroll 1
            for (int t = KW; t < nk - NS; ++t) { WAIT_VM(16); BARRIER(); issue(); }
            WAIT_VM(16); BARRIER();
            WAIT_VM(8);
            if (fin && lw == 0) poll_flags(P.flags(), lane == 0 ? FL_P + tile * 32 : -1, tag);
            BARRIER();
            WAIT_VM(0); BARRIER();
            BARRIER();
            if (!fin) BARRIER();
            if (blockIdx.x == 0 && lw == 0) {
#pragma unroll 1
                for (int i = 0; i < 4; ++i) poll_flags(P.flags(), FL_O + i * 64 + lane, tag);
                if (lane == 0) set_flag(P.flags(), FL_E, tag);
            }
        } else {
            const int fr = lane & 15, fq = lane >> 4;
            const int s0 = tm * 128 + wm * 64, d0 = tn * 128 + wn * 64;
            f32x4 acc[4][4] = {};
            auto publish_u = [&]() {
                WAIT_VM(0);
                if (lane == 0 && __hip_atomic_fetch_add((LAS unsigned*)(lds + LDS_CTL + 20), 1u, __ATOMIC_RELAXED, __HIP_MEMORY_SCOPE_WORKGROUP) == 3u)
                    set_flag(P.flags(), FL_U + um * 32 + un, tag);
            };
            bool upending = isM;
            if (isM && __builtin_amdgcn_readfirstlane(__hip_atomic_load((LAS unsigned*)(lds + LDS_CTL + 16), __ATOMIC_RELAXED, __HIP_MEMORY_SCOPE_WORKGROUP)) == 0u) { publish_u(); upending = false; }
            STAMP(3, 0);
            Reader<FRAG, 128, TR, 128, 4, 4> r2; r2.init(ring0, lane, wm, wn);
            Regs<4, 4> R0;
            int slot = 0;
            WAIT_LGKM0();
            BARRIER();
            STAMP(3, 1);
            r2.read(R0, 0);
            stage_step<NS, SB>(slot, r2, r2, R0, acc); stage_step<NS, SB>(slot, r2, r2, R0, acc);
            if (upending) publish_u();
            stage_step<NS, SB>(slot, r2, r2, R0, acc);
            stage_step<NS, SB>(slot, r2, r2, R0, acc); stage_step<NS, SB>(slot, r2, r2, R0, acc);
            stage_step<NS, SB>(slot, r2, r2, R0, acc); stage_step<NS, SB>(slot, r2, r2, R0, acc);
            Reader<TR, 128, FRAG, 128, 4, 4> r1; r1.init(ring0 + slot * SB, lane, wm, wn);
            stage_step<NS, SB>(slot, r2, r1, R0, acc);
            STAMP(3, 4);
            const int n1 = nf - 2;
            for (int t = 0; t < n1 / 4; ++t) {
                stage_step<NS, SB>(slot, r1, r1, R0, acc); stage_step<NS, SB>(slot, r1, r1, R0, acc);
                stage_step<NS, SB>(slot, r1, r1, R0, acc); stage_step<NS, SB>(slot, r1, r1, R0, acc);
            }
            for (int t = 0; t < (n1 & 3); ++t) stage_step<NS, SB>(slot, r1, r1, R0, acc);
            const __amdgpu_buffer_rsrc_t rp = rsrc(P.part() + (size_t)tile * 16384, 65536);
            u32x4 ex[4][4];
            if (fin) {
#pragma unroll
                for (int i = 0; i < 4; ++i)
#pragma unroll
                    for (int j = 0; j < 4; ++j) ex[i][j] = __builtin_amdgcn_raw_buffer_load_b128(rp, (wid * 4096 + (i * 4 + j) * 256 + lane * 4) * 4, 0, SC1);
            } else {
#pragma unroll
                for (int i = 0; i < 4; ++i) {
#pragma unroll
                    for (int j = 0; j < 4; ++j) { const u32x2 xv = *(const u32x2*)(P.x + (size_t)(s0 + i * 16 + fr) * D_ + d0 + j * 16 + fq * 4); ex[i][j][0] = xv[0]; ex[i][j][1] = xv[1]; }
                    ex[i][0][2] = P.b2[s0 + i * 16 + fr];
                }
            }
            SCHED0();
            stage_step<NS, SB>(slot, r1, r1, R0, acc); stage_step<NS, SB>(slot, r1, r1, R0, acc);
            STAMP(3, 2);
            if (!fin) {
#pragma unroll
                for (int i = 0; i < 4; ++i)
#pragma unroll
                    for (int j = 0; j < 4; ++j) {
                        const f32x4 v = acc[i][j] + (f32x4){lo16(ex[i][j][0]), hi16(ex[i][j][0]), lo16(ex[i][j][1]), hi16(ex[i][j][1])} + lo16(ex[i][0][2]);
                        __builtin_amdgcn_raw_buffer_store_b128(__builtin_bit_cast(u32x4, v), rp, (wid * 4096 + (i * 4 + j) * 256 + lane * 4) * 4, 0, SC1);
                    }
                WAIT_VM(0);
                BARRIER();
                if (tid == 0) set_flag(P.flags(), FL_P + tile * 32, tag);
            } else {
#pragma unroll
                for (int i = 0; i < 4; ++i) {
#pragma unroll
                    for (int j = 0; j < 4; ++j) acc[i][j] += __builtin_bit_cast(f32x4, ex[i][j]);
                    u16* rowp = P.out + (size_t)(s0 + i * 16 + fr) * D_ + d0 + (fq & 1) * 16 + (fq >> 1) * 8;
#pragma unroll
                    for (int jp = 0; jp < 2; ++jp) {
                        const f32x4 a = acc[i][2 * jp], b = acc[i][2 * jp + 1];
                        *(u32x4*)(rowp + jp * 32) = pair16(pack2(a[0], a[1]), pack2(a[2], a[3]), pack2(b[0], b[1]), pack2(b[2], b[3]));
                    }
                }
            }
            STAMP(3, 3);
        }
    }
}

extern "C" void kernel_launch(void* const* d_in, const int* in_sizes, int n_in, void* d_out, int out_size, void* d_ws, size_t ws_size, hipStream_t stream) {
    char* ws = (char*)d_ws;
    Params P;
    P.x = (const u16*)d_in[0]; P.wqkv = (const u16*)d_in[1]; P.wo = (const u16*)d_in[2];
    P.w1 = (const u16*)d_in[4]; P.b1 = (const u16*)d_in[5]; P.w2 = (const u16*)d_in[6]; P.b2 = (const u16*)d_in[7];
    P.out = (u16*)d_out; P.ws = ws;
    (void)hipFuncSetAttribute((const void*)k_main, hipFuncAttributeMaxDynamicSharedMemorySize, LDS_BYTES);
    k_main<<<256, 512, LDS_BYTES, stream>>>(P);
}
```

```cpp
#include <hip/hip_runtime.h>
#include <hip/hip_bf16.h>
#include <stdint.h>

#define LAS __attribute__((address_space(3)))
#define GAS __attribute__((address_space(1)))
typedef __bf16 bf16x8 __attribute__((ext_vector_type(8)));
typedef __bf16 bf16x4 __attribute__((ext_vector_type(4)));
typedef __bf16 bf16x2 __attribute__((ext_vector_type(2)));
typedef float f32x2 __attribute__((ext_vector_type(2)));
typedef float f32x4 __attribute__((ext_vector_type(4)));
typedef float f32x16 __attribute__((ext_vector_type(16)));
typedef unsigned u32x4 __attribute__((ext_vector_type(4)));
typedef unsigned u32x2 __attribute__((ext_vector_type(2)));
typedef unsigned short u16;
typedef unsigned long long u64;

constexpr int S_ = 2048, D_ = 1024, F_ = 3584, NH = 32;

constexpr size_t WS_FLAGS = 0;
constexpr size_t WS_STAT  = 1 << 16;
constexpr size_t WS_CS    = WS_STAT + (size_t)S_ * 8;
constexpr size_t WS_H     = 1 << 17;
constexpr size_t WS_QKV   = WS_H   + (size_t)S_ * D_ * 2;
constexpr size_t WS_U     = WS_QKV + (size_t)S_ * 3 * D_ * 2;
constexpr size_t WS_O     = WS_U   + (size_t)D_ * F_ * 2;
constexpr size_t WS_PART  = WS_O   + (size_t)S_ * D_ * 2;
constexpr int FL_E = 0;
constexpr int FL_L = 32;
constexpr int FL_C = 5 * 32;
constexpr int FL_U = 8 * 32;
constexpr int FL_Q = 16 * 32;
constexpr int FL_O = 32 * 32;
constexpr int FL_P = 40 * 32;

constexpr size_t WS_DBG = 200u << 20;
#define STAMP_DECL
#define STAMP(kid, i) do {} while (0)
#define STAMPL(kid, i) do {} while (0)
#define STAMP_OUT() do {} while (0)
#define WAIT_VM(n) asm volatile("s_waitcnt vmcnt(%0)" ::"n"(n) : "memory")
#define WAIT_LGKM0() __builtin_amdgcn_s_waitcnt(0xC07F)
#define CFENCE() asm volatile("" ::: "memory")
#define BARRIER() do { CFENCE(); __builtin_amdgcn_s_barrier(); CFENCE(); } while (0)
#define SCHED0() __builtin_amdgcn_sched_barrier(0)
#define RLX_AGENT __ATOMIC_RELAXED, __HIP_MEMORY_SCOPE_AGENT
constexpr int SC1 = 16;

__device__ __forceinline__ float bf2f(u16 v) { return __uint_as_float((unsigned)v << 16); }
__device__ __forceinline__ float lo16(unsigned v) { return __uint_as_float(v << 16); }
__device__ __forceinline__ float hi16(unsigned v) { return __uint_as_float(v & 0xffff0000u); }
__device__ __forceinline__ unsigned pack2(float lo, float hi) {
    f32x2 v = {lo, hi};
    bf16x2 b = __builtin_convertvector(v, bf16x2);
    return __builtin_bit_cast(unsigned, b);
}
__device__ __forceinline__ float rbf(float x) { return __uint_as_float(pack2(x, 0.f) << 16); }
template <int CTRL, int RMASK> __device__ __forceinline__ float dpp_add(float v) {
    return v + __builtin_bit_cast(float, __builtin_amdgcn_update_dpp(0, __builtin_bit_cast(int, v), CTRL, RMASK, 0xf, false));
}
__device__ __forceinline__ float wave_sum(float v) {
    v = dpp_add<0x111, 0xf>(v); v = dpp_add<0x112, 0xf>(v); v = dpp_add<0x114, 0xf>(v); v = dpp_add<0x118, 0xf>(v);
    v = dpp_add<0x142, 0xa>(v); v = dpp_add<0x143, 0xc>(v);
    return __builtin_bit_cast(float, __builtin_amdgcn_readlane(__builtin_bit_cast(int, v), 63));
}
__device__ __forceinline__ __amdgpu_buffer_rsrc_t rsrc(const void* p, unsigned bytes) { return __builtin_amdgcn_make_buffer_rsrc((void*)p, 0, bytes, 0x00020000); }

enum { ROW = 0, TR = 1, FRAG = 2 };
constexpr int PIECE = 1056, BLK = 8 * PIECE;
template <int T, int X> struct Op {
    static constexpr int BYTES = X * 132;
    __device__ static __forceinline__ unsigned src_off(int i, int lane, unsigned ld, unsigned cb) {
        const int blk = i >> 3, p = i & 7, pos = lane >> 3;
        if (T == TR) {
            if (X == 224 && blk == 3) {
                const int ps = lane >> 2, g = (p & 1) | ((ps >> 3) << 1), r = 32 * (p >> 1) + 8 * g + 4 * ((ps >> 2) & 1) + (ps & 3);
                return (unsigned)r * ld + cb + (unsigned)((lane & 3) * 16);
            }
            const int g = (p >> 2) | (pos & 2), r = 32 * (pos >> 2) + 8 * g + 4 * (pos & 1) + (p & 3);
            return (unsigned)r * ld + cb + (unsigned)((lane & 7) * 16);
        } else if (T == ROW) {
            const int j = ((pos >> 1) & 1) | ((pos & 1) << 1) | (pos & 4);
            return (unsigned)(blk * 64 + j * 8 + p) * ld + (unsigned)((lane & 7) * 16);
        } else {
            return (unsigned)blk * ld + (unsigned)(p * 1024 + lane * 16);
        }
    }
};
template <int AUX> __device__ __forceinline__ void glds16(const char* g, LAS char* l) { __builtin_amdgcn_global_load_lds((const GAS unsigned*)g, (LAS unsigned*)l, 16, 0, AUX); }
__device__ __forceinline__ void glds16_hidden_plain(const char* g, LAS char* l) {
    unsigned keep;
    asm volatile("s_mov_b32 %0, m0\n\ts_mov_b32 m0, %2\n\ts_nop 0\n\tglobal_load_lds_dwordx4 %1, off\n\ts_mov_b32 m0, %0"
                 : "=&s"(keep) : "v"(g), "s"(__builtin_amdgcn_readfirstlane((unsigned)(uintptr_t)l)) : "memory");
}
__device__ __forceinline__ void glds16_hidden(const char* g, LAS char* l) {
    unsigned keep;
    asm volatile("s_mov_b32 %0, m0\n\ts_mov_b32 m0, %2\n\ts_nop 0\n\tglobal_load_lds_dwordx4 %1, off sc1\n\ts_mov_b32 m0, %0"
                 : "=&s"(keep) : "v"(g), "s"(__builtin_amdgcn_readfirstlane((unsigned)(uintptr_t)l)) : "memory");
}
__device__ __forceinline__ void glds4(const char* g, LAS char* l) { __builtin_amdgcn_global_load_lds((const GAS unsigned*)g, (LAS unsigned*)l, 4, 0, 0); }

template <int T, int X> struct Addr {
    unsigned a, b;
    static constexpr int NV = (X == 224 || X == 192) ? 2 : 1;
    __device__ __forceinline__ void init(unsigned base, int lane, int w) {
        const int g = lane >> 4;
        b = 0;
        if (T == TR) {
            const int q = (lane & 15) >> 2, p = lane & 3;
            a = base + (unsigned)((q | ((g & 1) << 2)) * PIECE + (g >> 1) * 256 + p * 8 + (X == 128 ? w * BLK : w * 64));
            b = base + (unsigned)(3 * BLK + (g & 1) * PIECE + (g >> 1) * 512 + q * 64 + w * 32 + p * 8);
        } else if (T == ROW) {
            const int r = lane & 15;
            a = base + (unsigned)(w * BLK + (r & 7) * PIECE + (r >> 3) * 256 + g * 16);
        } else {
            a = base + (unsigned)(w * 4 * PIECE + lane * 16);
        }
    }
    __device__ __forceinline__ void step(unsigned d) { a += d; if (NV == 2) b += d; }
    __device__ __forceinline__ bf16x8 read(int n, int kk) const {
        if (T == TR) {
            LAS char* p; int t;
            if (X == 224 && n == 6) { p = (LAS char*)(uintptr_t)b + kk * 2 * PIECE; t = 256; }
            else if (X == 192) { p = (LAS char*)(uintptr_t)((n & 1) ? b : a) + kk * 512 + (n >> 1) * BLK; t = 128; }
            else { p = (LAS char*)(uintptr_t)a + kk * 512 + (X == 128 ? n * 32 : (n >> 1) * BLK + (n & 1) * 32); t = 128; }
            const bf16x4 lo = __builtin_amdgcn_ds_read_tr16_b64_v4bf16((LAS bf16x4*)p);
            const bf16x4 hi = __builtin_amdgcn_ds_read_tr16_b64_v4bf16((LAS bf16x4*)(p + t));
            return __builtin_shufflevector(lo, hi, 0, 1, 2, 3, 4, 5, 6, 7);
        } else if (T == ROW) {
            return *(LAS bf16x8*)((LAS char*)(uintptr_t)a + (n & 1) * 128 + (n >> 1) * 512 + kk * 64);
        } else {
            return *(LAS bf16x8*)((LAS char*)(uintptr_t)a + kk * BLK + n * PIECE);
        }
    }
};
template <int FM, int FN> struct Regs { bf16x8 m[FM], n[FN]; };
#define MFMA16(a, b, c) __builtin_amdgcn_mfma_f32_16x16x32_bf16(a, b, c, 0, 0, 0)

template <int TM, int BM, int TN, int BN, int FM, int FN> struct Reader {
    Addr<TM, BM> m; Addr<TN, BN> n;
    static constexpr int NDS = FM * (TM == TR ? 2 : 1) + FN * (TN == TR ? 2 : 1), NV = Addr<TM, BM>::NV + Addr<TN, BN>::NV;
    __device__ __forceinline__ void init(unsigned slot_base, int lane, int wm, int wn) { m.init(slot_base, lane, wm); n.init(slot_base + BM * 132, lane, wn); }
    __device__ __forceinline__ void read(Regs<FM, FN>& R, int kk) const {
#pragma unroll
        for (int i = 0; i < FM; ++i) R.m[i] = m.read(i, kk);
#pragma unroll
        for (int j = 0; j < FN; ++j) R.n[j] = n.read(j, kk);
    }
    __device__ __forceinline__ void step(unsigned d) { m.step(d); n.step(d); }
};
template <int AM, int FM, int FN> __device__ __forceinline__ void mma(f32x4 (&acc)[AM][FN], const Regs<FM, FN>& R) {
    const u32x4 one = {0x3f803f80u, 0x3f803f80u, 0x3f803f80u, 0x3f803f80u};
#pragma unroll
    for (int j = 0; j < FN; ++j) {
#pragma unroll
        for (int i = 0; i < FM; ++i) acc[i][j] = MFMA16(R.n[j], R.m[i], acc[i][j]);
        if (AM > FM && !(j & 1)) acc[AM - 1][j] = MFMA16(R.n[j], __builtin_bit_cast(bf16x8, one), acc[AM - 1][j]);
    }
}
template <int NDS, int NV, int NMF> __device__ __forceinline__ void interleave() {
#pragma unroll
    for (int i = 0; i < NMF; ++i) {
        __builtin_amdgcn_sched_group_barrier(0x8, 1, 0);
        if (i < NDS) __builtin_amdgcn_sched_group_barrier(0x80, 1, 0);
        if (i >= NMF - NV) __builtin_amdgcn_sched_group_barrier(0x2, 1, 0);
    }
}
template <int NS, int SB, class RD, class RDN, int AM, int FM, int FN>
__device__ __forceinline__ void stage_step(int& slot, RD& rd, RDN& rn, Regs<FM, FN>& R0, f32x4 (&acc)[AM][FN]) {
    constexpr int NMF = FM * FN + (AM - FM) * (FN / 2);
    Regs<FM, FN> R1;
    const unsigned d = (slot + 1 == NS) ? (unsigned)(-(NS - 1) * SB) : (unsigned)SB;
    slot = (slot + 1 == NS) ? 0 : slot + 1;
    rd.read(R1, 1);
    rn.step(d);
    mma(acc, R0);
    interleave<RD::NDS, RDN::NV, NMF>();
    SCHED0();
    WAIT_LGKM0();
    BARRIER();
    SCHED0();
    rn.read(R0, 0);
    mma(acc, R1);
    interleave<RDN::NDS, 0, NMF>();
    SCHED0();
}

template <int NM, int NN> struct Loader {
    static constexpr int NPW = NM + NN;
    unsigned om[NM], on[NN];
    template <int T> __device__ __forceinline__ void init_m(int lw, int lane, unsigned ld) {
#pragma unroll
        for (int i = 0; i < NM; ++i) om[i] = Op<T, NM * 32>::src_off(lw + 4 * i, lane, ld, (i >> 1) * 128);
    }
    template <int T> __device__ __forceinline__ void init_n(int lw, int lane, unsigned ld, unsigned c0 = 0, unsigned c1 = 128, unsigned c2 = 256, unsigned c3 = 384) {
#pragma unroll
        for (int i = 0; i < NN; ++i) on[i] = Op<T, NN * 32>::src_off(lw + 4 * i, lane, ld, (i >> 1) == 0 ? c0 : (i >> 1) == 1 ? c1 : (i >> 1) == 2 ? c2 : c3);
    }
    template <int AUX> __device__ __forceinline__ void issue_m(const char* gm, LAS char* dst, int lw) const {
#pragma unroll
        for (int i = 0; i < NM; ++i) glds16<AUX>(gm + om[i], dst + (lw + 4 * i) * PIECE);
    }
    template <int AUX> __device__ __forceinline__ void issue_n(const char* gn, LAS char* dst, int lw) const {
#pragma unroll
        for (int i = 0; i < NN; ++i) glds16<AUX>(gn + on[i], dst + NM * 32 * 132 + (lw + 4 * i) * PIECE);
    }
};
#define LEAD 3
__device__ __forceinline__ int korder(int p, int c, int nk) {
    const int r = (p - c) & 3, p0 = p - r;
    if (r > LEAD || p0 < 0 || p0 + LEAD >= nk) return p;
    return r == 0 ? p + LEAD : p - 1;
}
__device__ __forceinline__ f32x2 gelu2(f32x2 u) {
    const float c1 = -2.0f * 0.7978845608028654f * 1.4426950408889634f, c2 = c1 * 0.044715f;
    const f32x2 z = u * ((u * u) * c2 + c1);
    f32x2 e; e.x = __builtin_amdgcn_exp2f(z.x); e.y = __builtin_amdgcn_exp2f(z.y);
    e = e + 1.0f;
    f32x2 r; r.x = __builtin_amdgcn_rcpf(e.x); r.y = __builtin_amdgcn_rcpf(e.y);
    return u * r;
}
__device__ __forceinline__ u32x4 pair16(unsigned x0, unsigned x1, unsigned y0, unsigned y1) {
    auto a = __builtin_amdgcn_permlane16_swap(x0, y0, false, false);
    auto b = __builtin_amdgcn_permlane16_swap(x1, y1, false, false);
    return (u32x4){a[0], b[0], a[1], b[1]};
}
__device__ __forceinline__ bool flags_set(u64* flags, int idx, u64 tag) {
    const u64 v = __hip_atomic_load((GAS u64*)flags + (idx < 0 ? 0 : idx), RLX_AGENT);
    const bool ok = __builtin_amdgcn_ballot_w64(idx >= 0 && v - 1ull != tag) == 0ull;
    CFENCE();
    return ok;
}
__device__ __forceinline__ void poll_flags(u64* flags, int idx, u64 tag) {
#pragma unroll 1
    for (unsigned spins = 0; spins < (1u << 20); ++spins) {
        if (flags_set(flags, idx, tag)) break;
        __builtin_amdgcn_s_sleep(2);
    }
}
__device__ __forceinline__ void set_flag(u64* flags, int idx, u64 tag) {
    asm volatile("" : "+v"(tag));
    __hip_atomic_store((GAS u64*)flags + idx, tag + 1ull, RLX_AGENT);
}

struct Params {
    const u16 *x, *wqkv, *wo, *w1, *b1, *w2, *b2;
    u16* out;
    char* ws;
    __device__ __forceinline__ u16* h() const { return (u16*)(ws + WS_H); }
    __device__ __forceinline__ u16* qkv() const { return (u16*)(ws + WS_QKV); }
    __device__ __forceinline__ u16* U() const { return (u16*)(ws + WS_U); }
    __device__ __forceinline__ u16* o() const { return (u16*)(ws + WS_O); }
    __device__ __forceinline__ float* stat() const { return (float*)(ws + WS_STAT); }
    __device__ __forceinline__ float* cs() const { return (float*)(ws + WS_CS); }
    __device__ __forceinline__ float* part() const { return (float*)(ws + WS_PART); }
    __device__ __forceinline__ u64* flags() const { return (u64*)(ws + WS_FLAGS); }
};

constexpr int SB_M = (128 + 224) * 132, SB_Q = (128 + 192) * 132, SB_F = 256 * 132, NS_F = 4;
constexpr int LDS_QX = 3 * SB_Q;
constexpr int LDS_CTL = LDS_QX + 3072;
constexpr int LDS_AQ = 0, LDS_AK = 2 * SB_Q, LDS_AV = 128 * 1024, LDS_BYTES = 160 * 1024;
constexpr int LDS_BK = 16 * 1024, LDS_BV = 96 * 1024;
constexpr int LDS_SINK = LDS_BYTES - 256;
static_assert(LDS_CTL + 32 <= LDS_AV && 3 * SB_M <= LDS_SINK && 3 * SB_F <= LDS_AV && NS_F * SB_F <= LDS_BYTES, "LDS map");
#define NA 9
#define PF2 1
constexpr int KW = 8, NF_Q = 7 + NA, NF_M = 7 + 42 - NA;

__device__ __forceinline__ void prefetch_next(const Params& P, LAS char* lds, int batch, int lw, int lane) {
    const int piece = batch * 4 + lw;
    const char* src = piece < 7 ? (const char*)P.w2 + ((size_t)piece * 256 + blockIdx.x) * 8192 : (const char*)P.wo + (size_t)blockIdx.x * 8192;
    glds4(src + lane * 128, lds + LDS_SINK);
}
__device__ __forceinline__ int qkv_head(int tn, int blk) { return blk * NH + ((tn & ~3) | ((tn + blk) & 3)) * 2; }

__device__ __forceinline__ f32x2 ln_row(const u32x4 a, const u32x4 b, u32x4& oa, u32x4& ob) {
    f32x2 v[8];
#pragma unroll
    for (int i = 0; i < 4; ++i) { v[i] = (f32x2){lo16(a[i]), hi16(a[i])}; v[4 + i] = (f32x2){lo16(b[i]), hi16(b[i])}; }
    const f32x2 s2 = ((v[0] + v[1]) + (v[2] + v[3])) + ((v[4] + v[5]) + (v[6] + v[7]));
    const float mean = wave_sum(s2.x + s2.y) * (1.0f / D_);
    f32x2 q2 = {0.f, 0.f};
#pragma unroll
    for (int i = 0; i < 8; ++i) { const f32x2 d = v[i] - mean; q2 += d * d; }
    const float var = wave_sum(q2.x + q2.y) * (1.0f / D_);
    const float mb = rbf(mean), rs = __builtin_amdgcn_rcpf(rbf(__builtin_amdgcn_sqrtf(rbf(rbf(var) + 1e-5f))));
    const float mb2 = rbf((mean - mb) * rs), rs2 = __builtin_amdgcn_rcpf(rbf(__builtin_amdgcn_sqrtf(rbf(rbf(var * rs * rs) + 1e-5f))));
    const float g = rs * rs2, c = -mb2 * rs2;
#pragma unroll
    for (int i = 0; i < 8; ++i) {
        const f32x2 t = (v[i] - mb) * g + c;
        if (i < 4) oa[i] = pack2(t.x, t.y); else ob[i - 4] = pack2(t.x, t.y);
    }
    return (f32x2){mb, rs};
}

#define MFMA32(a, b, c) __builtin_amdgcn_mfma_f32_32x32x16_bf16(a, b, c, 0, 0, 0)
__device__ __forceinline__ int attn_dep(int hd, int qb, int lane) {
    const int which = lane >> 2, st = 2 * qb - 2 + (lane & 3), p = hd >> 1;
    if (lane >= 12 || st < 0 || (which == 0 && (lane & 3) < 2)) return -1;
    return FL_Q + st * 32 + ((p & ~3) | ((p - which) & 3));
}
template <int NW, int WHICH> __device__ __forceinline__ void attn_load(const Params& P, LAS char* dst, int hd, int qb, int w, int lane) {
    const char* g = (const char*)(P.qkv() + (size_t)(WHICH * NH + hd) * S_ * 32) + (WHICH == 2 ? lane & 3 : (lane & 3) ^ ((lane >> 4) & 3)) * 16;
#pragma unroll
    for (int i = 0; i < (WHICH == 0 ? 16 : 32) / NW; ++i) {
        const int pc = i * NW + w;
        int row = qb * 256 - (WHICH == 0 ? 0 : 256) + pc * 16 + (lane >> 2); row = row < 0 ? 0 : row;
        glds16_hidden(g + row * 64, dst + pc * 1024);
    }
}
#define PIN(x) asm volatile("" : "+v"(x))
__device__ __forceinline__ float max16(const f32x16& v) {
    float a = __builtin_fmaxf(__builtin_fmaxf(v[0], v[1]), v[2]), b = __builtin_fmaxf(__builtin_fmaxf(v[3], v[4]), v[5]);
    a = __builtin_fmaxf(__builtin_fmaxf(a, v[6]), v[7]); b = __builtin_fmaxf(__builtin_fmaxf(b, v[8]), v[9]);
    a = __builtin_fmaxf(__builtin_fmaxf(a, v[10]), v[11]); b = __builtin_fmaxf(__builtin_fmaxf(b, v[12]), v[13]);
    a = __builtin_fmaxf(__builtin_fmaxf(a, v[14]), v[15]);
    return __builtin_fmaxf(a, b);
}
__device__ __forceinline__ float attn_scores(LAS char* buf, LAS char* qbuf, int nbad, int wid, int lane, unsigned up, f32x16 (&sc)[8]) {
    const int r32 = lane & 31, hh = lane >> 5, sw = (r32 >> 2) & 3;
    LAS char* k0p = buf + (wid * 32 + r32) * 64 + ((hh ^ sw) * 16);
    LAS char* k1p = buf + (wid * 32 + r32) * 64 + (((2 + hh) ^ sw) * 16);
    const bf16x8 qf0 = *(LAS bf16x8*)(qbuf + (wid * 32 + r32) * 64 + ((hh ^ sw) * 16));
    const bf16x8 qf1 = *(LAS bf16x8*)(qbuf + (wid * 32 + r32) * 64 + (((2 + hh) ^ sw) * 16));
    bf16x8 ka[9], kb[9];
#pragma unroll
    for (int i = 0; i < 3; ++i) { const int j = i == 0 ? 8 : i - 1; ka[i] = *(LAS bf16x8*)(k0p + j * 2048); kb[i] = *(LAS bf16x8*)(k1p + j * 2048); SCHED0(); }
    const float NEG = -INFINITY;
    const f32x16 zero = {};
    const float b0 = nbad > 0 ? NEG : 0.f;
    f32x16 s8;
    float m = NEG;
#pragma unroll
    for (int i = 0; i < 11; ++i) {
        if (i + 3 < 9) { ka[i + 3] = *(LAS bf16x8*)(k0p + (i + 2) * 2048); kb[i + 3] = *(LAS bf16x8*)(k1p + (i + 2) * 2048); }
        if (i < 9) {
            const f32x16 t = MFMA32(kb[i], qf1, MFMA32(ka[i], qf0, zero));
            if (i == 0) s8 = t; else sc[i - 1] = t;
        }
        SCHED0();
        const int j = i - 3;
        if (j == 0) {
#pragma unroll
            for (int r = 0; r < 16; ++r) {
                const unsigned mk = (unsigned)__builtin_amdgcn_sbfe((int)up, (r & 3) + 8 * (r >> 2), 1);
                sc[0][r] = __uint_as_float(__builtin_amdgcn_bitop3_b32(mk, __float_as_uint(sc[0][r] + b0), __float_as_uint(s8[r]), 0xca));
            }
        }
        if (j >= 0) { const float mj = max16(sc[j]); m = __builtin_fmaxf(m, (j > 0 && j < nbad) ? NEG : mj); PIN(m); }
        SCHED0();
    }
    auto rr = __builtin_amdgcn_permlane32_swap(__float_as_uint(m), __float_as_uint(m), false, false);
    return __builtin_fmaxf(__uint_as_float(rr[0]), __uint_as_float(rr[1]));
}
struct PvState { f32x2 l0, l1; f32x16 o; bf16x8 vf[2], v8[2]; u32x4 pw[2], p8[2]; };
__device__ __forceinline__ bf16x8 v_read(LAS char* vp, int j, int s) {
    const bf16x4 lo = __builtin_amdgcn_ds_read_tr16_b64_v4bf16((LAS bf16x4*)(vp + j * 2048 + s * 1024));
    const bf16x4 hi = __builtin_amdgcn_ds_read_tr16_b64_v4bf16((LAS bf16x4*)(vp + j * 2048 + s * 1024 + 512));
    return __builtin_shufflevector(lo, hi, 0, 1, 2, 3, 4, 5, 6, 7);
}
template <int J0, int J1> __device__ __forceinline__ void attn_pv(LAS char* buf, int nbad, int wid, int lane, unsigned up, f32x16 (&sc)[8], float m, PvState& S) {
    const int hh = lane >> 5;
    LAS char* vp = buf + (wid * 32 + 4 * hh + ((lane & 15) >> 2)) * 64 + ((lane >> 4) & 1) * 32 + (lane & 3) * 8;
#pragma unroll
    for (int j = J0; j < J1; ++j) {
        bf16x8 vn[2] = {v_read(vp, j, 0), v_read(vp, j, 1)};
        if (j == 0) { S.v8[0] = v_read(vp, 8, 0); S.v8[1] = v_read(vp, 8, 1); }
        const float mj = (j > 0 && j < nbad) ? INFINITY : m;
        f32x2 p[8];
#pragma unroll
        for (int i = 0; i < 8; ++i) p[i] = (f32x2){sc[j][2 * i], sc[j][2 * i + 1]} - mj;
        SCHED0();
#pragma unroll
        for (int g = 0; g < 4; ++g) {
#pragma unroll
            for (int i = 2 * g; i < 2 * g + 2; ++i) { p[i].x = __builtin_amdgcn_exp2f(p[i].x); p[i].y = __builtin_amdgcn_exp2f(p[i].y); PIN(p[i]); }
            SCHED0();
            if (j > 0 && (g & 1) == 0) S.o = MFMA32(S.vf[g >> 1], __builtin_bit_cast(bf16x8, S.pw[g >> 1]), S.o);
            if (j == 1 && (g & 1) == 1) S.o = MFMA32(S.v8[g >> 1], __builtin_bit_cast(bf16x8, S.p8[g >> 1]), S.o);
            SCHED0();
        }
#pragma unroll
        for (int i = 0; i < 8; ++i) {
            S.pw[i >> 2][i & 3] = pack2(p[i].x, p[i].y);
            if (i & 1) S.l1 += p[i]; else S.l0 += p[i];
        }
        if (j == 0) {
#pragma unroll
            for (int i = 0; i < 8; ++i) {
                const int kr = ((2 * i) & 3) + 8 * (i >> 1);
                const unsigned mk = __builtin_amdgcn_bitop3_b32(0xffffu, (unsigned)__builtin_amdgcn_sbfe((int)up, kr, 1), (unsigned)__builtin_amdgcn_sbfe((int)up, kr + 1, 1), 0xca);
                S.p8[i >> 2][i & 3] = S.pw[i >> 2][i & 3] & ~mk; S.pw[i >> 2][i & 3] &= mk;
            }
        }
        S.vf[0] = vn[0]; S.vf[1] = vn[1];
        PIN(S.l0); PIN(S.l1); PIN(S.pw[0]); PIN(S.pw[1]);
        SCHED0();
    }
    if (J1 == 8) {
        S.o = MFMA32(S.vf[0], __builtin_bit_cast(bf16x8, S.pw[0]), S.o);
        S.o = MFMA32(S.vf[1], __builtin_bit_cast(bf16x8, S.pw[1]), S.o);
    }
}
__device__ __forceinline__ void attn_out(const Params& P, int hd, int qb, int wid, int lane, f32x2 l2, const f32x16& oacc) {
    const int r32 = lane & 31, hh = lane >> 5;
    float l = l2.x + l2.y;
    {
        auto rr = __builtin_amdgcn_permlane32_swap(__float_as_uint(l), __float_as_uint(l), false, false);
        l = __uint_as_float(rr[0]) + __uint_as_float(rr[1]);
    }
    const float inv = __builtin_amdgcn_rcpf(l);
    unsigned w[8];
#pragma unroll
    for (int g = 0; g < 4; ++g) { w[2 * g] = pack2(oacc[4 * g] * inv, oacc[4 * g + 1] * inv); w[2 * g + 1] = pack2(oacc[4 * g + 2] * inv, oacc[4 * g + 3] * inv); }
    const __amdgpu_buffer_rsrc_t rs = rsrc(P.o(), (unsigned)S_ * D_ * 2);
    const unsigned off = (unsigned)((hd * (S_ / 16) + qb * 16 + wid * 2 + (r32 >> 4)) * 1024 + (hh * 16 + (r32 & 15)) * 16);
#pragma unroll
    for (int gp = 0; gp < 2; ++gp) {
        auto a = __builtin_amdgcn_permlane32_swap(w[4 * gp], w[4 * gp + 2], false, false);
        auto b = __builtin_amdgcn_permlane32_swap(w[4 * gp + 1], w[4 * gp + 3], false, false);
        __builtin_amdgcn_raw_buffer_store_b128((u32x4){a[0], b[0], a[1], b[1]}, rs, off + gp * 512, 0, SC1);
    }
}

__global__ __launch_bounds__(512, 2) void k_main(const Params P) {
    extern __shared__ __attribute__((aligned(16))) char smem[];
    LAS char* lds = (LAS char*)smem;
    const int tid = threadIdx.x, lane = tid & 63, wid = __builtin_amdgcn_readfirstlane(tid >> 6);
    const int grp = blockIdx.x & 7, idx = blockIdx.x >> 3;
    const bool loader = wid >= 4;
    const int lw = wid - 4, wm = wid >> 1, wn = wid & 1;
    const unsigned ring0 = (unsigned)(uintptr_t)lds;
    const int pf_at = 4 + (idx % 12);
    const int i16 = idx & 15;
    const bool isM = idx < 16;
    const int fm = (grp >> 1) * 4 + (i16 >> 2), fn = (grp & 1) * 4 + (i16 & 3);
    const int um = (grp & 1) * 4 + (i16 >> 2), un = (grp >> 1) * 4 + (i16 & 3);
    const int hd = (grp & 1) * 8 + (i16 & 7), qb = (grp >> 1) * 2 + (i16 >> 3);
    STAMP_DECL;
    STAMP(1, 0);
    const u64 tag = __hip_atomic_load((GAS u64*)P.flags() + FL_E, RLX_AGENT);

    if (!isM && tid < 8) *(LAS unsigned*)(lds + LDS_CTL + tid * 4) = 0u;
    if (isM) {
        constexpr int NK = S_ / 64, SB = SB_M;
        const int pg = (grp >> 1) + 4 * (grp & 1);
        const int tm = um, tn = un, mi = pg * 16 + i16;
        const char* gm = (const char*)P.h() + tm * 256; const char* gn = (const char*)P.w1 + tn * 448;
        const int cls = (tm + tn) & 3;
        auto kof = [&](int u) { const int p0 = u - ((u - cls) & 3); return (((p0 == 1 || p0 == 2) ? u : korder(u, cls, NK)) + 4 * pg) & (NK - 1); };
        Loader<4, 7> L;
        {
            const int row = mi * 16 + wid * 2;
            const u32x4* px = (const u32x4*)(P.x + (size_t)row * D_);
            u32x4 a0 = px[lane], b0 = px[64 + lane], a1 = px[128 + lane], b1 = px[192 + lane];
            SCHED0();
            if (loader) {
                const unsigned c0 = (tn & 1) * 64;
                L.init_m<TR>(lw, lane, D_ * 2);
                L.init_n<TR>(lw, lane, F_ * 2, c0, c0 + 128, c0 + 256, (tn & 1) ? 0 : 384);
                asm volatile("" : "+v"(a0), "+v"(b0), "+v"(a1), "+v"(b1));
                L.issue_n<0>(gn + (size_t)kof(0) * (64 * F_ * 2), lds, lw);
            }
            STAMPL(0, 0);
            const __amdgpu_buffer_rsrc_t rh = rsrc(P.h(), (unsigned)S_ * D_ * 2);
            u32x4 oa0, ob0, oa1, ob1;
            const f32x2 s0 = ln_row(a0, b0, oa0, ob0), s1 = ln_row(a1, b1, oa1, ob1);
            __builtin_amdgcn_raw_buffer_store_b128(oa0, rh, (unsigned)(row * 2048 + lane * 16), 0, SC1);
            __builtin_amdgcn_raw_buffer_store_b128(ob0, rh, (unsigned)(row * 2048 + 1024 + lane * 16), 0, SC1);
            __builtin_amdgcn_raw_buffer_store_b128(oa1, rh, (unsigned)(row * 2048 + 2048 + lane * 16), 0, SC1);
            __builtin_amdgcn_raw_buffer_store_b128(ob1, rh, (unsigned)(row * 2048 + 3072 + lane * 16), 0, SC1);
            if (lane == 0) __builtin_amdgcn_raw_buffer_store_b128(__builtin_bit_cast(u32x4, (f32x4){s0.x, s0.y, s1.x, s1.y}), rsrc(P.stat(), S_ * 8), (unsigned)(row * 8), 0, SC1);
            SCHED0();
            STAMPL(0, 1);
        }
        if (loader) {
            WAIT_VM(0);
            BARRIER();
            STAMPL(0, 2);
            if (tid == 256 + 64) set_flag(P.flags(), FL_L + mi, tag);
#pragma unroll
            for (int u = 1; u < 3; ++u) L.issue_n<0>(gn + (size_t)kof(u) * (64 * F_ * 2), lds + u * SB, lw);
            if (lw == 0) poll_flags(P.flags(), lane < 16 ? FL_L + pg * 16 + lane : -1, tag);
            STAMPL(0, 3);
            BARRIER();
#pragma unroll
            for (int u = 0; u < 3; ++u) L.issue_m<SC1>(gm + (size_t)kof(u) * (64 * D_ * 2), lds + u * SB, lw);
            WAIT_VM(8); BARRIER();
            int slot = 0, t = 0;
            auto issue = [&]() {
                const int k = kof(t + 3);
                L.issue_m<SC1>(gm + (size_t)k * (64 * D_ * 2), lds + slot * SB, lw);
                L.issue_n<0>(gn + (size_t)k * (64 * F_ * 2), lds + slot * SB, lw);
                slot = (slot == 2) ? 0 : slot + 1;
            };
            WAIT_VM(4);
            if (lw == 0) poll_flags(P.flags(), lane < 16 ? FL_L + ((pg + 1) & 7) * 16 + lane : -1, tag);
            BARRIER(); issue(); ++t;
            WAIT_VM(L.NPW); BARRIER(); issue(); ++t;
            WAIT_VM(L.NPW);
            if (lw == 0) { poll_flags(P.flags(), FL_L + lane, tag); poll_flags(P.flags(), FL_L + 64 + lane, tag); }
            BARRIER(); issue(); ++t;
#pragma unroll 1
            for (int seg = 0; seg < 3; ++seg) {
                const int end = seg == 0 ? pf_at : seg == 1 ? pf_at + 12 : NK - 3;
#pragma unroll 1
                for (; t < end; ++t) { WAIT_VM(L.NPW); BARRIER(); issue(); }
                if (PF2 && seg < 2) prefetch_next(P, lds, seg, lw, lane);
            }
            WAIT_VM(L.NPW); BARRIER();
            WAIT_VM(0); BARRIER();
            BARRIER();
        } else {
            const int fr = lane & 15, fq = lane >> 4;
            const int fb = tn * 224 + ((tn & 1) ? 32 : 0) + wn * 32, fh = tn * 224 + ((tn & 1) ? 0 : 192) + wn * 16;
            WAIT_VM(0);
            WAIT_LGKM0();
            BARRIER();
            f32x4 acc[4][7];
#pragma unroll
            for (int j = 0; j < 7; ++j) {
                const u32x2 bb = *(const u32x2*)(P.b1 + (j < 6 ? fb + (j >> 1) * 64 + (j & 1) * 16 : fh) + fq * 4);
                const f32x4 bv = {lo16(bb[0]), hi16(bb[0]), lo16(bb[1]), hi16(bb[1])};
#pragma unroll
                for (int i = 0; i < 4; ++i) acc[i][j] = bv;
            }
            Reader<TR, 128, TR, 224, 4, 7> rd; rd.init(ring0, lane, wm, wn);
            Regs<4, 7> R0;
            int slot = 0;
            BARRIER();
            __builtin_amdgcn_s_waitcnt(0x0070);
            BARRIER();
            STAMP(1, 1);
            rd.read(R0, 0);
            for (int t = 0; t < NK / 4; ++t) {
                stage_step<3, SB>(slot, rd, rd, R0, acc); stage_step<3, SB>(slot, rd, rd, R0, acc);
                stage_step<3, SB>(slot, rd, rd, R0, acc); stage_step<3, SB>(slot, rd, rd, R0, acc);
            }
            STAMP(1, 2);
            const __amdgpu_buffer_rsrc_t rs = rsrc(P.U(), (unsigned)D_ * F_ * 2);
            const unsigned df = (unsigned)(tm * 8 + wm * 4);
            const unsigned ob = (unsigned)((fb >> 5) * 64 + df) * 1024u + (unsigned)((((fq & 1) * 2 + (fq >> 1)) * 16 + fr) * 16);
            const unsigned oh = (unsigned)((fh >> 5) * 64 + df + (fq & 1)) * 1024u + (unsigned)(((wn * 2 + (fq >> 1)) * 16 + fr) * 16);
            unsigned pk[4][7][2];
#pragma unroll
            for (int i = 0; i < 4; ++i) {
#pragma unroll
                for (int j = 0; j < 7; ++j) {
                    const f32x2 a = gelu2((f32x2){acc[i][j][0], acc[i][j][1]}), b = gelu2((f32x2){acc[i][j][2], acc[i][j][3]});
                    pk[i][j][0] = pack2(a.x, a.y); pk[i][j][1] = pack2(b.x, b.y);
                }
#pragma unroll
                for (int jp = 0; jp < 3; ++jp)
                    __builtin_amdgcn_raw_buffer_store_b128(pair16(pk[i][2 * jp][0], pk[i][2 * jp][1], pk[i][2 * jp + 1][0], pk[i][2 * jp + 1][1]), rs, ob + i * 1024 + jp * 2 * 65536, 0, SC1);
                if (i & 1)
                    __builtin_amdgcn_raw_buffer_store_b128(pair16(pk[i - 1][6][0], pk[i - 1][6][1], pk[i][6][0], pk[i][6][1]), rs, oh + (i - 1) * 1024, 0, SC1);
                SCHED0();
            }
            STAMP(1, 3);
        }
    } else {
        constexpr int NK = D_ / 64, SB = SB_Q;
        const int tm = fm;
        if (loader) {
            Loader<4, 6> L; L.init_m<ROW>(lw, lane, D_ * 2);
            L.init_n<TR>(lw, lane, 3 * D_ * 2, qkv_head(fn, 0) * 64, qkv_head(fn, 1) * 64, qkv_head(fn, 2) * 64);
            const char* gm = (const char*)P.x + (size_t)tm * 128 * D_ * 2; const char* gn = (const char*)P.wqkv;
            int slot = 0;
            auto issue = [&](int u) {
                const int k = korder(u & (NK - 1), (tm + fn) & 3, NK);
                L.issue_m<0>(gm + k * 128, lds + slot * SB, lw);
                L.issue_n<0>(gn + (size_t)k * (64 * 3 * D_ * 2) + (u >= NK ? 16 * 64 : 0), lds + slot * SB, lw);
                slot = (slot == 2) ? 0 : slot + 1;
            };
            issue(0); issue(1);
            WAIT_VM(L.NPW); BARRIER();
            issue(2);
            int t = 0;
#pragma unroll 1
            for (int seg = 0; seg < 4; ++seg) {
                const int end = seg == 0 ? 12 : seg == 1 ? 13 + (idx & 7) : seg == 2 ? 21 + (idx & 7) : 2 * NK - 3;
#pragma unroll 1
                for (; t < end; ++t) { WAIT_VM(L.NPW); BARRIER(); issue(t + 3); }
                if (seg == 0) {
                    if (lw == 0) {
                        poll_flags(P.flags(), lane < 8 ? FL_L + tm * 8 + lane : -1, tag);
                        glds16<SC1>((const char*)P.stat() + tm * 1024 + lane * 16, lds + LDS_QX);
                    }
                } else if (PF2 && seg < 3) prefetch_next(P, lds, seg - 1, lw, lane);
            }
            poll_flags(P.flags(), attn_dep(hd, qb, lane), tag);
            WAIT_VM(0);
            BARRIER();
            attn_load<4, 1>(P, lds + LDS_AK, hd, qb, lw, lane);
            attn_load<4, 2>(P, lds + LDS_AV, hd, qb, lw, lane);
            BARRIER();
            attn_load<4, 0>(P, lds + LDS_AQ, hd, qb, lw, lane);
            BARRIER();
        } else {
            const int fr = lane & 15, fq = lane >> 4;
            Reader<ROW, 128, TR, 192, 4, 6> rd; rd.init(ring0, lane, wm, wn);
            rd.n.b = rd.n.a + 32 - wm * 32; rd.n.a += wm * 32;
            Regs<4, 6> R0;
            int slot = 0;
            __builtin_amdgcn_s_waitcnt(0x0070);
            BARRIER();
            STAMP(1, 1);
            rd.read(R0, 0);
            const __amdgpu_buffer_rsrc_t rs = rsrc(P.qkv(), (unsigned)S_ * 3 * D_ * 2);
#pragma unroll 1
            for (int rnd = 0; rnd < 2; ++rnd) {
                f32x4 acc[5][6] = {};
                for (int t = 0; t < NK / 4; ++t) {
                    if (rnd == 1 && t == 1) WAIT_VM(0);
                    if (rnd == 1 && t == 2 && tid == 0) set_flag(P.flags(), FL_Q + tm * 32 + fn, tag);
                    stage_step<3, SB>(slot, rd, rd, R0, acc); stage_step<3, SB>(slot, rd, rd, R0, acc);
                    stage_step<3, SB>(slot, rd, rd, R0, acc); stage_step<3, SB>(slot, rd, rd, R0, acc);
                }
                STAMP(1, 2);
                LAS char* ex = lds + LDS_QX + 1024 + rnd * 1024;
                if (fr == 0) {
#pragma unroll
                    for (int jp = 0; jp < 3; ++jp) *(LAS f32x4*)(ex + wid * 256 + jp * 64 + fq * 16) = acc[4][2 * jp];
                }
                WAIT_LGKM0();
                CFENCE();
                __hip_atomic_store((LAS unsigned*)(lds + LDS_CTL + wid * 4), (unsigned)(rnd + 1), __ATOMIC_RELAXED, __HIP_MEMORY_SCOPE_WORKGROUP);
                const int s0 = tm * 128 + wm * 64;
                f32x2 st[4];
#pragma unroll
                for (int i = 0; i < 4; ++i) st[i] = *(LAS f32x2*)(lds + LDS_QX + (wm * 64 + i * 16 + fr) * 8);
                while (__builtin_amdgcn_readfirstlane(__hip_atomic_load((LAS unsigned*)(lds + LDS_CTL + (wid ^ 2) * 4), __ATOMIC_RELAXED, __HIP_MEMORY_SCOPE_WORKGROUP)) <= rnd) {}
                CFENCE();
#pragma unroll
                for (int jp = 0; jp < 3; ++jp) {
                    const int gh = qkv_head(fn, jp) + rnd * 16 + wn;
                    const float sc = jp == 0 ? 0.17677669529663687f * 1.4426950408889634f : 1.0f;
                    const f32x4 ca = acc[4][2 * jp], cb = *(LAS f32x4*)(ex + (wid ^ 2) * 256 + jp * 64 + fq * 16);
#pragma unroll
                    for (int i = 0; i < 4; ++i) {
                        const float r = st[i].y * sc, mr = -st[i].x * r;
                        const f32x4 a = acc[i][2 * jp] * r + ca * mr, b = acc[i][2 * jp + 1] * r + cb * mr;
                        __builtin_amdgcn_raw_buffer_store_b128(pair16(pack2(a[0], a[1]), pack2(a[2], a[3]), pack2(b[0], b[1]), pack2(b[2], b[3])), rs,
                                                               (unsigned)(((gh * S_ + s0 + i * 16 + fr) * 32 + ((fq & 1) ^ wm) * 16 + (fq >> 1) * 8) * 2), 0, SC1);
                    }
                }
            }
            STAMP(1, 3);
        }
    }

    constexpr int SB = SB_F, NS = NS_F;
    constexpr size_t OS = 2 * (S_ / 16) * 1024, WOS = 64 * D_ * 2, W2S = 64 * S_ * 2, US = 2 * 65536;
    const int tile = fm * 8 + fn, cls = (fm + fn) & 3, nf = isM ? NF_M : NF_Q;
    auto fof = [&](int i) {
        const int j = i < 7 ? (isM ? 0 : 7) + korder(i, cls, 7) : (isM ? 14 + NA : 14) + korder(i - 7, cls, nf - 7), k = 14 * (grp >> 1) + j;
        return k >= 56 ? k - 56 : k;
    };
    const char* g2 = (const char*)P.w2 + fm * 256;
    Loader<4, 4> L;
    if (loader) {
        L.init_m<TR>(lw, lane, S_ * 2);
        WAIT_VM(0);
        __builtin_amdgcn_s_waitcnt(0x0F70);
    }
    auto preload_w2 = [&]() {
#pragma unroll
        for (int u = 0; u < 2; ++u)
#pragma unroll
            for (int i = 0; i < 4; ++i) glds16_hidden_plain(g2 + fof(u) * W2S + L.om[i], lds + u * SB + (lw + 4 * i) * PIECE);
    };
    if (isM) {
        if (loader) preload_w2(); else WAIT_VM(0);
        BARRIER();
        if (tid == 0) set_flag(P.flags(), FL_U + um * 32 + un, tag);
        STAMP(2, 0);
    } else {
        if (loader) __builtin_amdgcn_s_setprio(1);
        WAIT_LGKM0();
        BARRIER();
#pragma unroll 1
        for (int it = 0; it < 2; ++it) {
            STAMP(2, 3 * it);
            f32x16 sc[8];
            int ln = lane;
            asm volatile("" : "+v"(ln));
            const int dg = (ln & 31) - 4 * (ln >> 5);
            const unsigned up = dg < 0 ? ~0u : ~1u << dg;
            const int nbad = qb == 0 ? 8 - wid : 0;
            SCHED0();
            const float m = attn_scores(lds + (it ? LDS_BK : LDS_AK), lds + LDS_AQ, nbad, wid, ln, up, sc);
            SCHED0();
            if (it == 0 || loader) WAIT_VM(0);
            BARRIER();
            if (it == 0) { if (tid == 0) set_flag(P.flags(), FL_Q + fm * 32 + fn + 8, tag); }
            else if (loader) preload_w2();
            STAMP(2, 3 * it + 1);
            PvState S;
            S.l0 = (f32x2){0.f, 0.f}; S.l1 = S.l0; S.o = (f32x16){};
            attn_pv<0, 8>(lds + (it ? LDS_BV : LDS_AV), nbad, wid, ln, up, sc, m, S);
            attn_out(P, hd + 16 * it, qb, wid, ln, S.l0 + S.l1, S.o);
            STAMP(2, 3 * it + 2);
            if (it == 0) {
                if (loader) {
                    poll_flags(P.flags(), attn_dep(hd + 16, qb, ln), tag);
                    attn_load<4, 0>(P, lds + LDS_AQ, hd + 16, qb, lw, ln);
                    attn_load<4, 1>(P, lds + LDS_BK, hd + 16, qb, lw, ln);
                    attn_load<4, 2>(P, lds + LDS_BV, hd + 16, qb, lw, ln);
                    WAIT_VM(8);
                }
                BARRIER();
            }
        }
        WAIT_LGKM0();
    }
    if (loader) {
        const char* go = (const char*)P.o() + (isM ? KW * OS : 0) + fm * 8192;
        const char* gw = (const char*)P.wo + (isM ? KW * WOS : 0) + fn * 256;
        const char* gu = (const char*)P.U() + fn * 8192;
        __builtin_amdgcn_s_setprio(0);
        L.init_n<FRAG>(lw, lane, US / 2);
        auto issue_f = [&](int u) {
            const int k = fof(u);
            L.issue_m<0>(g2 + k * W2S, lds + (u & (NS - 1)) * SB, lw); L.issue_n<SC1>(gu + k * US, lds + (u & (NS - 1)) * SB, lw);
        };
        poll_flags(P.flags(), lane < 2 ? FL_U + fn * 32 + (grp >> 1) * 4 + (isM ? 0 : 2) + lane : -1, tag);
#pragma unroll
        for (int u = 0; u < 2; ++u) L.issue_n<SC1>(gu + fof(u) * US, lds + u * SB, lw);
        WAIT_VM(4); BARRIER();
        issue_f(2); issue_f(3);
        WAIT_VM(16); BARRIER(); issue_f(4);
        WAIT_VM(16); BARRIER(); issue_f(5);
        WAIT_VM(16); BARRIER(); issue_f(6);
        const int udep = lane < 16 ? FL_U + fn * 32 + lane : -1;
        int nxt = 7;
        bool uready = false;
#pragma unroll 1
        for (int t = 3; t < 7; ++t) {
            if (t == 6 && !uready) { poll_flags(P.flags(), udep, tag); uready = true; for (; nxt < 10; ++nxt) issue_f(nxt); }
            if (nxt - (t + 2) >= 2) WAIT_VM(16); else WAIT_VM(0);
            BARRIER();
            if (!uready) uready = flags_set(P.flags(), udep, tag);
            if (uready) for (; nxt <= t + NS; ++nxt) issue_f(nxt);
        }
#pragma unroll 1
        for (int u = 7 + NS; u < nf; ++u) { WAIT_VM(16); BARRIER(); issue_f(u); }
        L.init_m<FRAG>(lw, lane, OS / 2); L.init_n<TR>(lw, lane, D_ * 2);
        if (lw == 0) poll_flags(P.flags(), lane < 16 ? FL_O + (fm >> 1) * 32 + (isM ? 16 : 0) + lane : -1, tag);
#pragma unroll 1
        for (int i = 0; i < KW; ++i) {
            WAIT_VM(16); BARRIER();
            const int k = korder(i, cls, KW);
            LAS char* dst = lds + ((nf + i) & (NS - 1)) * SB;
            L.issue_m<SC1>(go + k * OS, dst, lw); L.issue_n<0>(gw + k * WOS, dst, lw);
        }
        WAIT_VM(16); BARRIER();
        WAIT_VM(8);
        if (isM && lw == 0) poll_flags(P.flags(), lane == 0 ? FL_P + tile * 32 : -1, tag);
        BARRIER();
        WAIT_VM(0); BARRIER();
        BARRIER();
        if (!isM) BARRIER();
        if (blockIdx.x == 0 && lw == 0) {
#pragma unroll 1
            for (int i = 0; i < 4; ++i) poll_flags(P.flags(), FL_O + i * 64 + lane, tag);
            if (lane == 0) set_flag(P.flags(), FL_E, tag);
        }
    } else {
        const int fr = lane & 15, fq = lane >> 4;
        const int s0 = fm * 128 + wm * 64, d0 = fn * 128 + wn * 64;
        const __amdgpu_buffer_rsrc_t rp = rsrc(P.part() + (size_t)tile * 16384, 65536);
        f32x4 acc[4][4] = {};
        Reader<TR, 128, FRAG, 128, 4, 4> rf;
        Reader<FRAG, 128, TR, 128, 4, 4> rw;
        Regs<4, 4> R0;
        int slot = 0;
#define STEP(a, b) stage_step<NS, SB>(slot, a, b, R0, acc)
        rf.init(ring0, lane, wm, wn);
        BARRIER();
        STAMP(3, 1);
        rf.read(R0, 0);
        for (int t = 0; t < (nf - 1) / 4; ++t) {
            if (t == 1) WAIT_VM(0);
            if (t == 2 && !isM && tid == 0) { set_flag(P.flags(), FL_O + qb * 32 + hd, tag); set_flag(P.flags(), FL_O + qb * 32 + hd + 16, tag); }
            STEP(rf, rf); STEP(rf, rf); STEP(rf, rf); STEP(rf, rf);
        }
        for (int t = 0; t < ((nf - 1) & 3); ++t) STEP(rf, rf);
        rw.init(ring0 + slot * SB, lane, wm, wn);
        STEP(rf, rw);
        STAMP(3, 4);
        for (int t = 0; t < 2; ++t) { STEP(rw, rw); STEP(rw, rw); STEP(rw, rw); }
        u32x4 ex[4][4];
        if (isM) {
#pragma unroll
            for (int i = 0; i < 4; ++i)
#pragma unroll
                for (int j = 0; j < 4; ++j) ex[i][j] = __builtin_amdgcn_raw_buffer_load_b128(rp, (wid * 4096 + (i * 4 + j) * 256 + lane * 4) * 4, 0, SC1);
        } else {
#pragma unroll
            for (int i = 0; i < 4; ++i) {
#pragma unroll
                for (int j = 0; j < 4; ++j) { const u32x2 xv = *(const u32x2*)(P.x + (size_t)(s0 + i * 16 + fr) * D_ + d0 + j * 16 + fq * 4); ex[i][j][0] = xv[0]; ex[i][j][1] = xv[1]; }
                ex[i][0][2] = P.b2[s0 + i * 16 + fr];
            }
        }
        SCHED0();
        STEP(rw, rw); STEP(rw, rw);
        STAMP(3, 2);
        if (!isM) {
#pragma unroll
            for (int i = 0; i < 4; ++i)
#pragma unroll
                for (int j = 0; j < 4; ++j) {
                    const f32x4 v = acc[i][j] + (f32x4){lo16(ex[i][j][0]), hi16(ex[i][j][0]), lo16(ex[i][j][1]), hi16(ex[i][j][1])} + lo16(ex[i][0][2]);
                    __builtin_amdgcn_raw_buffer_store_b128(__builtin_bit_cast(u32x4, v), rp, (wid * 4096 + (i * 4 + j) * 256 + lane * 4) * 4, 0, SC1);
                }
            WAIT_VM(0);
            BARRIER();
            if (tid == 0) set_flag(P.flags(), FL_P + tile * 32, tag);
        } else {
#pragma unroll
            for (int i = 0; i < 4; ++i) {
#pragma unroll
                for (int j = 0; j < 4; ++j) acc[i][j] += __builtin_bit_cast(f32x4, ex[i][j]);
                u16* rowp = P.out + (size_t)(s0 + i * 16 + fr) * D_ + d0 + (fq & 1) * 16 + (fq >> 1) * 8;
#pragma unroll
                for (int jp = 0; jp < 2; ++jp) {
                    const f32x4 a = acc[i][2 * jp], b = acc[i][2 * jp + 1];
                    *(u32x4*)(rowp + jp * 32) = pair16(pack2(a[0], a[1]), pack2(a[2], a[3]), pack2(b[0], b[1]), pack2(b[2], b[3]));
                }
            }
        }
        STAMP(3, 3);
#undef STEP
    }
    STAMP_OUT();
}

extern "C" void kernel_launch(void* const* d_in, const int* in_sizes, int n_in, void* d_out, int out_size, void* d_ws, size_t ws_size, hipStream_t stream) {
    char* ws = (char*)d_ws;
    Params P;
    P.x = (const u16*)d_in[0]; P.wqkv = (const u16*)d_in[1]; P.wo = (const u16*)d_in[2];
    P.w1 = (const u16*)d_in[4]; P.b1 = (const u16*)d_in[5]; P.w2 = (const u16*)d_in[6]; P.b2 = (const u16*)d_in[7];
    P.out = (u16*)d_out; P.ws = ws;
    (void)hipFuncSetAttribute((const void*)k_main, hipFuncAttributeMaxDynamicSharedMemorySize, LDS_BYTES);
    k_main<<<256, 512, LDS_BYTES, stream>>>(P);
}
```

```cpp
#include <hip/hip_runtime.h>
#include <hip/hip_bf16.h>
#include <stdint.h>

#define LAS __attribute__((address_space(3)))
#define GAS __attribute__((address_space(1)))
typedef __bf16 bf16x8 __attribute__((ext_vector_type(8)));
typedef __bf16 bf16x4 __attribute__((ext_vector_type(4)));
typedef __bf16 bf16x2 __attribute__((ext_vector_type(2)));
typedef float f32x2 __attribute__((ext_vector_type(2)));
typedef float f32x4 __attribute__((ext_vector_type(4)));
typedef float f32x16 __attribute__((ext_vector_type(16)));
typedef unsigned u32x4 __attribute__((ext_vector_type(4)));
typedef unsigned u32x2 __attribute__((ext_vector_type(2)));
typedef unsigned short u16;
typedef unsigned long long u64;

constexpr int S_ = 2048, D_ = 1024, F_ = 3584, NH = 32;

constexpr size_t WS_FLAGS = 0;
constexpr size_t WS_STAT  = 1 << 16;
constexpr size_t WS_CS    = WS_STAT + (size_t)S_ * 8;
constexpr size_t WS_H     = 1 << 17;
constexpr size_t WS_QKV   = WS_H   + (size_t)S_ * D_ * 2;
constexpr size_t WS_U     = WS_QKV + (size_t)S_ * 3 * D_ * 2;
constexpr size_t WS_O     = WS_U   + (size_t)D_ * F_ * 2;
constexpr size_t WS_PART  = WS_O   + (size_t)S_ * D_ * 2;
constexpr int FL_E = 0;
constexpr int FL_L = 32;
constexpr int FL_C = 5 * 32;
constexpr int FL_U = 8 * 32;
constexpr int FL_Q = 16 * 32;
constexpr int FL_O = 32 * 32;
constexpr int FL_P = 40 * 32;

constexpr size_t WS_DBG = 200u << 20;
#define STAMP_DECL
#define STAMP(kid, i) do {} while (0)
#define STAMPL(kid, i) do {} while (0)
#define STAMP_OUT() do {} while (0)
#define WAIT_VM(n) asm volatile("s_waitcnt vmcnt(%0)" ::"n"(n) : "memory")
#define WAIT_LGKM0() __builtin_amdgcn_s_waitcnt(0xC07F)
#define CFENCE() asm volatile("" ::: "memory")
#define BARRIER() do { CFENCE(); __builtin_amdgcn_s_barrier(); CFENCE(); } while (0)
#define SCHED0() __builtin_amdgcn_sched_barrier(0)
#define RLX_AGENT __ATOMIC_RELAXED, __HIP_MEMORY_SCOPE_AGENT
constexpr int SC1 = 16;

__device__ __forceinline__ float bf2f(u16 v) { return __uint_as_float((unsigned)v << 16); }
__device__ __forceinline__ float lo16(unsigned v) { return __uint_as_float(v << 16); }
__device__ __forceinline__ float hi16(unsigned v) { return __uint_as_float(v & 0xffff0000u); }
__device__ __forceinline__ unsigned pack2(float lo, float hi) {
    f32x2 v = {lo, hi};
    bf16x2 b = __builtin_convertvector(v, bf16x2);
    return __builtin_bit_cast(unsigned, b);
}
__device__ __forceinline__ float rbf(float x) { return __uint_as_float(pack2(x, 0.f) << 16); }
template <int CTRL, int RMASK> __device__ __forceinline__ float dpp_add(float v) {
    return v + __builtin_bit_cast(float, __builtin_amdgcn_update_dpp(0, __builtin_bit_cast(int, v), CTRL, RMASK, 0xf, false));
}
__device__ __forceinline__ float wave_sum(float v) {
    v = dpp_add<0x111, 0xf>(v); v = dpp_add<0x112, 0xf>(v); v = dpp_add<0x114, 0xf>(v); v = dpp_add<0x118, 0xf>(v);
    v = dpp_add<0x142, 0xa>(v); v = dpp_add<0x143, 0xc>(v);
    return __builtin_bit_cast(float, __builtin_amdgcn_readlane(__builtin_bit_cast(int, v), 63));
}
__device__ __forceinline__ __amdgpu_buffer_rsrc_t rsrc(const void* p, unsigned bytes) { return __builtin_amdgcn_make_buffer_rsrc((void*)p, 0, bytes, 0x00020000); }

enum { ROW = 0, TR = 1, FRAG = 2 };
constexpr int PIECE = 1056, BLK = 8 * PIECE;
template <int T, int X> struct Op {
    static constexpr int BYTES = X * 132;
    __device__ static __forceinline__ unsigned src_off(int i, int lane, unsigned ld, unsigned cb) {
        const int blk = i >> 3, p = i & 7, pos = lane >> 3;
        if (T == TR) {
            if (X == 224 && blk == 3) {
                const int ps = lane >> 2, g = (p & 1) | ((ps >> 3) << 1), r = 32 * (p >> 1) + 8 * g + 4 * ((ps >> 2) & 1) + (ps & 3);
                return (unsigned)r * ld + cb + (unsigned)((lane & 3) * 16);
            }
            const int g = (p >> 2) | (pos & 2), r = 32 * (pos >> 2) + 8 * g + 4 * (pos & 1) + (p & 3);
            return (unsigned)r * ld + cb + (unsigned)((lane & 7) * 16);
        } else if (T == ROW) {
            const int j = ((pos >> 1) & 1) | ((pos & 1) << 1) | (pos & 4);
            return (unsigned)(blk * 64 + j * 8 + p) * ld + (unsigned)((lane & 7) * 16);
        } else {
            return (unsigned)blk * ld + (unsigned)(p * 1024 + lane * 16);
        }
    }
};
template <int AUX> __device__ __forceinline__ void glds16(const char* g, LAS char* l) { __builtin_amdgcn_global_load_lds((const GAS unsigned*)g, (LAS unsigned*)l, 16, 0, AUX); }
__device__ __forceinline__ void glds16_hidden_plain(const char* g, LAS char* l) {
    unsigned keep;
    asm volatile("s_mov_b32 %0, m0\n\ts_mov_b32 m0, %2\n\ts_nop 0\n\tglobal_load_lds_dwordx4 %1, off\n\ts_mov_b32 m0, %0"
                 : "=&s"(keep) : "v"(g), "s"(__builtin_amdgcn_readfirstlane((unsigned)(uintptr_t)l)) : "memory");
}
__device__ __forceinline__ void glds16_hidden(const char* g, LAS char* l) {
    unsigned keep;
    asm volatile("s_mov_b32 %0, m0\n\ts_mov_b32 m0, %2\n\ts_nop 0\n\tglobal_load_lds_dwordx4 %1, off sc1\n\ts_mov_b32 m0, %0"
                 : "=&s"(keep) : "v"(g), "s"(__builtin_amdgcn_readfirstlane((unsigned)(uintptr_t)l)) : "memory");
}
__device__ __forceinline__ void glds4(const char* g, LAS char* l) { __builtin_amdgcn_global_load_lds((const GAS unsigned*)g, (LAS unsigned*)l, 4, 0, 0); }

template <int T, int X> struct Addr {
    unsigned a, b;
    static constexpr int NV = (X == 224 || X == 192) ? 2 : 1;
    __device__ __forceinline__ void init(unsigned base, int lane, int w) {
        const int g = lane >> 4;
        b = 0;
        if (T == TR) {
            const int q = (lane & 15) >> 2, p = lane & 3;
            a = base + (unsigned)((q | ((g & 1) << 2)) * PIECE + (g >> 1) * 256 + p * 8 + (X == 128 ? w * BLK : w * 64));
            b = base + (unsigned)(3 * BLK + (g & 1) * PIECE + (g >> 1) * 512 + q * 64 + w * 32 + p * 8);
        } else if (T == ROW) {
            const int r = lane & 15;
            a = base + (unsigned)(w * BLK + (r & 7) * PIECE + (r >> 3) * 256 + g * 16);
        } else {
            a = base + (unsigned)(w * 4 * PIECE + lane * 16);
        }
    }
    __device__ __forceinline__ void step(unsigned d) { a += d; if (NV == 2) b += d; }
    __device__ __forceinline__ bf16x8 read(int n, int kk) const {
        if (T == TR) {
            LAS char* p; int t;
            if (X == 224 && n == 6) { p = (LAS char*)(uintptr_t)b + kk * 2 * PIECE; t = 256; }
            else if (X == 192) { p = (LAS char*)(uintptr_t)((n & 1) ? b : a) + kk * 512 + (n >> 1) * BLK; t = 128; }
            else { p = (LAS char*)(uintptr_t)a + kk * 512 + (X == 128 ? n * 32 : (n >> 1) * BLK + (n & 1) * 32); t = 128; }
            const bf16x4 lo = __builtin_amdgcn_ds_read_tr16_b64_v4bf16((LAS bf16x4*)p);
            const bf16x4 hi = __builtin_amdgcn_ds_read_tr16_b64_v4bf16((LAS bf16x4*)(p + t));
            return __builtin_shufflevector(lo, hi, 0, 1, 2, 3, 4, 5, 6, 7);
        } else if (T == ROW) {
            return *(LAS bf16x8*)((LAS char*)(uintptr_t)a + (n & 1) * 128 + (n >> 1) * 512 + kk * 64);
        } else {
            return *(LAS bf16x8*)((LAS char*)(uintptr_t)a + kk * BLK + n * PIECE);
        }
    }
};
template <int FM, int FN> struct Regs { bf16x8 m[FM], n[FN]; };
#define MFMA16(a, b, c) __builtin_amdgcn_mfma_f32_16x16x32_bf16(a, b, c, 0, 0, 0)

template <int TM, int BM, int TN, int BN, int FM, int FN> struct Reader {
    Addr<TM, BM> m; Addr<TN, BN> n;
    static constexpr int NDS = FM * (TM == TR ? 2 : 1) + FN * (TN == TR ? 2 : 1), NV = Addr<TM, BM>::NV + Addr<TN, BN>::NV;
    __device__ __forceinline__ void init(unsigned slot_base, int lane, int wm, int wn) { m.init(slot_base, lane, wm); n.init(slot_base + BM * 132, lane, wn); }
    __device__ __forceinline__ void read(Regs<FM, FN>& R, int kk) const {
#pragma unroll
        for (int i = 0; i < FM; ++i) R.m[i] = m.read(i, kk);
#pragma unroll
        for (int j = 0; j < FN; ++j) R.n[j] = n.read(j, kk);
    }
    __device__ __forceinline__ void step(unsigned d) { m.step(d); n.step(d); }
};
template <int AM, int FM, int FN> __device__ __forceinline__ void mma(f32x4 (&acc)[AM][FN], const Regs<FM, FN>& R) {
    const u32x4 one = {0x3f803f80u, 0x3f803f80u, 0x3f803f80u, 0x3f803f80u};
#pragma unroll
    for (int j = 0; j < FN; ++j) {
#pragma unroll
        for (int i = 0; i < FM; ++i) acc[i][j] = MFMA16(R.n[j], R.m[i], acc[i][j]);
        if (AM > FM && !(j & 1)) acc[AM - 1][j] = MFMA16(R.n[j], __builtin_bit_cast(bf16x8, one), acc[AM - 1][j]);
    }
}
template <int NDS, int NV, int NMF> __device__ __forceinline__ void interleave() {
#pragma unroll
    for (int i = 0; i < NMF; ++i) {
        __builtin_amdgcn_sched_group_barrier(0x8, 1, 0);
        if (i < NDS) __builtin_amdgcn_sched_group_barrier(0x80, 1, 0);
        if (i >= NMF - NV) __builtin_amdgcn_sched_group_barrier(0x2, 1, 0);
    }
}
template <int NS, int SB, class RD, class RDN, int AM, int FM, int FN>
__device__ __forceinline__ void stage_step(int& slot, RD& rd, RDN& rn, Regs<FM, FN>& R0, f32x4 (&acc)[AM][FN]) {
    constexpr int NMF = FM * FN + (AM - FM) * (FN / 2);
    Regs<FM, FN> R1;
    const unsigned d = (slot + 1 == NS) ? (unsigned)(-(NS - 1) * SB) : (unsigned)SB;
    slot = (slot + 1 == NS) ? 0 : slot + 1;
    rd.read(R1, 1);
    rn.step(d);
    mma(acc, R0);
    interleave<RD::NDS, RDN::NV, NMF>();
    SCHED0();
    WAIT_LGKM0();
    BARRIER();
    SCHED0();
    rn.read(R0, 0);
    mma(acc, R1);
    interleave<RDN::NDS, 0, NMF>();
    SCHED0();
}

template <int NM, int NN> struct Loader {
    static constexpr int NPW = NM + NN;
    unsigned om[NM], on[NN];
    template <int T> __device__ __forceinline__ void init_m(int lw, int lane, unsigned ld) {
#pragma unroll
        for (int i = 0; i < NM; ++i) om[i] = Op<T, NM * 32>::src_off(lw + 4 * i, lane, ld, (i >> 1) * 128);
    }
    template <int T> __device__ __forceinline__ void init_n(int lw, int lane, unsigned ld, unsigned c0 = 0, unsigned c1 = 128, unsigned c2 = 256, unsigned c3 = 384) {
#pragma unroll
        for (int i = 0; i < NN; ++i) on[i] = Op<T, NN * 32>::src_off(lw + 4 * i, lane, ld, (i >> 1) == 0 ? c0 : (i >> 1) == 1 ? c1 : (i >> 1) == 2 ? c2 : c3);
    }
    template <int AUX> __device__ __forceinline__ void issue_m(const char* gm, LAS char* dst, int lw) const {
#pragma unroll
        for (int i = 0; i < NM; ++i) glds16<AUX>(gm + om[i], dst + (lw + 4 * i) * PIECE);
    }
    template <int AUX> __device__ __forceinline__ void issue_n(const char* gn, LAS char* dst, int lw) const {
#pragma unroll
        for (int i = 0; i < NN; ++i) glds16<AUX>(gn + on[i], dst + NM * 32 * 132 + (lw + 4 * i) * PIECE);
    }
};
#define LEAD 3
__device__ __forceinline__ int korder(int p, int c, int nk) {
    const int r = (p - c) & 3, p0 = p - r;
    if (r > LEAD || p0 < 0 || p0 + LEAD >= nk) return p;
    return r == 0 ? p + LEAD : p - 1;
}
__device__ __forceinline__ f32x2 gelu2(f32x2 u) {
    const float c1 = -2.0f * 0.7978845608028654f * 1.4426950408889634f, c2 = c1 * 0.044715f;
    const f32x2 z = u * ((u * u) * c2 + c1);
    f32x2 e; e.x = __builtin_amdgcn_exp2f(z.x); e.y = __builtin_amdgcn_exp2f(z.y);
    e = e + 1.0f;
    f32x2 r; r.x = __builtin_amdgcn_rcpf(e.x); r.y = __builtin_amdgcn_rcpf(e.y);
    return u * r;
}
__device__ __forceinline__ u32x4 pair16(unsigned x0, unsigned x1, unsigned y0, unsigned y1) {
    auto a = __builtin_amdgcn_permlane16_swap(x0, y0, false, false);
    auto b = __builtin_amdgcn_permlane16_swap(x1, y1, false, false);
    return (u32x4){a[0], b[0], a[1], b[1]};
}
__device__ __forceinline__ bool flags_set(u64* flags, int idx, u64 tag) {
    const u64 v = __hip_atomic_load((GAS u64*)flags + (idx < 0 ? 0 : idx), RLX_AGENT);
    const bool ok = __builtin_amdgcn_ballot_w64(idx >= 0 && v - 1ull != tag) == 0ull;
    CFENCE();
    return ok;
}
__device__ __forceinline__ void poll_flags(u64* flags, int idx, u64 tag) {
#pragma unroll 1
    for (unsigned spins = 0; spins < (1u << 20); ++spins) {
        if (flags_set(flags, idx, tag)) break;
        __builtin_amdgcn_s_sleep(2);
    }
}
__device__ __forceinline__ void set_flag(u64* flags, int idx, u64 tag) {
    asm volatile("" : "+v"(tag));
    __hip_atomic_store((GAS u64*)flags + idx, tag + 1ull, RLX_AGENT);
}

struct Params {
    const u16 *x, *wqkv, *wo, *w1, *b1, *w2, *b2;
    u16* out;
    char* ws;
    __device__ __forceinline__ u16* h() const { return (u16*)(ws + WS_H); }
    __device__ __forceinline__ u16* qkv() const { return (u16*)(ws + WS_QKV); }
    __device__ __forceinline__ u16* U() const { return (u16*)(ws + WS_U); }
    __device__ __forceinline__ u16* o() const { return (u16*)(ws + WS_O); }
    __device__ __forceinline__ float* stat() const { return (float*)(ws + WS_STAT); }
    __device__ __forceinline__ float* cs() const { return (float*)(ws + WS_CS); }
    __device__ __forceinline__ float* part() const { return (float*)(ws + WS_PART); }
    __device__ __forceinline__ u64* flags() const { return (u64*)(ws + WS_FLAGS); }
};

constexpr int SB_M = (128 + 224) * 132, SB_Q = (128 + 192) * 132, SB_F = 256 * 132, NS_F = 4;
constexpr int LDS_QX = 3 * SB_Q;
constexpr int LDS_CTL = LDS_QX + 3072;
constexpr int LDS_AQ = 0, LDS_AK = 2 * SB_Q, LDS_AV = 128 * 1024, LDS_BYTES = 160 * 1024;
constexpr int LDS_BK = 16 * 1024, LDS_BV = 96 * 1024;
constexpr int LDS_SINK = LDS_BYTES - 256;
static_assert(LDS_CTL + 32 <= LDS_AV && 3 * SB_M <= LDS_SINK && 3 * SB_F <= LDS_AV && NS_F * SB_F <= LDS_BYTES, "LDS map");
#define NA 10
#define PF2 1
constexpr int KW = 8, NF_Q = 7 + NA, NF_M = 7 + 42 - NA;

__device__ __forceinline__ void prefetch_next(const Params& P, LAS char* lds, int batch, int lw, int lane) {
    const int piece = batch * 4 + lw;
    const char* src = piece < 7 ? (const char*)P.w2 + ((size_t)piece * 256 + blockIdx.x) * 8192 : (const char*)P.wo + (size_t)blockIdx.x * 8192;
    glds4(src + lane * 128, lds + LDS_SINK);
}
__device__ __forceinline__ int qkv_head(int tn, int blk) { return blk * NH + ((tn & ~3) | ((tn + blk) & 3)) * 2; }

__device__ __forceinline__ f32x2 ln_row(const u32x4 a, const u32x4 b, u32x4& oa, u32x4& ob) {
    f32x2 v[8];
#pragma unroll
    for (int i = 0; i < 4; ++i) { v[i] = (f32x2){lo16(a[i]), hi16(a[i])}; v[4 + i] = (f32x2){lo16(b[i]), hi16(b[i])}; }
    const f32x2 s2 = ((v[0] + v[1]) + (v[2] + v[3])) + ((v[4] + v[5]) + (v[6] + v[7]));
    const float mean = wave_sum(s2.x + s2.y) * (1.0f / D_);
    f32x2 q2 = {0.f, 0.f};
#pragma unroll
    for (int i = 0; i < 8; ++i) { const f32x2 d = v[i] - mean; q2 += d * d; }
    const float var = wave_sum(q2.x + q2.y) * (1.0f / D_);
    const float mb = rbf(mean), rs = __builtin_amdgcn_rcpf(rbf(__builtin_amdgcn_sqrtf(rbf(rbf(var) + 1e-5f))));
    const float mb2 = rbf((mean - mb) * rs), rs2 = __builtin_amdgcn_rcpf(rbf(__builtin_amdgcn_sqrtf(rbf(rbf(var * rs * rs) + 1e-5f))));
    const float g = rs * rs2, c = -mb2 * rs2;
#pragma unroll
    for (int i = 0; i < 8; ++i) {
        const f32x2 t = (v[i] - mb) * g + c;
        if (i < 4) oa[i] = pack2(t.x, t.y); else ob[i - 4] = pack2(t.x, t.y);
    }
    return (f32x2){mb, rs};
}

#define MFMA32(a, b, c) __builtin_amdgcn_mfma_f32_32x32x16_bf16(a, b, c, 0, 0, 0)
__device__ __forceinline__ int attn_dep(int hd, int qb, int lane) {
    const int which = lane >> 2, st = 2 * qb - 2 + (lane & 3), p = hd >> 1;
    if (lane >= 12 || st < 0 || (which == 0 && (lane & 3) < 2)) return -1;
    return FL_Q + st * 32 + ((p & ~3) | ((p - which) & 3));
}
template <int NW, int WHICH> __device__ __forceinline__ void attn_load(const Params& P, LAS char* dst, int hd, int qb, int w, int lane) {
    const char* g = (const char*)(P.qkv() + (size_t)(WHICH * NH + hd) * S_ * 32) + (WHICH == 2 ? lane & 3 : (lane & 3) ^ ((lane >> 4) & 3)) * 16;
#pragma unroll
    for (int i = 0; i < (WHICH == 0 ? 16 : 32) / NW; ++i) {
        const int pc = i * NW + w;
        int row = qb * 256 - (WHICH == 0 ? 0 : 256) + pc * 16 + (lane >> 2); row = row < 0 ? 0 : row;
        glds16_hidden(g + row * 64, dst + pc * 1024);
    }
}
#define PIN(x) asm volatile("" : "+v"(x))
__device__ __forceinline__ float max16(const f32x16& v) {
    float a = __builtin_fmaxf(__builtin_fmaxf(v[0], v[1]), v[2]), b = __builtin_fmaxf(__builtin_fmaxf(v[3], v[4]), v[5]);
    a = __builtin_fmaxf(__builtin_fmaxf(a, v[6]), v[7]); b = __builtin_fmaxf(__builtin_fmaxf(b, v[8]), v[9]);
    a = __builtin_fmaxf(__builtin_fmaxf(a, v[10]), v[11]); b = __builtin_fmaxf(__builtin_fmaxf(b, v[12]), v[13]);
    a = __builtin_fmaxf(__builtin_fmaxf(a, v[14]), v[15]);
    return __builtin_fmaxf(a, b);
}
__device__ __forceinline__ float attn_scores(LAS char* buf, LAS char* qbuf, int nbad, int wid, int lane, unsigned up, f32x16 (&sc)[8]) {
    const int r32 = lane & 31, hh = lane >> 5, sw = (r32 >> 2) & 3;
    LAS char* k0p = buf + (wid * 32 + r32) * 64 + ((hh ^ sw) * 16);
    LAS char* k1p = buf + (wid * 32 + r32) * 64 + (((2 + hh) ^ sw) * 16);
    const bf16x8 qf0 = *(LAS bf16x8*)(qbuf + (wid * 32 + r32) * 64 + ((hh ^ sw) * 16));
    const bf16x8 qf1 = *(LAS bf16x8*)(qbuf + (wid * 32 + r32) * 64 + (((2 + hh) ^ sw) * 16));
    bf16x8 ka[9], kb[9];
#pragma unroll
    for (int i = 0; i < 3; ++i) { const int j = i == 0 ? 8 : i - 1; ka[i] = *(LAS bf16x8*)(k0p + j * 2048); kb[i] = *(LAS bf16x8*)(k1p + j * 2048); SCHED0(); }
    const float NEG = -INFINITY;
    const f32x16 zero = {};
    const float b0 = nbad > 0 ? NEG : 0.f;
    f32x16 s8;
    float m = NEG;
#pragma unroll
    for (int i = 0; i < 11; ++i) {
        if (i + 3 < 9) { ka[i + 3] = *(LAS bf16x8*)(k0p + (i + 2) * 2048); kb[i + 3] = *(LAS bf16x8*)(k1p + (i + 2) * 2048); }
        if (i < 9) {
            const f32x16 t = MFMA32(kb[i], qf1, MFMA32(ka[i], qf0, zero));
            if (i == 0) s8 = t; else sc[i - 1] = t;
        }
        SCHED0();
        const int j = i - 3;
        if (j == 0) {
#pragma unroll
            for (int r = 0; r < 16; ++r) {
                const unsigned mk = (unsigned)__builtin_amdgcn_sbfe((int)up, (r & 3) + 8 * (r >> 2), 1);
                sc[0][r] = __uint_as_float(__builtin_amdgcn_bitop3_b32(mk, __float_as_uint(sc[0][r] + b0), __float_as_uint(s8[r]), 0xca));
            }
        }
        if (j >= 0) { const float mj = max16(sc[j]); m = __builtin_fmaxf(m, (j > 0 && j < nbad) ? NEG : mj); PIN(m); }
        SCHED0();
    }
    auto rr = __builtin_amdgcn_permlane32_swap(__float_as_uint(m), __float_as_uint(m), false, false);
    return __builtin_fmaxf(__uint_as_float(rr[0]), __uint_as_float(rr[1]));
}
struct PvState { f32x2 l0, l1; f32x16 o; bf16x8 vf[2], v8[2]; u32x4 pw[2], p8[2]; };
__device__ __forceinline__ bf16x8 v_read(LAS char* vp, int j, int s) {
    const bf16x4 lo = __builtin_amdgcn_ds_read_tr16_b64_v4bf16((LAS bf16x4*)(vp + j * 2048 + s * 1024));
    const bf16x4 hi = __builtin_amdgcn_ds_read_tr16_b64_v4bf16((LAS bf16x4*)(vp + j * 2048 + s * 1024 + 512));
    return __builtin_shufflevector(lo, hi, 0, 1, 2, 3, 4, 5, 6, 7);
}
template <int J0, int J1> __device__ __forceinline__ void attn_pv(LAS char* buf, int nbad, int wid, int lane, unsigned up, f32x16 (&sc)[8], float m, PvState& S) {
    const int hh = lane >> 5;
    LAS char* vp = buf + (wid * 32 + 4 * hh + ((lane & 15) >> 2)) * 64 + ((lane >> 4) & 1) * 32 + (lane & 3) * 8;
#pragma unroll
    for (int j = J0; j < J1; ++j) {
        bf16x8 vn[2] = {v_read(vp, j, 0), v_read(vp, j, 1)};
        if (j == 0) { S.v8[0] = v_read(vp, 8, 0); S.v8[1] = v_read(vp, 8, 1); }
        const float mj = (j > 0 && j < nbad) ? INFINITY : m;
        f32x2 p[8];
#pragma unroll
        for (int i = 0; i < 8; ++i) p[i] = (f32x2){sc[j][2 * i], sc[j][2 * i + 1]} - mj;
        SCHED0();
#pragma unroll
        for (int g = 0; g < 4; ++g) {
#pragma unroll
            for (int i = 2 * g; i < 2 * g + 2; ++i) { p[i].x = __builtin_amdgcn_exp2f(p[i].x); p[i].y = __builtin_amdgcn_exp2f(p[i].y); PIN(p[i]); }
            SCHED0();
            if (j > 0 && (g & 1) == 0) S.o = MFMA32(S.vf[g >> 1], __builtin_bit_cast(bf16x8, S.pw[g >> 1]), S.o);
            if (j == 1 && (g & 1) == 1) S.o = MFMA32(S.v8[g >> 1], __builtin_bit_cast(bf16x8, S.p8[g >> 1]), S.o);
            SCHED0();
        }
#pragma unroll
        for (int i = 0; i < 8; ++i) {
            S.pw[i >> 2][i & 3] = pack2(p[i].x, p[i].y);
            if (i & 1) S.l1 += p[i]; else S.l0 += p[i];
        }
        if (j == 0) {
#pragma unroll
            for (int i = 0; i < 8; ++i) {
                const int kr = ((2 * i) & 3) + 8 * (i >> 1);
                const unsigned mk = __builtin_amdgcn_bitop3_b32(0xffffu, (unsigned)__builtin_amdgcn_sbfe((int)up, kr, 1), (unsigned)__builtin_amdgcn_sbfe((int)up, kr + 1, 1), 0xca);
                S.p8[i >> 2][i & 3] = S.pw[i >> 2][i & 3] & ~mk; S.pw[i >> 2][i & 3] &= mk;
            }
        }
        S.vf[0] = vn[0]; S.vf[1] = vn[1];
        PIN(S.l0); PIN(S.l1); PIN(S.pw[0]); PIN(S.pw[1]);
        SCHED0();
    }
    if (J1 == 8) {
        S.o = MFMA32(S.vf[0], __builtin_bit_cast(bf16x8, S.pw[0]), S.o);
        S.o = MFMA32(S.vf[1], __builtin_bit_cast(bf16x8, S.pw[1]), S.o);
    }
}
__device__ __forceinline__ void attn_out(const Params& P, int hd, int qb, int wid, int lane, f32x2 l2, const f32x16& oacc) {
    const int r32 = lane & 31, hh = lane >> 5;
    float l = l2.x + l2.y;
    {
        auto rr = __builtin_amdgcn_permlane32_swap(__float_as_uint(l), __float_as_uint(l), false, false);
        l = __uint_as_float(rr[0]) + __uint_as_float(rr[1]);
    }
    const float inv = __builtin_amdgcn_rcpf(l);
    unsigned w[8];
#pragma unroll
    for (int g = 0; g < 4; ++g) { w[2 * g] = pack2(oacc[4 * g] * inv, oacc[4 * g + 1] * inv); w[2 * g + 1] = pack2(oacc[4 * g + 2] * inv, oacc[4 * g + 3] * inv); }
    const __amdgpu_buffer_rsrc_t rs = rsrc(P.o(), (unsigned)S_ * D_ * 2);
    const unsigned off = (unsigned)((hd * (S_ / 16) + qb * 16 + wid * 2 + (r32 >> 4)) * 1024 + (hh * 16 + (r32 & 15)) * 16);
#pragma unroll
    for (int gp = 0; gp < 2; ++gp) {
        auto a = __builtin_amdgcn_permlane32_swap(w[4 * gp], w[4 * gp + 2], false, false);
        auto b = __builtin_amdgcn_permlane32_swap(w[4 * gp + 1], w[4 * gp + 3], false, false);
        __builtin_amdgcn_raw_buffer_store_b128((u32x4){a[0], b[0], a[1], b[1]}, rs, off + gp * 512, 0, SC1);
    }
}

extern "C" __global__ __launch_bounds__(512, 2) void k_main(const Params P) {
    extern __shared__ __attribute__((aligned(16))) char smem[];
    LAS char* lds = (LAS char*)smem;
    const int tid = threadIdx.x, lane = tid & 63, wid = __builtin_amdgcn_readfirstlane(tid >> 6);
    const int grp = blockIdx.x & 7, idx = blockIdx.x >> 3;
    const bool loader = wid >= 4;
    const int lw = wid - 4, wm = wid >> 1, wn = wid & 1;
    const unsigned ring0 = (unsigned)(uintptr_t)lds;
    const int pf_at = 4 + (idx % 12);
    const int i16 = idx & 15;
    const bool isM = idx < 16;
    const int fm = (grp >> 1) * 4 + (i16 >> 2), fn = (grp & 1) * 4 + (i16 & 3);
    const int um = (grp & 1) * 4 + (i16 >> 2), un = (grp >> 1) * 4 + (i16 & 3);
    const int hd = (grp & 1) * 8 + (i16 & 7), qb = (grp >> 1) * 2 + (i16 >> 3);
    STAMP_DECL;
    STAMP(1, 0);
#define PFC 1
    if (PFC && wid == 0) {
        u64 pc; unsigned rel, len;
        asm volatile("s_getpc_b64 %0\n1:\n\ts_mov_b32 %1, 1b-k_main\n\ts_mov_b32 %2, 2f-k_main\n\t.subsection 1\n2:\n\t.subsection 0" : "=s"(pc), "=s"(rel), "=s"(len));
        const unsigned off = (unsigned)((idx + 32 * (lane & 15)) * 128);
        glds4((const char*)(pc - rel) + (off < len ? off : 0u), lds + LDS_SINK);
    }
    const u64 tag = __hip_atomic_load((GAS u64*)P.flags() + FL_E, RLX_AGENT);

    if (!isM && tid < 8) *(LAS unsigned*)(lds + LDS_CTL + tid * 4) = 0u;
    if (isM) {
        constexpr int NK = S_ / 64, SB = SB_M;
        const int pg = (grp >> 1) + 4 * (grp & 1);
        const int tm = um, tn = un, mi = pg * 16 + i16;
        const char* gm = (const char*)P.h() + tm * 256; const char* gn = (const char*)P.w1 + tn * 448;
        const int cls = (tm + tn) & 3;
        auto kof = [&](int u) { const int p0 = u - ((u - cls) & 3); return (((p0 == 1 || p0 == 2) ? u : korder(u, cls, NK)) + 4 * pg) & (NK - 1); };
        Loader<4, 7> L;
        {
            const int row = mi * 16 + wid * 2;
            const u32x4* px = (const u32x4*)(P.x + (size_t)row * D_);
            u32x4 a0 = px[lane], b0 = px[64 + lane], a1 = px[128 + lane], b1 = px[192 + lane];
            SCHED0();
            if (loader) {
                const unsigned c0 = (tn & 1) * 64;
                L.init_m<TR>(lw, lane, D_ * 2);
                L.init_n<TR>(lw, lane, F_ * 2, c0, c0 + 128, c0 + 256, (tn & 1) ? 0 : 384);
                asm volatile("" : "+v"(a0), "+v"(b0), "+v"(a1), "+v"(b1));
                L.issue_n<0>(gn + (size_t)kof(0) * (64 * F_ * 2), lds, lw);
            }
            STAMPL(0, 0);
            const __amdgpu_buffer_rsrc_t rh = rsrc(P.h(), (unsigned)S_ * D_ * 2);
            u32x4 oa0, ob0, oa1, ob1;
            const f32x2 s0 = ln_row(a0, b0, oa0, ob0), s1 = ln_row(a1, b1, oa1, ob1);
            __builtin_amdgcn_raw_buffer_store_b128(oa0, rh, (unsigned)(row * 2048 + lane * 16), 0, SC1);
            __builtin_amdgcn_raw_buffer_store_b128(ob0, rh, (unsigned)(row * 2048 + 1024 + lane * 16), 0, SC1);
            __builtin_amdgcn_raw_buffer_store_b128(oa1, rh, (unsigned)(row * 2048 + 2048 + lane * 16), 0, SC1);
            __builtin_amdgcn_raw_buffer_store_b128(ob1, rh, (unsigned)(row * 2048 + 3072 + lane * 16), 0, SC1);
            if (lane == 0) __builtin_amdgcn_raw_buffer_store_b128(__builtin_bit_cast(u32x4, (f32x4){s0.x, s0.y, s1.x, s1.y}), rsrc(P.stat(), S_ * 8), (unsigned)(row * 8), 0, SC1);
            SCHED0();
            STAMPL(0, 1);
        }
        if (loader) {
            WAIT_VM(0);
            BARRIER();
            STAMPL(0, 2);
            if (tid == 256 + 64) set_flag(P.flags(), FL_L + mi, tag);
#pragma unroll
            for (int u = 1; u < 3; ++u) L.issue_n<0>(gn + (size_t)kof(u) * (64 * F_ * 2), lds + u * SB, lw);
            if (lw == 0) poll_flags(P.flags(), lane < 16 ? FL_L + pg * 16 + lane : -1, tag);
            STAMPL(0, 3);
            BARRIER();
#pragma unroll
            for (int u = 0; u < 3; ++u) L.issue_m<SC1>(gm + (size_t)kof(u) * (64 * D_ * 2), lds + u * SB, lw);
            WAIT_VM(8); BARRIER();
            int slot = 0, t = 0;
            auto issue = [&]() {
                const int k = kof(t + 3);
                L.issue_m<SC1>(gm + (size_t)k * (64 * D_ * 2), lds + slot * SB, lw);
                L.issue_n<0>(gn + (size_t)k * (64 * F_ * 2), lds + slot * SB, lw);
                slot = (slot == 2) ? 0 : slot + 1;
            };
            WAIT_VM(4);
            if (lw == 0) poll_flags(P.flags(), lane < 16 ? FL_L + ((pg + 1) & 7) * 16 + lane : -1, tag);
            BARRIER(); issue(); ++t;
            WAIT_VM(L.NPW); BARRIER(); issue(); ++t;
            WAIT_VM(L.NPW);
            if (lw == 0) { poll_flags(P.flags(), FL_L + lane, tag); poll_flags(P.flags(), FL_L + 64 + lane, tag); }
            BARRIER(); issue(); ++t;
#pragma unroll 1
            for (int seg = 0; seg < 3; ++seg) {
                const int end = seg == 0 ? pf_at : seg == 1 ? pf_at + 12 : NK - 3;
#pragma unroll 1
                for (; t < end; ++t) { WAIT_VM(L.NPW); BARRIER(); issue(); }
                if (PF2 && seg < 2) prefetch_next(P, lds, seg, lw, lane);
            }
            WAIT_VM(L.NPW); BARRIER();
            WAIT_VM(0); BARRIER();
            BARRIER();
        } else {
            const int fr = lane & 15, fq = lane >> 4;
            const int fb = tn * 224 + ((tn & 1) ? 32 : 0) + wn * 32, fh = tn * 224 + ((tn & 1) ? 0 : 192) + wn * 16;
            WAIT_VM(0);
            WAIT_LGKM0();
            BARRIER();
            f32x4 acc[4][7];
#pragma unroll
            for (int j = 0; j < 7; ++j) {
                const u32x2 bb = *(const u32x2*)(P.b1 + (j < 6 ? fb + (j >> 1) * 64 + (j & 1) * 16 : fh) + fq * 4);
                const f32x4 bv = {lo16(bb[0]), hi16(bb[0]), lo16(bb[1]), hi16(bb[1])};
#pragma unroll
                for (int i = 0; i < 4; ++i) acc[i][j] = bv;
            }
            Reader<TR, 128, TR, 224, 4, 7> rd; rd.init(ring0, lane, wm, wn);
            Regs<4, 7> R0;
            int slot = 0;
            BARRIER();
            __builtin_amdgcn_s_waitcnt(0x0070);
            BARRIER();
            STAMP(1, 1);
            rd.read(R0, 0);
            for (int t = 0; t < NK / 4; ++t) {
                stage_step<3, SB>(slot, rd, rd, R0, acc); stage_step<3, SB>(slot, rd, rd, R0, acc);
                stage_step<3, SB>(slot, rd, rd, R0, acc); stage_step<3, SB>(slot, rd, rd, R0, acc);
            }
            STAMP(1, 2);
            const __amdgpu_buffer_rsrc_t rs = rsrc(P.U(), (unsigned)D_ * F_ * 2);
            const unsigned df = (unsigned)(tm * 8 + wm * 4);
            const unsigned ob = (unsigned)((fb >> 5) * 64 + df) * 1024u + (unsigned)((((fq & 1) * 2 + (fq >> 1)) * 16 + fr) * 16);
            const unsigned oh = (unsigned)((fh >> 5) * 64 + df + (fq & 1)) * 1024u + (unsigned)(((wn * 2 + (fq >> 1)) * 16 + fr) * 16);
            unsigned pk[4][7][2];
#pragma unroll
            for (int i = 0; i < 4; ++i) {
#pragma unroll
                for (int j = 0; j < 7; ++j) {
                    const f32x2 a = gelu2((f32x2){acc[i][j][0], acc[i][j][1]}), b = gelu2((f32x2){acc[i][j][2], acc[i][j][3]});
                    pk[i][j][0] = pack2(a.x, a.y); pk[i][j][1] = pack2(b.x, b.y);
                }
#pragma unroll
                for (int jp = 0; jp < 3; ++jp)
                    __builtin_amdgcn_raw_buffer_store_b128(pair16(pk[i][2 * jp][0], pk[i][2 * jp][1], pk[i][2 * jp + 1][0], pk[i][2 * jp + 1][1]), rs, ob + i * 1024 + jp * 2 * 65536, 0, SC1);
                if (i & 1)
                    __builtin_amdgcn_raw_buffer_store_b128(pair16(pk[i - 1][6][0], pk[i - 1][6][1], pk[i][6][0], pk[i][6][1]), rs, oh + (i - 1) * 1024, 0, SC1);
                SCHED0();
            }
            STAMP(1, 3);
        }
    } else {
        constexpr int NK = D_ / 64, SB = SB_Q;
        const int tm = fm;
        if (loader) {
            Loader<4, 6> L; L.init_m<ROW>(lw, lane, D_ * 2);
            L.init_n<TR>(lw, lane, 3 * D_ * 2, qkv_head(fn, 0) * 64, qkv_head(fn, 1) * 64, qkv_head(fn, 2) * 64);
            const char* gm = (const char*)P.x + (size_t)tm * 128 * D_ * 2; const char* gn = (const char*)P.wqkv;
            int slot = 0;
            auto issue = [&](int u) {
                const int k = korder(u & (NK - 1), (tm + fn) & 3, NK);
                L.issue_m<0>(gm + k * 128, lds + slot * SB, lw);
                L.issue_n<0>(gn + (size_t)k * (64 * 3 * D_ * 2) + (u >= NK ? 16 * 64 : 0), lds + slot * SB, lw);
                slot = (slot == 2) ? 0 : slot + 1;
            };
            issue(0); issue(1);
            WAIT_VM(L.NPW); BARRIER();
            issue(2);
            int t = 0;
#pragma unroll 1
            for (int seg = 0; seg < 4; ++seg) {
                const int end = seg == 0 ? 12 : seg == 1 ? 13 + (idx & 7) : seg == 2 ? 21 + (idx & 7) : 2 * NK - 3;
#pragma unroll 1
                for (; t < end; ++t) { WAIT_VM(L.NPW); BARRIER(); issue(t + 3); }
                if (seg == 0) {
                    if (lw == 0) {
                        poll_flags(P.flags(), lane < 8 ? FL_L + tm * 8 + lane : -1, tag);
                        glds16<SC1>((const char*)P.stat() + tm * 1024 + lane * 16, lds + LDS_QX);
                    }
                } else if (PF2 && seg < 3) prefetch_next(P, lds, seg - 1, lw, lane);
            }
            poll_flags(P.flags(), attn_dep(hd, qb, lane), tag);
            WAIT_VM(0);
            BARRIER();
            attn_load<4, 1>(P, lds + LDS_AK, hd, qb, lw, lane);
            attn_load<4, 2>(P, lds + LDS_AV, hd, qb, lw, lane);
            BARRIER();
            attn_load<4, 0>(P, lds + LDS_AQ, hd, qb, lw, lane);
            BARRIER();
        } else {
            const int fr = lane & 15, fq = lane >> 4;
            Reader<ROW, 128, TR, 192, 4, 6> rd; rd.init(ring0, lane, wm, wn);
            rd.n.b = rd.n.a + 32 - wm * 32; rd.n.a += wm * 32;
            Regs<4, 6> R0;
            int slot = 0;
            __builtin_amdgcn_s_waitcnt(0x0070);
            BARRIER();
            STAMP(1, 1);
            rd.read(R0, 0);
            const __amdgpu_buffer_rsrc_t rs = rsrc(P.qkv(), (unsigned)S_ * 3 * D_ * 2);
#pragma unroll 1
            for (int rnd = 0; rnd < 2; ++rnd) {
                f32x4 acc[5][6] = {};
                for (int t = 0; t < NK / 4; ++t) {
                    if (rnd == 1 && t == 1) WAIT_VM(0);
                    if (rnd == 1 && t == 2 && tid == 0) set_flag(P.flags(), FL_Q + tm * 32 + fn, tag);
                    stage_step<3, SB>(slot, rd, rd, R0, acc); stage_step<3, SB>(slot, rd, rd, R0, acc);
                    stage_step<3, SB>(slot, rd, rd, R0, acc); stage_step<3, SB>(slot, rd, rd, R0, acc);
                }
                STAMP(1, 2);
                LAS char* ex = lds + LDS_QX + 1024 + rnd * 1024;
                if (fr == 0) {
#pragma unroll
                    for (int jp = 0; jp < 3; ++jp) *(LAS f32x4*)(ex + wid * 256 + jp * 64 + fq * 16) = acc[4][2 * jp];
                }
                WAIT_LGKM0();
                CFENCE();
                __hip_atomic_store((LAS unsigned*)(lds + LDS_CTL + wid * 4), (unsigned)(rnd + 1), __ATOMIC_RELAXED, __HIP_MEMORY_SCOPE_WORKGROUP);
                const int s0 = tm * 128 + wm * 64;
                f32x2 st[4];
#pragma unroll
                for (int i = 0; i < 4; ++i) st[i] = *(LAS f32x2*)(lds + LDS_QX + (wm * 64 + i * 16 + fr) * 8);
                while (__builtin_amdgcn_readfirstlane(__hip_atomic_load((LAS unsigned*)(lds + LDS_CTL + (wid ^ 2) * 4), __ATOMIC_RELAXED, __HIP_MEMORY_SCOPE_WORKGROUP)) <= rnd) {}
                CFENCE();
#pragma unroll
                for (int jp = 0; jp < 3; ++jp) {
                    const int gh = qkv_head(fn, jp) + rnd * 16 + wn;
                    const float sc = jp == 0 ? 0.17677669529663687f * 1.4426950408889634f : 1.0f;
                    const f32x4 ca = acc[4][2 * jp], cb = *(LAS f32x4*)(ex + (wid ^ 2) * 256 + jp * 64 + fq * 16);
#pragma unroll
                    for (int i = 0; i < 4; ++i) {
                        const float r = st[i].y * sc, mr = -st[i].x * r;
                        const f32x4 a = acc[i][2 * jp] * r + ca * mr, b = acc[i][2 * jp + 1] * r + cb * mr;
                        __builtin_amdgcn_raw_buffer_store_b128(pair16(pack2(a[0], a[1]), pack2(a[2], a[3]), pack2(b[0], b[1]), pack2(b[2], b[3])), rs,
                                                               (unsigned)(((gh * S_ + s0 + i * 16 + fr) * 32 + ((fq & 1) ^ wm) * 16 + (fq >> 1) * 8) * 2), 0, SC1);
                    }
                }
            }
            STAMP(1, 3);
        }
    }

    constexpr int SB = SB_F, NS = NS_F;
    constexpr size_t OS = 2 * (S_ / 16) * 1024, WOS = 64 * D_ * 2, W2S = 64 * S_ * 2, US = 2 * 65536;
    const int tile = fm * 8 + fn, cls = (fm + fn) & 3, nf = isM ? NF_M : NF_Q;
    auto fof = [&](int i) {
        const int j = i < 7 ? (isM ? 0 : 7) + korder(i, cls, 7) : (isM ? 14 + NA : 14) + korder(i - 7, cls, nf - 7), k = 14 * (grp >> 1) + j;
        return k >= 56 ? k - 56 : k;
    };
    const char* g2 = (const char*)P.w2 + fm * 256;
    Loader<4, 4> L;
    if (loader) {
        L.init_m<TR>(lw, lane, S_ * 2);
        WAIT_VM(0);
        __builtin_amdgcn_s_waitcnt(0x0F70);
    }
    auto preload_w2 = [&]() {
#pragma unroll
        for (int u = 0; u < 2; ++u)
#pragma unroll
            for (int i = 0; i < 4; ++i) glds16_hidden_plain(g2 + fof(u) * W2S + L.om[i], lds + u * SB + (lw + 4 * i) * PIECE);
    };
    if (isM) {
        if (loader) preload_w2(); else WAIT_VM(0);
        BARRIER();
        if (tid == 0) set_flag(P.flags(), FL_U + um * 32 + un, tag);
        STAMP(2, 0);
    } else {
        if (loader) __builtin_amdgcn_s_setprio(1);
        WAIT_LGKM0();
        BARRIER();
#pragma unroll 1
        for (int it = 0; it < 2; ++it) {
            STAMP(2, 3 * it);
            f32x16 sc[8];
            int ln = lane;
            asm volatile("" : "+v"(ln));
            const int dg = (ln & 31) - 4 * (ln >> 5);
            const unsigned up = dg < 0 ? ~0u : ~1u << dg;
            const int nbad = qb == 0 ? 8 - wid : 0;
            SCHED0();
            const float m = attn_scores(lds + (it ? LDS_BK : LDS_AK), lds + LDS_AQ, nbad, wid, ln, up, sc);
            SCHED0();
            if (it == 0 || loader) WAIT_VM(0);
            BARRIER();
            if (it == 0) { if (tid == 0) set_flag(P.flags(), FL_Q + fm * 32 + fn + 8, tag); }
            else if (loader) preload_w2();
            STAMP(2, 3 * it + 1);
            PvState S;
            S.l0 = (f32x2){0.f, 0.f}; S.l1 = S.l0; S.o = (f32x16){};
            attn_pv<0, 8>(lds + (it ? LDS_BV : LDS_AV), nbad, wid, ln, up, sc, m, S);
            attn_out(P, hd + 16 * it, qb, wid, ln, S.l0 + S.l1, S.o);
            STAMP(2, 3 * it + 2);
            if (it == 0) {
                if (loader) {
                    poll_flags(P.flags(), attn_dep(hd + 16, qb, ln), tag);
                    attn_load<4, 0>(P, lds + LDS_AQ, hd + 16, qb, lw, ln);
                    attn_load<4, 1>(P, lds + LDS_BK, hd + 16, qb, lw, ln);
                    attn_load<4, 2>(P, lds + LDS_BV, hd + 16, qb, lw, ln);
                    WAIT_VM(8);
                }
                BARRIER();
            }
        }
        WAIT_LGKM0();
    }
    if (loader) {
        const char* go = (const char*)P.o() + (isM ? KW * OS : 0) + fm * 8192;
        const char* gw = (const char*)P.wo + (isM ? KW * WOS : 0) + fn * 256;
        const char* gu = (const char*)P.U() + fn * 8192;
        __builtin_amdgcn_s_setprio(0);
        L.init_n<FRAG>(lw, lane, US / 2);
        auto issue_f = [&](int u) {
            const int k = fof(u);
            L.issue_m<0>(g2 + k * W2S, lds + (u & (NS - 1)) * SB, lw); L.issue_n<SC1>(gu + k * US, lds + (u & (NS - 1)) * SB, lw);
        };
        poll_flags(P.flags(), lane < 2 ? FL_U + fn * 32 + (grp >> 1) * 4 + (isM ? 0 : 2) + lane : -1, tag);
#pragma unroll
        for (int u = 0; u < 2; ++u) L.issue_n<SC1>(gu + fof(u) * US, lds + u * SB, lw);
        WAIT_VM(4); BARRIER();
        issue_f(2); issue_f(3);
        WAIT_VM(16); BARRIER(); issue_f(4);
        WAIT_VM(16); BARRIER(); issue_f(5);
        WAIT_VM(16); BARRIER(); issue_f(6);
        const int udep = lane < 16 ? FL_U + fn * 32 + lane : -1;
        int nxt = 7;
        bool uready = false;
#pragma unroll 1
        for (int t = 3; t < 7; ++t) {
            if (t == 6 && !uready) { poll_flags(P.flags(), udep, tag); uready = true; for (; nxt < 10; ++nxt) issue_f(nxt); }
            if (nxt - (t + 2) >= 2) WAIT_VM(16); else WAIT_VM(0);
            BARRIER();
            if (!uready) uready = flags_set(P.flags(), udep, tag);
            if (uready) for (; nxt <= t + NS; ++nxt) issue_f(nxt);
        }
#pragma unroll 1
        for (int u = 7 + NS; u < nf; ++u) { WAIT_VM(16); BARRIER(); issue_f(u); }
        L.init_m<FRAG>(lw, lane, OS / 2); L.init_n<TR>(lw, lane, D_ * 2);
        if (lw == 0) poll_flags(P.flags(), lane < 16 ? FL_O + (fm >> 1) * 32 + (isM ? 16 : 0) + lane : -1, tag);
#pragma unroll 1
        for (int i = 0; i < KW; ++i) {
            WAIT_VM(16); BARRIER();
            const int k = korder(i, cls, KW);
            LAS char* dst = lds + ((nf + i) & (NS - 1)) * SB;
            L.issue_m<SC1>(go + k * OS, dst, lw); L.issue_n<0>(gw + k * WOS, dst, lw);
        }
        WAIT_VM(16); BARRIER();
        WAIT_VM(8);
        if (isM && lw == 0) poll_flags(P.flags(), lane == 0 ? FL_P + tile * 32 : -1, tag);
        BARRIER();
        WAIT_VM(0); BARRIER();
        BARRIER();
        if (!isM) BARRIER();
        if (blockIdx.x == 0 && lw == 0) {
#pragma unroll 1
            for (int i = 0; i < 4; ++i) poll_flags(P.flags(), FL_O + i * 64 + lane, tag);
            if (lane == 0) set_flag(P.flags(), FL_E, tag);
        }
    } else {
        const int fr = lane & 15, fq = lane >> 4;
        const int s0 = fm * 128 + wm * 64, d0 = fn * 128 + wn * 64;
        const __amdgpu_buffer_rsrc_t rp = rsrc(P.part() + (size_t)tile * 16384, 65536);
        f32x4 acc[4][4] = {};
        Reader<TR, 128, FRAG, 128, 4, 4> rf;
        Reader<FRAG, 128, TR, 128, 4, 4> rw;
        Regs<4, 4> R0;
        int slot = 0;
#define STEP(a, b) stage_step<NS, SB>(slot, a, b, R0, acc)
        rf.init(ring0, lane, wm, wn);
        BARRIER();
        STAMP(3, 1);
        rf.read(R0, 0);
        for (int t = 0; t < (nf - 1) / 4; ++t) {
            if (t == 1) WAIT_VM(0);
            if (t == 2 && !isM && tid == 0) { set_flag(P.flags(), FL_O + qb * 32 + hd, tag); set_flag(P.flags(), FL_O + qb * 32 + hd + 16, tag); }
            STEP(rf, rf); STEP(rf, rf); STEP(rf, rf); STEP(rf, rf);
        }
        for (int t = 0; t < ((nf - 1) & 3); ++t) STEP(rf, rf);
        rw.init(ring0 + slot * SB, lane, wm, wn);
        STEP(rf, rw);
        STAMP(3, 4);
        for (int t = 0; t < 2; ++t) { STEP(rw, rw); STEP(rw, rw); STEP(rw, rw); }
        u32x4 ex[4][4];
        if (isM) {
#pragma unroll
            for (int i = 0; i < 4; ++i)
#pragma unroll
                for (int j = 0; j < 4; ++j) ex[i][j] = __builtin_amdgcn_raw_buffer_load_b128(rp, (wid * 4096 + (i * 4 + j) * 256 + lane * 4) * 4, 0, SC1);
        } else {
#pragma unroll
            for (int i = 0; i < 4; ++i) {
#pragma unroll
                for (int j = 0; j < 4; ++j) { const u32x2 xv = *(const u32x2*)(P.x + (size_t)(s0 + i * 16 + fr) * D_ + d0 + j * 16 + fq * 4); ex[i][j][0] = xv[0]; ex[i][j][1] = xv[1]; }
                ex[i][0][2] = P.b2[s0 + i * 16 + fr];
            }
        }
        SCHED0();
        STEP(rw, rw); STEP(rw, rw);
        STAMP(3, 2);
        if (!isM) {
#pragma unroll
            for (int i = 0; i < 4; ++i)
#pragma unroll
                for (int j = 0; j < 4; ++j) {
                    const f32x4 v = acc[i][j] + (f32x4){lo16(ex[i][j][0]), hi16(ex[i][j][0]), lo16(ex[i][j][1]), hi16(ex[i][j][1])} + lo16(ex[i][0][2]);
                    __builtin_amdgcn_raw_buffer_store_b128(__builtin_bit_cast(u32x4, v), rp, (wid * 4096 + (i * 4 + j) * 256 + lane * 4) * 4, 0, SC1);
                }
            WAIT_VM(0);
            BARRIER();
            if (tid == 0) set_flag(P.flags(), FL_P + tile * 32, tag);
        } else {
#pragma unroll
            for (int i = 0; i < 4; ++i) {
#pragma unroll
                for (int j = 0; j < 4; ++j) acc[i][j] += __builtin_bit_cast(f32x4, ex[i][j]);
                u16* rowp = P.out + (size_t)(s0 + i * 16 + fr) * D_ + d0 + (fq & 1) * 16 + (fq >> 1) * 8;
#pragma unroll
                for (int jp = 0; jp < 2; ++jp) {
                    const f32x4 a = acc[i][2 * jp], b = acc[i][2 * jp + 1];
                    *(u32x4*)(rowp + jp * 32) = pair16(pack2(a[0], a[1]), pack2(a[2], a[3]), pack2(b[0], b[1]), pack2(b[2], b[3]));
                }
            }
        }
        STAMP(3, 3);
#undef STEP
    }
    STAMP_OUT();
}

extern "C" void kernel_launch(void* const* d_in, const int* in_sizes, int n_in, void* d_out, int out_size, void* d_ws, size_t ws_size, hipStream_t stream) {
    char* ws = (char*)d_ws;
    Params P;
    P.x = (const u16*)d_in[0]; P.wqkv = (const u16*)d_in[1]; P.wo = (const u16*)d_in[2];
    P.w1 = (const u16*)d_in[4]; P.b1 = (const u16*)d_in[5]; P.w2 = (const u16*)d_in[6]; P.b2 = (const u16*)d_in[7];
    P.out = (u16*)d_out; P.ws = ws;
    (void)hipFuncSetAttribute((const void*)k_main, hipFuncAttributeMaxDynamicSharedMemorySize, LDS_BYTES);
    k_main<<<256, 512, LDS_BYTES, stream>>>(P);
}
```

```cpp
#include <hip/hip_runtime.h>
#include <hip/hip_bf16.h>
#include <stdint.h>

#define LAS __attribute__((address_space(3)))
#define GAS __attribute__((address_space(1)))
typedef __bf16 bf16x8 __attribute__((ext_vector_type(8)));
typedef __bf16 bf16x4 __attribute__((ext_vector_type(4)));
typedef __bf16 bf16x2 __attribute__((ext_vector_type(2)));
typedef float f32x2 __attribute__((ext_vector_type(2)));
typedef float f32x4 __attribute__((ext_vector_type(4)));
typedef float f32x16 __attribute__((ext_vector_type(16)));
typedef unsigned u32x4 __attribute__((ext_vector_type(4)));
typedef unsigned u32x2 __attribute__((ext_vector_type(2)));
typedef unsigned short u16;
typedef unsigned long long u64;

constexpr int S_ = 2048, D_ = 1024, F_ = 3584, NH = 32;

constexpr size_t WS_FLAGS = 0;
constexpr size_t WS_STAT  = 1 << 16;
constexpr size_t WS_CS    = WS_STAT + (size_t)S_ * 8;
constexpr size_t WS_H     = 1 << 17;
constexpr size_t WS_QKV   = WS_H   + (size_t)S_ * D_ * 2;
constexpr size_t WS_U     = WS_QKV + (size_t)S_ * 3 * D_ * 2;
constexpr size_t WS_O     = WS_U   + (size_t)D_ * F_ * 2;
constexpr size_t WS_PART  = WS_O   + (size_t)S_ * D_ * 2;
constexpr int FL_E = 0;
constexpr int FL_L = 32;
constexpr int FL_C = 5 * 32;
constexpr int FL_U = 8 * 32;
constexpr int FL_Q = 16 * 32;
constexpr int FL_O = 32 * 32;
constexpr int FL_P = 40 * 32;

constexpr size_t WS_DBG = 200u << 20;
#define STAMP_DECL
#define STAMP(kid, i) do {} while (0)
#define STAMPL(kid, i) do {} while (0)
#define STAMP_OUT() do {} while (0)
#define WAIT_VM(n) asm volatile("s_waitcnt vmcnt(%0)" ::"n"(n) : "memory")
#define WAIT_LGKM0() __builtin_amdgcn_s_waitcnt(0xC07F)
#define CFENCE() asm volatile("" ::: "memory")
#define BARRIER() do { CFENCE(); __builtin_amdgcn_s_barrier(); CFENCE(); } while (0)
#define SCHED0() __builtin_amdgcn_sched_barrier(0)
#define RLX_AGENT __ATOMIC_RELAXED, __HIP_MEMORY_SCOPE_AGENT
constexpr int SC1 = 16;

__device__ __forceinline__ float bf2f(u16 v) { return __uint_as_float((unsigned)v << 16); }
__device__ __forceinline__ float lo16(unsigned v) { return __uint_as_float(v << 16); }
__device__ __forceinline__ float hi16(unsigned v) { return __uint_as_float(v & 0xffff0000u); }
__device__ __forceinline__ unsigned pack2(float lo, float hi) {
    f32x2 v = {lo, hi};
    bf16x2 b = __builtin_convertvector(v, bf16x2);
    return __builtin_bit_cast(unsigned, b);
}
__device__ __forceinline__ float rbf(float x) { return __uint_as_float(pack2(x, 0.f) << 16); }
template <int CTRL, int RMASK> __device__ __forceinline__ float dpp_add(float v) {
    return v + __builtin_bit_cast(float, __builtin_amdgcn_update_dpp(0, __builtin_bit_cast(int, v), CTRL, RMASK, 0xf, false));
}
__device__ __forceinline__ float wave_sum(float v) {
    v = dpp_add<0x111, 0xf>(v); v = dpp_add<0x112, 0xf>(v); v = dpp_add<0x114, 0xf>(v); v = dpp_add<0x118, 0xf>(v);
    v = dpp_add<0x142, 0xa>(v); v = dpp_add<0x143, 0xc>(v);
    return __builtin_bit_cast(float, __builtin_amdgcn_readlane(__builtin_bit_cast(int, v), 63));
}
__device__ __forceinline__ __amdgpu_buffer_rsrc_t rsrc(const void* p, unsigned bytes) { return __builtin_amdgcn_make_buffer_rsrc((void*)p, 0, bytes, 0x00020000); }

enum { ROW = 0, TR = 1, FRAG = 2 };
constexpr int PIECE = 1056, BLK = 8 * PIECE;
template <int T, int X> struct Op {
    static constexpr int BYTES = X * 132;
    __device__ static __forceinline__ unsigned src_off(int i, int lane, unsigned ld, unsigned cb) {
        const int blk = i >> 3, p = i & 7, pos = lane >> 3;
        if (T == TR) {
            if (X == 224 && blk == 3) {
                const int ps = lane >> 2, g = (p & 1) | ((ps >> 3) << 1), r = 32 * (p >> 1) + 8 * g + 4 * ((ps >> 2) & 1) + (ps & 3);
                return (unsigned)r * ld + cb + (unsigned)((lane & 3) * 16);
            }
            const int g = (p >> 2) | (pos & 2), r = 32 * (pos >> 2) + 8 * g + 4 * (pos & 1) + (p & 3);
            return (unsigned)r * ld + cb + (unsigned)((lane & 7) * 16);
        } else if (T == ROW) {
            const int j = ((pos >> 1) & 1) | ((pos & 1) << 1) | (pos & 4);
            return (unsigned)(blk * 64 + j * 8 + p) * ld + (unsigned)((lane & 7) * 16);
        } else {
            return (unsigned)blk * ld + (unsigned)(p * 1024 + lane * 16);
        }
    }
};
template <int AUX> __device__ __forceinline__ void glds16(const char* g, LAS char* l) { __builtin_amdgcn_global_load_lds((const GAS unsigned*)g, (LAS unsigned*)l, 16, 0, AUX); }
__device__ __forceinline__ void glds16_hidden_plain(const char* g, LAS char* l) {
    unsigned keep;
    asm volatile("s_mov_b32 %0, m0\n\ts_mov_b32 m0, %2\n\ts_nop 0\n\tglobal_load_lds_dwordx4 %1, off\n\ts_mov_b32 m0, %0"
                 : "=&s"(keep) : "v"(g), "s"(__builtin_amdgcn_readfirstlane((unsigned)(uintptr_t)l)) : "memory");
}
__device__ __forceinline__ void glds16_hidden(const char* g, LAS char* l) {
    unsigned keep;
    asm volatile("s_mov_b32 %0, m0\n\ts_mov_b32 m0, %2\n\ts_nop 0\n\tglobal_load_lds_dwordx4 %1, off sc1\n\ts_mov_b32 m0, %0"
                 : "=&s"(keep) : "v"(g), "s"(__builtin_amdgcn_readfirstlane((unsigned)(uintptr_t)l)) : "memory");
}
__device__ __forceinline__ void glds4(const char* g, LAS char* l) { __builtin_amdgcn_global_load_lds((const GAS unsigned*)g, (LAS unsigned*)l, 4, 0, 0); }

template <int T, int X> struct Addr {
    unsigned a, b;
    static constexpr int NV = (X == 224 || X == 192) ? 2 : 1;
    __device__ __forceinline__ void init(unsigned base, int lane, int w) {
        const int g = lane >> 4;
        b = 0;
        if (T == TR) {
            const int q = (lane & 15) >> 2, p = lane & 3;
            a = base + (unsigned)((q | ((g & 1) << 2)) * PIECE + (g >> 1) * 256 + p * 8 + (X == 128 ? w * BLK : w * 64));
            b = base + (unsigned)(3 * BLK + (g & 1) * PIECE + (g >> 1) * 512 + q * 64 + w * 32 + p * 8);
        } else if (T == ROW) {
            const int r = lane & 15;
            a = base + (unsigned)(w * BLK + (r & 7) * PIECE + (r >> 3) * 256 + g * 16);
        } else {
            a = base + (unsigned)(w * 4 * PIECE + lane * 16);
        }
    }
    __device__ __forceinline__ void step(unsigned d) { a += d; if (NV == 2) b += d; }
    __device__ __forceinline__ bf16x8 read(int n, int kk) const {
        if (T == TR) {
            LAS char* p; int t;
            if (X == 224 && n == 6) { p = (LAS char*)(uintptr_t)b + kk * 2 * PIECE; t = 256; }
            else if (X == 192) { p = (LAS char*)(uintptr_t)((n & 1) ? b : a) + kk * 512 + (n >> 1) * BLK; t = 128; }
            else { p = (LAS char*)(uintptr_t)a + kk * 512 + (X == 128 ? n * 32 : (n >> 1) * BLK + (n & 1) * 32); t = 128; }
            const bf16x4 lo = __builtin_amdgcn_ds_read_tr16_b64_v4bf16((LAS bf16x4*)p);
            const bf16x4 hi = __builtin_amdgcn_ds_read_tr16_b64_v4bf16((LAS bf16x4*)(p + t));
            return __builtin_shufflevector(lo, hi, 0, 1, 2, 3, 4, 5, 6, 7);
        } else if (T == ROW) {
            return *(LAS bf16x8*)((LAS char*)(uintptr_t)a + (n & 1) * 128 + (n >> 1) * 512 + kk * 64);
        } else {
            return *(LAS bf16x8*)((LAS char*)(uintptr_t)a + kk * BLK + n * PIECE);
        }
    }
};
template <int FM, int FN> struct Regs { bf16x8 m[FM], n[FN]; };
#define MFMA16(a, b, c) __builtin_amdgcn_mfma_f32_16x16x32_bf16(a, b, c, 0, 0, 0)

template <int TM, int BM, int TN, int BN, int FM, int FN> struct Reader {
    Addr<TM, BM> m; Addr<TN, BN> n;
    static constexpr int NDS = FM * (TM == TR ? 2 : 1) + FN * (TN == TR ? 2 : 1), NV = Addr<TM, BM>::NV + Addr<TN, BN>::NV;
    __device__ __forceinline__ void init(unsigned slot_base, int lane, int wm, int wn) { m.init(slot_base, lane, wm); n.init(slot_base + BM * 132, lane, wn); }
    __device__ __forceinline__ void read(Regs<FM, FN>& R, int kk) const {
#pragma unroll
        for (int i = 0; i < FM; ++i) R.m[i] = m.read(i, kk);
#pragma unroll
        for (int j = 0; j < FN; ++j) R.n[j] = n.read(j, kk);
    }
    __device__ __forceinline__ void step(unsigned d) { m.step(d); n.step(d); }
};
template <int AM, int FM, int FN> __device__ __forceinline__ void mma(f32x4 (&acc)[AM][FN], const Regs<FM, FN>& R) {
    const u32x4 one = {0x3f803f80u, 0x3f803f80u, 0x3f803f80u, 0x3f803f80u};
#pragma unroll
    for (int j = 0; j < FN; ++j) {
#pragma unroll
        for (int i = 0; i < FM; ++i) acc[i][j] = MFMA16(R.n[j], R.m[i], acc[i][j]);
        if (AM > FM && !(j & 1)) acc[AM - 1][j] = MFMA16(R.n[j], __builtin_bit_cast(bf16x8, one), acc[AM - 1][j]);
    }
}
template <int NDS, int NV, int NMF> __device__ __forceinline__ void interleave() {
#pragma unroll
    for (int i = 0; i < NMF; ++i) {
        __builtin_amdgcn_sched_group_barrier(0x8, 1, 0);
        if (i < NDS) __builtin_amdgcn_sched_group_barrier(0x80, 1, 0);
        if (i >= NMF - NV) __builtin_amdgcn_sched_group_barrier(0x2, 1, 0);
    }
}
template <int NS, int SB, class RD, class RDN, int AM, int FM, int FN>
__device__ __forceinline__ void stage_step(int& slot, RD& rd, RDN& rn, Regs<FM, FN>& R0, f32x4 (&acc)[AM][FN]) {
    constexpr int NMF = FM * FN + (AM - FM) * (FN / 2);
    Regs<FM, FN> R1;
    const unsigned d = (slot + 1 == NS) ? (unsigned)(-(NS - 1) * SB) : (unsigned)SB;
    slot = (slot + 1 == NS) ? 0 : slot + 1;
    rd.read(R1, 1);
    rn.step(d);
    mma(acc, R0);
    interleave<RD::NDS, RDN::NV, NMF>();
    SCHED0();
    WAIT_LGKM0();
    BARRIER();
    SCHED0();
    rn.read(R0, 0);
    mma(acc, R1);
    interleave<RDN::NDS, 0, NMF>();
    SCHED0();
}

template <int NM, int NN> struct Loader {
    static constexpr int NPW = NM + NN;
    unsigned om[NM], on[NN];
    template <int T> __device__ __forceinline__ void init_m(int lw, int lane, unsigned ld) {
#pragma unroll
        for (int i = 0; i < NM; ++i) om[i] = Op<T, NM * 32>::src_off(lw + 4 * i, lane, ld, (i >> 1) * 128);
    }
    template <int T> __device__ __forceinline__ void init_n(int lw, int lane, unsigned ld, unsigned c0 = 0, unsigned c1 = 128, unsigned c2 = 256, unsigned c3 = 384) {
#pragma unroll
        for (int i = 0; i < NN; ++i) on[i] = Op<T, NN * 32>::src_off(lw + 4 * i, lane, ld, (i >> 1) == 0 ? c0 : (i >> 1) == 1 ? c1 : (i >> 1) == 2 ? c2 : c3);
    }
    template <int AUX> __device__ __forceinline__ void issue_m(const char* gm, LAS char* dst, int lw) const {
#pragma unroll
        for (int i = 0; i < NM; ++i) glds16<AUX>(gm + om[i], dst + (lw + 4 * i) * PIECE);
    }
    template <int AUX> __device__ __forceinline__ void issue_n(const char* gn, LAS char* dst, int lw) const {
#pragma unroll
        for (int i = 0; i < NN; ++i) glds16<AUX>(gn + on[i], dst + NM * 32 * 132 + (lw + 4 * i) * PIECE);
    }
};
#define LEAD 2
__device__ __forceinline__ int korder(int p, int c, int nk) {
    const int r = (p - c) & 3, p0 = p - r;
    if (r > LEAD || p0 < 0 || p0 + LEAD >= nk) return p;
    return r == 0 ? p + LEAD : p - 1;
}
__device__ __forceinline__ f32x2 gelu2(f32x2 u) {
    const float c1 = -2.0f * 0.7978845608028654f * 1.4426950408889634f, c2 = c1 * 0.044715f;
    const f32x2 z = u * ((u * u) * c2 + c1);
    f32x2 e; e.x = __builtin_amdgcn_exp2f(z.x); e.y = __builtin_amdgcn_exp2f(z.y);
    e = e + 1.0f;
    f32x2 r; r.x = __builtin_amdgcn_rcpf(e.x); r.y = __builtin_amdgcn_rcpf(e.y);
    return u * r;
}
__device__ __forceinline__ u32x4 pair16(unsigned x0, unsigned x1, unsigned y0, unsigned y1) {
    auto a = __builtin_amdgcn_permlane16_swap(x0, y0, false, false);
    auto b = __builtin_amdgcn_permlane16_swap(x1, y1, false, false);
    return (u32x4){a[0], b[0], a[1], b[1]};
}
__device__ __forceinline__ bool flags_set(u64* flags, int idx, u64 tag) {
    const u64 v = __hip_atomic_load((GAS u64*)flags + (idx < 0 ? 0 : idx), RLX_AGENT);
    const bool ok = __builtin_amdgcn_ballot_w64(idx >= 0 && v - 1ull != tag) == 0ull;
    CFENCE();
    return ok;
}
__device__ __forceinline__ void poll_flags(u64* flags, int idx, u64 tag) {
#pragma unroll 1
    for (unsigned spins = 0; spins < (1u << 20); ++spins) {
        if (flags_set(flags, idx, tag)) break;
        __builtin_amdgcn_s_sleep(2);
    }
}
__device__ __forceinline__ void set_flag(u64* flags, int idx, u64 tag) {
    asm volatile("" : "+v"(tag));
    __hip_atomic_store((GAS u64*)flags + idx, tag + 1ull, RLX_AGENT);
}

struct Params {
    const u16 *x, *wqkv, *wo, *w1, *b1, *w2, *b2;
    u16* out;
    char* ws;
    __device__ __forceinline__ u16* h() const { return out; }
    __device__ __forceinline__ u16* qkv() const { return (u16*)(ws + WS_QKV); }
    __device__ __forceinline__ u16* U() const { return (u16*)(ws + WS_U); }
    __device__ __forceinline__ u16* o() const { return (u16*)(ws + WS_QKV); }
    __device__ __forceinline__ float* stat() const { return (float*)(ws + WS_STAT); }
    __device__ __forceinline__ u64* flags() const { return (u64*)(ws + WS_FLAGS); }
};

constexpr int SB_M = (128 + 224) * 132, SB_Q = (128 + 192) * 132, SB_F = 256 * 132, NS_F = 4;
constexpr int LDS_QX = 3 * SB_Q;
constexpr int LDS_CTL = LDS_QX + 3072;
constexpr int LDS_AQ = 0, LDS_AK = 2 * SB_Q, LDS_AV = 128 * 1024, LDS_BYTES = 160 * 1024;
constexpr int LDS_BK = 16 * 1024, LDS_BV = 96 * 1024;
constexpr int LDS_MC = LDS_BYTES - 512;
constexpr int LDS_SINK = LDS_BYTES - 256;
static_assert(LDS_CTL + 32 <= LDS_AV && 3 * SB_M <= LDS_SINK && 3 * SB_F <= LDS_AV && NS_F * SB_F <= LDS_BYTES, "LDS map");
#define NA 15
#define NT1 0
#define NTQ 0
#define NT2 0
#define NTX 0
#define ROTQ 4
#define ROTF 1
#define ROTW 2
constexpr int KW = 8, NF_Q = 7 + NA, NF_M = 7 + 42 - NA;

#define SPF 0
__device__ __forceinline__ void scalar_warm(const char* p, unsigned& sink) {
    if (SPF == 4) asm volatile("s_load_dword %0, %1, 0x0\n\ts_load_dword %0, %1, 0x40\n\ts_load_dword %0, %1, 0x80\n\ts_load_dword %0, %1, 0xc0" : "+s"(sink) : "s"(p));
    if (SPF == 8) asm volatile("s_load_dword %0, %1, 0x0\n\ts_load_dword %0, %1, 0x40\n\ts_load_dword %0, %1, 0x80\n\ts_load_dword %0, %1, 0xc0\n\t"
                               "s_load_dword %0, %1, 0x100\n\ts_load_dword %0, %1, 0x140\n\ts_load_dword %0, %1, 0x180\n\ts_load_dword %0, %1, 0x1c0" : "+s"(sink) : "s"(p));
}
__device__ __forceinline__ int qkv_head(int tn, int blk) { return blk * NH + ((tn & ~3) | ((tn + blk) & 3)) * 2; }

__device__ __forceinline__ f32x2 ln_row(const u32x4 a, const u32x4 b, u32x4& oa, u32x4& ob) {
    f32x2 v[8];
#pragma unroll
    for (int i = 0; i < 4; ++i) { v[i] = (f32x2){lo16(a[i]), hi16(a[i])}; v[4 + i] = (f32x2){lo16(b[i]), hi16(b[i])}; }
    const f32x2 s2 = ((v[0] + v[1]) + (v[2] + v[3])) + ((v[4] + v[5]) + (v[6] + v[7]));
    const float mean = wave_sum(s2.x + s2.y) * (1.0f / D_);
    f32x2 q2 = {0.f, 0.f};
#pragma unroll
    for (int i = 0; i < 8; ++i) { const f32x2 d = v[i] - mean; q2 += d * d; }
    const float var = wave_sum(q2.x + q2.y) * (1.0f / D_);
    const float mb = rbf(mean), rs = __builtin_amdgcn_rcpf(rbf(__builtin_amdgcn_sqrtf(rbf(rbf(var) + 1e-5f))));
    const float mb2 = rbf((mean - mb) * rs), rs2 = __builtin_amdgcn_rcpf(rbf(__builtin_amdgcn_sqrtf(rbf(rbf(var * rs * rs) + 1e-5f))));
    const float g = rs * rs2, c = -mb2 * rs2;
#pragma unroll
    for (int i = 0; i < 8; ++i) {
        const f32x2 t = (v[i] - mb) * g + c;
        if (i < 4) oa[i] = pack2(t.x, t.y); else ob[i - 4] = pack2(t.x, t.y);
    }
    return (f32x2){mb, rs};
}

#define MFMA32(a, b, c) __builtin_amdgcn_mfma_f32_32x32x16_bf16(a, b, c, 0, 0, 0)
__device__ __forceinline__ int attn_dep(int hd, int qb, int lane) {
    const int which = lane >> 2, st = 2 * qb - 2 + (lane & 3), p = hd >> 1;
    if (lane >= 12 || st < 0 || (which == 0 && (lane & 3) < 2)) return -1;
    return FL_Q + st * 32 + ((p & ~3) | ((p - which) & 3));
}
template <int NW, int WHICH> __device__ __forceinline__ void attn_load(const Params& P, LAS char* dst, int hd, int qb, int w, int lane) {
    const char* g = (const char*)(P.qkv() + (size_t)(WHICH * NH + hd) * S_ * 32) + (WHICH == 2 ? lane & 3 : (lane & 3) ^ ((lane >> 4) & 3)) * 16;
#pragma unroll
    for (int i = 0; i < (WHICH == 0 ? 16 : 32) / NW; ++i) {
        const int pc = i * NW + w;
        int row = qb * 256 - (WHICH == 0 ? 0 : 256) + pc * 16 + (lane >> 2); row = row < 0 ? 0 : row;
        glds16_hidden(g + row * 64, dst + pc * 1024);
    }
}
#define PIN(x) asm volatile("" : "+v"(x))
__device__ __forceinline__ float max16(const f32x16& v) {
    float a = __builtin_fmaxf(__builtin_fmaxf(v[0], v[1]), v[2]), b = __builtin_fmaxf(__builtin_fmaxf(v[3], v[4]), v[5]);
    a = __builtin_fmaxf(__builtin_fmaxf(a, v[6]), v[7]); b = __builtin_fmaxf(__builtin_fmaxf(b, v[8]), v[9]);
    a = __builtin_fmaxf(__builtin_fmaxf(a, v[10]), v[11]); b = __builtin_fmaxf(__builtin_fmaxf(b, v[12]), v[13]);
    a = __builtin_fmaxf(__builtin_fmaxf(a, v[14]), v[15]);
    return __builtin_fmaxf(a, b);
}
__device__ __forceinline__ float attn_scores(LAS char* buf, LAS char* qbuf, int nbad, int wid, int lane, unsigned up, f32x16 (&sc)[8]) {
    const int r32 = lane & 31, hh = lane >> 5, sw = (r32 >> 2) & 3;
    LAS char* k0p = buf + (wid * 32 + r32) * 64 + ((hh ^ sw) * 16);
    LAS char* k1p = buf + (wid * 32 + r32) * 64 + (((2 + hh) ^ sw) * 16);
    const bf16x8 qf0 = *(LAS bf16x8*)(qbuf + (wid * 32 + r32) * 64 + ((hh ^ sw) * 16));
    const bf16x8 qf1 = *(LAS bf16x8*)(qbuf + (wid * 32 + r32) * 64 + (((2 + hh) ^ sw) * 16));
    bf16x8 ka[9], kb[9];
#pragma unroll
    for (int i = 0; i < 3; ++i) { const int j = i == 0 ? 8 : i - 1; ka[i] = *(LAS bf16x8*)(k0p + j * 2048); kb[i] = *(LAS bf16x8*)(k1p + j * 2048); SCHED0(); }
    const float NEG = -INFINITY;
    const f32x16 zero = {};
    const float b0 = nbad > 0 ? NEG : 0.f;
    f32x16 s8;
    float m = NEG;
#pragma unroll
    for (int i = 0; i < 11; ++i) {
        if (i + 3 < 9) { ka[i + 3] = *(LAS bf16x8*)(k0p + (i + 2) * 2048); kb[i + 3] = *(LAS bf16x8*)(k1p + (i + 2) * 2048); }
        if (i < 9) {
            const f32x16 t = MFMA32(kb[i], qf1, MFMA32(ka[i], qf0, zero));
            if (i == 0) s8 = t; else sc[i - 1] = t;
        }
        SCHED0();
        const int j = i - 3;
        if (j == 0) {
#pragma unroll
            for (int r = 0; r < 16; ++r) {
                const unsigned mk = (unsigned)__builtin_amdgcn_sbfe((int)up, (r & 3) + 8 * (r >> 2), 1);
                sc[0][r] = __uint_as_float(__builtin_amdgcn_bitop3_b32(mk, __float_as_uint(sc[0][r] + b0), __float_as_uint(s8[r]), 0xca));
            }
        }
        if (j >= 0) { const float mj = max16(sc[j]); m = __builtin_fmaxf(m, (j > 0 && j < nbad) ? NEG : mj); PIN(m); }
        SCHED0();
    }
    auto rr = __builtin_amdgcn_permlane32_swap(__float_as_uint(m), __float_as_uint(m), false, false);
    return __builtin_fmaxf(__uint_as_float(rr[0]), __uint_as_float(rr[1]));
}
struct PvState { f32x2 l0, l1; f32x16 o; bf16x8 vf[2], v8[2]; u32x4 pw[2], p8[2]; };
__device__ __forceinline__ bf16x8 v_read(LAS char* vp, int j, int s) {
    const bf16x4 lo = __builtin_amdgcn_ds_read_tr16_b64_v4bf16((LAS bf16x4*)(vp + j * 2048 + s * 1024));
    const bf16x4 hi = __builtin_amdgcn_ds_read_tr16_b64_v4bf16((LAS bf16x4*)(vp + j * 2048 + s * 1024 + 512));
    return __builtin_shufflevector(lo, hi, 0, 1, 2, 3, 4, 5, 6, 7);
}
template <int J0, int J1> __device__ __forceinline__ void attn_pv(LAS char* buf, int nbad, int wid, int lane, unsigned up, f32x16 (&sc)[8], float m, PvState& S) {
    const int hh = lane >> 5;
    LAS char* vp = buf + (wid * 32 + 4 * hh + ((lane & 15) >> 2)) * 64 + ((lane >> 4) & 1) * 32 + (lane & 3) * 8;
#pragma unroll
    for (int j = J0; j < J1; ++j) {
        bf16x8 vn[2] = {v_read(vp, j, 0), v_read(vp, j, 1)};
        if (j == 0) { S.v8[0] = v_read(vp, 8, 0); S.v8[1] = v_read(vp, 8, 1); }
        const float mj = (j > 0 && j < nbad) ? INFINITY : m;
        f32x2 p[8];
#pragma unroll
        for (int i = 0; i < 8; ++i) p[i] = (f32x2){sc[j][2 * i], sc[j][2 * i + 1]} - mj;
        SCHED0();
#pragma unroll
        for (int g = 0; g < 4; ++g) {
#pragma unroll
            for (int i = 2 * g; i < 2 * g + 2; ++i) { p[i].x = __builtin_amdgcn_exp2f(p[i].x); p[i].y = __builtin_amdgcn_exp2f(p[i].y); PIN(p[i]); }
            SCHED0();
            if (j > 0 && (g & 1) == 0) S.o = MFMA32(S.vf[g >> 1], __builtin_bit_cast(bf16x8, S.pw[g >> 1]), S.o);
            if (j == 1 && (g & 1) == 1) S.o = MFMA32(S.v8[g >> 1], __builtin_bit_cast(bf16x8, S.p8[g >> 1]), S.o);
            SCHED0();
        }
#pragma unroll
        for (int i = 0; i < 8; ++i) {
            S.pw[i >> 2][i & 3] = pack2(p[i].x, p[i].y);
            if (i & 1) S.l1 += p[i]; else S.l0 += p[i];
        }
        if (j == 0) {
#pragma unroll
            for (int i = 0; i < 8; ++i) {
                const int kr = ((2 * i) & 3) + 8 * (i >> 1);
                const unsigned mk = __builtin_amdgcn_bitop3_b32(0xffffu, (unsigned)__builtin_amdgcn_sbfe((int)up, kr, 1), (unsigned)__builtin_amdgcn_sbfe((int)up, kr + 1, 1), 0xca);
                S.p8[i >> 2][i & 3] = S.pw[i >> 2][i & 3] & ~mk; S.pw[i >> 2][i & 3] &= mk;
            }
        }
        S.vf[0] = vn[0]; S.vf[1] = vn[1];
        PIN(S.l0); PIN(S.l1); PIN(S.pw[0]); PIN(S.pw[1]);
        SCHED0();
    }
    if (J1 == 8) {
        S.o = MFMA32(S.vf[0], __builtin_bit_cast(bf16x8, S.pw[0]), S.o);
        S.o = MFMA32(S.vf[1], __builtin_bit_cast(bf16x8, S.pw[1]), S.o);
    }
}
__device__ __forceinline__ void attn_out(const Params& P, int hd, int qb, int wid, int lane, f32x2 l2, const f32x16& oacc) {
    const int r32 = lane & 31, hh = lane >> 5;
    float l = l2.x + l2.y;
    {
        auto rr = __builtin_amdgcn_permlane32_swap(__float_as_uint(l), __float_as_uint(l), false, false);
        l = __uint_as_float(rr[0]) + __uint_as_float(rr[1]);
    }
    const float inv = __builtin_amdgcn_rcpf(l);
    unsigned w[8];
#pragma unroll
    for (int g = 0; g < 4; ++g) { w[2 * g] = pack2(oacc[4 * g] * inv, oacc[4 * g + 1] * inv); w[2 * g + 1] = pack2(oacc[4 * g + 2] * inv, oacc[4 * g + 3] * inv); }
    const __amdgpu_buffer_rsrc_t rs = rsrc(P.o(), (unsigned)S_ * D_ * 2);
    const unsigned off = (unsigned)((hd * (S_ / 16) + qb * 16 + wid * 2 + (r32 >> 4)) * 1024 + (hh * 16 + (r32 & 15)) * 16);
#pragma unroll
    for (int gp = 0; gp < 2; ++gp) {
        auto a = __builtin_amdgcn_permlane32_swap(w[4 * gp], w[4 * gp + 2], false, false);
        auto b = __builtin_amdgcn_permlane32_swap(w[4 * gp + 1], w[4 * gp + 3], false, false);
        __builtin_amdgcn_raw_buffer_store_b128((u32x4){a[0], b[0], a[1], b[1]}, rs, off + gp * 512, 0, SC1);
    }
}

extern "C" __global__ __launch_bounds__(512, 2) void k_main(const Params P) {
    extern __shared__ __attribute__((aligned(16))) char smem[];
    LAS char* lds = (LAS char*)smem;
    const int tid = threadIdx.x, lane = tid & 63, wid = __builtin_amdgcn_readfirstlane(tid >> 6);
    const int grp = blockIdx.x & 7, idx = blockIdx.x >> 3;
    const bool loader = wid >= 4;
    const int lw = wid - 4, wm = wid >> 1, wn = wid & 1;
    const unsigned ring0 = (unsigned)(uintptr_t)lds;
    const int i16 = idx & 15;
    const bool isM = idx < 16;
    const int fm = (grp >> 1) * 4 + (i16 >> 2), fn = (grp & 1) * 4 + (i16 & 3);
    const int um = (grp & 1) * 4 + (i16 >> 2), un = (grp >> 1) * 4 + (i16 & 3);
    const int hd = (grp & 1) * 8 + (i16 & 7), qb = (grp >> 1) * 2 + (i16 >> 3);
    STAMP_DECL;
    STAMP(1, 0);
#define PFC 1
    if (PFC && wid == 0) {
        u64 pc; unsigned rel, len;
        asm volatile("s_getpc_b64 %0\n1:\n\ts_mov_b32 %1, 1b-k_main\n\ts_mov_b32 %2, 2f-k_main\n\t.subsection 1\n2:\n\t.subsection 0" : "=s"(pc), "=s"(rel), "=s"(len));
        const unsigned off = (unsigned)((idx + 32 * (lane & 15)) * 128);
        glds4((const char*)(pc - rel) + (off < len ? off : 0u), lds + LDS_SINK);
    }
    const u64 tag = __hip_atomic_load((GAS u64*)P.flags() + FL_E, RLX_AGENT);

    unsigned ssink = 0;
    auto swarm = [&](int t) {
        if (SPF == 0) return;
        const int n = t * SPF;
        int f = (n >> 4) * 256 + ((grp & 1) * 32 + idx) * 4 + lw + (SPF == 4 ? 1792 : 0) + 896 * (grp >> 1);
        f = f >= F_ ? f - F_ : f;
        scalar_warm((const char*)P.w2 + (size_t)f * (S_ * 2) + (grp >> 1) * 1024 + (n & 15) * 64, ssink);
    };
    if (tid < 8) *(LAS unsigned*)(lds + (isM ? LDS_MC : LDS_CTL) + tid * 4) = 0u;
    if (isM) {
        constexpr int NK = S_ / 64, SB = SB_M;
        const int pg = (grp >> 1) + 4 * (grp & 1);
        const int tm = um, tn = un, mi = pg * 16 + i16;
        const char* gm = (const char*)P.h() + tm * 256; const char* gn = (const char*)P.w1 + tn * 448;
        const int cls = (tm + tn) & 3;
        auto kof = [&](int u) { const int p0 = u - ((u - cls) & 3); return (((p0 == 1 || p0 == 2) ? u : korder(u, cls, NK)) + 4 * pg) & (NK - 1); };
        Loader<4, 7> L;
        {
            const int row = mi * 16 + wid * 2;
            const u32x4* px = (const u32x4*)(P.x + (size_t)row * D_);
            u32x4 a0, b0, a1, b1;
            if (NTX) { a0 = __builtin_nontemporal_load(px + lane); b0 = __builtin_nontemporal_load(px + 64 + lane); a1 = __builtin_nontemporal_load(px + 128 + lane); b1 = __builtin_nontemporal_load(px + 192 + lane); }
            else { a0 = px[lane]; b0 = px[64 + lane]; a1 = px[128 + lane]; b1 = px[192 + lane]; }
            SCHED0();
            if (loader) {
                const unsigned c0 = (tn & 1) * 64;
                L.init_m<TR>(lw, lane, D_ * 2);
                L.init_n<TR>(lw, lane, F_ * 2, c0, c0 + 128, c0 + 256, (tn & 1) ? 0 : 384);
                asm volatile("" : "+v"(a0), "+v"(b0), "+v"(a1), "+v"(b1));
                L.issue_n<NT1>(gn + (size_t)kof(0) * (64 * F_ * 2), lds, lw);
            }
            STAMPL(0, 0);
            const __amdgpu_buffer_rsrc_t rh = rsrc(P.h(), (unsigned)S_ * D_ * 2);
            u32x4 oa0, ob0, oa1, ob1;
            const f32x2 s0 = ln_row(a0, b0, oa0, ob0), s1 = ln_row(a1, b1, oa1, ob1);
            __builtin_amdgcn_raw_buffer_store_b128(oa0, rh, (unsigned)(row * 2048 + lane * 16), 0, SC1);
            __builtin_amdgcn_raw_buffer_store_b128(ob0, rh, (unsigned)(row * 2048 + 1024 + lane * 16), 0, SC1);
            __builtin_amdgcn_raw_buffer_store_b128(oa1, rh, (unsigned)(row * 2048 + 2048 + lane * 16), 0, SC1);
            __builtin_amdgcn_raw_buffer_store_b128(ob1, rh, (unsigned)(row * 2048 + 3072 + lane * 16), 0, SC1);
            if (lane == 0) __builtin_amdgcn_raw_buffer_store_b128(__builtin_bit_cast(u32x4, (f32x4){s0.x, s0.y, s1.x, s1.y}), rsrc(P.stat(), S_ * 8), (unsigned)(row * 8), 0, SC1);
            SCHED0();
            STAMPL(0, 1);
        }
        if (loader) {
            WAIT_VM(0);
            BARRIER();
            STAMPL(0, 2);
            if (tid == 256 + 64) set_flag(P.flags(), FL_L + mi, tag);
#define ORD 0
            if (ORD == 0) {
#pragma unroll
                for (int u = 1; u < 3; ++u) L.issue_n<NT1>(gn + (size_t)kof(u) * (64 * F_ * 2), lds + u * SB, lw);
            }
            if (lw == 0) poll_flags(P.flags(), lane < 16 ? FL_L + pg * 16 + lane : -1, tag);
            STAMPL(0, 3);
            BARRIER();
            if (ORD == 0) {
#pragma unroll
                for (int u = 0; u < 3; ++u) L.issue_m<SC1>(gm + (size_t)kof(u) * (64 * D_ * 2), lds + u * SB, lw);
                WAIT_VM(8); BARRIER();
                WAIT_VM(4);
            } else {
                L.issue_m<SC1>(gm + (size_t)kof(0) * (64 * D_ * 2), lds, lw);
                L.issue_m<SC1>(gm + (size_t)kof(1) * (64 * D_ * 2), lds + SB, lw);
                if (ORD == 1) L.issue_n<NT1>(gn + (size_t)kof(1) * (64 * F_ * 2), lds + SB, lw);
                L.issue_m<SC1>(gm + (size_t)kof(2) * (64 * D_ * 2), lds + 2 * SB, lw);
                L.issue_n<NT1>(gn + (size_t)kof(2) * (64 * F_ * 2), lds + 2 * SB, lw);
                if (ORD == 1) WAIT_VM(22); else WAIT_VM(15);
                BARRIER();
                WAIT_VM(11);
            }
            int slot = 0, t = 0;
            auto issue = [&]() {
                const int k = kof(t + 3);
                L.issue_m<SC1>(gm + (size_t)k * (64 * D_ * 2), lds + slot * SB, lw);
                L.issue_n<NT1>(gn + (size_t)k * (64 * F_ * 2), lds + slot * SB, lw);
                slot = (slot == 2) ? 0 : slot + 1;
            };
            if (lw == 0) poll_flags(P.flags(), lane < 16 ? FL_L + ((pg + 1) & 7) * 16 + lane : -1, tag);
            BARRIER(); issue(); ++t;
            WAIT_VM(L.NPW); BARRIER(); issue(); ++t;
            WAIT_VM(L.NPW);
            if (lw == 0) { poll_flags(P.flags(), FL_L + lane, tag); poll_flags(P.flags(), FL_L + 64 + lane, tag); }
            BARRIER(); issue(); ++t;
            swarm(0); swarm(1);
#pragma unroll 1
            for (; t < NK - 3; ++t) { WAIT_VM(L.NPW); BARRIER(); issue(); swarm(t - 1); }
            asm volatile("s_waitcnt lgkmcnt(0)" : "+s"(ssink));
#define PF3 0
            auto warm = [&](int r) {
                const int n = ((r * 16 + i16) * 4 + lw) * 7 + (lane >> 3), row = n * 2 + (grp & 1);
                int f = row + 896 * (grp >> 1); f = f >= F_ ? f - F_ : f;
                if (r < PF3 && lane < 56) glds4((const char*)P.w2 + (size_t)f * (S_ * 2) + (grp >> 1) * 1024 + (lane & 7) * 128, lds + LDS_SINK);
            };
            warm(0);
            WAIT_VM(L.NPW + (PF3 > 0)); BARRIER();
            WAIT_VM((PF3 > 0) + 0); BARRIER();
            BARRIER();
            WAIT_VM(0); warm(1);
            WAIT_VM(0); warm(2);
            WAIT_VM(0); warm(3);
        } else {
            const int fr = lane & 15, fq = lane >> 4;
            const int fb = tn * 224 + ((tn & 1) ? 32 : 0) + wn * 32, fh = tn * 224 + ((tn & 1) ? 0 : 192) + wn * 16;
            WAIT_VM(0);
            WAIT_LGKM0();
            BARRIER();
            f32x4 acc[4][7];
#pragma unroll
            for (int j = 0; j < 7; ++j) {
                const u32x2 bb = *(const u32x2*)(P.b1 + (j < 6 ? fb + (j >> 1) * 64 + (j & 1) * 16 : fh) + fq * 4);
                const f32x4 bv = {lo16(bb[0]), hi16(bb[0]), lo16(bb[1]), hi16(bb[1])};
#pragma unroll
                for (int i = 0; i < 4; ++i) acc[i][j] = bv;
            }
            Reader<TR, 128, TR, 224, 4, 7> rd; rd.init(ring0, lane, wm, wn);
            Regs<4, 7> R0;
            int slot = 0;
            BARRIER();
            __builtin_amdgcn_s_waitcnt(0x0070);
            BARRIER();
            STAMP(1, 1);
            rd.read(R0, 0);
            for (int t = 0; t < NK / 4; ++t) {
                stage_step<3, SB>(slot, rd, rd, R0, acc); stage_step<3, SB>(slot, rd, rd, R0, acc);
                stage_step<3, SB>(slot, rd, rd, R0, acc); stage_step<3, SB>(slot, rd, rd, R0, acc);
            }
            STAMP(1, 2);
            const __amdgpu_buffer_rsrc_t rs = rsrc(P.U(), (unsigned)D_ * F_ * 2);
            const unsigned df = (unsigned)(tm * 8 + wm * 4);
            const unsigned ob = (unsigned)((fb >> 5) * 64 + df) * 1024u + (unsigned)((((fq & 1) * 2 + (fq >> 1)) * 16 + fr) * 16);
            const unsigned oh = (unsigned)((fh >> 5) * 64 + df + (fq & 1)) * 1024u + (unsigned)(((wn * 2 + (fq >> 1)) * 16 + fr) * 16);
            unsigned pk[4][7][2];
#pragma unroll
            for (int i = 0; i < 4; ++i) {
#pragma unroll
                for (int j = 0; j < 7; ++j) {
                    const f32x2 a = gelu2((f32x2){acc[i][j][0], acc[i][j][1]}), b = gelu2((f32x2){acc[i][j][2], acc[i][j][3]});
                    pk[i][j][0] = pack2(a.x, a.y); pk[i][j][1] = pack2(b.x, b.y);
                }
#pragma unroll
                for (int jp = 0; jp < 3; ++jp)
                    __builtin_amdgcn_raw_buffer_store_b128(pair16(pk[i][2 * jp][0], pk[i][2 * jp][1], pk[i][2 * jp + 1][0], pk[i][2 * jp + 1][1]), rs, ob + i * 1024 + jp * 2 * 65536, 0, SC1);
                if (i & 1)
                    __builtin_amdgcn_raw_buffer_store_b128(pair16(pk[i - 1][6][0], pk[i - 1][6][1], pk[i][6][0], pk[i][6][1]), rs, oh + (i - 1) * 1024, 0, SC1);
                SCHED0();
            }
            STAMP(1, 3);
        }
    } else {
        constexpr int NK = D_ / 64, SB = SB_Q;
        const int tm = fm;
        if (loader) {
            Loader<4, 6> L; L.init_m<ROW>(lw, lane, D_ * 2);
            L.init_n<TR>(lw, lane, 3 * D_ * 2, qkv_head(fn, 0) * 64, qkv_head(fn, 1) * 64, qkv_head(fn, 2) * 64);
            const char* gm = (const char*)P.x + (size_t)tm * 128 * D_ * 2; const char* gn = (const char*)P.wqkv;
            int slot = 0;
            auto issue = [&](int u) {
                const int k = (korder(u & (NK - 1), (tm + fn) & 3, NK) + ROTQ * (grp >> 1)) & (NK - 1);
                L.issue_m<0>(gm + k * 128, lds + slot * SB, lw);
                L.issue_n<NTQ>(gn + (size_t)k * (64 * 3 * D_ * 2) + (u >= NK ? 16 * 64 : 0), lds + slot * SB, lw);
                slot = (slot == 2) ? 0 : slot + 1;
            };
            issue(0); issue(1);
            WAIT_VM(L.NPW); BARRIER();
            issue(2);
            int t = 0;
            WAIT_VM(L.NPW); BARRIER(); issue(3); ++t;
#pragma unroll 1
            for (int seg = 0; seg < 2; ++seg) {
                const int end = seg == 0 ? 12 : 2 * NK - 3;
#pragma unroll 1
                for (; t < end; ++t) { WAIT_VM(L.NPW); BARRIER(); issue(t + 3); swarm(t - 1); }
                asm volatile("s_waitcnt lgkmcnt(0)" : "+s"(ssink));
                if (seg == 0 && lw == 0) {
                    poll_flags(P.flags(), lane < 8 ? FL_L + tm * 8 + lane : -1, tag);
                    glds16<SC1>((const char*)P.stat() + tm * 1024 + lane * 16, lds + LDS_QX);
                }
            }
            poll_flags(P.flags(), attn_dep(hd, qb, lane), tag);
            WAIT_VM(0);
            BARRIER();
            attn_load<4, 1>(P, lds + LDS_AK, hd, qb, lw, lane);
            attn_load<4, 2>(P, lds + LDS_AV, hd, qb, lw, lane);
            BARRIER();
            attn_load<4, 0>(P, lds + LDS_AQ, hd, qb, lw, lane);
            BARRIER();
        } else {
            const int fr = lane & 15, fq = lane >> 4;
            Reader<ROW, 128, TR, 192, 4, 6> rd; rd.init(ring0, lane, wm, wn);
            rd.n.b = rd.n.a + 32 - wm * 32; rd.n.a += wm * 32;
            Regs<4, 6> R0;
            int slot = 0;
            __builtin_amdgcn_s_waitcnt(0x0070);
            BARRIER();
            STAMP(1, 1);
            rd.read(R0, 0);
            const __amdgpu_buffer_rsrc_t rs = rsrc(P.qkv(), (unsigned)S_ * 3 * D_ * 2);
#pragma unroll 1
            for (int rnd = 0; rnd < 2; ++rnd) {
                f32x4 acc[5][6] = {};
                for (int t = 0; t < NK / 4; ++t) {
                    if (rnd == 1 && t == 1) WAIT_VM(0);
                    if (rnd == 1 && t == 2 && tid == 0) set_flag(P.flags(), FL_Q + tm * 32 + fn, tag);
                    stage_step<3, SB>(slot, rd, rd, R0, acc); stage_step<3, SB>(slot, rd, rd, R0, acc);
                    stage_step<3, SB>(slot, rd, rd, R0, acc); stage_step<3, SB>(slot, rd, rd, R0, acc);
                }
                STAMP(1, 2);
                LAS char* ex = lds + LDS_QX + 1024 + rnd * 1024;
                if (fr == 0) {
#pragma unroll
                    for (int jp = 0; jp < 3; ++jp) *(LAS f32x4*)(ex + wid * 256 + jp * 64 + fq * 16) = acc[4][2 * jp];
                }
                WAIT_LGKM0();
                CFENCE();
                __hip_atomic_store((LAS unsigned*)(lds + LDS_CTL + wid * 4), (unsigned)(rnd + 1), __ATOMIC_RELAXED, __HIP_MEMORY_SCOPE_WORKGROUP);
                const int s0 = tm * 128 + wm * 64;
                f32x2 st[4];
#pragma unroll
                for (int i = 0; i < 4; ++i) st[i] = *(LAS f32x2*)(lds + LDS_QX + (wm * 64 + i * 16 + fr) * 8);
                while (__builtin_amdgcn_readfirstlane(__hip_atomic_load((LAS unsigned*)(lds + LDS_CTL + (wid ^ 2) * 4), __ATOMIC_RELAXED, __HIP_MEMORY_SCOPE_WORKGROUP)) <= rnd) {}
                CFENCE();
#pragma unroll
                for (int jp = 0; jp < 3; ++jp) {
                    const int gh = qkv_head(fn, jp) + rnd * 16 + wn;
                    const float sc = jp == 0 ? 0.17677669529663687f * 1.4426950408889634f : 1.0f;
                    const f32x4 ca = acc[4][2 * jp], cb = *(LAS f32x4*)(ex + (wid ^ 2) * 256 + jp * 64 + fq * 16);
#pragma unroll
                    for (int i = 0; i < 4; ++i) {
                        const float r = st[i].y * sc, mr = -st[i].x * r;
                        const f32x4 a = acc[i][2 * jp] * r + ca * mr, b = acc[i][2 * jp + 1] * r + cb * mr;
                        __builtin_amdgcn_raw_buffer_store_b128(pair16(pack2(a[0], a[1]), pack2(a[2], a[3]), pack2(b[0], b[1]), pack2(b[2], b[3])), rs,
                                                               (unsigned)(((gh * S_ + s0 + i * 16 + fr) * 32 + ((fq & 1) ^ wm) * 16 + (fq >> 1) * 8) * 2), 0, SC1);
                    }
                }
            }
            STAMP(1, 3);
        }
    }

    constexpr int SB = SB_F, NS = NS_F;
    constexpr size_t OS = 2 * (S_ / 16) * 1024, WOS = 64 * D_ * 2, W2S = 64 * S_ * 2, US = 2 * 65536;
    const int tile = fm * 8 + fn, cls = (fm + fn) & 3, nf = isM ? NF_M : NF_Q;
    auto fof = [&](int i) {
        int r = korder(i - 7, cls, nf - 7) + (ROTF ? (grp & 1) * ((nf - 7) >> 1) : 0);
        r = r >= nf - 7 ? r - (nf - 7) : r;
        const int j = i < 7 ? (isM ? 0 : 7) + korder(i, cls, 7) : (isM ? 14 + NA : 14) + r, k = 14 * (grp >> 1) + j;
        return k >= 56 ? k - 56 : k;
    };
    const char* g2 = (const char*)P.w2 + fm * 256;
    Loader<4, 4> L;
    if (loader) {
        L.init_m<TR>(lw, lane, S_ * 2);
        WAIT_VM(0);
        __builtin_amdgcn_s_waitcnt(0x0F70);
    }
    auto preload_w2 = [&]() {
#pragma unroll
        for (int u = 0; u < 2; ++u)
#pragma unroll
            for (int i = 0; i < 4; ++i) glds16_hidden_plain(g2 + fof(u) * W2S + L.om[i], lds + u * SB + (lw + 4 * i) * PIECE);
    };
    if (isM) {
        if (loader) preload_w2();
        else {
            WAIT_VM(0);
            if (lane == 0 && __hip_atomic_fetch_add((LAS unsigned*)(lds + LDS_MC), 1u, __ATOMIC_RELAXED, __HIP_MEMORY_SCOPE_WORKGROUP) == 3u)
                set_flag(P.flags(), FL_U + um * 32 + un, tag);
        }
        STAMP(2, 0);
    } else {
        if (loader) __builtin_amdgcn_s_setprio(1);
        WAIT_LGKM0();
        BARRIER();
#pragma unroll 1
        for (int it = 0; it < 2; ++it) {
            STAMP(2, 3 * it);
            f32x16 sc[8];
            int ln = lane;
            asm volatile("" : "+v"(ln));
            const int dg = (ln & 31) - 4 * (ln >> 5);
            const unsigned up = dg < 0 ? ~0u : ~1u << dg;
            const int nbad = qb == 0 ? 8 - wid : 0;
            SCHED0();
            const float m = attn_scores(lds + (it ? LDS_BK : LDS_AK), lds + LDS_AQ, nbad, wid, ln, up, sc);
            SCHED0();
            if (it == 0 || loader) WAIT_VM(0);
            BARRIER();
            if (it == 0) { if (tid == 0) set_flag(P.flags(), FL_Q + fm * 32 + fn + 8, tag); }
            else if (loader) preload_w2();
            STAMP(2, 3 * it + 1);
            PvState S;
            S.l0 = (f32x2){0.f, 0.f}; S.l1 = S.l0; S.o = (f32x16){};
            attn_pv<0, 8>(lds + (it ? LDS_BV : LDS_AV), nbad, wid, ln, up, sc, m, S);
            attn_out(P, hd + 16 * it, qb, wid, ln, S.l0 + S.l1, S.o);
            STAMP(2, 3 * it + 2);
            if (it == 0) {
                if (loader) {
                    poll_flags(P.flags(), attn_dep(hd + 16, qb, ln), tag);
                    attn_load<4, 0>(P, lds + LDS_AQ, hd + 16, qb, lw, ln);
                    attn_load<4, 1>(P, lds + LDS_BK, hd + 16, qb, lw, ln);
                    attn_load<4, 2>(P, lds + LDS_BV, hd + 16, qb, lw, ln);
                    WAIT_VM(8);
                }
                BARRIER();
            }
        }
        WAIT_LGKM0();
    }
    if (loader) {
        const char* go = (const char*)P.o() + (isM ? KW * OS : 0) + fm * 8192;
        const char* gw = (const char*)P.wo + (isM ? KW * WOS : 0) + fn * 256;
        const char* gu = (const char*)P.U() + fn * 8192;
        __builtin_amdgcn_s_setprio(0);
        L.init_n<FRAG>(lw, lane, US / 2);
        auto issue_f = [&](int u) {
            const int k = fof(u);
            L.issue_m<NT2>(g2 + k * W2S, lds + (u & (NS - 1)) * SB, lw); L.issue_n<SC1>(gu + k * US, lds + (u & (NS - 1)) * SB, lw);
        };
        poll_flags(P.flags(), lane < 2 ? FL_U + fn * 32 + (grp >> 1) * 4 + (isM ? 0 : 2) + lane : -1, tag);
#pragma unroll
        for (int u = 0; u < 2; ++u) L.issue_n<SC1>(gu + fof(u) * US, lds + u * SB, lw);
        WAIT_VM(4); BARRIER();
        issue_f(2); issue_f(3);
        WAIT_VM(16); BARRIER(); issue_f(4);
        WAIT_VM(16); BARRIER(); issue_f(5);
        WAIT_VM(16); BARRIER(); issue_f(6);
        const int udep = lane < 16 ? FL_U + fn * 32 + lane : -1;
        int nxt = 7;
        bool uready = false;
#pragma unroll 1
        for (int t = 3; t < 7; ++t) {
            if (t == 6 && !uready) { poll_flags(P.flags(), udep, tag); uready = true; for (; nxt < 10; ++nxt) issue_f(nxt); }
            if (nxt - (t + 2) >= 2) WAIT_VM(16); else WAIT_VM(0);
            BARRIER();
            if (!uready) uready = flags_set(P.flags(), udep, tag);
            if (uready) for (; nxt <= t + NS; ++nxt) issue_f(nxt);
        }
#pragma unroll 1
        for (int u = 7 + NS; u < nf; ++u) { WAIT_VM(16); BARRIER(); issue_f(u); }
        L.init_m<FRAG>(lw, lane, OS / 2); L.init_n<TR>(lw, lane, D_ * 2);
        if (lw == 0) poll_flags(P.flags(), lane < 16 ? FL_O + (fm >> 1) * 32 + (isM ? 16 : 0) + lane : -1, tag);
#pragma unroll 1
        for (int i = 0; i < KW; ++i) {
            WAIT_VM(16); BARRIER();
            const int k = (korder(i, cls, KW) + ROTW * (grp >> 1)) & (KW - 1);
            LAS char* dst = lds + ((nf + i) & (NS - 1)) * SB;
            L.issue_m<SC1>(go + k * OS, dst, lw); L.issue_n<NT2>(gw + k * WOS, dst, lw);
        }
        WAIT_VM(16); BARRIER();
        WAIT_VM(8);
        if (isM && lw == 0) poll_flags(P.flags(), lane == 0 ? FL_P + tile * 32 : -1, tag);
        BARRIER();
        WAIT_VM(0); BARRIER();
        BARRIER();
        if (!isM) BARRIER();
        if (blockIdx.x == 0 && lw == 0) {
#pragma unroll 1
            for (int i = 0; i < 4; ++i) poll_flags(P.flags(), FL_O + i * 64 + lane, tag);
            if (lane == 0) set_flag(P.flags(), FL_E, tag);
        }
    } else {
        const int fr = lane & 15, fq = lane >> 4;
        const int s0 = fm * 128 + wm * 64, d0 = fn * 128 + wn * 64;
        const __amdgpu_buffer_rsrc_t ro = rsrc(P.out, (unsigned)S_ * D_ * 2);
        f32x4 acc[4][4] = {};
        Reader<TR, 128, FRAG, 128, 4, 4> rf;
        Reader<FRAG, 128, TR, 128, 4, 4> rw;
        Regs<4, 4> R0;
        int slot = 0;
#define STEP(a, b) stage_step<NS, SB>(slot, a, b, R0, acc)
        rf.init(ring0, lane, wm, wn);
        BARRIER();
        STAMP(3, 1);
        rf.read(R0, 0);
        for (int t = 0; t < (nf - 1) / 4; ++t) {
            if (t == 1) WAIT_VM(0);
            if (t == 2 && !isM && tid == 0) { set_flag(P.flags(), FL_O + qb * 32 + hd, tag); set_flag(P.flags(), FL_O + qb * 32 + hd + 16, tag); }
            STEP(rf, rf); STEP(rf, rf); STEP(rf, rf); STEP(rf, rf);
        }
        for (int t = 0; t < ((nf - 1) & 3); ++t) STEP(rf, rf);
        rw.init(ring0 + slot * SB, lane, wm, wn);
        STEP(rf, rw);
        STAMP(3, 4);
        for (int t = 0; t < 2; ++t) { STEP(rw, rw); STEP(rw, rw); STEP(rw, rw); }
        const unsigned eo = (unsigned)(((s0 + fr) * D_ + d0 + (fq & 1) * 16 + (fq >> 1) * 8) * 2);
        u32x4 ex[4][2];
        unsigned bv[4] = {0u, 0u, 0u, 0u};
        if (isM) {
#pragma unroll
            for (int i = 0; i < 4; ++i)
#pragma unroll
                for (int jp = 0; jp < 2; ++jp) ex[i][jp] = __builtin_amdgcn_raw_buffer_load_b128(ro, eo + i * (16 * D_ * 2) + jp * 64, 0, SC1);
        } else {
#pragma unroll
            for (int i = 0; i < 4; ++i) {
#pragma unroll
                for (int jp = 0; jp < 2; ++jp) ex[i][jp] = *(const u32x4*)((const char*)P.x + eo + i * (16 * D_ * 2) + jp * 64);
                bv[i] = P.b2[s0 + i * 16 + fr];
            }
        }
        SCHED0();
        STEP(rw, rw); STEP(rw, rw);
        STAMP(3, 2);
#pragma unroll
        for (int i = 0; i < 4; ++i)
#pragma unroll
            for (int jp = 0; jp < 2; ++jp) {
                auto e0 = __builtin_amdgcn_permlane16_swap(ex[i][jp][0], ex[i][jp][2], false, false);
                auto e1 = __builtin_amdgcn_permlane16_swap(ex[i][jp][1], ex[i][jp][3], false, false);
                const f32x4 a = acc[i][2 * jp] + (f32x4){lo16(e0[0]), hi16(e0[0]), lo16(e1[0]), hi16(e1[0])} + lo16(bv[i]);
                const f32x4 b = acc[i][2 * jp + 1] + (f32x4){lo16(e0[1]), hi16(e0[1]), lo16(e1[1]), hi16(e1[1])} + lo16(bv[i]);
                __builtin_amdgcn_raw_buffer_store_b128(pair16(pack2(a[0], a[1]), pack2(a[2], a[3]), pack2(b[0], b[1]), pack2(b[2], b[3])), ro, eo + i * (16 * D_ * 2) + jp * 64, 0, SC1);
            }
        if (!isM) {
            WAIT_VM(0);
            BARRIER();
            if (tid == 0) set_flag(P.flags(), FL_P + tile * 32, tag);
        }
        STAMP(3, 3);
#undef STEP
    }
    STAMP_OUT();
}

extern "C" void kernel_launch(void* const* d_in, const int* in_sizes, int n_in, void* d_out, int out_size, void* d_ws, size_t ws_size, hipStream_t stream) {
    char* ws = (char*)d_ws;
    Params P;
    P.x = (const u16*)d_in[0]; P.wqkv = (const u16*)d_in[1]; P.wo = (const u16*)d_in[2];
    P.w1 = (const u16*)d_in[4]; P.b1 = (const u16*)d_in[5]; P.w2 = (const u16*)d_in[6]; P.b2 = (const u16*)d_in[7];
    P.out = (u16*)d_out; P.ws = ws;
    (void)hipFuncSetAttribute((const void*)k_main, hipFuncAttributeMaxDynamicSharedMemorySize, LDS_BYTES);
    k_main<<<256, 512, LDS_BYTES, stream>>>(P);
}
```

```cpp
#include <hip/hip_runtime.h>
#include <hip/hip_bf16.h>
#include <stdint.h>

#define LAS __attribute__((address_space(3)))
#define GAS __attribute__((address_space(1)))
typedef __bf16 bf16x8 __attribute__((ext_vector_type(8)));
typedef __bf16 bf16x4 __attribute__((ext_vector_type(4)));
typedef __bf16 bf16x2 __attribute__((ext_vector_type(2)));
typedef float f32x2 __attribute__((ext_vector_type(2)));
typedef float f32x4 __attribute__((ext_vector_type(4)));
typedef float f32x16 __attribute__((ext_vector_type(16)));
typedef unsigned u32x4 __attribute__((ext_vector_type(4)));
typedef unsigned u32x2 __attribute__((ext_vector_type(2)));
typedef unsigned short u16;
typedef unsigned long long u64;

constexpr int S_ = 2048, D_ = 1024, F_ = 3584, NH = 32;

constexpr size_t WS_FLAGS = 0;
constexpr size_t WS_STAT  = 1 << 16;
constexpr size_t WS_CS    = WS_STAT + (size_t)S_ * 8;
constexpr size_t WS_H     = 1 << 17;
constexpr size_t WS_QKV   = WS_H   + (size_t)S_ * D_ * 2;
constexpr size_t WS_U     = WS_QKV + (size_t)S_ * 3 * D_ * 2;
constexpr size_t WS_O     = WS_U   + (size_t)D_ * F_ * 2;
constexpr size_t WS_PART  = WS_O   + (size_t)S_ * D_ * 2;
constexpr int FL_E = 0;
constexpr int FL_L = 32;
constexpr int FL_C = 5 * 32;
constexpr int FL_U = 8 * 32;
constexpr int FL_Q = 16 * 32;
constexpr int FL_O = 32 * 32;
constexpr int FL_P = 40 * 32;

constexpr size_t WS_DBG = 200u << 20;
#define STAMP_DECL unsigned ts_r = 0
#define STAMP_(kid, i) do { if (((kid) == 1 && (i) == 0) || ((kid) == 1 && (i) == 0)) { __builtin_amdgcn_sched_barrier(0); const unsigned r_ = (unsigned)__builtin_amdgcn_s_memrealtime(); __builtin_amdgcn_s_waitcnt(0xC07F); \
    ts_r = lane == (kid) * 8 + (i) ? r_ : ts_r; asm volatile("" : "+v"(ts_r)); __builtin_amdgcn_sched_barrier(0); } } while (0)
#define STAMP(kid, i) STAMP_(kid, i)
#define STAMPL(kid, i) STAMP_(kid, i)
#define STAMP_OUT() do { if (wid == 0 && isM && (blockIdx.x & 127) == 7) { const unsigned t0_ = __builtin_amdgcn_readlane(ts_r, 8), d_ = __builtin_amdgcn_readlane(ts_r, 8) - t0_; \
    while ((unsigned)__builtin_amdgcn_s_memrealtime() - t0_ < 7000u + (d_ < 10000u ? d_ : 0u)) __builtin_amdgcn_s_sleep(1); } } while (0)
#define WAIT_VM(n) asm volatile("s_waitcnt vmcnt(%0)" ::"n"(n) : "memory")
#define WAIT_LGKM0() __builtin_amdgcn_s_waitcnt(0xC07F)
#define CFENCE() asm volatile("" ::: "memory")
#define BARRIER() do { CFENCE(); __builtin_amdgcn_s_barrier(); CFENCE(); } while (0)
#define SCHED0() __builtin_amdgcn_sched_barrier(0)
#define RLX_AGENT __ATOMIC_RELAXED, __HIP_MEMORY_SCOPE_AGENT
constexpr int SC1 = 16;

__device__ __forceinline__ float bf2f(u16 v) { return __uint_as_float((unsigned)v << 16); }
__device__ __forceinline__ float lo16(unsigned v) { return __uint_as_float(v << 16); }
__device__ __forceinline__ float hi16(unsigned v) { return __uint_as_float(v & 0xffff0000u); }
__device__ __forceinline__ unsigned pack2(float lo, float hi) {
    f32x2 v = {lo, hi};
    bf16x2 b = __builtin_convertvector(v, bf16x2);
    return __builtin_bit_cast(unsigned, b);
}
__device__ __forceinline__ float rbf(float x) { return __uint_as_float(pack2(x, 0.f) << 16); }
template <int CTRL, int RMASK> __device__ __forceinline__ float dpp_add(float v) {
    return v + __builtin_bit_cast(float, __builtin_amdgcn_update_dpp(0, __builtin_bit_cast(int, v), CTRL, RMASK, 0xf, false));
}
__device__ __forceinline__ float wave_sum(float v) {
    v = dpp_add<0x111, 0xf>(v); v = dpp_add<0x112, 0xf>(v); v = dpp_add<0x114, 0xf>(v); v = dpp_add<0x118, 0xf>(v);
    v = dpp_add<0x142, 0xa>(v); v = dpp_add<0x143, 0xc>(v);
    return __builtin_bit_cast(float, __builtin_amdgcn_readlane(__builtin_bit_cast(int, v), 63));
}
__device__ __forceinline__ __amdgpu_buffer_rsrc_t rsrc(const void* p, unsigned bytes) { return __builtin_amdgcn_make_buffer_rsrc((void*)p, 0, bytes, 0x00020000); }

enum { ROW = 0, TR = 1, FRAG = 2 };
constexpr int PIECE = 1056, BLK = 8 * PIECE;
template <int T, int X> struct Op {
    static constexpr int BYTES = X * 132;
    __device__ static __forceinline__ unsigned src_off(int i, int lane, unsigned ld, unsigned cb) {
        const int blk = i >> 3, p = i & 7, pos = lane >> 3;
        if (T == TR) {
            if (X == 224 && blk == 3) {
                const int ps = lane >> 2, g = (p & 1) | ((ps >> 3) << 1), r = 32 * (p >> 1) + 8 * g + 4 * ((ps >> 2) & 1) + (ps & 3);
                return (unsigned)r * ld + cb + (unsigned)((lane & 3) * 16);
            }
            const int g = (p >> 2) | (pos & 2), r = 32 * (pos >> 2) + 8 * g + 4 * (pos & 1) + (p & 3);
            return (unsigned)r * ld + cb + (unsigned)((lane & 7) * 16);
        } else if (T == ROW) {
            const int j = ((pos >> 1) & 1) | ((pos & 1) << 1) | (pos & 4);
            return (unsigned)(blk * 64 + j * 8 + p) * ld + (unsigned)((lane & 7) * 16);
        } else {
            return (unsigned)blk * ld + (unsigned)(p * 1024 + lane * 16);
        }
    }
};
template <int AUX> __device__ __forceinline__ void glds16(const char* g, LAS char* l) { __builtin_amdgcn_global_load_lds((const GAS unsigned*)g, (LAS unsigned*)l, 16, 0, AUX); }
__device__ __forceinline__ void glds16_hidden_plain(const char* g, LAS char* l) {
    unsigned keep;
    asm volatile("s_mov_b32 %0, m0\n\ts_mov_b32 m0, %2\n\ts_nop 0\n\tglobal_load_lds_dwordx4 %1, off\n\ts_mov_b32 m0, %0"
                 : "=&s"(keep) : "v"(g), "s"(__builtin_amdgcn_readfirstlane((unsigned)(uintptr_t)l)) : "memory");
}
__device__ __forceinline__ void glds16_hidden(const char* g, LAS char* l) {
    unsigned keep;
    asm volatile("s_mov_b32 %0, m0\n\ts_mov_b32 m0, %2\n\ts_nop 0\n\tglobal_load_lds_dwordx4 %1, off sc1\n\ts_mov_b32 m0, %0"
                 : "=&s"(keep) : "v"(g), "s"(__builtin_amdgcn_readfirstlane((unsigned)(uintptr_t)l)) : "memory");
}
__device__ __forceinline__ void glds4(const char* g, LAS char* l) { __builtin_amdgcn_global_load_lds((const GAS unsigned*)g, (LAS unsigned*)l, 4, 0, 0); }

template <int T, int X> struct Addr {
    unsigned a, b;
    static constexpr int NV = (X == 224 || X == 192) ? 2 : 1;
    __device__ __forceinline__ void init(unsigned base, int lane, int w) {
        const int g = lane >> 4;
        b = 0;
        if (T == TR) {
            const int q = (lane & 15) >> 2, p = lane & 3;
            a = base + (unsigned)((q | ((g & 1) << 2)) * PIECE + (g >> 1) * 256 + p * 8 + (X == 128 ? w * BLK : w * 64));
            b = base + (unsigned)(3 * BLK + (g & 1) * PIECE + (g >> 1) * 512 + q * 64 + w * 32 + p * 8);
        } else if (T == ROW) {
            const int r = lane & 15;
            a = base + (unsigned)(w * BLK + (r & 7) * PIECE + (r >> 3) * 256 + g * 16);
        } else {
            a = base + (unsigned)(w * 4 * PIECE + lane * 16);
        }
    }
    __device__ __forceinline__ void step(unsigned d) { a += d; if (NV == 2) b += d; }
    __device__ __forceinline__ bf16x8 read(int n, int kk) const {
        if (T == TR) {
            LAS char* p; int t;
            if (X == 224 && n == 6) { p = (LAS char*)(uintptr_t)b + kk * 2 * PIECE; t = 256; }
            else if (X == 192) { p = (LAS char*)(uintptr_t)((n & 1) ? b : a) + kk * 512 + (n >> 1) * BLK; t = 128; }
            else { p = (LAS char*)(uintptr_t)a + kk * 512 + (X == 128 ? n * 32 : (n >> 1) * BLK + (n & 1) * 32); t = 128; }
            const bf16x4 lo = __builtin_amdgcn_ds_read_tr16_b64_v4bf16((LAS bf16x4*)p);
            const bf16x4 hi = __builtin_amdgcn_ds_read_tr16_b64_v4bf16((LAS bf16x4*)(p + t));
            return __builtin_shufflevector(lo, hi, 0, 1, 2, 3, 4, 5, 6, 7);
        } else if (T == ROW) {
            return *(LAS bf16x8*)((LAS char*)(uintptr_t)a + (n & 1) * 128 + (n >> 1) * 512 + kk * 64);
        } else {
            return *(LAS bf16x8*)((LAS char*)(uintptr_t)a + kk * BLK + n * PIECE);
        }
    }
};
template <int FM, int FN> struct Regs { bf16x8 m[FM], n[FN]; };
#define MFMA16(a, b, c) __builtin_amdgcn_mfma_f32_16x16x32_bf16(a, b, c, 0, 0, 0)

template <int TM, int BM, int TN, int BN, int FM, int FN> struct Reader {
    Addr<TM, BM> m; Addr<TN, BN> n;
    static constexpr int NDS = FM * (TM == TR ? 2 : 1) + FN * (TN == TR ? 2 : 1), NV = Addr<TM, BM>::NV + Addr<TN, BN>::NV;
    __device__ __forceinline__ void init(unsigned slot_base, int lane, int wm, int wn) { m.init(slot_base, lane, wm); n.init(slot_base + BM * 132, lane, wn); }
    __device__ __forceinline__ void read(Regs<FM, FN>& R, int kk) const {
#pragma unroll
        for (int i = 0; i < FM; ++i) R.m[i] = m.read(i, kk);
#pragma unroll
        for (int j = 0; j < FN; ++j) R.n[j] = n.read(j, kk);
    }
    __device__ __forceinline__ void step(unsigned d) { m.step(d); n.step(d); }
};
template <int AM, int FM, int FN> __device__ __forceinline__ void mma(f32x4 (&acc)[AM][FN], const Regs<FM, FN>& R) {
    const u32x4 one = {0x3f803f80u, 0x3f803f80u, 0x3f803f80u, 0x3f803f80u};
#pragma unroll
    for (int j = 0; j < FN; ++j) {
#pragma unroll
        for (int i = 0; i < FM; ++i) acc[i][j] = MFMA16(R.n[j], R.m[i], acc[i][j]);
        if (AM > FM && !(j & 1)) acc[AM - 1][j] = MFMA16(R.n[j], __builtin_bit_cast(bf16x8, one), acc[AM - 1][j]);
    }
}
template <int NDS, int NV, int NMF> __device__ __forceinline__ void interleave() {
#pragma unroll
    for (int i = 0; i < NMF; ++i) {
        __builtin_amdgcn_sched_group_barrier(0x8, 1, 0);
        if (i < NDS) __builtin_amdgcn_sched_group_barrier(0x80, 1, 0);
        if (i >= NMF - NV) __builtin_amdgcn_sched_group_barrier(0x2, 1, 0);
    }
}
template <int NS, int SB, class RD, class RDN, int AM, int FM, int FN>
__device__ __forceinline__ void stage_step(int& slot, RD& rd, RDN& rn, Regs<FM, FN>& R0, f32x4 (&acc)[AM][FN]) {
    constexpr int NMF = FM * FN + (AM - FM) * (FN / 2);
    Regs<FM, FN> R1;
    const unsigned d = (slot + 1 == NS) ? (unsigned)(-(NS - 1) * SB) : (unsigned)SB;
    slot = (slot + 1 == NS) ? 0 : slot + 1;
    rd.read(R1, 1);
    rn.step(d);
    mma(acc, R0);
    interleave<RD::NDS, RDN::NV, NMF>();
    SCHED0();
    WAIT_LGKM0();
    BARRIER();
    SCHED0();
    rn.read(R0, 0);
    mma(acc, R1);
    interleave<RDN::NDS, 0, NMF>();
    SCHED0();
}

template <int NM, int NN> struct Loader {
    static constexpr int NPW = NM + NN;
    unsigned om[NM], on[NN];
    template <int T> __device__ __forceinline__ void init_m(int lw, int lane, unsigned ld) {
#pragma unroll
        for (int i = 0; i < NM; ++i) om[i] = Op<T, NM * 32>::src_off(lw + 4 * i, lane, ld, (i >> 1) * 128);
    }
    template <int T> __device__ __forceinline__ void init_n(int lw, int lane, unsigned ld, unsigned c0 = 0, unsigned c1 = 128, unsigned c2 = 256, unsigned c3 = 384) {
#pragma unroll
        for (int i = 0; i < NN; ++i) on[i] = Op<T, NN * 32>::src_off(lw + 4 * i, lane, ld, (i >> 1) == 0 ? c0 : (i >> 1) == 1 ? c1 : (i >> 1) == 2 ? c2 : c3);
    }
    template <int AUX> __device__ __forceinline__ void issue_m(const char* gm, LAS char* dst, int lw) const {
#pragma unroll
        for (int i = 0; i < NM; ++i) glds16<AUX>(gm + om[i], dst + (lw + 4 * i) * PIECE);
    }
    template <int AUX> __device__ __forceinline__ void issue_n(const char* gn, LAS char* dst, int lw) const {
#pragma unroll
        for (int i = 0; i < NN; ++i) glds16<AUX>(gn + on[i], dst + NM * 32 * 132 + (lw + 4 * i) * PIECE);
    }
};
#define LEAD 2
__device__ __forceinline__ int korder(int p, int c, int nk) {
    const int r = (p - c) & 3, p0 = p - r;
    if (r > LEAD || p0 < 0 || p0 + LEAD >= nk) return p;
    return r == 0 ? p + LEAD : p - 1;
}
__device__ __forceinline__ f32x2 gelu2(f32x2 u) {
    const float c1 = -2.0f * 0.7978845608028654f * 1.4426950408889634f, c2 = c1 * 0.044715f;
    const f32x2 z = u * ((u * u) * c2 + c1);
    f32x2 e; e.x = __builtin_amdgcn_exp2f(z.x); e.y = __builtin_amdgcn_exp2f(z.y);
    e = e + 1.0f;
    f32x2 r; r.x = __builtin_amdgcn_rcpf(e.x); r.y = __builtin_amdgcn_rcpf(e.y);
    return u * r;
}
__device__ __forceinline__ u32x4 pair16(unsigned x0, unsigned x1, unsigned y0, unsigned y1) {
    auto a = __builtin_amdgcn_permlane16_swap(x0, y0, false, false);
    auto b = __builtin_amdgcn_permlane16_swap(x1, y1, false, false);
    return (u32x4){a[0], b[0], a[1], b[1]};
}
__device__ __forceinline__ bool flags_set(u64* flags, int idx, u64 tag) {
    const u64 v = __hip_atomic_load((GAS u64*)flags + (idx < 0 ? 0 : idx), RLX_AGENT);
    const bool ok = __builtin_amdgcn_ballot_w64(idx >= 0 && v - 1ull != tag) == 0ull;
    CFENCE();
    return ok;
}
__device__ __forceinline__ void poll_flags(u64* flags, int idx, u64 tag) {
#pragma unroll 1
    for (unsigned spins = 0; spins < (1u << 20); ++spins) {
        if (flags_set(flags, idx, tag)) break;
        __builtin_amdgcn_s_sleep(2);
    }
}
__device__ __forceinline__ void set_flag(u64* flags, int idx, u64 tag) {
    asm volatile("" : "+v"(tag));
    __hip_atomic_store((GAS u64*)flags + idx, tag + 1ull, RLX_AGENT);
}

struct Params {
    const u16 *x, *wqkv, *wo, *w1, *b1, *w2, *b2;
    u16* out;
    char* ws;
    __device__ __forceinline__ u16* h() const { return out; }
    __device__ __forceinline__ u16* qkv() const { return (u16*)(ws + WS_QKV); }
    __device__ __forceinline__ u16* U() const { return (u16*)(ws + WS_U); }
    __device__ __forceinline__ u16* o() const { return (u16*)(ws + WS_QKV); }
    __device__ __forceinline__ float* stat() const { return (float*)(ws + WS_STAT); }
    __device__ __forceinline__ u64* flags() const { return (u64*)(ws + WS_FLAGS); }
};

constexpr int SB_M = (128 + 224) * 132, SB_Q = (128 + 192) * 132, SB_F = 256 * 132, NS_F = 4;
constexpr int LDS_QX = 3 * SB_Q;
constexpr int LDS_CTL = LDS_QX + 3072;
constexpr int LDS_AQ = 0, LDS_AK = 2 * SB_Q, LDS_AV = 128 * 1024, LDS_BYTES = 160 * 1024;
constexpr int LDS_BK = 16 * 1024, LDS_BV = 96 * 1024;
constexpr int LDS_MC = LDS_BYTES - 512;
constexpr int LDS_SINK = LDS_BYTES - 256;
static_assert(LDS_CTL + 32 <= LDS_AV && 3 * SB_M <= LDS_SINK && 3 * SB_F <= LDS_AV && NS_F * SB_F <= LDS_BYTES, "LDS map");
#define NA 15
#define NT1 0
#define NTQ 0
#define NT2 0
#define NTX 0
#define ROTQ 4
#define ROTF 1
#define ROTW 2
constexpr int KW = 8, NF_Q = 7 + NA, NF_M = 7 + 42 - NA;

#define SPF 0
__device__ __forceinline__ void scalar_warm(const char* p, unsigned& sink) {
    if (SPF == 4) asm volatile("s_load_dword %0, %1, 0x0\n\ts_load_dword %0, %1, 0x40\n\ts_load_dword %0, %1, 0x80\n\ts_load_dword %0, %1, 0xc0" : "+s"(sink) : "s"(p));
    if (SPF == 8) asm volatile("s_load_dword %0, %1, 0x0\n\ts_load_dword %0, %1, 0x40\n\ts_load_dword %0, %1, 0x80\n\ts_load_dword %0, %1, 0xc0\n\t"
                               "s_load_dword %0, %1, 0x100\n\ts_load_dword %0, %1, 0x140\n\ts_load_dword %0, %1, 0x180\n\ts_load_dword %0, %1, 0x1c0" : "+s"(sink) : "s"(p));
}
__device__ __forceinline__ int qkv_head(int tn, int blk) { return blk * NH + ((tn & ~3) | ((tn + blk) & 3)) * 2; }

__device__ __forceinline__ f32x2 ln_row(const u32x4 a, const u32x4 b, u32x4& oa, u32x4& ob) {
    f32x2 v[8];
#pragma unroll
    for (int i = 0; i < 4; ++i) { v[i] = (f32x2){lo16(a[i]), hi16(a[i])}; v[4 + i] = (f32x2){lo16(b[i]), hi16(b[i])}; }
    const f32x2 s2 = ((v[0] + v[1]) + (v[2] + v[3])) + ((v[4] + v[5]) + (v[6] + v[7]));
    const float mean = wave_sum(s2.x + s2.y) * (1.0f / D_);
    f32x2 q2 = {0.f, 0.f};
#pragma unroll
    for (int i = 0; i < 8; ++i) { const f32x2 d = v[i] - mean; q2 += d * d; }
    const float var = wave_sum(q2.x + q2.y) * (1.0f / D_);
    const float mb = rbf(mean), rs = __builtin_amdgcn_rcpf(rbf(__builtin_amdgcn_sqrtf(rbf(rbf(var) + 1e-5f))));
    const float mb2 = rbf((mean - mb) * rs), rs2 = __builtin_amdgcn_rcpf(rbf(__builtin_amdgcn_sqrtf(rbf(rbf(var * rs * rs) + 1e-5f))));
    const float g = rs * rs2, c = -mb2 * rs2;
#pragma unroll
    for (int i = 0; i < 8; ++i) {
        const f32x2 t = (v[i] - mb) * g + c;
        if (i < 4) oa[i] = pack2(t.x, t.y); else ob[i - 4] = pack2(t.x, t.y);
    }
    return (f32x2){mb, rs};
}

#define MFMA32(a, b, c) __builtin_amdgcn_mfma_f32_32x32x16_bf16(a, b, c, 0, 0, 0)
__device__ __forceinline__ int attn_dep(int hd, int qb, int lane) {
    const int which = lane >> 2, st = 2 * qb - 2 + (lane & 3), p = hd >> 1;
    if (lane >= 12 || st < 0 || (which == 0 && (lane & 3) < 2)) return -1;
    return FL_Q + st * 32 + ((p & ~3) | ((p - which) & 3));
}
template <int NW, int WHICH> __device__ __forceinline__ void attn_load(const Params& P, LAS char* dst, int hd, int qb, int w, int lane) {
    const char* g = (const char*)(P.qkv() + (size_t)(WHICH * NH + hd) * S_ * 32) + (WHICH == 2 ? lane & 3 : (lane & 3) ^ ((lane >> 4) & 3)) * 16;
#pragma unroll
    for (int i = 0; i < (WHICH == 0 ? 16 : 32) / NW; ++i) {
        const int pc = i * NW + w;
        int row = qb * 256 - (WHICH == 0 ? 0 : 256) + pc * 16 + (lane >> 2); row = row < 0 ? 0 : row;
        glds16_hidden(g + row * 64, dst + pc * 1024);
    }
}
#define PIN(x) asm volatile("" : "+v"(x))
__device__ __forceinline__ float max16(const f32x16& v) {
    float a = __builtin_fmaxf(__builtin_fmaxf(v[0], v[1]), v[2]), b = __builtin_fmaxf(__builtin_fmaxf(v[3], v[4]), v[5]);
    a = __builtin_fmaxf(__builtin_fmaxf(a, v[6]), v[7]); b = __builtin_fmaxf(__builtin_fmaxf(b, v[8]), v[9]);
    a = __builtin_fmaxf(__builtin_fmaxf(a, v[10]), v[11]); b = __builtin_fmaxf(__builtin_fmaxf(b, v[12]), v[13]);
    a = __builtin_fmaxf(__builtin_fmaxf(a, v[14]), v[15]);
    return __builtin_fmaxf(a, b);
}
__device__ __forceinline__ float attn_scores(LAS char* buf, LAS char* qbuf, int nbad, int wid, int lane, unsigned up, f32x16 (&sc)[8]) {
    const int r32 = lane & 31, hh = lane >> 5, sw = (r32 >> 2) & 3;
    LAS char* k0p = buf + (wid * 32 + r32) * 64 + ((hh ^ sw) * 16);
    LAS char* k1p = buf + (wid * 32 + r32) * 64 + (((2 + hh) ^ sw) * 16);
    const bf16x8 qf0 = *(LAS bf16x8*)(qbuf + (wid * 32 + r32) * 64 + ((hh ^ sw) * 16));
    const bf16x8 qf1 = *(LAS bf16x8*)(qbuf + (wid * 32 + r32) * 64 + (((2 + hh) ^ sw) * 16));
    bf16x8 ka[9], kb[9];
#pragma unroll
    for (int i = 0; i < 3; ++i) { const int j = i == 0 ? 8 : i - 1; ka[i] = *(LAS bf16x8*)(k0p + j * 2048); kb[i] = *(LAS bf16x8*)(k1p + j * 2048); SCHED0(); }
    const float NEG = -INFINITY;
    const f32x16 zero = {};
    const float b0 = nbad > 0 ? NEG : 0.f;
    f32x16 s8;
    float m = NEG;
#pragma unroll
    for (int i = 0; i < 11; ++i) {
        if (i + 3 < 9) { ka[i + 3] = *(LAS bf16x8*)(k0p + (i + 2) * 2048); kb[i + 3] = *(LAS bf16x8*)(k1p + (i + 2) * 2048); }
        if (i < 9) {
            const f32x16 t = MFMA32(kb[i], qf1, MFMA32(ka[i], qf0, zero));
            if (i == 0) s8 = t; else sc[i - 1] = t;
        }
        SCHED0();
        const int j = i - 3;
        if (j == 0) {
#pragma unroll
            for (int r = 0; r < 16; ++r) {
                const unsigned mk = (unsigned)__builtin_amdgcn_sbfe((int)up, (r & 3) + 8 * (r >> 2), 1);
                sc[0][r] = __uint_as_float(__builtin_amdgcn_bitop3_b32(mk, __float_as_uint(sc[0][r] + b0), __float_as_uint(s8[r]), 0xca));
            }
        }
        if (j >= 0) { const float mj = max16(sc[j]); m = __builtin_fmaxf(m, (j > 0 && j < nbad) ? NEG : mj); PIN(m); }
        SCHED0();
    }
    auto rr = __builtin_amdgcn_permlane32_swap(__float_as_uint(m), __float_as_uint(m), false, false);
    return __builtin_fmaxf(__uint_as_float(rr[0]), __uint_as_float(rr[1]));
}
struct PvState { f32x2 l0, l1; f32x16 o; bf16x8 vf[2], v8[2]; u32x4 pw[2], p8[2]; };
__device__ __forceinline__ bf16x8 v_read(LAS char* vp, int j, int s) {
    const bf16x4 lo = __builtin_amdgcn_ds_read_tr16_b64_v4bf16((LAS bf16x4*)(vp + j * 2048 + s * 1024));
    const bf16x4 hi = __builtin_amdgcn_ds_read_tr16_b64_v4bf16((LAS bf16x4*)(vp + j * 2048 + s * 1024 + 512));
    return __builtin_shufflevector(lo, hi, 0, 1, 2, 3, 4, 5, 6, 7);
}
template <int J0, int J1> __device__ __forceinline__ void attn_pv(LAS char* buf, int nbad, int wid, int lane, unsigned up, f32x16 (&sc)[8], float m, PvState& S) {
    const int hh = lane >> 5;
    LAS char* vp = buf + (wid * 32 + 4 * hh + ((lane & 15) >> 2)) * 64 + ((lane >> 4) & 1) * 32 + (lane & 3) * 8;
#pragma unroll
    for (int j = J0; j < J1; ++j) {
        bf16x8 vn[2] = {v_read(vp, j, 0), v_read(vp, j, 1)};
        if (j == 0) { S.v8[0] = v_read(vp, 8, 0); S.v8[1] = v_read(vp, 8, 1); }
        const float mj = (j > 0 && j < nbad) ? INFINITY : m;
        f32x2 p[8];
#pragma unroll
        for (int i = 0; i < 8; ++i) p[i] = (f32x2){sc[j][2 * i], sc[j][2 * i + 1]} - mj;
        SCHED0();
#pragma unroll
        for (int g = 0; g < 4; ++g) {
#pragma unroll
            for (int i = 2 * g; i < 2 * g + 2; ++i) { p[i].x = __builtin_amdgcn_exp2f(p[i].x); p[i].y = __builtin_amdgcn_exp2f(p[i].y); PIN(p[i]); }
            SCHED0();
            if (j > 0 && (g & 1) == 0) S.o = MFMA32(S.vf[g >> 1], __builtin_bit_cast(bf16x8, S.pw[g >> 1]), S.o);
            if (j == 1 && (g & 1) == 1) S.o = MFMA32(S.v8[g >> 1], __builtin_bit_cast(bf16x8, S.p8[g >> 1]), S.o);
            SCHED0();
        }
#pragma unroll
        for (int i = 0; i < 8; ++i) {
            S.pw[i >> 2][i & 3] = pack2(p[i].x, p[i].y);
            if (i & 1) S.l1 += p[i]; else S.l0 += p[i];
        }
        if (j == 0) {
#pragma unroll
            for (int i = 0; i < 8; ++i) {
                const int kr = ((2 * i) & 3) + 8 * (i >> 1);
                const unsigned mk = __builtin_amdgcn_bitop3_b32(0xffffu, (unsigned)__builtin_amdgcn_sbfe((int)up, kr, 1), (unsigned)__builtin_amdgcn_sbfe((int)up, kr + 1, 1), 0xca);
                S.p8[i >> 2][i & 3] = S.pw[i >> 2][i & 3] & ~mk; S.pw[i >> 2][i & 3] &= mk;
            }
        }
        S.vf[0] = vn[0]; S.vf[1] = vn[1];
        PIN(S.l0); PIN(S.l1); PIN(S.pw[0]); PIN(S.pw[1]);
        SCHED0();
    }
    if (J1 == 8) {
        S.o = MFMA32(S.vf[0], __builtin_bit_cast(bf16x8, S.pw[0]), S.o);
        S.o = MFMA32(S.vf[1], __builtin_bit_cast(bf16x8, S.pw[1]), S.o);
    }
}
__device__ __forceinline__ void attn_out(const Params& P, int hd, int qb, int wid, int lane, f32x2 l2, const f32x16& oacc) {
    const int r32 = lane & 31, hh = lane >> 5;
    float l = l2.x + l2.y;
    {
        auto rr = __builtin_amdgcn_permlane32_swap(__float_as_uint(l), __float_as_uint(l), false, false);
        l = __uint_as_float(rr[0]) + __uint_as_float(rr[1]);
    }
    const float inv = __builtin_amdgcn_rcpf(l);
    unsigned w[8];
#pragma unroll
    for (int g = 0; g < 4; ++g) { w[2 * g] = pack2(oacc[4 * g] * inv, oacc[4 * g + 1] * inv); w[2 * g + 1] = pack2(oacc[4 * g + 2] * inv, oacc[4 * g + 3] * inv); }
    const __amdgpu_buffer_rsrc_t rs = rsrc(P.o(), (unsigned)S_ * D_ * 2);
    const unsigned off = (unsigned)((hd * (S_ / 16) + qb * 16 + wid * 2 + (r32 >> 4)) * 1024 + (hh * 16 + (r32 & 15)) * 16);
#pragma unroll
    for (int gp = 0; gp < 2; ++gp) {
        auto a = __builtin_amdgcn_permlane32_swap(w[4 * gp], w[4 * gp + 2], false, false);
        auto b = __builtin_amdgcn_permlane32_swap(w[4 * gp + 1], w[4 * gp + 3], false, false);
        __builtin_amdgcn_raw_buffer_store_b128((u32x4){a[0], b[0], a[1], b[1]}, rs, off + gp * 512, 0, SC1);
    }
}

extern "C" __global__ __launch_bounds__(512, 2) void k_main(const Params P) {
    extern __shared__ __attribute__((aligned(16))) char smem[];
    LAS char* lds = (LAS char*)smem;
    const int tid = threadIdx.x, lane = tid & 63, wid = __builtin_amdgcn_readfirstlane(tid >> 6);
    const int grp = blockIdx.x & 7, idx = blockIdx.x >> 3;
    const bool loader = wid >= 4;
    const int lw = wid - 4, wm = wid >> 1, wn = wid & 1;
    const unsigned ring0 = (unsigned)(uintptr_t)lds;
    const int i16 = idx & 15;
    const bool isM = idx < 16;
    const int fm = (grp >> 1) * 4 + (i16 >> 2), fn = (grp & 1) * 4 + (i16 & 3);
    const int um = (grp & 1) * 4 + (i16 >> 2), un = (grp >> 1) * 4 + (i16 & 3);
    const int hd = (grp & 1) * 8 + (i16 & 7), qb = (grp >> 1) * 2 + (i16 >> 3);
    STAMP_DECL;
    STAMP(1, 0);
#define PFC 1
    if (PFC && wid == 0) {
        u64 pc; unsigned rel, len;
        asm volatile("s_getpc_b64 %0\n1:\n\ts_mov_b32 %1, 1b-k_main\n\ts_mov_b32 %2, 2f-k_main\n\t.subsection 1\n2:\n\t.subsection 0" : "=s"(pc), "=s"(rel), "=s"(len));
        const unsigned off = (unsigned)((idx + 32 * (lane & 15)) * 128);
        glds4((const char*)(pc - rel) + (off < len ? off : 0u), lds + LDS_SINK);
    }
    const u64 tag = __hip_atomic_load((GAS u64*)P.flags() + FL_E, RLX_AGENT);

    unsigned ssink = 0;
    auto swarm = [&](int t) {
        if (SPF == 0) return;
        const int n = t * SPF;
        int f = (n >> 4) * 256 + ((grp & 1) * 32 + idx) * 4 + lw + (SPF == 4 ? 1792 : 0) + 896 * (grp >> 1);
        f = f >= F_ ? f - F_ : f;
        scalar_warm((const char*)P.w2 + (size_t)f * (S_ * 2) + (grp >> 1) * 1024 + (n & 15) * 64, ssink);
    };
    if (tid < 8) *(LAS unsigned*)(lds + (isM ? LDS_MC : LDS_CTL) + tid * 4) = 0u;
    if (isM) {
        constexpr int NK = S_ / 64, SB = SB_M;
        const int pg = (grp >> 1) + 4 * (grp & 1);
        const int tm = um, tn = un, mi = pg * 16 + i16;
        const char* gm = (const char*)P.h() + tm * 256; const char* gn = (const char*)P.w1 + tn * 448;
        const int cls = (tm + tn) & 3;
        auto kof = [&](int u) { const int p0 = u - ((u - cls) & 3); return (((p0 == 1 || p0 == 2) ? u : korder(u, cls, NK)) + 4 * pg) & (NK - 1); };
        Loader<4, 7> L;
        {
            const int row = mi * 16 + wid * 2;
            const u32x4* px = (const u32x4*)(P.x + (size_t)row * D_);
            u32x4 a0, b0, a1, b1;
            if (NTX) { a0 = __builtin_nontemporal_load(px + lane); b0 = __builtin_nontemporal_load(px + 64 + lane); a1 = __builtin_nontemporal_load(px + 128 + lane); b1 = __builtin_nontemporal_load(px + 192 + lane); }
            else { a0 = px[lane]; b0 = px[64 + lane]; a1 = px[128 + lane]; b1 = px[192 + lane]; }
            SCHED0();
            if (loader) {
                const unsigned c0 = (tn & 1) * 64;
                L.init_m<TR>(lw, lane, D_ * 2);
                L.init_n<TR>(lw, lane, F_ * 2, c0, c0 + 128, c0 + 256, (tn & 1) ? 0 : 384);
                asm volatile("" : "+v"(a0), "+v"(b0), "+v"(a1), "+v"(b1));
                L.issue_n<NT1>(gn + (size_t)kof(0) * (64 * F_ * 2), lds, lw);
            }
            STAMPL(0, 0);
            const __amdgpu_buffer_rsrc_t rh = rsrc(P.h(), (unsigned)S_ * D_ * 2);
            u32x4 oa0, ob0, oa1, ob1;
            const f32x2 s0 = ln_row(a0, b0, oa0, ob0), s1 = ln_row(a1, b1, oa1, ob1);
            __builtin_amdgcn_raw_buffer_store_b128(oa0, rh, (unsigned)(row * 2048 + lane * 16), 0, SC1);
            __builtin_amdgcn_raw_buffer_store_b128(ob0, rh, (unsigned)(row * 2048 + 1024 + lane * 16), 0, SC1);
            __builtin_amdgcn_raw_buffer_store_b128(oa1, rh, (unsigned)(row * 2048 + 2048 + lane * 16), 0, SC1);
            __builtin_amdgcn_raw_buffer_store_b128(ob1, rh, (unsigned)(row * 2048 + 3072 + lane * 16), 0, SC1);
            if (lane == 0) __builtin_amdgcn_raw_buffer_store_b128(__builtin_bit_cast(u32x4, (f32x4){s0.x, s0.y, s1.x, s1.y}), rsrc(P.stat(), S_ * 8), (unsigned)(row * 8), 0, SC1);
            SCHED0();
            STAMPL(0, 1);
        }
        if (loader) {
            WAIT_VM(0);
            BARRIER();
            STAMPL(0, 2);
            if (tid == 256 + 64) set_flag(P.flags(), FL_L + mi, tag);
#define ORD 0
            if (ORD == 0) {
#pragma unroll
                for (int u = 1; u < 3; ++u) L.issue_n<NT1>(gn + (size_t)kof(u) * (64 * F_ * 2), lds + u * SB, lw);
            }
            if (lw == 0) poll_flags(P.flags(), lane < 16 ? FL_L + pg * 16 + lane : -1, tag);
            STAMPL(0, 3);
            BARRIER();
            if (ORD == 0) {
#pragma unroll
                for (int u = 0; u < 3; ++u) L.issue_m<SC1>(gm + (size_t)kof(u) * (64 * D_ * 2), lds + u * SB, lw);
                WAIT_VM(8); BARRIER();
                WAIT_VM(4);
            } else {
                L.issue_m<SC1>(gm + (size_t)kof(0) * (64 * D_ * 2), lds, lw);
                L.issue_m<SC1>(gm + (size_t)kof(1) * (64 * D_ * 2), lds + SB, lw);
                if (ORD == 1) L.issue_n<NT1>(gn + (size_t)kof(1) * (64 * F_ * 2), lds + SB, lw);
                L.issue_m<SC1>(gm + (size_t)kof(2) * (64 * D_ * 2), lds + 2 * SB, lw);
                L.issue_n<NT1>(gn + (size_t)kof(2) * (64 * F_ * 2), lds + 2 * SB, lw);
                if (ORD == 1) WAIT_VM(22); else WAIT_VM(15);
                BARRIER();
                WAIT_VM(11);
            }
            int slot = 0, t = 0;
            auto issue = [&]() {
                const int k = kof(t + 3);
                L.issue_m<SC1>(gm + (size_t)k * (64 * D_ * 2), lds + slot * SB, lw);
                L.issue_n<NT1>(gn + (size_t)k * (64 * F_ * 2), lds + slot * SB, lw);
                slot = (slot == 2) ? 0 : slot + 1;
            };
            if (lw == 0) poll_flags(P.flags(), lane < 16 ? FL_L + ((pg + 1) & 7) * 16 + lane : -1, tag);
            BARRIER(); issue(); ++t;
            WAIT_VM(L.NPW); BARRIER(); issue(); ++t;
            WAIT_VM(L.NPW);
            if (lw == 0) { poll_flags(P.flags(), FL_L + lane, tag); poll_flags(P.flags(), FL_L + 64 + lane, tag); }
            BARRIER(); issue(); ++t;
            swarm(0); swarm(1);
#pragma unroll 1
            for (; t < NK - 3; ++t) { WAIT_VM(L.NPW); BARRIER(); issue(); swarm(t - 1); }
            asm volatile("s_waitcnt lgkmcnt(0)" : "+s"(ssink));
#define PF3 2
            auto warm = [&](int r) {
                const int n = ((r * 16 + i16) * 4 + lw) * 7 + (lane >> 3), row = n * 2 + (grp & 1);
                int f = row + 896 * (grp >> 1); f = f >= F_ ? f - F_ : f;
                if (r < PF3 && lane < 56) glds4((const char*)P.w2 + (size_t)f * (S_ * 2) + (grp >> 1) * 1024 + (lane & 7) * 128, lds + LDS_SINK);
            };
            warm(0);
            WAIT_VM(L.NPW + (PF3 > 0)); BARRIER();
            WAIT_VM((PF3 > 0) + 0); BARRIER();
            BARRIER();
            WAIT_VM(0); warm(1);
            WAIT_VM(0); warm(2);
            WAIT_VM(0); warm(3);
        } else {
            const int fr = lane & 15, fq = lane >> 4;
            const int fb = tn * 224 + ((tn & 1) ? 32 : 0) + wn * 32, fh = tn * 224 + ((tn & 1) ? 0 : 192) + wn * 16;
            WAIT_VM(0);
            WAIT_LGKM0();
            BARRIER();
            f32x4 acc[4][7];
#pragma unroll
            for (int j = 0; j < 7; ++j) {
                const u32x2 bb = *(const u32x2*)(P.b1 + (j < 6 ? fb + (j >> 1) * 64 + (j & 1) * 16 : fh) + fq * 4);
                const f32x4 bv = {lo16(bb[0]), hi16(bb[0]), lo16(bb[1]), hi16(bb[1])};
#pragma unroll
                for (int i = 0; i < 4; ++i) acc[i][j] = bv;
            }
            Reader<TR, 128, TR, 224, 4, 7> rd; rd.init(ring0, lane, wm, wn);
            Regs<4, 7> R0;
            int slot = 0;
            BARRIER();
            __builtin_amdgcn_s_waitcnt(0x0070);
            BARRIER();
            STAMP(1, 1);
            rd.read(R0, 0);
            for (int t = 0; t < NK / 4; ++t) {
                stage_step<3, SB>(slot, rd, rd, R0, acc); stage_step<3, SB>(slot, rd, rd, R0, acc);
                stage_step<3, SB>(slot, rd, rd, R0, acc); stage_step<3, SB>(slot, rd, rd, R0, acc);
            }
            STAMP(1, 2);
            const __amdgpu_buffer_rsrc_t rs = rsrc(P.U(), (unsigned)D_ * F_ * 2);
            const unsigned df = (unsigned)(tm * 8 + wm * 4);
            const unsigned ob = (unsigned)((fb >> 5) * 64 + df) * 1024u + (unsigned)((((fq & 1) * 2 + (fq >> 1)) * 16 + fr) * 16);
            const unsigned oh = (unsigned)((fh >> 5) * 64 + df + (fq & 1)) * 1024u + (unsigned)(((wn * 2 + (fq >> 1)) * 16 + fr) * 16);
            unsigned pk[4][7][2];
#pragma unroll
            for (int i = 0; i < 4; ++i) {
#pragma unroll
                for (int j = 0; j < 7; ++j) {
                    const f32x2 a = gelu2((f32x2){acc[i][j][0], acc[i][j][1]}), b = gelu2((f32x2){acc[i][j][2], acc[i][j][3]});
                    pk[i][j][0] = pack2(a.x, a.y); pk[i][j][1] = pack2(b.x, b.y);
                }
#pragma unroll
                for (int jp = 0; jp < 3; ++jp)
                    __builtin_amdgcn_raw_buffer_store_b128(pair16(pk[i][2 * jp][0], pk[i][2 * jp][1], pk[i][2 * jp + 1][0], pk[i][2 * jp + 1][1]), rs, ob + i * 1024 + jp * 2 * 65536, 0, SC1);
                if (i & 1)
                    __builtin_amdgcn_raw_buffer_store_b128(pair16(pk[i - 1][6][0], pk[i - 1][6][1], pk[i][6][0], pk[i][6][1]), rs, oh + (i - 1) * 1024, 0, SC1);
                SCHED0();
            }
            STAMP(1, 3);
        }
    } else {
        constexpr int NK = D_ / 64, SB = SB_Q;
        const int tm = fm;
        if (loader) {
            Loader<4, 6> L; L.init_m<ROW>(lw, lane, D_ * 2);
            L.init_n<TR>(lw, lane, 3 * D_ * 2, qkv_head(fn, 0) * 64, qkv_head(fn, 1) * 64, qkv_head(fn, 2) * 64);
            const char* gm = (const char*)P.x + (size_t)tm * 128 * D_ * 2; const char* gn = (const char*)P.wqkv;
            int slot = 0;
            auto issue = [&](int u) {
                const int k = (korder(u & (NK - 1), (tm + fn) & 3, NK) + ROTQ * (grp >> 1)) & (NK - 1);
                L.issue_m<0>(gm + k * 128, lds + slot * SB, lw);
                L.issue_n<NTQ>(gn + (size_t)k * (64 * 3 * D_ * 2) + (u >= NK ? 16 * 64 : 0), lds + slot * SB, lw);
                slot = (slot == 2) ? 0 : slot + 1;
            };
            issue(0); issue(1);
            WAIT_VM(L.NPW); BARRIER();
            issue(2);
            int t = 0;
            WAIT_VM(L.NPW); BARRIER(); issue(3); ++t;
#pragma unroll 1
            for (int seg = 0; seg < 2; ++seg) {
                const int end = seg == 0 ? 12 : 2 * NK - 3;
#pragma unroll 1
                for (; t < end; ++t) { WAIT_VM(L.NPW); BARRIER(); issue(t + 3); swarm(t - 1); }
                asm volatile("s_waitcnt lgkmcnt(0)" : "+s"(ssink));
                if (seg == 0 && lw == 0) {
                    poll_flags(P.flags(), lane < 8 ? FL_L + tm * 8 + lane : -1, tag);
                    glds16<SC1>((const char*)P.stat() + tm * 1024 + lane * 16, lds + LDS_QX);
                }
            }
            poll_flags(P.flags(), attn_dep(hd, qb, lane), tag);
            WAIT_VM(0);
            BARRIER();
            attn_load<4, 1>(P, lds + LDS_AK, hd, qb, lw, lane);
            attn_load<4, 2>(P, lds + LDS_AV, hd, qb, lw, lane);
            BARRIER();
            attn_load<4, 0>(P, lds + LDS_AQ, hd, qb, lw, lane);
            BARRIER();
        } else {
            const int fr = lane & 15, fq = lane >> 4;
            Reader<ROW, 128, TR, 192, 4, 6> rd; rd.init(ring0, lane, wm, wn);
            rd.n.b = rd.n.a + 32 - wm * 32; rd.n.a += wm * 32;
            Regs<4, 6> R0;
            int slot = 0;
            __builtin_amdgcn_s_waitcnt(0x0070);
            BARRIER();
            STAMP(1, 1);
            rd.read(R0, 0);
            const __amdgpu_buffer_rsrc_t rs = rsrc(P.qkv(), (unsigned)S_ * 3 * D_ * 2);
#pragma unroll 1
            for (int rnd = 0; rnd < 2; ++rnd) {
                f32x4 acc[5][6] = {};
                for (int t = 0; t < NK / 4; ++t) {
                    if (rnd == 1 && t == 1) WAIT_VM(0);
                    if (rnd == 1 && t == 2 && tid == 0) set_flag(P.flags(), FL_Q + tm * 32 + fn, tag);
                    stage_step<3, SB>(slot, rd, rd, R0, acc); stage_step<3, SB>(slot, rd, rd, R0, acc);
                    stage_step<3, SB>(slot, rd, rd, R0, acc); stage_step<3, SB>(slot, rd, rd, R0, acc);
                }
                STAMP(1, 2);
                LAS char* ex = lds + LDS_QX + 1024 + rnd * 1024;
                if (fr == 0) {
#pragma unroll
                    for (int jp = 0; jp < 3; ++jp) *(LAS f32x4*)(ex + wid * 256 + jp * 64 + fq * 16) = acc[4][2 * jp];
                }
                WAIT_LGKM0();
                CFENCE();
                __hip_atomic_store((LAS unsigned*)(lds + LDS_CTL + wid * 4), (unsigned)(rnd + 1), __ATOMIC_RELAXED, __HIP_MEMORY_SCOPE_WORKGROUP);
                const int s0 = tm * 128 + wm * 64;
                f32x2 st[4];
#pragma unroll
                for (int i = 0; i < 4; ++i) st[i] = *(LAS f32x2*)(lds + LDS_QX + (wm * 64 + i * 16 + fr) * 8);
                while (__builtin_amdgcn_readfirstlane(__hip_atomic_load((LAS unsigned*)(lds + LDS_CTL + (wid ^ 2) * 4), __ATOMIC_RELAXED, __HIP_MEMORY_SCOPE_WORKGROUP)) <= rnd) {}
                CFENCE();
#pragma unroll
                for (int jp = 0; jp < 3; ++jp) {
                    const int gh = qkv_head(fn, jp) + rnd * 16 + wn;
                    const float sc = jp == 0 ? 0.17677669529663687f * 1.4426950408889634f : 1.0f;
                    const f32x4 ca = acc[4][2 * jp], cb = *(LAS f32x4*)(ex + (wid ^ 2) * 256 + jp * 64 + fq * 16);
#pragma unroll
                    for (int i = 0; i < 4; ++i) {
                        const float r = st[i].y * sc, mr = -st[i].x * r;
                        const f32x4 a = acc[i][2 * jp] * r + ca * mr, b = acc[i][2 * jp + 1] * r + cb * mr;
                        __builtin_amdgcn_raw_buffer_store_b128(pair16(pack2(a[0], a[1]), pack2(a[2], a[3]), pack2(b[0], b[1]), pack2(b[2], b[3])), rs,
                                                               (unsigned)(((gh * S_ + s0 + i * 16 + fr) * 32 + ((fq & 1) ^ wm) * 16 + (fq >> 1) * 8) * 2), 0, SC1);
                    }
                }
            }
            STAMP(1, 3);
        }
    }

    constexpr int SB = SB_F, NS = NS_F;
    constexpr size_t OS = 2 * (S_ / 16) * 1024, WOS = 64 * D_ * 2, W2S = 64 * S_ * 2, US = 2 * 65536;
    const int tile = fm * 8 + fn, cls = (fm + fn) & 3, nf = isM ? NF_M : NF_Q;
    auto fof = [&](int i) {
        int r = korder(i - 7, cls, nf - 7) + (ROTF ? (grp & 1) * ((nf - 7) >> 1) : 0);
        r = r >= nf - 7 ? r - (nf - 7) : r;
        const int j = i < 7 ? (isM ? 0 : 7) + korder(i, cls, 7) : (isM ? 14 + NA : 14) + r, k = 14 * (grp >> 1) + j;
        return k >= 56 ? k - 56 : k;
    };
    const char* g2 = (const char*)P.w2 + fm * 256;
    Loader<4, 4> L;
    if (loader) {
        L.init_m<TR>(lw, lane, S_ * 2);
        WAIT_VM(0);
        __builtin_amdgcn_s_waitcnt(0x0F70);
    }
    auto preload_w2 = [&]() {
#pragma unroll
        for (int u = 0; u < 2; ++u)
#pragma unroll
            for (int i = 0; i < 4; ++i) glds16_hidden_plain(g2 + fof(u) * W2S + L.om[i], lds + u * SB + (lw + 4 * i) * PIECE);
    };
    if (isM) {
        if (loader) preload_w2();
        else {
            WAIT_VM(0);
            if (lane == 0 && __hip_atomic_fetch_add((LAS unsigned*)(lds + LDS_MC), 1u, __ATOMIC_RELAXED, __HIP_MEMORY_SCOPE_WORKGROUP) == 3u)
                set_flag(P.flags(), FL_U + um * 32 + un, tag);
        }
        STAMP(2, 0);
    } else {
        if (loader) __builtin_amdgcn_s_setprio(1);
        WAIT_LGKM0();
        BARRIER();
#pragma unroll 1
        for (int it = 0; it < 2; ++it) {
            STAMP(2, 3 * it);
            f32x16 sc[8];
            int ln = lane;
            asm volatile("" : "+v"(ln));
            const int dg = (ln & 31) - 4 * (ln >> 5);
            const unsigned up = dg < 0 ? ~0u : ~1u << dg;
            const int nbad = qb == 0 ? 8 - wid : 0;
            SCHED0();
            const float m = attn_scores(lds + (it ? LDS_BK : LDS_AK), lds + LDS_AQ, nbad, wid, ln, up, sc);
            SCHED0();
            if (it == 0 || loader) WAIT_VM(0);
            BARRIER();
            if (it == 0) { if (tid == 0) set_flag(P.flags(), FL_Q + fm * 32 + fn + 8, tag); }
            else if (loader) preload_w2();
            STAMP(2, 3 * it + 1);
            PvState S;
            S.l0 = (f32x2){0.f, 0.f}; S.l1 = S.l0; S.o = (f32x16){};
            attn_pv<0, 8>(lds + (it ? LDS_BV : LDS_AV), nbad, wid, ln, up, sc, m, S);
            attn_out(P, hd + 16 * it, qb, wid, ln, S.l0 + S.l1, S.o);
            STAMP(2, 3 * it + 2);
            if (it == 0) {
                if (loader) {
                    poll_flags(P.flags(), attn_dep(hd + 16, qb, ln), tag);
                    attn_load<4, 0>(P, lds + LDS_AQ, hd + 16, qb, lw, ln);
                    attn_load<4, 1>(P, lds + LDS_BK, hd + 16, qb, lw, ln);
                    attn_load<4, 2>(P, lds + LDS_BV, hd + 16, qb, lw, ln);
                    WAIT_VM(8);
                }
                BARRIER();
            }
        }
        WAIT_LGKM0();
    }
    if (loader) {
        const char* go = (const char*)P.o() + (isM ? KW * OS : 0) + fm * 8192;
        const char* gw = (const char*)P.wo + (isM ? KW * WOS : 0) + fn * 256;
        const char* gu = (const char*)P.U() + fn * 8192;
        __builtin_amdgcn_s_setprio(0);
        L.init_n<FRAG>(lw, lane, US / 2);
        auto issue_f = [&](int u) {
            const int k = fof(u);
            L.issue_m<NT2>(g2 + k * W2S, lds + (u & (NS - 1)) * SB, lw); L.issue_n<SC1>(gu + k * US, lds + (u & (NS - 1)) * SB, lw);
        };
        poll_flags(P.flags(), lane < 2 ? FL_U + fn * 32 + (grp >> 1) * 4 + (isM ? 0 : 2) + lane : -1, tag);
#pragma unroll
        for (int u = 0; u < 2; ++u) L.issue_n<SC1>(gu + fof(u) * US, lds + u * SB, lw);
        WAIT_VM(4); BARRIER();
        issue_f(2); issue_f(3);
        WAIT_VM(16); BARRIER(); issue_f(4);
        WAIT_VM(16); BARRIER(); issue_f(5);
        WAIT_VM(16); BARRIER(); issue_f(6);
        const int udep = lane < 16 ? FL_U + fn * 32 + lane : -1;
        int nxt = 7;
        bool uready = false;
#pragma unroll 1
        for (int t = 3; t < 7; ++t) {
            if (t == 6 && !uready) { poll_flags(P.flags(), udep, tag); uready = true; for (; nxt < 10; ++nxt) issue_f(nxt); }
            if (nxt - (t + 2) >= 2) WAIT_VM(16); else WAIT_VM(0);
            BARRIER();
            if (!uready) uready = flags_set(P.flags(), udep, tag);
            if (uready) for (; nxt <= t + NS; ++nxt) issue_f(nxt);
        }
#pragma unroll 1
        for (int u = 7 + NS; u < nf; ++u) { WAIT_VM(16); BARRIER(); issue_f(u); }
        L.init_m<FRAG>(lw, lane, OS / 2); L.init_n<TR>(lw, lane, D_ * 2);
        if (lw == 0) poll_flags(P.flags(), lane < 16 ? FL_O + (fm >> 1) * 32 + (isM ? 16 : 0) + lane : -1, tag);
#pragma unroll 1
        for (int i = 0; i < KW; ++i) {
            WAIT_VM(16); BARRIER();
            const int k = (korder(i, cls, KW) + ROTW * (grp >> 1)) & (KW - 1);
            LAS char* dst = lds + ((nf + i) & (NS - 1)) * SB;
            L.issue_m<SC1>(go + k * OS, dst, lw); L.issue_n<NT2>(gw + k * WOS, dst, lw);
        }
        WAIT_VM(16); BARRIER();
        WAIT_VM(8);
        if (isM && lw == 0) poll_flags(P.flags(), lane == 0 ? FL_P + tile * 32 : -1, tag);
        BARRIER();
        WAIT_VM(0); BARRIER();
        BARRIER();
        if (!isM) BARRIER();
        if (blockIdx.x == 0 && lw == 0) {
#pragma unroll 1
            for (int i = 0; i < 4; ++i) poll_flags(P.flags(), FL_O + i * 64 + lane, tag);
            if (lane == 0) set_flag(P.flags(), FL_E, tag);
        }
    } else {
        const int fr = lane & 15, fq = lane >> 4;
        const int s0 = fm * 128 + wm * 64, d0 = fn * 128 + wn * 64;
        const __amdgpu_buffer_rsrc_t ro = rsrc(P.out, (unsigned)S_ * D_ * 2);
        f32x4 acc[4][4] = {};
        Reader<TR, 128, FRAG, 128, 4, 4> rf;
        Reader<FRAG, 128, TR, 128, 4, 4> rw;
        Regs<4, 4> R0;
        int slot = 0;
#define STEP(a, b) stage_step<NS, SB>(slot, a, b, R0, acc)
        rf.init(ring0, lane, wm, wn);
        BARRIER();
        STAMP(3, 1);
        rf.read(R0, 0);
        for (int t = 0; t < (nf - 1) / 4; ++t) {
            if (t == 1) WAIT_VM(0);
            if (t == 2 && !isM && tid == 0) { set_flag(P.flags(), FL_O + qb * 32 + hd, tag); set_flag(P.flags(), FL_O + qb * 32 + hd + 16, tag); }
            STEP(rf, rf); STEP(rf, rf); STEP(rf, rf); STEP(rf, rf);
        }
        for (int t = 0; t < ((nf - 1) & 3); ++t) STEP(rf, rf);
        rw.init(ring0 + slot * SB, lane, wm, wn);
        STEP(rf, rw);
        STAMP(3, 4);
        for (int t = 0; t < 2; ++t) { STEP(rw, rw); STEP(rw, rw); STEP(rw, rw); }
        const unsigned eo = (unsigned)(((s0 + fr) * D_ + d0 + (fq & 1) * 16 + (fq >> 1) * 8) * 2);
        u32x4 ex[4][2];
        unsigned bv[4] = {0u, 0u, 0u, 0u};
        if (isM) {
#pragma unroll
            for (int i = 0; i < 4; ++i)
#pragma unroll
                for (int jp = 0; jp < 2; ++jp) ex[i][jp] = __builtin_amdgcn_raw_buffer_load_b128(ro, eo + i * (16 * D_ * 2) + jp * 64, 0, SC1);
        } else {
#pragma unroll
            for (int i = 0; i < 4; ++i) {
#pragma unroll
                for (int jp = 0; jp < 2; ++jp) ex[i][jp] = *(const u32x4*)((const char*)P.x + eo + i * (16 * D_ * 2) + jp * 64);
                bv[i] = P.b2[s0 + i * 16 + fr];
            }
        }
        SCHED0();
        STEP(rw, rw); STEP(rw, rw);
        STAMP(3, 2);
#pragma unroll
        for (int i = 0; i < 4; ++i)
#pragma unroll
            for (int jp = 0; jp < 2; ++jp) {
                auto e0 = __builtin_amdgcn_permlane16_swap(ex[i][jp][0], ex[i][jp][2], false, false);
                auto e1 = __builtin_amdgcn_permlane16_swap(ex[i][jp][1], ex[i][jp][3], false, false);
                const f32x4 a = acc[i][2 * jp] + (f32x4){lo16(e0[0]), hi16(e0[0]), lo16(e1[0]), hi16(e1[0])} + lo16(bv[i]);
                const f32x4 b = acc[i][2 * jp + 1] + (f32x4){lo16(e0[1]), hi16(e0[1]), lo16(e1[1]), hi16(e1[1])} + lo16(bv[i]);
                __builtin_amdgcn_raw_buffer_store_b128(pair16(pack2(a[0], a[1]), pack2(a[2], a[3]), pack2(b[0], b[1]), pack2(b[2], b[3])), ro, eo + i * (16 * D_ * 2) + jp * 64, 0, SC1);
            }
        if (!isM) {
            WAIT_VM(0);
            BARRIER();
            if (tid == 0) set_flag(P.flags(), FL_P + tile * 32, tag);
        }
        STAMP(3, 3);
#undef STEP
    }
    STAMP_OUT();
}

extern "C" void kernel_launch(void* const* d_in, const int* in_sizes, int n_in, void* d_out, int out_size, void* d_ws, size_t ws_size, hipStream_t stream) {
    char* ws = (char*)d_ws;
    Params P;
    P.x = (const u16*)d_in[0]; P.wqkv = (const u16*)d_in[1]; P.wo = (const u16*)d_in[2];
    P.w1 = (const u16*)d_in[4]; P.b1 = (const u16*)d_in[5]; P.w2 = (const u16*)d_in[6]; P.b2 = (const u16*)d_in[7];
    P.out = (u16*)d_out; P.ws = ws;
    (void)hipFuncSetAttribute((const void*)k_main, hipFuncAttributeMaxDynamicSharedMemorySize, LDS_BYTES);
    k_main<<<256, 512, LDS_BYTES, stream>>>(P);
}
```

```cpp
#include <hip/hip_runtime.h>
#include <hip/hip_bf16.h>
#include <stdint.h>

#define LAS __attribute__((address_space(3)))
#define GAS __attribute__((address_space(1)))
typedef __bf16 bf16x8 __attribute__((ext_vector_type(8)));
typedef __bf16 bf16x4 __attribute__((ext_vector_type(4)));
typedef __bf16 bf16x2 __attribute__((ext_vector_type(2)));
typedef float f32x2 __attribute__((ext_vector_type(2)));
typedef float f32x4 __attribute__((ext_vector_type(4)));
typedef float f32x16 __attribute__((ext_vector_type(16)));
typedef unsigned u32x4 __attribute__((ext_vector_type(4)));
typedef unsigned u32x2 __attribute__((ext_vector_type(2)));
typedef unsigned short u16;
typedef unsigned long long u64;

constexpr int S_ = 2048, D_ = 1024, F_ = 3584, NH = 32;

constexpr size_t WS_FLAGS = 0;
constexpr size_t WS_STAT  = 1 << 16;
constexpr size_t WS_CS    = WS_STAT + (size_t)S_ * 8;
constexpr size_t WS_H     = 1 << 17;
constexpr size_t WS_QKV   = WS_H   + (size_t)S_ * D_ * 2;
constexpr size_t WS_U     = WS_QKV + (size_t)S_ * 3 * D_ * 2;
constexpr size_t WS_O     = WS_U   + (size_t)D_ * F_ * 2;
constexpr size_t WS_PART  = WS_O   + (size_t)S_ * D_ * 2;
constexpr int FL_E = 0;
constexpr int FL_L = 32;
constexpr int FL_C = 5 * 32;
constexpr int FL_U = 8 * 32;
constexpr int FL_Q = 16 * 32;
constexpr int FL_O = 32 * 32;
constexpr int FL_P = 40 * 32;

constexpr size_t WS_DBG = 200u << 20;
#define STAMP_DECL
#define STAMP(kid, i) do {} while (0)
#define STAMPL(kid, i) do {} while (0)
#define STAMP_OUT() do {} while (0)
#define WAIT_VM(n) asm volatile("s_waitcnt vmcnt(%0)" ::"n"(n) : "memory")
#define WAIT_LGKM0() __builtin_amdgcn_s_waitcnt(0xC07F)
#define CFENCE() asm volatile("" ::: "memory")
#define BARRIER() do { CFENCE(); __builtin_amdgcn_s_barrier(); CFENCE(); } while (0)
#define SCHED0() __builtin_amdgcn_sched_barrier(0)
#define RLX_AGENT __ATOMIC_RELAXED, __HIP_MEMORY_SCOPE_AGENT
constexpr int SC1 = 16;

__device__ __forceinline__ float bf2f(u16 v) { return __uint_as_float((unsigned)v << 16); }
__device__ __forceinline__ float lo16(unsigned v) { return __uint_as_float(v << 16); }
__device__ __forceinline__ float hi16(unsigned v) { return __uint_as_float(v & 0xffff0000u); }
__device__ __forceinline__ unsigned pack2(float lo, float hi) {
    f32x2 v = {lo, hi};
    bf16x2 b = __builtin_convertvector(v, bf16x2);
    return __builtin_bit_cast(unsigned, b);
}
__device__ __forceinline__ float rbf(float x) { return __uint_as_float(pack2(x, 0.f) << 16); }
template <int CTRL, int RMASK> __device__ __forceinline__ float dpp_add(float v) {
    return v + __builtin_bit_cast(float, __builtin_amdgcn_update_dpp(0, __builtin_bit_cast(int, v), CTRL, RMASK, 0xf, false));
}
__device__ __forceinline__ float wave_sum(float v) {
    v = dpp_add<0x111, 0xf>(v); v = dpp_add<0x112, 0xf>(v); v = dpp_add<0x114, 0xf>(v); v = dpp_add<0x118, 0xf>(v);
    v = dpp_add<0x142, 0xa>(v); v = dpp_add<0x143, 0xc>(v);
    return __builtin_bit_cast(float, __builtin_amdgcn_readlane(__builtin_bit_cast(int, v), 63));
}
__device__ __forceinline__ __amdgpu_buffer_rsrc_t rsrc(const void* p, unsigned bytes) { return __builtin_amdgcn_make_buffer_rsrc((void*)p, 0, bytes, 0x00020000); }

enum { ROW = 0, TR = 1, FRAG = 2 };
constexpr int PIECE = 1056, BLK = 8 * PIECE;
template <int T, int X> struct Op {
    static constexpr int BYTES = X * 132;
    __device__ static __forceinline__ unsigned src_off(int i, int lane, unsigned ld, unsigned cb) {
        const int blk = i >> 3, p = i & 7, pos = lane >> 3;
        if (T == TR) {
            if (X == 224 && blk == 3) {
                const int ps = lane >> 2, g = (p & 1) | ((ps >> 3) << 1), r = 32 * (p >> 1) + 8 * g + 4 * ((ps >> 2) & 1) + (ps & 3);
                return (unsigned)r * ld + cb + (unsigned)((lane & 3) * 16);
            }
            const int g = (p >> 2) | (pos & 2), r = 32 * (pos >> 2) + 8 * g + 4 * (pos & 1) + (p & 3);
            return (unsigned)r * ld + cb + (unsigned)((lane & 7) * 16);
        } else if (T == ROW) {
            const int j = ((pos >> 1) & 1) | ((pos & 1) << 1) | (pos & 4);
            return (unsigned)(blk * 64 + j * 8 + p) * ld + (unsigned)((lane & 7) * 16);
        } else {
            return (unsigned)blk * ld + (unsigned)(p * 1024 + lane * 16);
        }
    }
};
template <int AUX> __device__ __forceinline__ void glds16(const char* g, LAS char* l) { __builtin_amdgcn_global_load_lds((const GAS unsigned*)g, (LAS unsigned*)l, 16, 0, AUX); }
__device__ __forceinline__ void glds16_hidden_plain(const char* g, LAS char* l) {
    unsigned keep;
    asm volatile("s_mov_b32 %0, m0\n\ts_mov_b32 m0, %2\n\ts_nop 0\n\tglobal_load_lds_dwordx4 %1, off\n\ts_mov_b32 m0, %0"
                 : "=&s"(keep) : "v"(g), "s"(__builtin_amdgcn_readfirstlane((unsigned)(uintptr_t)l)) : "memory");
}
__device__ __forceinline__ void glds16_hidden(const char* g, LAS char* l) {
    unsigned keep;
    asm volatile("s_mov_b32 %0, m0\n\ts_mov_b32 m0, %2\n\ts_nop 0\n\tglobal_load_lds_dwordx4 %1, off sc1\n\ts_mov_b32 m0, %0"
                 : "=&s"(keep) : "v"(g), "s"(__builtin_amdgcn_readfirstlane((unsigned)(uintptr_t)l)) : "memory");
}
__device__ __forceinline__ void glds4(const char* g, LAS char* l) { __builtin_amdgcn_global_load_lds((const GAS unsigned*)g, (LAS unsigned*)l, 4, 0, 0); }

template <int T, int X> struct Addr {
    unsigned a, b;
    static constexpr int NV = (X == 224 || X == 192) ? 2 : 1;
    __device__ __forceinline__ void init(unsigned base, int lane, int w) {
        const int g = lane >> 4;
        b = 0;
        if (T == TR) {
            const int q = (lane & 15) >> 2, p = lane & 3;
            a = base + (unsigned)((q | ((g & 1) << 2)) * PIECE + (g >> 1) * 256 + p * 8 + (X == 128 ? w * BLK : w * 64));
            b = base + (unsigned)(3 * BLK + (g & 1) * PIECE + (g >> 1) * 512 + q * 64 + w * 32 + p * 8);
        } else if (T == ROW) {
            const int r = lane & 15;
            a = base + (unsigned)(w * BLK + (r & 7) * PIECE + (r >> 3) * 256 + g * 16);
        } else {
            a = base + (unsigned)(w * 4 * PIECE + lane * 16);
        }
    }
    __device__ __forceinline__ void step(unsigned d) { a += d; if (NV == 2) b += d; }
    __device__ __forceinline__ bf16x8 read(int n, int kk) const {
        if (T == TR) {
            LAS char* p; int t;
            if (X == 224 && n == 6) { p = (LAS char*)(uintptr_t)b + kk * 2 * PIECE; t = 256; }
            else if (X == 192) { p = (LAS char*)(uintptr_t)((n & 1) ? b : a) + kk * 512 + (n >> 1) * BLK; t = 128; }
            else { p = (LAS char*)(uintptr_t)a + kk * 512 + (X == 128 ? n * 32 : (n >> 1) * BLK + (n & 1) * 32); t = 128; }
            const bf16x4 lo = __builtin_amdgcn_ds_read_tr16_b64_v4bf16((LAS bf16x4*)p);
            const bf16x4 hi = __builtin_amdgcn_ds_read_tr16_b64_v4bf16((LAS bf16x4*)(p + t));
            return __builtin_shufflevector(lo, hi, 0, 1, 2, 3, 4, 5, 6, 7);
        } else if (T == ROW) {
            return *(LAS bf16x8*)((LAS char*)(uintptr_t)a + (n & 1) * 128 + (n >> 1) * 512 + kk * 64);
        } else {
            return *(LAS bf16x8*)((LAS char*)(uintptr_t)a + kk * BLK + n * PIECE);
        }
    }
};
template <int FM, int FN> struct Regs { bf16x8 m[FM], n[FN]; };
#define MFMA16(a, b, c) __builtin_amdgcn_mfma_f32_16x16x32_bf16(a, b, c, 0, 0, 0)

template <int TM, int BM, int TN, int BN, int FM, int FN> struct Reader {
    Addr<TM, BM> m; Addr<TN, BN> n;
    static constexpr int NDS = FM * (TM == TR ? 2 : 1) + FN * (TN == TR ? 2 : 1), NV = Addr<TM, BM>::NV + Addr<TN, BN>::NV;
    __device__ __forceinline__ void init(unsigned slot_base, int lane, int wm, int wn) { m.init(slot_base, lane, wm); n.init(slot_base + BM * 132, lane, wn); }
    __device__ __forceinline__ void read(Regs<FM, FN>& R, int kk) const {
#pragma unroll
        for (int i = 0; i < FM; ++i) R.m[i] = m.read(i, kk);
#pragma unroll
        for (int j = 0; j < FN; ++j) R.n[j] = n.read(j, kk);
    }
    __device__ __forceinline__ void step(unsigned d) { m.step(d); n.step(d); }
};
template <int AM, int FM, int FN> __device__ __forceinline__ void mma(f32x4 (&acc)[AM][FN], const Regs<FM, FN>& R) {
    const u32x4 one = {0x3f803f80u, 0x3f803f80u, 0x3f803f80u, 0x3f803f80u};
#pragma unroll
    for (int j = 0; j < FN; ++j) {
#pragma unroll
        for (int i = 0; i < FM; ++i) acc[i][j] = MFMA16(R.n[j], R.m[i], acc[i][j]);
        if (AM > FM && !(j & 1)) acc[AM - 1][j] = MFMA16(R.n[j], __builtin_bit_cast(bf16x8, one), acc[AM - 1][j]);
    }
}
template <int NDS, int NV, int NMF> __device__ __forceinline__ void interleave() {
#pragma unroll
    for (int i = 0; i < NMF; ++i) {
        __builtin_amdgcn_sched_group_barrier(0x8, 1, 0);
        if (i < NDS) __builtin_amdgcn_sched_group_barrier(0x80, 1, 0);
        if (i >= NMF - NV) __builtin_amdgcn_sched_group_barrier(0x2, 1, 0);
    }
}
template <int NS, int SB, class RD, class RDN, int AM, int FM, int FN>
__device__ __forceinline__ void stage_step(int& slot, RD& rd, RDN& rn, Regs<FM, FN>& R0, f32x4 (&acc)[AM][FN]) {
    constexpr int NMF = FM * FN + (AM - FM) * (FN / 2);
    Regs<FM, FN> R1;
    const unsigned d = (slot + 1 == NS) ? (unsigned)(-(NS - 1) * SB) : (unsigned)SB;
    slot = (slot + 1 == NS) ? 0 : slot + 1;
    rd.read(R1, 1);
    rn.step(d);
    mma(acc, R0);
    interleave<RD::NDS, RDN::NV, NMF>();
    SCHED0();
    WAIT_LGKM0();
    BARRIER();
    SCHED0();
    rn.read(R0, 0);
    mma(acc, R1);
    interleave<RDN::NDS, 0, NMF>();
    SCHED0();
}

template <int NM, int NN> struct Loader {
    static constexpr int NPW = NM + NN;
    unsigned om[NM], on[NN];
    template <int T> __device__ __forceinline__ void init_m(int lw, int lane, unsigned ld) {
#pragma unroll
        for (int i = 0; i < NM; ++i) om[i] = Op<T, NM * 32>::src_off(lw + 4 * i, lane, ld, (i >> 1) * 128);
    }
    template <int T> __device__ __forceinline__ void init_n(int lw, int lane, unsigned ld, unsigned c0 = 0, unsigned c1 = 128, unsigned c2 = 256, unsigned c3 = 384) {
#pragma unroll
        for (int i = 0; i < NN; ++i) on[i] = Op<T, NN * 32>::src_off(lw + 4 * i, lane, ld, (i >> 1) == 0 ? c0 : (i >> 1) == 1 ? c1 : (i >> 1) == 2 ? c2 : c3);
    }
    template <int AUX> __device__ __forceinline__ void issue_m(const char* gm, LAS char* dst, int lw) const {
#pragma unroll
        for (int i = 0; i < NM; ++i) glds16<AUX>(gm + om[i], dst + (lw + 4 * i) * PIECE);
    }
    template <int AUX> __device__ __forceinline__ void issue_n(const char* gn, LAS char* dst, int lw) const {
#pragma unroll
        for (int i = 0; i < NN; ++i) glds16<AUX>(gn + on[i], dst + NM * 32 * 132 + (lw + 4 * i) * PIECE);
    }
};
#define LEAD 2
__device__ __forceinline__ int korder(int p, int c, int nk) {
    const int r = (p - c) & 3, p0 = p - r;
    if (r > LEAD || p0 < 0 || p0 + LEAD >= nk) return p;
    return r == 0 ? p + LEAD : p - 1;
}
__device__ __forceinline__ f32x2 gelu2(f32x2 u) {
    const float c1 = -2.0f * 0.7978845608028654f * 1.4426950408889634f, c2 = c1 * 0.044715f;
    const f32x2 z = u * ((u * u) * c2 + c1);
    f32x2 e; e.x = __builtin_amdgcn_exp2f(z.x); e.y = __builtin_amdgcn_exp2f(z.y);
    e = e + 1.0f;
    f32x2 r; r.x = __builtin_amdgcn_rcpf(e.x); r.y = __builtin_amdgcn_rcpf(e.y);
    return u * r;
}
__device__ __forceinline__ u32x4 pair16(unsigned x0, unsigned x1, unsigned y0, unsigned y1) {
    auto a = __builtin_amdgcn_permlane16_swap(x0, y0, false, false);
    auto b = __builtin_amdgcn_permlane16_swap(x1, y1, false, false);
    return (u32x4){a[0], b[0], a[1], b[1]};
}
__device__ __forceinline__ bool flags_set(u64* flags, int idx, u64 tag) {
    const u64 v = __hip_atomic_load((GAS u64*)flags + (idx < 0 ? 0 : idx), RLX_AGENT);
    const bool ok = __builtin_amdgcn_ballot_w64(idx >= 0 && v - 1ull != tag) == 0ull;
    CFENCE();
    return ok;
}
__device__ __forceinline__ void poll_flags(u64* flags, int idx, u64 tag) {
#pragma unroll 1
    for (unsigned spins = 0; spins < (1u << 20); ++spins) {
        if (flags_set(flags, idx, tag)) break;
        __builtin_amdgcn_s_sleep(2);
    }
}
__device__ __forceinline__ void set_flag(u64* flags, int idx, u64 tag) {
    asm volatile("" : "+v"(tag));
    __hip_atomic_store((GAS u64*)flags + idx, tag + 1ull, RLX_AGENT);
}

struct Params {
    const u16 *x, *wqkv, *wo, *w1, *b1, *w2, *b2;
    u16* out;
    char* ws;
    __device__ __forceinline__ u16* h() const { return out; }
    __device__ __forceinline__ u16* qkv() const { return (u16*)(ws + WS_QKV); }
    __device__ __forceinline__ u16* U() const { return (u16*)(ws + WS_U); }
    __device__ __forceinline__ u16* o() const { return (u16*)(ws + WS_QKV); }
    __device__ __forceinline__ float* stat() const { return (float*)(ws + WS_STAT); }
    __device__ __forceinline__ u64* flags() const { return (u64*)(ws + WS_FLAGS); }
};

constexpr int SB_M = (128 + 224) * 132, SB_Q = (128 + 192) * 132, SB_F = 256 * 132, NS_F = 4;
constexpr int LDS_QX = 3 * SB_Q;
constexpr int LDS_CTL = LDS_QX + 3072;
constexpr int LDS_AQ = 0, LDS_AK = 2 * SB_Q, LDS_AV = 128 * 1024, LDS_BYTES = 160 * 1024;
constexpr int LDS_BK = 16 * 1024, LDS_BV = 96 * 1024;
constexpr int LDS_MC = LDS_BYTES - 512;
constexpr int LDS_SINK = LDS_BYTES - 256;
static_assert(LDS_CTL + 32 <= LDS_AV && 3 * SB_M <= LDS_SINK && 3 * SB_F <= LDS_AV && NS_F * SB_F <= LDS_BYTES, "LDS map");
#define NA 15
#define NT1 0
#define NTQ 0
#define NT2 0
#define NTX 0
#define ROTQ 4
#define ROTF 1
#define ROTW 2
constexpr int KW = 8, NF_Q = 7 + NA, NF_M = 7 + 42 - NA;

#define SPF 0
__device__ __forceinline__ void scalar_warm(const char* p, unsigned& sink) {
    if (SPF == 4) asm volatile("s_load_dword %0, %1, 0x0\n\ts_load_dword %0, %1, 0x40\n\ts_load_dword %0, %1, 0x80\n\ts_load_dword %0, %1, 0xc0" : "+s"(sink) : "s"(p));
    if (SPF == 8) asm volatile("s_load_dword %0, %1, 0x0\n\ts_load_dword %0, %1, 0x40\n\ts_load_dword %0, %1, 0x80\n\ts_load_dword %0, %1, 0xc0\n\t"
                               "s_load_dword %0, %1, 0x100\n\ts_load_dword %0, %1, 0x140\n\ts_load_dword %0, %1, 0x180\n\ts_load_dword %0, %1, 0x1c0" : "+s"(sink) : "s"(p));
}
__device__ __forceinline__ int qkv_head(int tn, int blk) { return blk * NH + ((tn & ~3) | ((tn + blk) & 3)) * 2; }

__device__ __forceinline__ f32x2 ln_row(const u32x4 a, const u32x4 b, u32x4& oa, u32x4& ob) {
    f32x2 v[8];
#pragma unroll
    for (int i = 0; i < 4; ++i) { v[i] = (f32x2){lo16(a[i]), hi16(a[i])}; v[4 + i] = (f32x2){lo16(b[i]), hi16(b[i])}; }
    const f32x2 s2 = ((v[0] + v[1]) + (v[2] + v[3])) + ((v[4] + v[5]) + (v[6] + v[7]));
    const float mean = wave_sum(s2.x + s2.y) * (1.0f / D_);
    f32x2 q2 = {0.f, 0.f};
#pragma unroll
    for (int i = 0; i < 8; ++i) { const f32x2 d = v[i] - mean; q2 += d * d; }
    const float var = wave_sum(q2.x + q2.y) * (1.0f / D_);
    const float mb = rbf(mean), rs = __builtin_amdgcn_rcpf(rbf(__builtin_amdgcn_sqrtf(rbf(rbf(var) + 1e-5f))));
    const float mb2 = rbf((mean - mb) * rs), rs2 = __builtin_amdgcn_rcpf(rbf(__builtin_amdgcn_sqrtf(rbf(rbf(var * rs * rs) + 1e-5f))));
    const float g = rs * rs2, c = -mb2 * rs2;
#pragma unroll
    for (int i = 0; i < 8; ++i) {
        const f32x2 t = (v[i] - mb) * g + c;
        if (i < 4) oa[i] = pack2(t.x, t.y); else ob[i - 4] = pack2(t.x, t.y);
    }
    return (f32x2){mb, rs};
}

#define MFMA32(a, b, c) __builtin_amdgcn_mfma_f32_32x32x16_bf16(a, b, c, 0, 0, 0)
__device__ __forceinline__ int attn_dep(int hd, int qb, int lane) {
    const int which = lane >> 2, st = 2 * qb - 2 + (lane & 3), p = hd >> 1;
    if (lane >= 12 || st < 0 || (which == 0 && (lane & 3) < 2)) return -1;
    return FL_Q + st * 32 + ((p & ~3) | ((p - which) & 3));
}
template <int NW, int WHICH> __device__ __forceinline__ void attn_load(const Params& P, LAS char* dst, int hd, int qb, int w, int lane) {
    const char* g = (const char*)(P.qkv() + (size_t)(WHICH * NH + hd) * S_ * 32) + (WHICH == 2 ? lane & 3 : (lane & 3) ^ ((lane >> 4) & 3)) * 16;
#pragma unroll
    for (int i = 0; i < (WHICH == 0 ? 16 : 32) / NW; ++i) {
        const int pc = i * NW + w;
        int row = qb * 256 - (WHICH == 0 ? 0 : 256) + pc * 16 + (lane >> 2); row = row < 0 ? 0 : row;
        glds16_hidden(g + row * 64, dst + pc * 1024);
    }
}
#define PIN(x) asm volatile("" : "+v"(x))
__device__ __forceinline__ float max16(const f32x16& v) {
    float a = __builtin_fmaxf(__builtin_fmaxf(v[0], v[1]), v[2]), b = __builtin_fmaxf(__builtin_fmaxf(v[3], v[4]), v[5]);
    a = __builtin_fmaxf(__builtin_fmaxf(a, v[6]), v[7]); b = __builtin_fmaxf(__builtin_fmaxf(b, v[8]), v[9]);
    a = __builtin_fmaxf(__builtin_fmaxf(a, v[10]), v[11]); b = __builtin_fmaxf(__builtin_fmaxf(b, v[12]), v[13]);
    a = __builtin_fmaxf(__builtin_fmaxf(a, v[14]), v[15]);
    return __builtin_fmaxf(a, b);
}
__device__ __forceinline__ float attn_scores(LAS char* buf, LAS char* qbuf, int nbad, int wid, int lane, unsigned up, f32x16 (&sc)[8]) {
    const int r32 = lane & 31, hh = lane >> 5, sw = (r32 >> 2) & 3;
    LAS char* k0p = buf + (wid * 32 + r32) * 64 + ((hh ^ sw) * 16);
    LAS char* k1p = buf + (wid * 32 + r32) * 64 + (((2 + hh) ^ sw) * 16);
    const bf16x8 qf0 = *(LAS bf16x8*)(qbuf + (wid * 32 + r32) * 64 + ((hh ^ sw) * 16));
    const bf16x8 qf1 = *(LAS bf16x8*)(qbuf + (wid * 32 + r32) * 64 + (((2 + hh) ^ sw) * 16));
    bf16x8 ka[9], kb[9];
#pragma unroll
    for (int i = 0; i < 3; ++i) { const int j = i == 0 ? 8 : i - 1; ka[i] = *(LAS bf16x8*)(k0p + j * 2048); kb[i] = *(LAS bf16x8*)(k1p + j * 2048); SCHED0(); }
    const float NEG = -INFINITY;
    const f32x16 zero = {};
    const float b0 = nbad > 0 ? NEG : 0.f;
    f32x16 s8;
    float m = NEG;
#pragma unroll
    for (int i = 0; i < 11; ++i) {
        if (i + 3 < 9) { ka[i + 3] = *(LAS bf16x8*)(k0p + (i + 2) * 2048); kb[i + 3] = *(LAS bf16x8*)(k1p + (i + 2) * 2048); }
        if (i < 9) {
            const f32x16 t = MFMA32(kb[i], qf1, MFMA32(ka[i], qf0, zero));
            if (i == 0) s8 = t; else sc[i - 1] = t;
        }
        SCHED0();
        const int j = i - 3;
        if (j == 0) {
#pragma unroll
            for (int r = 0; r < 16; ++r) {
                const unsigned mk = (unsigned)__builtin_amdgcn_sbfe((int)up, (r & 3) + 8 * (r >> 2), 1);
                sc[0][r] = __uint_as_float(__builtin_amdgcn_bitop3_b32(mk, __float_as_uint(sc[0][r] + b0), __float_as_uint(s8[r]), 0xca));
            }
        }
        if (j >= 0) { const float mj = max16(sc[j]); m = __builtin_fmaxf(m, (j > 0 && j < nbad) ? NEG : mj); PIN(m); }
        SCHED0();
    }
    auto rr = __builtin_amdgcn_permlane32_swap(__float_as_uint(m), __float_as_uint(m), false, false);
    return __builtin_fmaxf(__uint_as_float(rr[0]), __uint_as_float(rr[1]));
}
struct PvState { f32x2 l0, l1; f32x16 o; bf16x8 vf[2], v8[2]; u32x4 pw[2], p8[2]; };
__device__ __forceinline__ bf16x8 v_read(LAS char* vp, int j, int s) {
    const bf16x4 lo = __builtin_amdgcn_ds_read_tr16_b64_v4bf16((LAS bf16x4*)(vp + j * 2048 + s * 1024));
    const bf16x4 hi = __builtin_amdgcn_ds_read_tr16_b64_v4bf16((LAS bf16x4*)(vp + j * 2048 + s * 1024 + 512));
    return __builtin_shufflevector(lo, hi, 0, 1, 2, 3, 4, 5, 6, 7);
}
template <int J0, int J1> __device__ __forceinline__ void attn_pv(LAS char* buf, int nbad, int wid, int lane, unsigned up, f32x16 (&sc)[8], float m, PvState& S) {
    const int hh = lane >> 5;
    LAS char* vp = buf + (wid * 32 + 4 * hh + ((lane & 15) >> 2)) * 64 + ((lane >> 4) & 1) * 32 + (lane & 3) * 8;
#pragma unroll
    for (int j = J0; j < J1; ++j) {
        bf16x8 vn[2] = {v_read(vp, j, 0), v_read(vp, j, 1)};
        if (j == 0) { S.v8[0] = v_read(vp, 8, 0); S.v8[1] = v_read(vp, 8, 1); }
        const float mj = (j > 0 && j < nbad) ? INFINITY : m;
        f32x2 p[8];
#pragma unroll
        for (int i = 0; i < 8; ++i) p[i] = (f32x2){sc[j][2 * i], sc[j][2 * i + 1]} - mj;
        SCHED0();
#pragma unroll
        for (int g = 0; g < 4; ++g) {
#pragma unroll
            for (int i = 2 * g; i < 2 * g + 2; ++i) { p[i].x = __builtin_amdgcn_exp2f(p[i].x); p[i].y = __builtin_amdgcn_exp2f(p[i].y); PIN(p[i]); }
            SCHED0();
            if (j > 0 && (g & 1) == 0) S.o = MFMA32(S.vf[g >> 1], __builtin_bit_cast(bf16x8, S.pw[g >> 1]), S.o);
            if (j == 1 && (g & 1) == 1) S.o = MFMA32(S.v8[g >> 1], __builtin_bit_cast(bf16x8, S.p8[g >> 1]), S.o);
            SCHED0();
        }
#pragma unroll
        for (int i = 0; i < 8; ++i) {
            S.pw[i >> 2][i & 3] = pack2(p[i].x, p[i].y);
            if (i & 1) S.l1 += p[i]; else S.l0 += p[i];
        }
        if (j == 0) {
#pragma unroll
            for (int i = 0; i < 8; ++i) {
                const int kr = ((2 * i) & 3) + 8 * (i >> 1);
                const unsigned mk = __builtin_amdgcn_bitop3_b32(0xffffu, (unsigned)__builtin_amdgcn_sbfe((int)up, kr, 1), (unsigned)__builtin_amdgcn_sbfe((int)up, kr + 1, 1), 0xca);
                S.p8[i >> 2][i & 3] = S.pw[i >> 2][i & 3] & ~mk; S.pw[i >> 2][i & 3] &= mk;
            }
        }
        S.vf[0] = vn[0]; S.vf[1] = vn[1];
        PIN(S.l0); PIN(S.l1); PIN(S.pw[0]); PIN(S.pw[1]);
        SCHED0();
    }
    if (J1 == 8) {
        S.o = MFMA32(S.vf[0], __builtin_bit_cast(bf16x8, S.pw[0]), S.o);
        S.o = MFMA32(S.vf[1], __builtin_bit_cast(bf16x8, S.pw[1]), S.o);
    }
}
__device__ __forceinline__ void attn_out(const Params& P, int hd, int qb, int wid, int lane, f32x2 l2, const f32x16& oacc) {
    const int r32 = lane & 31, hh = lane >> 5;
    float l = l2.x + l2.y;
    {
        auto rr = __builtin_amdgcn_permlane32_swap(__float_as_uint(l), __float_as_uint(l), false, false);
        l = __uint_as_float(rr[0]) + __uint_as_float(rr[1]);
    }
    const float inv = __builtin_amdgcn_rcpf(l);
    unsigned w[8];
#pragma unroll
    for (int g = 0; g < 4; ++g) { w[2 * g] = pack2(oacc[4 * g] * inv, oacc[4 * g + 1] * inv); w[2 * g + 1] = pack2(oacc[4 * g + 2] * inv, oacc[4 * g + 3] * inv); }
    const __amdgpu_buffer_rsrc_t rs = rsrc(P.o(), (unsigned)S_ * D_ * 2);
    const unsigned off = (unsigned)((hd * (S_ / 16) + qb * 16 + wid * 2 + (r32 >> 4)) * 1024 + (hh * 16 + (r32 & 15)) * 16);
#pragma unroll
    for (int gp = 0; gp < 2; ++gp) {
        auto a = __builtin_amdgcn_permlane32_swap(w[4 * gp], w[4 * gp + 2], false, false);
        auto b = __builtin_amdgcn_permlane32_swap(w[4 * gp + 1], w[4 * gp + 3], false, false);
        __builtin_amdgcn_raw_buffer_store_b128((u32x4){a[0], b[0], a[1], b[1]}, rs, off + gp * 512, 0, SC1);
    }
}

extern "C" __global__ __launch_bounds__(512, 2) void k_main(const Params P) {
    extern __shared__ __attribute__((aligned(16))) char smem[];
    LAS char* lds = (LAS char*)smem;
    const int tid = threadIdx.x, lane = tid & 63, wid = __builtin_amdgcn_readfirstlane(tid >> 6);
    const int grp = blockIdx.x & 7, idx = blockIdx.x >> 3;
    const bool loader = wid >= 4;
    const int lw = wid - 4, wm = wid >> 1, wn = wid & 1;
    const unsigned ring0 = (unsigned)(uintptr_t)lds;
    const int i16 = idx & 15;
    const bool isM = idx < 16;
    const int fm = (grp >> 1) * 4 + (i16 >> 2), fn = (grp & 1) * 4 + (i16 & 3);
    const int um = (grp & 1) * 4 + (i16 >> 2), un = (grp >> 1) * 4 + (i16 & 3);
    const int hd = (grp & 1) * 8 + (i16 & 7), qb = (grp >> 1) * 2 + (i16 >> 3);
    STAMP_DECL;
    STAMP(1, 0);
#define PFC 1
    if (PFC && wid == 0) {
        u64 pc; unsigned rel, len;
        asm volatile("s_getpc_b64 %0\n1:\n\ts_mov_b32 %1, 1b-k_main\n\ts_mov_b32 %2, 2f-k_main\n\t.subsection 1\n2:\n\t.subsection 0" : "=s"(pc), "=s"(rel), "=s"(len));
        const unsigned off = (unsigned)((idx + 32 * (lane & 15)) * 128);
        glds4((const char*)(pc - rel) + (off < len ? off : 0u), lds + LDS_SINK);
    }
    const u64 tag = __hip_atomic_load((GAS u64*)P.flags() + FL_E, RLX_AGENT);

    unsigned ssink = 0;
    auto swarm = [&](int t) {
        if (SPF == 0) return;
        const int n = t * SPF;
        int f = (n >> 4) * 256 + ((grp & 1) * 32 + idx) * 4 + lw + (SPF == 4 ? 1792 : 0) + 896 * (grp >> 1);
        f = f >= F_ ? f - F_ : f;
        scalar_warm((const char*)P.w2 + (size_t)f * (S_ * 2) + (grp >> 1) * 1024 + (n & 15) * 64, ssink);
    };
    if (tid < 8) *(LAS unsigned*)(lds + (isM ? LDS_MC : LDS_CTL) + tid * 4) = 0u;
    if (isM) {
        constexpr int NK = S_ / 64, SB = SB_M;
        const int pg = (grp >> 1) + 4 * (grp & 1);
        const int tm = um, tn = un, mi = pg * 16 + i16;
        const char* gm = (const char*)P.h() + tm * 256; const char* gn = (const char*)P.w1 + tn * 448;
        const int cls = (tm + tn) & 3;
        auto kof = [&](int u) { const int p0 = u - ((u - cls) & 3); return (((p0 == 1 || p0 == 2) ? u : korder(u, cls, NK)) + 4 * pg) & (NK - 1); };
        Loader<4, 7> L;
        {
            const int row = mi * 16 + wid * 2;
            const u32x4* px = (const u32x4*)(P.x + (size_t)row * D_);
            u32x4 a0, b0, a1, b1;
            if (NTX) { a0 = __builtin_nontemporal_load(px + lane); b0 = __builtin_nontemporal_load(px + 64 + lane); a1 = __builtin_nontemporal_load(px + 128 + lane); b1 = __builtin_nontemporal_load(px + 192 + lane); }
            else { a0 = px[lane]; b0 = px[64 + lane]; a1 = px[128 + lane]; b1 = px[192 + lane]; }
            SCHED0();
            if (loader) {
                const unsigned c0 = (tn & 1) * 64;
                L.init_m<TR>(lw, lane, D_ * 2);
                L.init_n<TR>(lw, lane, F_ * 2, c0, c0 + 128, c0 + 256, (tn & 1) ? 0 : 384);
                asm volatile("" : "+v"(a0), "+v"(b0), "+v"(a1), "+v"(b1));
                L.issue_n<NT1>(gn + (size_t)kof(0) * (64 * F_ * 2), lds, lw);
            }
            STAMPL(0, 0);
            const __amdgpu_buffer_rsrc_t rh = rsrc(P.h(), (unsigned)S_ * D_ * 2);
            u32x4 oa0, ob0, oa1, ob1;
            const f32x2 s0 = ln_row(a0, b0, oa0, ob0), s1 = ln_row(a1, b1, oa1, ob1);
            __builtin_amdgcn_raw_buffer_store_b128(oa0, rh, (unsigned)(row * 2048 + lane * 16), 0, SC1);
            __builtin_amdgcn_raw_buffer_store_b128(ob0, rh, (unsigned)(row * 2048 + 1024 + lane * 16), 0, SC1);
            __builtin_amdgcn_raw_buffer_store_b128(oa1, rh, (unsigned)(row * 2048 + 2048 + lane * 16), 0, SC1);
            __builtin_amdgcn_raw_buffer_store_b128(ob1, rh, (unsigned)(row * 2048 + 3072 + lane * 16), 0, SC1);
            if (lane == 0) __builtin_amdgcn_raw_buffer_store_b128(__builtin_bit_cast(u32x4, (f32x4){s0.x, s0.y, s1.x, s1.y}), rsrc(P.stat(), S_ * 8), (unsigned)(row * 8), 0, SC1);
            SCHED0();
            STAMPL(0, 1);
        }
        if (loader) {
            WAIT_VM(0);
            BARRIER();
            STAMPL(0, 2);
            if (tid == 256 + 64) set_flag(P.flags(), FL_L + mi, tag);
#define ORD 0
            if (ORD == 0) {
#pragma unroll
                for (int u = 1; u < 3; ++u) L.issue_n<NT1>(gn + (size_t)kof(u) * (64 * F_ * 2), lds + u * SB, lw);
            }
            if (lw == 0) poll_flags(P.flags(), lane < 16 ? FL_L + pg * 16 + lane : -1, tag);
            STAMPL(0, 3);
            BARRIER();
            if (ORD == 0) {
#pragma unroll
                for (int u = 0; u < 3; ++u) L.issue_m<SC1>(gm + (size_t)kof(u) * (64 * D_ * 2), lds + u * SB, lw);
                WAIT_VM(8); BARRIER();
                WAIT_VM(4);
            } else {
                L.issue_m<SC1>(gm + (size_t)kof(0) * (64 * D_ * 2), lds, lw);
                L.issue_m<SC1>(gm + (size_t)kof(1) * (64 * D_ * 2), lds + SB, lw);
                if (ORD == 1) L.issue_n<NT1>(gn + (size_t)kof(1) * (64 * F_ * 2), lds + SB, lw);
                L.issue_m<SC1>(gm + (size_t)kof(2) * (64 * D_ * 2), lds + 2 * SB, lw);
                L.issue_n<NT1>(gn + (size_t)kof(2) * (64 * F_ * 2), lds + 2 * SB, lw);
                if (ORD == 1) WAIT_VM(22); else WAIT_VM(15);
                BARRIER();
                WAIT_VM(11);
            }
            int slot = 0, t = 0;
            auto issue = [&]() {
                const int k = kof(t + 3);
                L.issue_m<SC1>(gm + (size_t)k * (64 * D_ * 2), lds + slot * SB, lw);
                L.issue_n<NT1>(gn + (size_t)k * (64 * F_ * 2), lds + slot * SB, lw);
                slot = (slot == 2) ? 0 : slot + 1;
            };
            if (lw == 0) poll_flags(P.flags(), lane < 16 ? FL_L + ((pg + 1) & 7) * 16 + lane : -1, tag);
            BARRIER(); issue(); ++t;
            WAIT_VM(L.NPW); BARRIER(); issue(); ++t;
            WAIT_VM(L.NPW);
            if (lw == 0) { poll_flags(P.flags(), FL_L + lane, tag); poll_flags(P.flags(), FL_L + 64 + lane, tag); }
            BARRIER(); issue(); ++t;
            swarm(0); swarm(1);
#pragma unroll 1
            for (; t < NK - 3; ++t) { WAIT_VM(L.NPW); BARRIER(); issue(); swarm(t - 1); }
            asm volatile("s_waitcnt lgkmcnt(0)" : "+s"(ssink));
#define PF3 2
            auto warm = [&](int r) {
                const int n = ((r * 16 + i16) * 4 + lw) * 7 + (lane >> 3), row = n * 2 + (grp & 1);
                int f = row + 896 * (grp >> 1); f = f >= F_ ? f - F_ : f;
                if (r < PF3 && lane < 56) glds4((const char*)P.w2 + (size_t)f * (S_ * 2) + (grp >> 1) * 1024 + (lane & 7) * 128, lds + LDS_SINK);
            };
            warm(0);
            WAIT_VM(L.NPW + (PF3 > 0)); BARRIER();
            WAIT_VM((PF3 > 0) + 0); BARRIER();
            BARRIER();
            WAIT_VM(0); warm(1);
            WAIT_VM(0); warm(2);
            WAIT_VM(0); warm(3);
        } else {
            const int fr = lane & 15, fq = lane >> 4;
            const int fb = tn * 224 + ((tn & 1) ? 32 : 0) + wn * 32, fh = tn * 224 + ((tn & 1) ? 0 : 192) + wn * 16;
            WAIT_VM(0);
            WAIT_LGKM0();
            BARRIER();
            f32x4 acc[4][7];
#pragma unroll
            for (int j = 0; j < 7; ++j) {
                const u32x2 bb = *(const u32x2*)(P.b1 + (j < 6 ? fb + (j >> 1) * 64 + (j & 1) * 16 : fh) + fq * 4);
                const f32x4 bv = {lo16(bb[0]), hi16(bb[0]), lo16(bb[1]), hi16(bb[1])};
#pragma unroll
                for (int i = 0; i < 4; ++i) acc[i][j] = bv;
            }
            Reader<TR, 128, TR, 224, 4, 7> rd; rd.init(ring0, lane, wm, wn);
            Regs<4, 7> R0;
            int slot = 0;
            BARRIER();
            __builtin_amdgcn_s_waitcnt(0x0070);
            BARRIER();
            STAMP(1, 1);
            rd.read(R0, 0);
            for (int t = 0; t < NK / 4; ++t) {
                stage_step<3, SB>(slot, rd, rd, R0, acc); stage_step<3, SB>(slot, rd, rd, R0, acc);
                stage_step<3, SB>(slot, rd, rd, R0, acc); stage_step<3, SB>(slot, rd, rd, R0, acc);
            }
            STAMP(1, 2);
            const __amdgpu_buffer_rsrc_t rs = rsrc(P.U(), (unsigned)D_ * F_ * 2);
            const unsigned df = (unsigned)(tm * 8 + wm * 4);
            const unsigned ob = (unsigned)((fb >> 5) * 64 + df) * 1024u + (unsigned)((((fq & 1) * 2 + (fq >> 1)) * 16 + fr) * 16);
            const unsigned oh = (unsigned)((fh >> 5) * 64 + df + (fq & 1)) * 1024u + (unsigned)(((wn * 2 + (fq >> 1)) * 16 + fr) * 16);
            unsigned pk[4][7][2];
#pragma unroll
            for (int i = 0; i < 4; ++i) {
#pragma unroll
                for (int j = 0; j < 7; ++j) {
                    const f32x2 a = gelu2((f32x2){acc[i][j][0], acc[i][j][1]}), b = gelu2((f32x2){acc[i][j][2], acc[i][j][3]});
                    pk[i][j][0] = pack2(a.x, a.y); pk[i][j][1] = pack2(b.x, b.y);
                }
#pragma unroll
                for (int jp = 0; jp < 3; ++jp)
                    __builtin_amdgcn_raw_buffer_store_b128(pair16(pk[i][2 * jp][0], pk[i][2 * jp][1], pk[i][2 * jp + 1][0], pk[i][2 * jp + 1][1]), rs, ob + i * 1024 + jp * 2 * 65536, 0, SC1);
                if (i & 1)
                    __builtin_amdgcn_raw_buffer_store_b128(pair16(pk[i - 1][6][0], pk[i - 1][6][1], pk[i][6][0], pk[i][6][1]), rs, oh + (i - 1) * 1024, 0, SC1);
                SCHED0();
            }
            STAMP(1, 3);
        }
    } else {
        constexpr int NK = D_ / 64, SB = SB_Q;
        const int tm = fm;
        if (loader) {
            Loader<4, 6> L; L.init_m<ROW>(lw, lane, D_ * 2);
            L.init_n<TR>(lw, lane, 3 * D_ * 2, qkv_head(fn, 0) * 64, qkv_head(fn, 1) * 64, qkv_head(fn, 2) * 64);
            const char* gm = (const char*)P.x + (size_t)tm * 128 * D_ * 2; const char* gn = (const char*)P.wqkv;
            int slot = 0;
            auto issue = [&](int u) {
                const int k = (korder(u & (NK - 1), (tm + fn) & 3, NK) + ROTQ * (grp >> 1)) & (NK - 1);
                L.issue_m<0>(gm + k * 128, lds + slot * SB, lw);
                L.issue_n<NTQ>(gn + (size_t)k * (64 * 3 * D_ * 2) + (u >= NK ? 16 * 64 : 0), lds + slot * SB, lw);
                slot = (slot == 2) ? 0 : slot + 1;
            };
            issue(0); issue(1);
            WAIT_VM(L.NPW); BARRIER();
            issue(2);
            int t = 0;
            WAIT_VM(L.NPW); BARRIER(); issue(3); ++t;
#pragma unroll 1
            for (int seg = 0; seg < 2; ++seg) {
                const int end = seg == 0 ? 12 : 2 * NK - 3;
#pragma unroll 1
                for (; t < end; ++t) { WAIT_VM(L.NPW); BARRIER(); issue(t + 3); swarm(t - 1); }
                asm volatile("s_waitcnt lgkmcnt(0)" : "+s"(ssink));
                if (seg == 0 && lw == 0) {
                    poll_flags(P.flags(), lane < 8 ? FL_L + tm * 8 + lane : -1, tag);
                    glds16<SC1>((const char*)P.stat() + tm * 1024 + lane * 16, lds + LDS_QX);
                }
            }
            poll_flags(P.flags(), attn_dep(hd, qb, lane), tag);
            WAIT_VM(0);
            BARRIER();
            attn_load<4, 1>(P, lds + LDS_AK, hd, qb, lw, lane);
            attn_load<4, 2>(P, lds + LDS_AV, hd, qb, lw, lane);
            BARRIER();
            attn_load<4, 0>(P, lds + LDS_AQ, hd, qb, lw, lane);
            BARRIER();
        } else {
            const int fr = lane & 15, fq = lane >> 4;
            Reader<ROW, 128, TR, 192, 4, 6> rd; rd.init(ring0, lane, wm, wn);
            rd.n.b = rd.n.a + 32 - wm * 32; rd.n.a += wm * 32;
            Regs<4, 6> R0;
            int slot = 0;
            __builtin_amdgcn_s_waitcnt(0x0070);
            BARRIER();
            STAMP(1, 1);
            rd.read(R0, 0);
            const __amdgpu_buffer_rsrc_t rs = rsrc(P.qkv(), (unsigned)S_ * 3 * D_ * 2);
#pragma unroll 1
            for (int rnd = 0; rnd < 2; ++rnd) {
                f32x4 acc[5][6] = {};
                for (int t = 0; t < NK / 4; ++t) {
                    if (rnd == 1 && t == 1) WAIT_VM(0);
                    if (rnd == 1 && t == 2 && tid == 0) set_flag(P.flags(), FL_Q + tm * 32 + fn, tag);
                    stage_step<3, SB>(slot, rd, rd, R0, acc); stage_step<3, SB>(slot, rd, rd, R0, acc);
                    stage_step<3, SB>(slot, rd, rd, R0, acc); stage_step<3, SB>(slot, rd, rd, R0, acc);
                }
                STAMP(1, 2);
                LAS char* ex = lds + LDS_QX + 1024 + rnd * 1024;
                if (fr == 0) {
#pragma unroll
                    for (int jp = 0; jp < 3; ++jp) *(LAS f32x4*)(ex + wid * 256 + jp * 64 + fq * 16) = acc[4][2 * jp];
                }
                WAIT_LGKM0();
                CFENCE();
                __hip_atomic_store((LAS unsigned*)(lds + LDS_CTL + wid * 4), (unsigned)(rnd + 1), __ATOMIC_RELAXED, __HIP_MEMORY_SCOPE_WORKGROUP);
                const int s0 = tm * 128 + wm * 64;
                f32x2 st[4];
#pragma unroll
                for (int i = 0; i < 4; ++i) st[i] = *(LAS f32x2*)(lds + LDS_QX + (wm * 64 + i * 16 + fr) * 8);
                while (__builtin_amdgcn_readfirstlane(__hip_atomic_load((LAS unsigned*)(lds + LDS_CTL + (wid ^ 2) * 4), __ATOMIC_RELAXED, __HIP_MEMORY_SCOPE_WORKGROUP)) <= rnd) {}
                CFENCE();
#pragma unroll
                for (int jp = 0; jp < 3; ++jp) {
                    const int gh = qkv_head(fn, jp) + rnd * 16 + wn;
                    const float sc = jp == 0 ? 0.17677669529663687f * 1.4426950408889634f : 1.0f;
                    const f32x4 ca = acc[4][2 * jp], cb = *(LAS f32x4*)(ex + (wid ^ 2) * 256 + jp * 64 + fq * 16);
#pragma unroll
                    for (int i = 0; i < 4; ++i) {
                        const float r = st[i].y * sc, mr = -st[i].x * r;
                        const f32x4 a = acc[i][2 * jp] * r + ca * mr, b = acc[i][2 * jp + 1] * r + cb * mr;
                        __builtin_amdgcn_raw_buffer_store_b128(pair16(pack2(a[0], a[1]), pack2(a[2], a[3]), pack2(b[0], b[1]), pack2(b[2], b[3])), rs,
                                                               (unsigned)(((gh * S_ + s0 + i * 16 + fr) * 32 + ((fq & 1) ^ wm) * 16 + (fq >> 1) * 8) * 2), 0, SC1);
                    }
                }
            }
            STAMP(1, 3);
        }
    }

    constexpr int SB = SB_F, NS = NS_F;
    constexpr size_t OS = 2 * (S_ / 16) * 1024, WOS = 64 * D_ * 2, W2S = 64 * S_ * 2, US = 2 * 65536;
    const int tile = fm * 8 + fn, cls = (fm + fn) & 3, nf = isM ? NF_M : NF_Q;
    auto fof = [&](int i) {
        int r = korder(i - 7, cls, nf - 7) + (ROTF ? (grp & 1) * ((nf - 7) >> 1) : 0);
        r = r >= nf - 7 ? r - (nf - 7) : r;
        const int j = i < 7 ? (isM ? 0 : 7) + korder(i, cls, 7) : (isM ? 14 + NA : 14) + r, k = 14 * (grp >> 1) + j;
        return k >= 56 ? k - 56 : k;
    };
    const char* g2 = (const char*)P.w2 + fm * 256;
    Loader<4, 4> L;
    if (loader) {
        L.init_m<TR>(lw, lane, S_ * 2);
        WAIT_VM(0);
        __builtin_amdgcn_s_waitcnt(0x0F70);
    }
    auto preload_w2 = [&]() {
#pragma unroll
        for (int u = 0; u < 2; ++u)
#pragma unroll
            for (int i = 0; i < 4; ++i) glds16_hidden_plain(g2 + fof(u) * W2S + L.om[i], lds + u * SB + (lw + 4 * i) * PIECE);
    };
    if (isM) {
        if (loader) preload_w2();
        else {
            WAIT_VM(0);
            if (lane == 0 && __hip_atomic_fetch_add((LAS unsigned*)(lds + LDS_MC), 1u, __ATOMIC_RELAXED, __HIP_MEMORY_SCOPE_WORKGROUP) == 3u)
                set_flag(P.flags(), FL_U + um * 32 + un, tag);
        }
        STAMP(2, 0);
    } else {
        if (loader) __builtin_amdgcn_s_setprio(1);
        WAIT_LGKM0();
        BARRIER();
#pragma unroll 1
        for (int it = 0; it < 2; ++it) {
            STAMP(2, 3 * it);
            f32x16 sc[8];
            int ln = lane;
            asm volatile("" : "+v"(ln));
            const int dg = (ln & 31) - 4 * (ln >> 5);
            const unsigned up = dg < 0 ? ~0u : ~1u << dg;
            const int nbad = qb == 0 ? 8 - wid : 0;
            SCHED0();
            const float m = attn_scores(lds + (it ? LDS_BK : LDS_AK), lds + LDS_AQ, nbad, wid, ln, up, sc);
            SCHED0();
            if (it == 0 || loader) WAIT_VM(0);
            BARRIER();
            if (it == 0) { if (tid == 0) set_flag(P.flags(), FL_Q + fm * 32 + fn + 8, tag); }
            else if (loader) preload_w2();
            STAMP(2, 3 * it + 1);
            PvState S;
            S.l0 = (f32x2){0.f, 0.f}; S.l1 = S.l0; S.o = (f32x16){};
            attn_pv<0, 8>(lds + (it ? LDS_BV : LDS_AV), nbad, wid, ln, up, sc, m, S);
            attn_out(P, hd + 16 * it, qb, wid, ln, S.l0 + S.l1, S.o);
            STAMP(2, 3 * it + 2);
            if (it == 0) {
                if (loader) {
                    poll_flags(P.flags(), attn_dep(hd + 16, qb, ln), tag);
                    attn_load<4, 0>(P, lds + LDS_AQ, hd + 16, qb, lw, ln);
                    attn_load<4, 1>(P, lds + LDS_BK, hd + 16, qb, lw, ln);
                    attn_load<4, 2>(P, lds + LDS_BV, hd + 16, qb, lw, ln);
                    WAIT_VM(8);
                }
                BARRIER();
            }
        }
        WAIT_LGKM0();
    }
    if (loader) {
        const char* go = (const char*)P.o() + (isM ? KW * OS : 0) + fm * 8192;
        const char* gw = (const char*)P.wo + (isM ? KW * WOS : 0) + fn * 256;
        const char* gu = (const char*)P.U() + fn * 8192;
        __builtin_amdgcn_s_setprio(0);
        L.init_n<FRAG>(lw, lane, US / 2);
        auto issue_f = [&](int u) {
            const int k = fof(u);
            L.issue_m<NT2>(g2 + k * W2S, lds + (u & (NS - 1)) * SB, lw); L.issue_n<SC1>(gu + k * US, lds + (u & (NS - 1)) * SB, lw);
        };
        poll_flags(P.flags(), lane < 2 ? FL_U + fn * 32 + (grp >> 1) * 4 + (isM ? 0 : 2) + lane : -1, tag);
#pragma unroll
        for (int u = 0; u < 2; ++u) L.issue_n<SC1>(gu + fof(u) * US, lds + u * SB, lw);
        WAIT_VM(4); BARRIER();
        issue_f(2); issue_f(3);
        WAIT_VM(16); BARRIER(); issue_f(4);
        WAIT_VM(16); BARRIER(); issue_f(5);
        WAIT_VM(16); BARRIER(); issue_f(6);
        const int udep = lane < 16 ? FL_U + fn * 32 + lane : -1;
        int nxt = 7;
        bool uready = false;
#pragma unroll 1
        for (int t = 3; t < 7; ++t) {
            if (t == 6 && !uready) { poll_flags(P.flags(), udep, tag); uready = true; for (; nxt < 10; ++nxt) issue_f(nxt); }
            if (nxt - (t + 2) >= 2) WAIT_VM(16); else WAIT_VM(0);
            BARRIER();
            if (!uready) uready = flags_set(P.flags(), udep, tag);
            if (uready) for (; nxt <= t + NS; ++nxt) issue_f(nxt);
        }
#pragma unroll 1
        for (int u = 7 + NS; u < nf; ++u) { WAIT_VM(16); BARRIER(); issue_f(u); }
        L.init_m<FRAG>(lw, lane, OS / 2); L.init_n<TR>(lw, lane, D_ * 2);
        if (lw == 0) poll_flags(P.flags(), lane < 16 ? FL_O + (fm >> 1) * 32 + (isM ? 16 : 0) + lane : -1, tag);
#pragma unroll 1
        for (int i = 0; i < KW; ++i) {
            WAIT_VM(16); BARRIER();
            const int k = (korder(i, cls, KW) + ROTW * (grp >> 1)) & (KW - 1);
            LAS char* dst = lds + ((nf + i) & (NS - 1)) * SB;
            L.issue_m<SC1>(go + k * OS, dst, lw); L.issue_n<NT2>(gw + k * WOS, dst, lw);
        }
        WAIT_VM(16); BARRIER();
        WAIT_VM(8);
        if (isM && lw == 0) poll_flags(P.flags(), lane == 0 ? FL_P + tile * 32 : -1, tag);
        BARRIER();
        WAIT_VM(0); BARRIER();
        BARRIER();
        if (!isM) BARRIER();
        if (blockIdx.x == 0 && lw == 0) {
#pragma unroll 1
            for (int i = 0; i < 4; ++i) poll_flags(P.flags(), FL_O + i * 64 + lane, tag);
            if (lane == 0) set_flag(P.flags(), FL_E, tag);
        }
    } else {
        const int fr = lane & 15, fq = lane >> 4;
        const int s0 = fm * 128 + wm * 64, d0 = fn * 128 + wn * 64;
        const __amdgpu_buffer_rsrc_t ro = rsrc(P.out, (unsigned)S_ * D_ * 2);
        f32x4 acc[4][4] = {};
        Reader<TR, 128, FRAG, 128, 4, 4> rf;
        Reader<FRAG, 128, TR, 128, 4, 4> rw;
        Regs<4, 4> R0;
        int slot = 0;
#define STEP(a, b) stage_step<NS, SB>(slot, a, b, R0, acc)
        rf.init(ring0, lane, wm, wn);
        BARRIER();
        STAMP(3, 1);
        rf.read(R0, 0);
        for (int t = 0; t < (nf - 1) / 4; ++t) {
            if (t == 1) WAIT_VM(0);
            if (t == 2 && !isM && tid == 0) { set_flag(P.flags(), FL_O + qb * 32 + hd, tag); set_flag(P.flags(), FL_O + qb * 32 + hd + 16, tag); }
            STEP(rf, rf); STEP(rf, rf); STEP(rf, rf); STEP(rf, rf);
        }
        for (int t = 0; t < ((nf - 1) & 3); ++t) STEP(rf, rf);
        rw.init(ring0 + slot * SB, lane, wm, wn);
        STEP(rf, rw);
        STAMP(3, 4);
        for (int t = 0; t < 2; ++t) { STEP(rw, rw); STEP(rw, rw); STEP(rw, rw); }
        const unsigned eo = (unsigned)(((s0 + fr) * D_ + d0 + (fq & 1) * 16 + (fq >> 1) * 8) * 2);
        u32x4 ex[4][2];
        unsigned bv[4] = {0u, 0u, 0u, 0u};
        if (isM) {
#pragma unroll
            for (int i = 0; i < 4; ++i)
#pragma unroll
                for (int jp = 0; jp < 2; ++jp) ex[i][jp] = __builtin_amdgcn_raw_buffer_load_b128(ro, eo + i * (16 * D_ * 2) + jp * 64, 0, SC1);
        } else {
#pragma unroll
            for (int i = 0; i < 4; ++i) {
#pragma unroll
                for (int jp = 0; jp < 2; ++jp) ex[i][jp] = *(const u32x4*)((const char*)P.x + eo + i * (16 * D_ * 2) + jp * 64);
                bv[i] = P.b2[s0 + i * 16 + fr];
            }
        }
        SCHED0();
        STEP(rw, rw); STEP(rw, rw);
        STAMP(3, 2);
#pragma unroll
        for (int i = 0; i < 4; ++i)
#pragma unroll
            for (int jp = 0; jp < 2; ++jp) {
                auto e0 = __builtin_amdgcn_permlane16_swap(ex[i][jp][0], ex[i][jp][2], false, false);
                auto e1 = __builtin_amdgcn_permlane16_swap(ex[i][jp][1], ex[i][jp][3], false, false);
                const f32x4 a = acc[i][2 * jp] + (f32x4){lo16(e0[0]), hi16(e0[0]), lo16(e1[0]), hi16(e1[0])} + lo16(bv[i]);
                const f32x4 b = acc[i][2 * jp + 1] + (f32x4){lo16(e0[1]), hi16(e0[1]), lo16(e1[1]), hi16(e1[1])} + lo16(bv[i]);
                __builtin_amdgcn_raw_buffer_store_b128(pair16(pack2(a[0], a[1]), pack2(a[2], a[3]), pack2(b[0], b[1]), pack2(b[2], b[3])), ro, eo + i * (16 * D_ * 2) + jp * 64, 0, SC1);
            }
        if (!isM) {
            WAIT_VM(0);
            BARRIER();
            if (tid == 0) set_flag(P.flags(), FL_P + tile * 32, tag);
        }
        STAMP(3, 3);
#undef STEP
    }
    STAMP_OUT();
}

extern "C" void kernel_launch(void* const* d_in, const int* in_sizes, int n_in, void* d_out, int out_size, void* d_ws, size_t ws_size, hipStream_t stream) {
    char* ws = (char*)d_ws;
    Params P;
    P.x = (const u16*)d_in[0]; P.wqkv = (const u16*)d_in[1]; P.wo = (const u16*)d_in[2];
    P.w1 = (const u16*)d_in[4]; P.b1 = (const u16*)d_in[5]; P.w2 = (const u16*)d_in[6]; P.b2 = (const u16*)d_in[7];
    P.out = (u16*)d_out; P.ws = ws;
    (void)hipFuncSetAttribute((const void*)k_main, hipFuncAttributeMaxDynamicSharedMemorySize, LDS_BYTES);
    k_main<<<256, 512, LDS_BYTES, stream>>>(P);
}
```

```cpp
#include <hip/hip_runtime.h>
#include <hip/hip_bf16.h>
#include <stdint.h>
#define MORD 1

#define LAS __attribute__((address_space(3)))
#define GAS __attribute__((address_space(1)))
typedef __bf16 bf16x8 __attribute__((ext_vector_type(8)));
typedef __bf16 bf16x4 __attribute__((ext_vector_type(4)));
typedef __bf16 bf16x2 __attribute__((ext_vector_type(2)));
typedef float f32x2 __attribute__((ext_vector_type(2)));
typedef float f32x4 __attribute__((ext_vector_type(4)));
typedef float f32x16 __attribute__((ext_vector_type(16)));
typedef unsigned u32x4 __attribute__((ext_vector_type(4)));
typedef unsigned u32x2 __attribute__((ext_vector_type(2)));
typedef unsigned short u16;
typedef unsigned long long u64;

constexpr int S_ = 2048, D_ = 1024, F_ = 3584, NH = 32;

constexpr size_t WS_FLAGS = 0;
constexpr size_t WS_STAT  = 1 << 16;
constexpr size_t WS_CS    = WS_STAT + (size_t)S_ * 8;
constexpr size_t WS_H     = 1 << 17;
constexpr size_t WS_QKV   = WS_H   + (size_t)S_ * D_ * 2;
constexpr size_t WS_U     = WS_QKV + (size_t)S_ * 3 * D_ * 2;
constexpr size_t WS_O     = WS_U   + (size_t)D_ * F_ * 2;
constexpr size_t WS_PART  = WS_O   + (size_t)S_ * D_ * 2;
constexpr int FL_E = 0;
constexpr int FL_L = 32;
constexpr int FL_C = 5 * 32;
constexpr int FL_U = 8 * 32;
constexpr int FL_Q = 16 * 32;
constexpr int FL_O = 32 * 32;
constexpr int FL_P = 40 * 32;

constexpr size_t WS_DBG = 200u << 20;
#define STAMP_DECL
#define STAMP(kid, i) do {} while (0)
#define STAMPL(kid, i) do {} while (0)
#define STAMP_OUT() do {} while (0)
#define WAIT_VM(n) asm volatile("s_waitcnt vmcnt(%0)" ::"n"(n) : "memory")
#define WAIT_LGKM0() __builtin_amdgcn_s_waitcnt(0xC07F)
#define CFENCE() asm volatile("" ::: "memory")
#define BARRIER() do { CFENCE(); __builtin_amdgcn_s_barrier(); CFENCE(); } while (0)
#define SCHED0() __builtin_amdgcn_sched_barrier(0)
#define RLX_AGENT __ATOMIC_RELAXED, __HIP_MEMORY_SCOPE_AGENT
constexpr int SC1 = 16;

__device__ __forceinline__ float bf2f(u16 v) { return __uint_as_float((unsigned)v << 16); }
__device__ __forceinline__ float lo16(unsigned v) { return __uint_as_float(v << 16); }
__device__ __forceinline__ float hi16(unsigned v) { return __uint_as_float(v & 0xffff0000u); }
__device__ __forceinline__ unsigned pack2(float lo, float hi) {
    f32x2 v = {lo, hi};
    bf16x2 b = __builtin_convertvector(v, bf16x2);
    return __builtin_bit_cast(unsigned, b);
}
__device__ __forceinline__ float rbf(float x) { return __uint_as_float(pack2(x, 0.f) << 16); }
template <int CTRL, int RMASK> __device__ __forceinline__ float dpp_add(float v) {
    return v + __builtin_bit_cast(float, __builtin_amdgcn_update_dpp(0, __builtin_bit_cast(int, v), CTRL, RMASK, 0xf, false));
}
__device__ __forceinline__ float wave_sum(float v) {
    v = dpp_add<0x111, 0xf>(v); v = dpp_add<0x112, 0xf>(v); v = dpp_add<0x114, 0xf>(v); v = dpp_add<0x118, 0xf>(v);
    v = dpp_add<0x142, 0xa>(v); v = dpp_add<0x143, 0xc>(v);
    return __builtin_bit_cast(float, __builtin_amdgcn_readlane(__builtin_bit_cast(int, v), 63));
}
__device__ __forceinline__ __amdgpu_buffer_rsrc_t rsrc(const void* p, unsigned bytes) { return __builtin_amdgcn_make_buffer_rsrc((void*)p, 0, bytes, 0x00020000); }

enum { ROW = 0, TR = 1, FRAG = 2 };
constexpr int PIECE = 1056, BLK = 8 * PIECE;
template <int T, int X> struct Op {
    static constexpr int BYTES = X * 132;
    __device__ static __forceinline__ unsigned src_off(int i, int lane, unsigned ld, unsigned cb) {
        const int blk = i >> 3, p = i & 7, pos = lane >> 3;
        if (T == TR) {
            if (X == 224 && blk == 3) {
                const int ps = lane >> 2, g = (p & 1) | ((ps >> 3) << 1), r = 32 * (p >> 1) + 8 * g + 4 * ((ps >> 2) & 1) + (ps & 3);
                return (unsigned)r * ld + cb + (unsigned)((lane & 3) * 16);
            }
            const int g = (p >> 2) | (pos & 2), r = 32 * (pos >> 2) + 8 * g + 4 * (pos & 1) + (p & 3);
            return (unsigned)r * ld + cb + (unsigned)((lane & 7) * 16);
        } else if (T == ROW) {
            const int j = ((pos >> 1) & 1) | ((pos & 1) << 1) | (pos & 4);
            return (unsigned)(blk * 64 + j * 8 + p) * ld + (unsigned)((lane & 7) * 16);
        } else {
            return (unsigned)blk * ld + (unsigned)(p * 1024 + lane * 16);
        }
    }
};
template <int AUX> __device__ __forceinline__ void glds16(const char* g, LAS char* l) { __builtin_amdgcn_global_load_lds((const GAS unsigned*)g, (LAS unsigned*)l, 16, 0, AUX); }
__device__ __forceinline__ void glds16_hidden_plain(const char* g, LAS char* l) {
    unsigned keep;
    asm volatile("s_mov_b32 %0, m0\n\ts_mov_b32 m0, %2\n\ts_nop 0\n\tglobal_load_lds_dwordx4 %1, off\n\ts_mov_b32 m0, %0"
                 : "=&s"(keep) : "v"(g), "s"(__builtin_amdgcn_readfirstlane((unsigned)(uintptr_t)l)) : "memory");
}
__device__ __forceinline__ void glds16_hidden(const char* g, LAS char* l) {
    unsigned keep;
    asm volatile("s_mov_b32 %0, m0\n\ts_mov_b32 m0, %2\n\ts_nop 0\n\tglobal_load_lds_dwordx4 %1, off sc1\n\ts_mov_b32 m0, %0"
                 : "=&s"(keep) : "v"(g), "s"(__builtin_amdgcn_readfirstlane((unsigned)(uintptr_t)l)) : "memory");
}
__device__ __forceinline__ void glds4(const char* g, LAS char* l) { __builtin_amdgcn_global_load_lds((const GAS unsigned*)g, (LAS unsigned*)l, 4, 0, 0); }

template <int T, int X> struct Addr {
    unsigned a, b;
    static constexpr int NV = (X == 224 || X == 192) ? 2 : 1;
    __device__ __forceinline__ void init(unsigned base, int lane, int w) {
        const int g = lane >> 4;
        b = 0;
        if (T == TR) {
            const int q = (lane & 15) >> 2, p = lane & 3;
            a = base + (unsigned)((q | ((g & 1) << 2)) * PIECE + (g >> 1) * 256 + p * 8 + (X == 128 ? w * BLK : w * 64));
            b = base + (unsigned)(3 * BLK + (g & 1) * PIECE + (g >> 1) * 512 + q * 64 + w * 32 + p * 8);
        } else if (T == ROW) {
            const int r = lane & 15;
            a = base + (unsigned)(w * BLK + (r & 7) * PIECE + (r >> 3) * 256 + g * 16);
        } else {
            a = base + (unsigned)(w * 4 * PIECE + lane * 16);
        }
    }
    __device__ __forceinline__ void step(unsigned d) { a += d; if (NV == 2) b += d; }
    __device__ __forceinline__ bf16x8 read(int n, int kk) const {
        if (T == TR) {
            LAS char* p; int t;
            if (X == 224 && n == 6) { p = (LAS char*)(uintptr_t)b + kk * 2 * PIECE; t = 256; }
            else if (X == 192) { p = (LAS char*)(uintptr_t)((n & 1) ? b : a) + kk * 512 + (n >> 1) * BLK; t = 128; }
            else { p = (LAS char*)(uintptr_t)a + kk * 512 + (X == 128 ? n * 32 : (n >> 1) * BLK + (n & 1) * 32); t = 128; }
            const bf16x4 lo = __builtin_amdgcn_ds_read_tr16_b64_v4bf16((LAS bf16x4*)p);
            const bf16x4 hi = __builtin_amdgcn_ds_read_tr16_b64_v4bf16((LAS bf16x4*)(p + t));
            return __builtin_shufflevector(lo, hi, 0, 1, 2, 3, 4, 5, 6, 7);
        } else if (T == ROW) {
            return *(LAS bf16x8*)((LAS char*)(uintptr_t)a + (n & 1) * 128 + (n >> 1) * 512 + kk * 64);
        } else {
            return *(LAS bf16x8*)((LAS char*)(uintptr_t)a + kk * BLK + n * PIECE);
        }
    }
};
template <int FM, int FN> struct Regs { bf16x8 m[FM], n[FN]; };
#define MFMA16(a, b, c) __builtin_amdgcn_mfma_f32_16x16x32_bf16(a, b, c, 0, 0, 0)

template <int TM, int BM, int TN, int BN, int FM, int FN> struct Reader {
    Addr<TM, BM> m; Addr<TN, BN> n;
    static constexpr int NDS = FM * (TM == TR ? 2 : 1) + FN * (TN == TR ? 2 : 1), NV = Addr<TM, BM>::NV + Addr<TN, BN>::NV;
    __device__ __forceinline__ void init(unsigned slot_base, int lane, int wm, int wn) { m.init(slot_base, lane, wm); n.init(slot_base + BM * 132, lane, wn); }
    __device__ __forceinline__ void read(Regs<FM, FN>& R, int kk) const {
#pragma unroll
        for (int i = 0; i < FM; ++i) R.m[i] = m.read(i, kk);
#pragma unroll
        for (int j = 0; j < FN; ++j) R.n[j] = n.read(j, kk);
    }
    __device__ __forceinline__ void step(unsigned d) { m.step(d); n.step(d); }
};
template <int AM, int FM, int FN> __device__ __forceinline__ void mma(f32x4 (&acc)[AM][FN], const Regs<FM, FN>& R) {
    const u32x4 one = {0x3f803f80u, 0x3f803f80u, 0x3f803f80u, 0x3f803f80u};
#pragma unroll
    for (int j = 0; j < FN; ++j) {
        if (MORD == 1 && AM > FM && !(j & 1)) acc[AM - 1][j] = MFMA16(R.n[j], __builtin_bit_cast(bf16x8, one), acc[AM - 1][j]);
#pragma unroll
        for (int ii = 0; ii < FM; ++ii) { const int i = (MORD == 1 && (j & 1)) ? FM - 1 - ii : ii; acc[i][j] = MFMA16(R.n[j], R.m[i], acc[i][j]); }
        if (MORD == 0 && AM > FM && !(j & 1)) acc[AM - 1][j] = MFMA16(R.n[j], __builtin_bit_cast(bf16x8, one), acc[AM - 1][j]);
    }
}
template <int AM, int FM, int FN> __device__ __forceinline__ void pin_acc(f32x4 (&acc)[AM][FN]) {
#pragma unroll
    for (int j = 0; j < FN; ++j) {
        asm volatile("" : "+v"(acc[0][j]), "+v"(acc[1][j]), "+v"(acc[2][j]), "+v"(acc[3][j]));
        if (AM > FM && !(j & 1)) asm volatile("" : "+v"(acc[AM - 1][j]));
    }
}
template <int NDS, int NV, int NMF> __device__ __forceinline__ void interleave() {
#pragma unroll
    for (int i = 0; i < NMF; ++i) {
        __builtin_amdgcn_sched_group_barrier(0x8, 1, 0);
        if (i < NDS) __builtin_amdgcn_sched_group_barrier(0x80, 1, 0);
        if (i >= NMF - NV) __builtin_amdgcn_sched_group_barrier(0x2, 1, 0);
    }
}
template <int NS, int SB, class RD, class RDN, int AM, int FM, int FN>
__device__ __forceinline__ void stage_step(int& slot, RD& rd, RDN& rn, Regs<FM, FN>& R0, f32x4 (&acc)[AM][FN]) {
    constexpr int NMF = FM * FN + (AM - FM) * (FN / 2);
    Regs<FM, FN> R1;
    const unsigned d = (slot + 1 == NS) ? (unsigned)(-(NS - 1) * SB) : (unsigned)SB;
    slot = (slot + 1 == NS) ? 0 : slot + 1;
    rd.read(R1, 1);
    rn.step(d);
    mma(acc, R0);
    interleave<RD::NDS, RDN::NV, NMF>();
    SCHED0();
    pin_acc<AM, FM, FN>(acc);
    WAIT_LGKM0();
    BARRIER();
    SCHED0();
    rn.read(R0, 0);
    mma(acc, R1);
    interleave<RDN::NDS, 0, NMF>();
    SCHED0();
    pin_acc<AM, FM, FN>(acc);
    SCHED0();
}

template <int NM, int NN> struct Loader {
    static constexpr int NPW = NM + NN;
    unsigned om[NM], on[NN];
    template <int T> __device__ __forceinline__ void init_m(int lw, int lane, unsigned ld) {
#pragma unroll
        for (int i = 0; i < NM; ++i) om[i] = Op<T, NM * 32>::src_off(lw + 4 * i, lane, ld, (i >> 1) * 128);
    }
    template <int T> __device__ __forceinline__ void init_n(int lw, int lane, unsigned ld, unsigned c0 = 0, unsigned c1 = 128, unsigned c2 = 256, unsigned c3 = 384) {
#pragma unroll
        for (int i = 0; i < NN; ++i) on[i] = Op<T, NN * 32>::src_off(lw + 4 * i, lane, ld, (i >> 1) == 0 ? c0 : (i >> 1) == 1 ? c1 : (i >> 1) == 2 ? c2 : c3);
    }
    template <int AUX> __device__ __forceinline__ void issue_m(const char* gm, LAS char* dst, int lw) const {
#pragma unroll
        for (int i = 0; i < NM; ++i) glds16<AUX>(gm + om[i], dst + (lw + 4 * i) * PIECE);
    }
    template <int AUX> __device__ __forceinline__ void issue_n(const char* gn, LAS char* dst, int lw) const {
#pragma unroll
        for (int i = 0; i < NN; ++i) glds16<AUX>(gn + on[i], dst + NM * 32 * 132 + (lw + 4 * i) * PIECE);
    }
};
#define LEAD 2
__device__ __forceinline__ int korder(int p, int c, int nk) {
    const int r = (p - c) & 3, p0 = p - r;
    if (r > LEAD || p0 < 0 || p0 + LEAD >= nk) return p;
    return r == 0 ? p + LEAD : p - 1;
}
__device__ __forceinline__ f32x2 gelu2(f32x2 u) {
    const float c1 = -2.0f * 0.7978845608028654f * 1.4426950408889634f, c2 = c1 * 0.044715f;
    const f32x2 z = u * ((u * u) * c2 + c1);
    f32x2 e; e.x = __builtin_amdgcn_exp2f(z.x); e.y = __builtin_amdgcn_exp2f(z.y);
    e = e + 1.0f;
    f32x2 r; r.x = __builtin_amdgcn_rcpf(e.x); r.y = __builtin_amdgcn_rcpf(e.y);
    return u * r;
}
__device__ __forceinline__ u32x4 pair16(unsigned x0, unsigned x1, unsigned y0, unsigned y1) {
    auto a = __builtin_amdgcn_permlane16_swap(x0, y0, false, false);
    auto b = __builtin_amdgcn_permlane16_swap(x1, y1, false, false);
    return (u32x4){a[0], b[0], a[1], b[1]};
}
__device__ __forceinline__ bool flags_set(u64* flags, int idx, u64 tag) {
    const u64 v = __hip_atomic_load((GAS u64*)flags + (idx < 0 ? 0 : idx), RLX_AGENT);
    const bool ok = __builtin_amdgcn_ballot_w64(idx >= 0 && v - 1ull != tag) == 0ull;
    CFENCE();
    return ok;
}
__device__ __forceinline__ void poll_flags(u64* flags, int idx, u64 tag) {
#pragma unroll 1
    for (unsigned spins = 0; spins < (1u << 20); ++spins) {
        if (flags_set(flags, idx, tag)) break;
        __builtin_amdgcn_s_sleep(2);
    }
}
__device__ __forceinline__ void peek_issue(u64& v, u64* flags, int idx) {
    asm volatile("global_load_dwordx2 %0, %1, off sc1" : "=v"(v) : "v"((GAS u64*)flags + (idx < 0 ? 0 : idx)) : "memory");
}
__device__ __forceinline__ bool peek_ok(u64& v, int idx, u64 tag) {
    asm volatile("" : "+v"(v) :: "memory");
    return __builtin_amdgcn_ballot_w64(idx >= 0 && v - 1ull != tag) == 0ull;
}
__device__ __forceinline__ void set_flag(u64* flags, int idx, u64 tag) {
    asm volatile("" : "+v"(tag));
    __hip_atomic_store((GAS u64*)flags + idx, tag + 1ull, RLX_AGENT);
}

struct Params {
    const u16 *x, *wqkv, *wo, *w1, *b1, *w2, *b2;
    u16* out;
    char* ws;
    __device__ __forceinline__ u16* h() const { return out; }
    __device__ __forceinline__ u16* qkv() const { return (u16*)(ws + WS_QKV); }
    __device__ __forceinline__ u16* U() const { return (u16*)(ws + WS_U); }
    __device__ __forceinline__ u16* o() const { return (u16*)(ws + WS_QKV); }
    __device__ __forceinline__ float* stat() const { return (float*)(ws + WS_STAT); }
    __device__ __forceinline__ u64* flags() const { return (u64*)(ws + WS_FLAGS); }
};

constexpr int SB_M = (128 + 224) * 132, SB_Q = (128 + 192) * 132, SB_F = 256 * 132, NS_F = 4;
constexpr int LDS_QX = 3 * SB_Q;
constexpr int LDS_CTL = LDS_QX + 3072;
constexpr int LDS_AQ = 0, LDS_AK = 2 * SB_Q, LDS_AV = 128 * 1024, LDS_BYTES = 160 * 1024;
constexpr int LDS_BK = 16 * 1024, LDS_BV = 96 * 1024;
constexpr int LDS_MC = LDS_BYTES - 512;
constexpr int LDS_X = 72 * 1024;
constexpr int LDS_EX = 136 * 1024;
constexpr int LDS_SINK = LDS_BYTES - 256;
static_assert(LDS_CTL + 32 <= LDS_AV && 3 * SB_M <= LDS_SINK && 3 * SB_F <= LDS_AV && NS_F * SB_F <= LDS_EX && LDS_EX + 16384 + 256 <= LDS_MC, "LDS map");
#define NA 19
#define NT1 0
#define NTQ 0
#define NT2 0
#define NTX 0
#define ROTQ 4
#define ROTF 1
#define ROTW 4
constexpr int KW = 16, NF_Q = 7 + NA, NF_M = 7 + 42 - NA;

#define SPF 0
__device__ __forceinline__ void scalar_warm(const char* p, unsigned& sink) {
    if (SPF == 4) asm volatile("s_load_dword %0, %1, 0x0\n\ts_load_dword %0, %1, 0x40\n\ts_load_dword %0, %1, 0x80\n\ts_load_dword %0, %1, 0xc0" : "+s"(sink) : "s"(p));
    if (SPF == 8) asm volatile("s_load_dword %0, %1, 0x0\n\ts_load_dword %0, %1, 0x40\n\ts_load_dword %0, %1, 0x80\n\ts_load_dword %0, %1, 0xc0\n\t"
                               "s_load_dword %0, %1, 0x100\n\ts_load_dword %0, %1, 0x140\n\ts_load_dword %0, %1, 0x180\n\ts_load_dword %0, %1, 0x1c0" : "+s"(sink) : "s"(p));
}
__device__ __forceinline__ int qkv_head(int tn, int blk) { return blk * NH + ((tn & ~3) | ((tn + blk) & 3)) * 2; }

__device__ __forceinline__ f32x2 ln_row(const u32x4 a, const u32x4 b, u32x4& oa, u32x4& ob) {
    f32x2 v[8];
#pragma unroll
    for (int i = 0; i < 4; ++i) { v[i] = (f32x2){lo16(a[i]), hi16(a[i])}; v[4 + i] = (f32x2){lo16(b[i]), hi16(b[i])}; }
    const f32x2 s2 = ((v[0] + v[1]) + (v[2] + v[3])) + ((v[4] + v[5]) + (v[6] + v[7]));
    const float mean = wave_sum(s2.x + s2.y) * (1.0f / D_);
    f32x2 q2 = {0.f, 0.f};
#pragma unroll
    for (int i = 0; i < 8; ++i) { const f32x2 d = v[i] - mean; q2 += d * d; }
    const float var = wave_sum(q2.x + q2.y) * (1.0f / D_);
    const float mb = rbf(mean), rs = __builtin_amdgcn_rcpf(rbf(__builtin_amdgcn_sqrtf(rbf(rbf(var) + 1e-5f))));
    const float mb2 = rbf((mean - mb) * rs), rs2 = __builtin_amdgcn_rcpf(rbf(__builtin_amdgcn_sqrtf(rbf(rbf(var * rs * rs) + 1e-5f))));
    const float g = rs * rs2, c = -mb2 * rs2;
#pragma unroll
    for (int i = 0; i < 8; ++i) {
        const f32x2 t = (v[i] - mb) * g + c;
        if (i < 4) oa[i] = pack2(t.x, t.y); else ob[i - 4] = pack2(t.x, t.y);
    }
    return (f32x2){mb, rs};
}

#define MFMA32(a, b, c) __builtin_amdgcn_mfma_f32_32x32x16_bf16(a, b, c, 0, 0, 0)
__device__ __forceinline__ int attn_dep(int hd, int qb, int lane) {
    const int which = lane >> 2, st = 2 * qb - 2 + (lane & 3), p = hd >> 1;
    if (lane >= 12 || st < 0 || (which == 0 && (lane & 3) < 2)) return -1;
    return FL_Q + st * 32 + ((p & ~3) | ((p - which) & 3));
}
__device__ __forceinline__ void glds16x4_hidden(const char* g, unsigned l) {
    unsigned keep;
    asm volatile("s_mov_b32 %0, m0\n\ts_mov_b32 m0, %2\n\ts_nop 0\n\tglobal_load_lds_dwordx4 %1, off sc1\n\tglobal_load_lds_dwordx4 %1, off offset:1024 sc1\n\t"
                 "global_load_lds_dwordx4 %1, off offset:2048 sc1\n\tglobal_load_lds_dwordx4 %1, off offset:3072 sc1\n\ts_mov_b32 m0, %0"
                 : "=&s"(keep) : "v"(g), "s"(__builtin_amdgcn_readfirstlane(l)) : "memory");
}
template <int WHICH> __device__ __forceinline__ void attn_load(const Params& P, unsigned dst, int hd, int qb, int w, int lane) {
    const int row = WHICH == 0 ? qb * 256 + w * 64 : qb * 256 - ((qb == 0 && w < 2) ? 0 : 256) + w * 128;
    const char* g = (const char*)(P.qkv() + ((size_t)(WHICH * NH + hd) * S_ + row) * 32) + (lane >> 2) * 64 + (WHICH == 2 ? lane & 3 : (lane & 3) ^ ((lane >> 4) & 3)) * 16;
    glds16x4_hidden(g, dst + w * (WHICH == 0 ? 4096 : 8192));
    if (WHICH != 0) glds16x4_hidden(g + 4096, dst + w * 8192 + 4096);
}
#define PIN(x) asm volatile("" : "+v"(x))
__device__ __forceinline__ float max16(const f32x16& v) {
    float a = __builtin_fmaxf(__builtin_fmaxf(v[0], v[1]), v[2]), b = __builtin_fmaxf(__builtin_fmaxf(v[3], v[4]), v[5]);
    a = __builtin_fmaxf(__builtin_fmaxf(a, v[6]), v[7]); b = __builtin_fmaxf(__builtin_fmaxf(b, v[8]), v[9]);
    a = __builtin_fmaxf(__builtin_fmaxf(a, v[10]), v[11]); b = __builtin_fmaxf(__builtin_fmaxf(b, v[12]), v[13]);
    a = __builtin_fmaxf(__builtin_fmaxf(a, v[14]), v[15]);
    return __builtin_fmaxf(a, b);
}
__device__ __forceinline__ float attn_scores(LAS char* buf, LAS char* qbuf, int nbad, int wid, int lane, unsigned up, f32x16 (&sc)[8]) {
    const int r32 = lane & 31, hh = lane >> 5, sw = (r32 >> 2) & 3;
    LAS char* k0p = buf + (wid * 32 + r32) * 64 + ((hh ^ sw) * 16);
    LAS char* k1p = buf + (wid * 32 + r32) * 64 + (((2 + hh) ^ sw) * 16);
    const bf16x8 qf0 = *(LAS bf16x8*)(qbuf + (wid * 32 + r32) * 64 + ((hh ^ sw) * 16));
    const bf16x8 qf1 = *(LAS bf16x8*)(qbuf + (wid * 32 + r32) * 64 + (((2 + hh) ^ sw) * 16));
    bf16x8 ka[9], kb[9];
#pragma unroll
    for (int i = 0; i < 3; ++i) { const int j = i == 0 ? 8 : i - 1; ka[i] = *(LAS bf16x8*)(k0p + j * 2048); kb[i] = *(LAS bf16x8*)(k1p + j * 2048); SCHED0(); }
    const float NEG = -INFINITY;
    const f32x16 zero = {};
    const float b0 = nbad > 0 ? NEG : 0.f;
    f32x16 s8;
    float m = NEG;
#pragma unroll
    for (int i = 0; i < 11; ++i) {
        if (i + 3 < 9) { ka[i + 3] = *(LAS bf16x8*)(k0p + (i + 2) * 2048); kb[i + 3] = *(LAS bf16x8*)(k1p + (i + 2) * 2048); }
        if (i < 9) {
            const f32x16 t = MFMA32(kb[i], qf1, MFMA32(ka[i], qf0, zero));
            if (i == 0) s8 = t; else sc[i - 1] = t;
        }
        SCHED0();
        const int j = i - 3;
        if (j == 0) {
#pragma unroll
            for (int r = 0; r < 16; ++r) {
                const unsigned mk = (unsigned)__builtin_amdgcn_sbfe((int)up, (r & 3) + 8 * (r >> 2), 1);
                sc[0][r] = __uint_as_float(__builtin_amdgcn_bitop3_b32(mk, __float_as_uint(sc[0][r] + b0), __float_as_uint(s8[r]), 0xca));
            }
        }
        if (j >= 0) { const float mj = max16(sc[j]); m = __builtin_fmaxf(m, (j > 0 && j < nbad) ? NEG : mj); PIN(m); }
        SCHED0();
    }
    auto rr = __builtin_amdgcn_permlane32_swap(__float_as_uint(m), __float_as_uint(m), false, false);
    return __builtin_fmaxf(__uint_as_float(rr[0]), __uint_as_float(rr[1]));
}
struct PvState { f32x2 l0, l1; f32x16 o; bf16x8 vf[2], v8[2]; u32x4 pw[2], p8[2]; };
__device__ __forceinline__ bf16x8 v_read(LAS char* vp, int j, int s) {
    const bf16x4 lo = __builtin_amdgcn_ds_read_tr16_b64_v4bf16((LAS bf16x4*)(vp + j * 2048 + s * 1024));
    const bf16x4 hi = __builtin_amdgcn_ds_read_tr16_b64_v4bf16((LAS bf16x4*)(vp + j * 2048 + s * 1024 + 512));
    return __builtin_shufflevector(lo, hi, 0, 1, 2, 3, 4, 5, 6, 7);
}
template <int J0, int J1> __device__ __forceinline__ void attn_pv(LAS char* buf, int nbad, int wid, int lane, unsigned up, f32x16 (&sc)[8], float m, PvState& S) {
    const int hh = lane >> 5;
    LAS char* vp = buf + (wid * 32 + 4 * hh + ((lane & 15) >> 2)) * 64 + ((lane >> 4) & 1) * 32 + (lane & 3) * 8;
#pragma unroll
    for (int j = J0; j < J1; ++j) {
        bf16x8 vn[2] = {v_read(vp, j, 0), v_read(vp, j, 1)};
        if (j == 0) { S.v8[0] = v_read(vp, 8, 0); S.v8[1] = v_read(vp, 8, 1); }
        const float mj = (j > 0 && j < nbad) ? INFINITY : m;
        f32x2 p[8];
#pragma unroll
        for (int i = 0; i < 8; ++i) p[i] = (f32x2){sc[j][2 * i], sc[j][2 * i + 1]} - mj;
        SCHED0();
#pragma unroll
        for (int g = 0; g < 4; ++g) {
#pragma unroll
            for (int i = 2 * g; i < 2 * g + 2; ++i) { p[i].x = __builtin_amdgcn_exp2f(p[i].x); p[i].y = __builtin_amdgcn_exp2f(p[i].y); PIN(p[i]); }
            SCHED0();
            if (j > 0 && (g & 1) == 0) S.o = MFMA32(S.vf[g >> 1], __builtin_bit_cast(bf16x8, S.pw[g >> 1]), S.o);
            if (j == 1 && (g & 1) == 1) S.o = MFMA32(S.v8[g >> 1], __builtin_bit_cast(bf16x8, S.p8[g >> 1]), S.o);
            SCHED0();
        }
#pragma unroll
        for (int i = 0; i < 8; ++i) {
            S.pw[i >> 2][i & 3] = pack2(p[i].x, p[i].y);
            if (i & 1) S.l1 += p[i]; else S.l0 += p[i];
        }
        if (j == 0) {
#pragma unroll
            for (int i = 0; i < 8; ++i) {
                const int kr = ((2 * i) & 3) + 8 * (i >> 1);
                const unsigned mk = __builtin_amdgcn_bitop3_b32(0xffffu, (unsigned)__builtin_amdgcn_sbfe((int)up, kr, 1), (unsigned)__builtin_amdgcn_sbfe((int)up, kr + 1, 1), 0xca);
                S.p8[i >> 2][i & 3] = S.pw[i >> 2][i & 3] & ~mk; S.pw[i >> 2][i & 3] &= mk;
            }
        }
        S.vf[0] = vn[0]; S.vf[1] = vn[1];
        PIN(S.l0); PIN(S.l1); PIN(S.pw[0]); PIN(S.pw[1]);
        SCHED0();
    }
    if (J1 == 8) {
        S.o = MFMA32(S.vf[0], __builtin_bit_cast(bf16x8, S.pw[0]), S.o);
        S.o = MFMA32(S.vf[1], __builtin_bit_cast(bf16x8, S.pw[1]), S.o);
    }
}
__device__ __forceinline__ void attn_out(const Params& P, int hd, int qb, int wid, int lane, f32x2 l2, const f32x16& oacc) {
    const int r32 = lane & 31, hh = lane >> 5;
    float l = l2.x + l2.y;
    {
        auto rr = __builtin_amdgcn_permlane32_swap(__float_as_uint(l), __float_as_uint(l), false, false);
        l = __uint_as_float(rr[0]) + __uint_as_float(rr[1]);
    }
    const float inv = __builtin_amdgcn_rcpf(l);
    unsigned w[8];
#pragma unroll
    for (int g = 0; g < 4; ++g) { w[2 * g] = pack2(oacc[4 * g] * inv, oacc[4 * g + 1] * inv); w[2 * g + 1] = pack2(oacc[4 * g + 2] * inv, oacc[4 * g + 3] * inv); }
    const __amdgpu_buffer_rsrc_t rs = rsrc(P.o(), (unsigned)S_ * D_ * 2);
    const unsigned off = (unsigned)((hd * (S_ / 16) + qb * 16 + wid * 2 + (r32 >> 4)) * 1024 + (hh * 16 + (r32 & 15)) * 16);
#pragma unroll
    for (int gp = 0; gp < 2; ++gp) {
        auto a = __builtin_amdgcn_permlane32_swap(w[4 * gp], w[4 * gp + 2], false, false);
        auto b = __builtin_amdgcn_permlane32_swap(w[4 * gp + 1], w[4 * gp + 3], false, false);
        __builtin_amdgcn_raw_buffer_store_b128((u32x4){a[0], b[0], a[1], b[1]}, rs, off + gp * 512, 0, SC1);
    }
}

extern "C" __global__ __launch_bounds__(512, 2) void k_main(const Params P) {
    extern __shared__ __attribute__((aligned(16))) char smem[];
    LAS char* lds = (LAS char*)smem;
    const int tid = threadIdx.x, lane = tid & 63, wid = __builtin_amdgcn_readfirstlane(tid >> 6);
    const int grp = blockIdx.x & 7, idx = blockIdx.x >> 3;
    const bool loader = wid >= 4;
    const int lw = wid - 4, wm = wid >> 1, wn = wid & 1;
    const unsigned ring0 = (unsigned)(uintptr_t)lds;
    const int i16 = idx & 15;
    const bool isM = idx < 16;
    const int fm = (grp >> 1) * 4 + (i16 >> 2), fn = (grp & 1) * 4 + (i16 & 3);
    const int um = (grp & 1) * 4 + (i16 >> 2), un = (grp >> 1) * 4 + (i16 & 3);
    const int hd = (grp & 1) * 8 + (i16 & 7), qb = (grp >> 1) * 2 + (i16 >> 3);
    STAMP_DECL;
    STAMP(1, 0);
#define PFC 1
    if (PFC && wid == 0) {
        u64 pc; unsigned rel, len;
        asm volatile("s_getpc_b64 %0\n1:\n\ts_mov_b32 %1, 1b-k_main\n\ts_mov_b32 %2, 2f-k_main\n\t.subsection 1\n2:\n\t.subsection 0" : "=s"(pc), "=s"(rel), "=s"(len));
        const unsigned off = (unsigned)((idx + 32 * (lane & 15)) * 128);
        unsigned keep;
        asm volatile("s_mov_b32 %0, m0\n\ts_mov_b32 m0, %2\n\ts_nop 0\n\tglobal_load_lds_dword %1, off\n\ts_mov_b32 m0, %0"
                     : "=&s"(keep) : "v"((const char*)(pc - rel) + (off < len ? off : 0u)), "s"(ring0 + LDS_SINK) : "memory");
    }
    u64 tag;
    asm volatile("global_load_dwordx2 %0, %1, off sc1" : "=v"(tag) : "v"((GAS u64*)P.flags() + FL_E) : "memory");

    unsigned ssink = 0;
    auto swarm = [&](int t) {
        if (SPF == 0) return;
        const int n = t * SPF;
        int f = (n >> 4) * 256 + ((grp & 1) * 32 + idx) * 4 + lw + (SPF == 4 ? 1792 : 0) + 896 * (grp >> 1);
        f = f >= F_ ? f - F_ : f;
        scalar_warm((const char*)P.w2 + (size_t)f * (S_ * 2) + (grp >> 1) * 1024 + (n & 15) * 64, ssink);
    };
    if (tid < 8) *(LAS unsigned*)(lds + (isM ? LDS_MC : LDS_CTL) + tid * 4) = 0u;
    const unsigned eo = (unsigned)(((fm * 128 + wm * 64 + (lane & 15)) * D_ + fn * 128 + wn * 64 + ((lane >> 4) & 1) * 16 + (lane >> 5) * 8) * 2);
    if (isM) {
        constexpr int NK = S_ / 64, SB = SB_M;
        const int pg = (grp >> 1) + 4 * (grp & 1);
        const int tm = um, tn = un, mi = pg * 16 + i16;
        const char* gm = (const char*)P.h() + tm * 256; const char* gn = (const char*)P.w1 + tn * 448;
        const int cls = (tm + tn) & 3;
        auto kof = [&](int u) { const int p0 = u - ((u - cls) & 3); return (((p0 == 1 || p0 == 2) ? u : korder(u, cls, NK)) + 4 * pg) & (NK - 1); };
        Loader<4, 7> L;
        {
            const int row = mi * 16 + wid * 2;
            const u32x4* px = (const u32x4*)(P.x + (size_t)row * D_);
            u32x4 a0, b0, a1, b1;
            if (NTX) { a0 = __builtin_nontemporal_load(px + lane); b0 = __builtin_nontemporal_load(px + 64 + lane); a1 = __builtin_nontemporal_load(px + 128 + lane); b1 = __builtin_nontemporal_load(px + 192 + lane); }
            else { a0 = px[lane]; b0 = px[64 + lane]; a1 = px[128 + lane]; b1 = px[192 + lane]; }
            SCHED0();
            if (loader) {
                const unsigned c0 = (tn & 1) * 64;
                L.init_m<TR>(lw, lane, D_ * 2);
                L.init_n<TR>(lw, lane, F_ * 2, c0, c0 + 128, c0 + 256, (tn & 1) ? 0 : 384);
            }
            asm volatile("" : "+v"(a0), "+v"(b0), "+v"(a1), "+v"(b1));
            if (loader) {
#pragma unroll
                for (int i = 0; i < 7; ++i) glds16_hidden_plain(gn + (size_t)kof(0) * (64 * F_ * 2) + L.on[i], lds + 128 * 132 + (lw + 4 * i) * PIECE);
            }
            STAMPL(0, 0);
            const __amdgpu_buffer_rsrc_t rh = rsrc(P.h(), (unsigned)S_ * D_ * 2);
            u32x4 oa0, ob0, oa1, ob1;
            const f32x2 s0 = ln_row(a0, b0, oa0, ob0), s1 = ln_row(a1, b1, oa1, ob1);
            __builtin_amdgcn_raw_buffer_store_b128(oa0, rh, (unsigned)(row * 2048 + lane * 16), 0, SC1);
            __builtin_amdgcn_raw_buffer_store_b128(ob0, rh, (unsigned)(row * 2048 + 1024 + lane * 16), 0, SC1);
            __builtin_amdgcn_raw_buffer_store_b128(oa1, rh, (unsigned)(row * 2048 + 2048 + lane * 16), 0, SC1);
            __builtin_amdgcn_raw_buffer_store_b128(ob1, rh, (unsigned)(row * 2048 + 3072 + lane * 16), 0, SC1);
            if (lane == 0) __builtin_amdgcn_raw_buffer_store_b128(__builtin_bit_cast(u32x4, (f32x4){s0.x, s0.y, s1.x, s1.y}), rsrc(P.stat(), S_ * 8), (unsigned)(row * 8), 0, SC1);
            SCHED0();
            STAMPL(0, 1);
        }
        if (loader) {
            WAIT_VM(0);
            BARRIER();
            STAMPL(0, 2);
            if (tid == 256 + 64) set_flag(P.flags(), FL_L + mi, tag);
#define ORD 0
            if (ORD == 0) {
#pragma unroll
                for (int u = 1; u < 3; ++u) L.issue_n<NT1>(gn + (size_t)kof(u) * (64 * F_ * 2), lds + u * SB, lw);
            }
            if (lw == 0) poll_flags(P.flags(), lane < 16 ? FL_L + pg * 16 + lane : -1, tag);
            STAMPL(0, 3);
            BARRIER();
            if (ORD == 0) {
#pragma unroll
                for (int u = 0; u < 3; ++u) L.issue_m<SC1>(gm + (size_t)kof(u) * (64 * D_ * 2), lds + u * SB, lw);
                WAIT_VM(8); BARRIER();
                WAIT_VM(4);
            } else {
                L.issue_m<SC1>(gm + (size_t)kof(0) * (64 * D_ * 2), lds, lw);
                L.issue_m<SC1>(gm + (size_t)kof(1) * (64 * D_ * 2), lds + SB, lw);
                if (ORD == 1) L.issue_n<NT1>(gn + (size_t)kof(1) * (64 * F_ * 2), lds + SB, lw);
                L.issue_m<SC1>(gm + (size_t)kof(2) * (64 * D_ * 2), lds + 2 * SB, lw);
                L.issue_n<NT1>(gn + (size_t)kof(2) * (64 * F_ * 2), lds + 2 * SB, lw);
                if (ORD == 1) WAIT_VM(22); else WAIT_VM(15);
                BARRIER();
                WAIT_VM(11);
            }
#define LPRIO1 0
            __builtin_amdgcn_s_setprio(LPRIO1);
            int slot = 0, t = 0;
            auto issue = [&]() {
                const int k = kof(t + 3);
                L.issue_m<SC1>(gm + (size_t)k * (64 * D_ * 2), lds + slot * SB, lw);
                L.issue_n<NT1>(gn + (size_t)k * (64 * F_ * 2), lds + slot * SB, lw);
                slot = (slot == 2) ? 0 : slot + 1;
            };
            if (lw == 0) poll_flags(P.flags(), lane < 16 ? FL_L + ((pg + 1) & 7) * 16 + lane : -1, tag);
            BARRIER(); issue(); ++t;
            WAIT_VM(L.NPW); BARRIER(); issue(); ++t;
            WAIT_VM(L.NPW);
            if (lw == 0) { poll_flags(P.flags(), FL_L + lane, tag); poll_flags(P.flags(), FL_L + 64 + lane, tag); }
            BARRIER(); issue(); ++t;
            swarm(0); swarm(1);
#pragma unroll 1
            for (; t < NK - 3; ++t) { WAIT_VM(L.NPW); BARRIER(); issue(); swarm(t - 1); }
            asm volatile("s_waitcnt lgkmcnt(0)" : "+s"(ssink));
#define PF3 2
            auto warm = [&](int r) {
                const int n = ((r * 16 + i16) * 4 + lw) * 7 + (lane >> 3), row = n * 2 + (grp & 1);
                int f = row + 896 * (grp >> 1); f = f >= F_ ? f - F_ : f;
                if (r < PF3 && lane < 56) glds4((const char*)P.w2 + (size_t)f * (S_ * 2) + (grp >> 1) * 1024 + (lane & 7) * 128, lds + LDS_SINK);
            };
            __builtin_amdgcn_s_setprio(0);
            warm(0);
            WAIT_VM(L.NPW + (PF3 > 0)); BARRIER();
            WAIT_VM((PF3 > 0) + 0); BARRIER();
            BARRIER();
#pragma unroll
            for (int i = 0; i < 8; ++i)
                glds16<0>((const char*)P.x + ((size_t)(fm * 128 + i * 16 + (lane >> 2)) * D_ + fn * 128 + lw * 32 + (lane & 3) * 8) * 2, lds + LDS_X + (lw * 8 + i) * 1024);
            glds4((const char*)P.b2 + fm * 256 + lane * 4, lds + LDS_X + 32768);
            WAIT_VM(0); warm(1);
            WAIT_VM(0); warm(2);
            WAIT_VM(0); warm(3);
            __builtin_amdgcn_s_waitcnt(0x0F70);
        } else {
            const int fr = lane & 15, fq = lane >> 4;
            const int fb = tn * 224 + ((tn & 1) ? 32 : 0) + wn * 32, fh = tn * 224 + ((tn & 1) ? 0 : 192) + wn * 16;
            WAIT_VM(0);
            WAIT_LGKM0();
            BARRIER();
            f32x4 acc[4][7];
#pragma unroll
            for (int j = 0; j < 7; ++j) {
                const u32x2 bb = *(const u32x2*)(P.b1 + (j < 6 ? fb + (j >> 1) * 64 + (j & 1) * 16 : fh) + fq * 4);
                const f32x4 bv = {lo16(bb[0]), hi16(bb[0]), lo16(bb[1]), hi16(bb[1])};
#pragma unroll
                for (int i = 0; i < 4; ++i) acc[i][j] = bv;
            }
            Reader<TR, 128, TR, 224, 4, 7> rd; rd.init(ring0, lane, wm, wn);
            Regs<4, 7> R0;
            int slot = 0;
            BARRIER();
            __builtin_amdgcn_s_waitcnt(0x0070);
            BARRIER();
            STAMP(1, 1);
            rd.read(R0, 0);
            for (int t = 0; t < NK / 4; ++t) {
                stage_step<3, SB>(slot, rd, rd, R0, acc); stage_step<3, SB>(slot, rd, rd, R0, acc);
                stage_step<3, SB>(slot, rd, rd, R0, acc); stage_step<3, SB>(slot, rd, rd, R0, acc);
            }
            STAMP(1, 2);
            const __amdgpu_buffer_rsrc_t rs = rsrc(P.U(), (unsigned)D_ * F_ * 2);
            const unsigned df = (unsigned)(tm * 8 + wm * 4);
            const unsigned ob = (unsigned)((fb >> 5) * 64 + df) * 1024u + (unsigned)((((fq & 1) * 2 + (fq >> 1)) * 16 + fr) * 16);
            const unsigned oh = (unsigned)((fh >> 5) * 64 + df + (fq & 1)) * 1024u + (unsigned)(((wn * 2 + (fq >> 1)) * 16 + fr) * 16);
            unsigned pk[4][7][2];
#pragma unroll
            for (int i = 0; i < 4; ++i) {
#pragma unroll
                for (int j = 0; j < 7; ++j) {
                    const f32x2 a = gelu2((f32x2){acc[i][j][0], acc[i][j][1]}), b = gelu2((f32x2){acc[i][j][2], acc[i][j][3]});
                    pk[i][j][0] = pack2(a.x, a.y); pk[i][j][1] = pack2(b.x, b.y);
                }
#pragma unroll
                for (int jp = 0; jp < 3; ++jp)
                    __builtin_amdgcn_raw_buffer_store_b128(pair16(pk[i][2 * jp][0], pk[i][2 * jp][1], pk[i][2 * jp + 1][0], pk[i][2 * jp + 1][1]), rs, ob + i * 1024 + jp * 2 * 65536, 0, SC1);
                if (i & 1)
                    __builtin_amdgcn_raw_buffer_store_b128(pair16(pk[i - 1][6][0], pk[i - 1][6][1], pk[i][6][0], pk[i][6][1]), rs, oh + (i - 1) * 1024, 0, SC1);
                SCHED0();
            }
            STAMP(1, 3);
        }
    } else {
        constexpr int NK = D_ / 64, SB = SB_Q;
        const int tm = fm;
        if (loader) {
            Loader<4, 6> L; L.init_m<ROW>(lw, lane, D_ * 2);
            L.init_n<TR>(lw, lane, 3 * D_ * 2, qkv_head(fn, 0) * 64, qkv_head(fn, 1) * 64, qkv_head(fn, 2) * 64);
            const char* gm = (const char*)P.x + (size_t)tm * 128 * D_ * 2; const char* gn = (const char*)P.wqkv;
            int slot = 0;
            auto issue = [&](int u) {
                const int k = (korder(u & (NK - 1), (tm + fn) & 3, NK) + ROTQ * (grp >> 1)) & (NK - 1);
                L.issue_m<0>(gm + k * 128, lds + slot * SB, lw);
                L.issue_n<NTQ>(gn + (size_t)k * (64 * 3 * D_ * 2) + (u >= NK ? 16 * 64 : 0), lds + slot * SB, lw);
                slot = (slot == 2) ? 0 : slot + 1;
            };
            __builtin_amdgcn_s_setprio(LPRIO1);
            issue(0); issue(1);
            WAIT_VM(L.NPW); BARRIER();
            issue(2);
            int t = 0;
            WAIT_VM(L.NPW); BARRIER(); issue(3); ++t;
#pragma unroll 1
            for (int seg = 0; seg < 2; ++seg) {
                const int end = seg == 0 ? 12 : 2 * NK - 3;
#pragma unroll 1
                for (; t < end; ++t) { WAIT_VM(L.NPW); BARRIER(); issue(t + 3); swarm(t - 1); }
                asm volatile("s_waitcnt lgkmcnt(0)" : "+s"(ssink));
                if (seg == 0 && lw == 0) {
                    poll_flags(P.flags(), lane < 8 ? FL_L + tm * 8 + lane : -1, tag);
                    glds16<SC1>((const char*)P.stat() + tm * 1024 + lane * 16, lds + LDS_QX);
                }
            }
            poll_flags(P.flags(), attn_dep(hd, qb, lane), tag);
            WAIT_VM(0);
            BARRIER();
            attn_load<1>(P, ring0 + LDS_AK, hd, qb, lw, lane);
            attn_load<2>(P, ring0 + LDS_AV, hd, qb, lw, lane);
            BARRIER();
            attn_load<0>(P, ring0 + LDS_AQ, hd, qb, lw, lane);
            BARRIER();
            __builtin_amdgcn_s_waitcnt(0x0F70);
            CFENCE();
        } else {
            const int fr = lane & 15, fq = lane >> 4;
            Reader<ROW, 128, TR, 192, 4, 6> rd; rd.init(ring0, lane, wm, wn);
            rd.n.b = rd.n.a + 32 - wm * 32; rd.n.a += wm * 32;
            Regs<4, 6> R0;
            int slot = 0;
            __builtin_amdgcn_s_waitcnt(0x0070);
            BARRIER();
            STAMP(1, 1);
            rd.read(R0, 0);
            const __amdgpu_buffer_rsrc_t rs = rsrc(P.qkv(), (unsigned)S_ * 3 * D_ * 2);
#pragma unroll 1
            for (int rnd = 0; rnd < 2; ++rnd) {
                f32x4 acc[5][6] = {};
                for (int t = 0; t < NK / 4; ++t) {
                    if (__builtin_expect(rnd == 1 && (t == 1 || t == 2), 0)) {
                        if (t == 1) WAIT_VM(0);
                        else if (tid == 0) set_flag(P.flags(), FL_Q + tm * 32 + fn, tag);
                    }
                    stage_step<3, SB>(slot, rd, rd, R0, acc); stage_step<3, SB>(slot, rd, rd, R0, acc);
                    stage_step<3, SB>(slot, rd, rd, R0, acc); stage_step<3, SB>(slot, rd, rd, R0, acc);
                }
                STAMP(1, 2);
                LAS char* ex = lds + LDS_QX + 1024 + rnd * 1024;
                if (fr == 0) {
#pragma unroll
                    for (int jp = 0; jp < 3; ++jp) *(LAS f32x4*)(ex + wid * 256 + jp * 64 + fq * 16) = acc[4][2 * jp];
                }
                WAIT_LGKM0();
                CFENCE();
                __hip_atomic_store((LAS unsigned*)(lds + LDS_CTL + wid * 4), (unsigned)(rnd + 1), __ATOMIC_RELAXED, __HIP_MEMORY_SCOPE_WORKGROUP);
                const int s0 = tm * 128 + wm * 64;
                f32x2 st[4];
#pragma unroll
                for (int i = 0; i < 4; ++i) st[i] = *(LAS f32x2*)(lds + LDS_QX + (wm * 64 + i * 16 + fr) * 8);
                while (__builtin_amdgcn_readfirstlane(__hip_atomic_load((LAS unsigned*)(lds + LDS_CTL + (wid ^ 2) * 4), __ATOMIC_RELAXED, __HIP_MEMORY_SCOPE_WORKGROUP)) <= rnd) {}
                CFENCE();
#pragma unroll
                for (int jp = 0; jp < 3; ++jp) {
                    const int gh = qkv_head(fn, jp) + rnd * 16 + wn;
                    const float sc = jp == 0 ? 0.17677669529663687f * 1.4426950408889634f : 1.0f;
                    const f32x4 ca = acc[4][2 * jp], cb = *(LAS f32x4*)(ex + (wid ^ 2) * 256 + jp * 64 + fq * 16);
#pragma unroll
                    for (int i = 0; i < 4; ++i) {
                        const float r = st[i].y * sc, mr = -st[i].x * r;
                        const f32x4 a = acc[i][2 * jp] * r + ca * mr, b = acc[i][2 * jp + 1] * r + cb * mr;
                        __builtin_amdgcn_raw_buffer_store_b128(pair16(pack2(a[0], a[1]), pack2(a[2], a[3]), pack2(b[0], b[1]), pack2(b[2], b[3])), rs,
                                                               (unsigned)(((gh * S_ + s0 + i * 16 + fr) * 32 + ((fq & 1) ^ wm) * 16 + (fq >> 1) * 8) * 2), 0, SC1);
                    }
                }
            }
            STAMP(1, 3);
        }
    }

    constexpr int SB = SB_F, NS = NS_F;
    constexpr size_t OS = 2 * (S_ / 16) * 1024, WOS = 64 * D_ * 2, W2S = 64 * S_ * 2, US = 2 * 65536;
    const int tile = fm * 8 + fn, cls = (fm + fn) & 3, nf = isM ? NF_M : NF_Q;
    auto fof = [&](int i) {
        int r = korder(i - 7, cls, nf - 7) + (ROTF ? (grp & 1) * ((nf - 7) >> 1) : 0);
        r = r >= nf - 7 ? r - (nf - 7) : r;
        const int j = i < 7 ? (isM ? 0 : 7) + korder(i, cls, 7) : (isM ? 14 + NA : 14) + r, k = 14 * (grp >> 1) + j;
        return k >= 56 ? k - 56 : k;
    };
    const char* g2 = (const char*)P.w2 + fm * 256;
    Loader<4, 4> L;
    if (loader) {
        L.init_m<TR>(lw, lane, S_ * 2);
        WAIT_VM(0);
    }
    auto preload_w2 = [&]() {
        int k0 = fof(0), k1 = fof(1), w = lw;
        asm volatile("" : "+s"(k0), "+s"(k1), "+s"(w));
#pragma unroll
        for (int u = 0; u < 2; ++u)
#pragma unroll
            for (int i = 0; i < 4; ++i) glds16_hidden_plain(g2 + (u ? k1 : k0) * W2S + L.om[i], lds + u * SB + (w + 4 * i) * PIECE);
    };
    if (isM) {
        if (loader) preload_w2();
        else {
            WAIT_VM(0);
            if (lane == 0 && __hip_atomic_fetch_add((LAS unsigned*)(lds + LDS_MC), 1u, __ATOMIC_RELAXED, __HIP_MEMORY_SCOPE_WORKGROUP) == 3u)
                set_flag(P.flags(), FL_U + um * 32 + un, tag);
        }
        STAMP(2, 0);
        BARRIER();
    } else {
        if (loader) __builtin_amdgcn_s_setprio(1);
        WAIT_LGKM0();
        BARRIER();
#pragma unroll 1
        for (int it = 0; it < 2; ++it) {
            STAMP(2, 3 * it);
            f32x16 sc[8];
            int ln = lane;
            asm volatile("" : "+v"(ln));
            const int dg = (ln & 31) - 4 * (ln >> 5);
            const unsigned up = dg < 0 ? ~0u : ~1u << dg;
            int nbad = qb == 0 ? 8 - wid : 0;
            asm volatile("" : "+s"(nbad));
            SCHED0();
            const float m = attn_scores(lds + (it ? LDS_BK : LDS_AK), lds + LDS_AQ, nbad, wid, ln, up, sc);
            SCHED0();
            WAIT_VM(0);
            BARRIER();
            if (tid == 0) set_flag(P.flags(), it == 0 ? FL_Q + fm * 32 + fn + 8 : FL_O + qb * 32 + hd, tag);
            if (it == 1 && loader) preload_w2();
            STAMP(2, 3 * it + 1);
            PvState S;
            S.l0 = (f32x2){0.f, 0.f}; S.l1 = S.l0; S.o = (f32x16){};
            attn_pv<0, 8>(lds + (it ? LDS_BV : LDS_AV), nbad, wid, ln, up, sc, m, S);
            attn_out(P, hd + 16 * it, qb, wid, ln, S.l0 + S.l1, S.o);
            STAMP(2, 3 * it + 2);
            if (it == 0) {
                if (loader) {
                    __builtin_amdgcn_s_setprio(0);
                    int h2 = hd + 16, q2 = qb, w2 = lw;
                    asm volatile("" : "+s"(h2), "+s"(q2), "+s"(w2));
                    poll_flags(P.flags(), attn_dep(h2, q2, ln), tag);
                    attn_load<0>(P, ring0 + LDS_AQ, h2, q2, w2, ln);
                    attn_load<1>(P, ring0 + LDS_BK, h2, q2, w2, ln);
                    attn_load<2>(P, ring0 + LDS_BV, h2, q2, w2, ln);
                    WAIT_VM(8);
                    __builtin_amdgcn_s_setprio(1);
                }
                BARRIER();
            }
        }
        WAIT_LGKM0();
    }
    const int own = isM ? 0 : 1;
    if (loader) {
        const char* go = (const char*)P.o() + fm * 8192 + own * 4096;
        const char* gw = (const char*)P.wo + fn * 256;
        const char* gu = (const char*)P.U() + fn * 8192;
#define LPRIO 0
        __builtin_amdgcn_s_setprio(0);
        L.init_n<FRAG>(lw, lane, US / 2);
        auto issue_f = [&](int u) {
            const int k = fof(u);
            L.issue_m<NT2>(g2 + k * W2S, lds + (u & (NS - 1)) * SB, lw); L.issue_n<SC1>(gu + k * US, lds + (u & (NS - 1)) * SB, lw);
        };
        const int udep = lane < 16 ? FL_U + fn * 32 + lane : -1, odep = lane < 32 ? FL_O + (fm >> 1) * 32 + lane : -1, pdep = lane == 0 ? FL_P + tile * 32 + own : -1;
        u64 pk;
        poll_flags(P.flags(), lane < 2 ? FL_U + fn * 32 + (grp >> 1) * 4 + (isM ? 0 : 2) + lane : -1, tag);
#pragma unroll
        for (int u = 0; u < 2; ++u) L.issue_n<SC1>(gu + fof(u) * US, lds + u * SB, lw);
        WAIT_VM(4); BARRIER();
        __builtin_amdgcn_s_setprio(LPRIO);
        issue_f(2); issue_f(3);
        WAIT_VM(16); BARRIER(); issue_f(4);
        WAIT_VM(16); BARRIER(); peek_issue(pk, P.flags(), udep); issue_f(5);
        WAIT_VM(16); BARRIER(); issue_f(6);
        WAIT_VM(16);
        int nxt = 7;
        if (!peek_ok(pk, udep, tag)) {
            bool uready = false;
#pragma unroll 1
            for (int t = 3; t < 7; ++t) {
                if (t == 6 && !uready) { poll_flags(P.flags(), udep, tag); uready = true; for (; nxt < 10; ++nxt) issue_f(nxt); }
                if (nxt - (t + 2) >= 2) WAIT_VM(16); else WAIT_VM(0);
                BARRIER();
                if (!uready) uready = flags_set(P.flags(), udep, tag);
                if (uready) for (; nxt <= t + NS; ++nxt) issue_f(nxt);
            }
        }
#pragma unroll 1
        for (; nxt < nf - 2; ++nxt) { WAIT_VM(16); BARRIER(); issue_f(nxt); }
        const unsigned xo = (unsigned)(((fm * 128 + (lane >> 2)) * D_ + fn * 128 + lw * 32 + (lane & 3) * 8) * 2);
        constexpr unsigned ROWS16 = 16 * D_ * 2;
        WAIT_VM(16); BARRIER(); peek_issue(pk, P.flags(), odep);
        issue_f(nf - 2);
        WAIT_VM(16); BARRIER(); issue_f(nf - 1);
        L.init_n<TR>(lw, lane, D_ * 2);
        const unsigned oo = (unsigned)(lw * 1024 + lane * 16);
        auto issue_w = [&](int i) {
            const int k = (korder(i, cls, KW) + ROTW * (grp >> 1)) & (KW - 1);
            LAS char* dst = lds + ((nf + i) & (NS - 1)) * SB;
            glds16<SC1>(go + k * OS + oo, dst + (own * 4 + lw) * PIECE); glds16<SC1>(go + k * OS + OS / 2 + oo, dst + (8 + own * 4 + lw) * PIECE);
            L.issue_n<NT2>(gw + k * WOS, dst, lw);
        };
        WAIT_VM(16);
        if (!peek_ok(pk, odep, tag)) poll_flags(P.flags(), odep, tag);
        STAMPL(0, 4);
        BARRIER(); issue_w(0);
        WAIT_VM(14); BARRIER(); issue_w(1);
#pragma unroll 1
        for (int i = 2; i < 5; ++i) { WAIT_VM(12); BARRIER(); issue_w(i); }
        WAIT_VM(12); BARRIER();
        STAMPL(0, 5);
        issue_w(5);
        {
            u32x4 t0, t1, t2, t3;
            asm volatile("ds_read_b128 %0, %4\n\tds_read_b128 %1, %4 offset:1024\n\tds_read_b128 %2, %4 offset:2048\n\tds_read_b128 %3, %4 offset:3072\n\ts_waitcnt lgkmcnt(0)"
                         : "=&v"(t0), "=&v"(t1), "=&v"(t2), "=&v"(t3) : "v"(ring0 + LDS_EX + lw * 4096 + lane * 16) : "memory");
            const __amdgpu_buffer_rsrc_t ro = rsrc(P.out, (unsigned)S_ * D_ * 2);
            const unsigned so = xo + (1 - own) * 4 * ROWS16;
            __builtin_amdgcn_raw_buffer_store_b128(t0, ro, so, 0, SC1); __builtin_amdgcn_raw_buffer_store_b128(t1, ro, so + ROWS16, 0, SC1);
            __builtin_amdgcn_raw_buffer_store_b128(t2, ro, so + 2 * ROWS16, 0, SC1); __builtin_amdgcn_raw_buffer_store_b128(t3, ro, so + 3 * ROWS16, 0, SC1);
        }
#pragma unroll 1
        for (int i = 6; i < 9; ++i) { WAIT_VM(16); BARRIER(); issue_w(i); }
        WAIT_VM(12); BARRIER();
        STAMPL(0, 6);
        issue_w(9);
        asm volatile("global_store_dwordx2 %0, %1, off sc1" ::"v"((GAS u64*)P.flags() + FL_P + tile * 32 + 1 - own), "v"(tag + 1ull) : "memory");
#pragma unroll 1
        for (int i = 10; i < 13; ++i) { WAIT_VM(13); BARRIER(); issue_w(i); }
        WAIT_VM(12); BARRIER(); issue_w(13);
        WAIT_VM(12); BARRIER(); STAMPL(0, 7); issue_w(14);
        WAIT_VM(12); BARRIER(); issue_w(15);
        peek_issue(pk, P.flags(), pdep);
        WAIT_VM(13); BARRIER();
        WAIT_VM(7); BARRIER();
        WAIT_VM(0);
        auto load_partial = [&]() {
#pragma unroll
            for (int i = 0; i < 4; ++i) glds16<SC1>((const char*)P.out + xo + (own * 4 + i) * ROWS16, lds + LDS_EX + (lw * 4 + i) * 1024);
        };
        const bool early = peek_ok(pk, pdep, tag);
        if (early) load_partial();
        BARRIER();
        BARRIER();
        if (!early) { poll_flags(P.flags(), pdep, tag); load_partial(); }
        WAIT_VM(0); BARRIER();
        if (blockIdx.x == 0 && lw == 0) {
#pragma unroll 1
            for (int i = 0; i < 4; ++i) poll_flags(P.flags(), FL_O + i * 64 + lane, tag);
            if (lane == 0) set_flag(P.flags(), FL_E, tag);
        }
    } else {
        const __amdgpu_buffer_rsrc_t ro = rsrc(P.out, (unsigned)S_ * D_ * 2);
        f32x4 acc[4][4] = {};
        if (isM) {
#pragma unroll
            for (int i = 0; i < 4; ++i)
#pragma unroll
                for (int jp = 0; jp < 2; ++jp) {
                    LAS char* xp = lds + LDS_X + ((wn * 2 + jp) * 8 + wm * 4 + i) * 1024 + (lane & 15) * 64 + (lane >> 4) * 8;
                    const u32x2 e0 = *(LAS u32x2*)xp, e1 = *(LAS u32x2*)(xp + 32);
                    const float bias = bf2f(*(LAS u16*)(lds + LDS_X + 32768 + (wm * 64 + i * 16 + (lane & 15)) * 2));
                    acc[i][2 * jp] = (f32x4){lo16(e0[0]), hi16(e0[0]), lo16(e0[1]), hi16(e0[1])} + bias;
                    acc[i][2 * jp + 1] = (f32x4){lo16(e1[0]), hi16(e1[0]), lo16(e1[1]), hi16(e1[1])} + bias;
                }
        }
        pin_acc<4, 4, 4>(acc);
        Reader<TR, 128, FRAG, 128, 4, 4> rf;
        Reader<FRAG, 128, TR, 128, 4, 4> rw;
        Regs<4, 4> R0;
        LAS char* exl = lds + LDS_EX + wn * 8192 + lane * 16;
        int slot = 0;
#define STEP(a, b) stage_step<NS, SB>(slot, a, b, R0, acc)
        rf.init(ring0, lane, wm, wn);
        WAIT_LGKM0();
        BARRIER();
        STAMP(3, 1);
        rf.read(R0, 0);
        for (int t = 0; t < (nf - 2) / 4; ++t) {
            if (__builtin_expect(!isM && (t == 1 || t == 2), 0)) {
                if (t == 1) WAIT_VM(0);
                else if (tid == 0) set_flag(P.flags(), FL_O + qb * 32 + hd + 16, tag);
            }
            STEP(rf, rf); STEP(rf, rf); STEP(rf, rf); STEP(rf, rf);
        }
        static_assert((NF_M & 3) == (NF_Q & 3), "NA must be odd");
#pragma unroll
        for (int t = 0; t < ((NF_M - 2) & 3); ++t) STEP(rf, rf);
        STEP(rf, rf);
        rw.init(ring0 + slot * SB, lane, wm, wn);
        STEP(rf, rw);
        STAMP(3, 4);
        if (wm != own) {
            BARRIER();
#pragma unroll
            for (int i = 0; i < 4; ++i)
#pragma unroll
                for (int jp = 0; jp < 2; ++jp) {
                    const f32x4 a = acc[i][2 * jp], b = acc[i][2 * jp + 1];
                    *(LAS u32x4*)(exl + jp * 4096 + i * 1024) = (u32x4){pack2(a[0], a[1]), pack2(a[2], a[3]), pack2(b[0], b[1]), pack2(b[2], b[3])};
                }
            WAIT_LGKM0();
        } else {
            for (int t = 0; t < 4; ++t) { STEP(rw, rw); STEP(rw, rw); STEP(rw, rw); STEP(rw, rw); }
            STAMP(3, 2);
            BARRIER();
#pragma unroll
            for (int i = 0; i < 4; ++i)
#pragma unroll
                for (int jp = 0; jp < 2; ++jp) {
                    const u32x4 e = *(LAS u32x4*)(exl + jp * 4096 + i * 1024);
                    const f32x4 a = acc[i][2 * jp] + (f32x4){lo16(e[0]), hi16(e[0]), lo16(e[1]), hi16(e[1])}, b = acc[i][2 * jp + 1] + (f32x4){lo16(e[2]), hi16(e[2]), lo16(e[3]), hi16(e[3])};
                    __builtin_amdgcn_raw_buffer_store_b128(pair16(pack2(a[0], a[1]), pack2(a[2], a[3]), pack2(b[0], b[1]), pack2(b[2], b[3])), ro, eo + i * (16 * D_ * 2) + jp * 64, 0, SC1);
                }
            STAMP(3, 3);
        }
#undef STEP
    }
    STAMP_OUT();
}

extern "C" void kernel_launch(void* const* d_in, const int* in_sizes, int n_in, void* d_out, int out_size, void* d_ws, size_t ws_size, hipStream_t stream) {
    char* ws = (char*)d_ws;
    Params P;
    P.x = (const u16*)d_in[0]; P.wqkv = (const u16*)d_in[1]; P.wo = (const u16*)d_in[2];
    P.w1 = (const u16*)d_in[4]; P.b1 = (const u16*)d_in[5]; P.w2 = (const u16*)d_in[6]; P.b2 = (const u16*)d_in[7];
    P.out = (u16*)d_out; P.ws = ws;
    (void)hipFuncSetAttribute((const void*)k_main, hipFuncAttributeMaxDynamicSharedMemorySize, LDS_BYTES);
    k_main<<<256, 512, LDS_BYTES, stream>>>(P);
}
```

```cpp
#include <hip/hip_runtime.h>
#include <hip/hip_bf16.h>
#include <stdint.h>
#define MORD 1

#define LAS __attribute__((address_space(3)))
#define GAS __attribute__((address_space(1)))
typedef __bf16 bf16x8 __attribute__((ext_vector_type(8)));
typedef __bf16 bf16x4 __attribute__((ext_vector_type(4)));
typedef __bf16 bf16x2 __attribute__((ext_vector_type(2)));
typedef float f32x2 __attribute__((ext_vector_type(2)));
typedef float f32x4 __attribute__((ext_vector_type(4)));
typedef float f32x16 __attribute__((ext_vector_type(16)));
typedef unsigned u32x4 __attribute__((ext_vector_type(4)));
typedef unsigned u32x2 __attribute__((ext_vector_type(2)));
typedef unsigned short u16;
typedef unsigned long long u64;

constexpr int S_ = 2048, D_ = 1024, F_ = 3584, NH = 32;

constexpr size_t WS_FLAGS = 0;
constexpr size_t WS_STAT  = 1 << 16;
constexpr size_t WS_QKV   = (1 << 17) + (size_t)S_ * D_ * 2;
constexpr size_t WS_U     = WS_QKV + (size_t)S_ * 3 * D_ * 2;
constexpr int FL_E = 0;
constexpr int FL_L = 32;
constexpr int FL_U = 8 * 32;
constexpr int FL_Q = 16 * 32;
constexpr int FL_O = 32 * 32;
constexpr int FL_P = 40 * 32;

constexpr size_t WS_DBG = 200u << 20;
#define STAMP_DECL
#define STAMP(kid, i) do {} while (0)
#define STAMPL(kid, i) do {} while (0)
#define STAMP_OUT() do {} while (0)
#define WAIT_VM(n) asm volatile("s_waitcnt vmcnt(%0)" ::"n"(n) : "memory")
#define WAIT_LGKM0() __builtin_amdgcn_s_waitcnt(0xC07F)
#define CFENCE() asm volatile("" ::: "memory")
#define BARRIER() do { CFENCE(); __builtin_amdgcn_s_barrier(); CFENCE(); } while (0)
#define SCHED0() __builtin_amdgcn_sched_barrier(0)
#define RLX_AGENT __ATOMIC_RELAXED, __HIP_MEMORY_SCOPE_AGENT
constexpr int SC1 = 16;

__device__ __forceinline__ float bf2f(u16 v) { return __uint_as_float((unsigned)v << 16); }
__device__ __forceinline__ float lo16(unsigned v) { return __uint_as_float(v << 16); }
__device__ __forceinline__ float hi16(unsigned v) { return __uint_as_float(v & 0xffff0000u); }
__device__ __forceinline__ unsigned pack2(float lo, float hi) {
    f32x2 v = {lo, hi};
    bf16x2 b = __builtin_convertvector(v, bf16x2);
    return __builtin_bit_cast(unsigned, b);
}
__device__ __forceinline__ float rbf(float x) { return __uint_as_float(pack2(x, 0.f) << 16); }
template <int CTRL, int RMASK> __device__ __forceinline__ float dpp_add(float v) {
    return v + __builtin_bit_cast(float, __builtin_amdgcn_update_dpp(0, __builtin_bit_cast(int, v), CTRL, RMASK, 0xf, false));
}
__device__ __forceinline__ float wave_sum(float v) {
    v = dpp_add<0x111, 0xf>(v); v = dpp_add<0x112, 0xf>(v); v = dpp_add<0x114, 0xf>(v); v = dpp_add<0x118, 0xf>(v);
    v = dpp_add<0x142, 0xa>(v); v = dpp_add<0x143, 0xc>(v);
    return __builtin_bit_cast(float, __builtin_amdgcn_readlane(__builtin_bit_cast(int, v), 63));
}
__device__ __forceinline__ __amdgpu_buffer_rsrc_t rsrc(const void* p, unsigned bytes) { return __builtin_amdgcn_make_buffer_rsrc((void*)p, 0, bytes, 0x00020000); }

enum { ROW = 0, TR = 1, FRAG = 2 };
constexpr int PIECE = 1056, BLK = 8 * PIECE;
template <int T, int X> struct Op {
    static constexpr int BYTES = X * 132;
    __device__ static __forceinline__ unsigned src_off(int blk, int p, int lane, unsigned ld, unsigned cb) {
        const int pos = lane >> 3;
        if (T == TR) {
            if (X == 224 && blk == 3) {
                const int ps = lane >> 2, g = (p & 1) | ((ps >> 3) << 1), r = 32 * (p >> 1) + 8 * g + 4 * ((ps >> 2) & 1) + (ps & 3);
                return (unsigned)r * ld + cb + (unsigned)((lane & 3) * 16);
            }
            const int g = (p >> 2) | (pos & 2), r = 32 * (pos >> 2) + 8 * g + 4 * (pos & 1) + (p & 3);
            return (unsigned)r * ld + cb + (unsigned)((lane & 7) * 16);
        } else if (T == ROW) {
            const int j = ((pos >> 1) & 1) | ((pos & 1) << 1) | (pos & 4);
            return (unsigned)(blk * 64 + j * 8 + p) * ld + (unsigned)((lane & 7) * 16);
        } else {
            return (unsigned)blk * ld + (unsigned)(p * 1024 + lane * 16);
        }
    }
};
template <bool SC, int IMM> __device__ __forceinline__ void dma16(const char* base, unsigned off, unsigned l) {
    if (SC) asm volatile("s_add_u32 m0, %2, %3\n\ts_nop 0\n\tglobal_load_lds_dwordx4 %0, %1 sc1" ::"v"(off), "s"(base), "s"(l), "n"(IMM) : "scc", "memory");
    else    asm volatile("s_add_u32 m0, %2, %3\n\ts_nop 0\n\tglobal_load_lds_dwordx4 %0, %1" ::"v"(off), "s"(base), "s"(l), "n"(IMM) : "scc", "memory");
}
#define DMA_FIRST(sc) "s_add_u32 m0, %1, %2\n\ts_nop 0\n\tglobal_load_lds_dwordx4 %3, %0" sc
#define DMA_NEXT(n, sc) "\n\ts_addk_i32 m0, 4224\n\ts_nop 0\n\tglobal_load_lds_dwordx4 %" #n ", %0" sc
#define DMA_G4(sc) DMA_FIRST(sc) DMA_NEXT(4, sc) DMA_NEXT(5, sc) DMA_NEXT(6, sc)
template <bool SC, int IMM, int N> __device__ __forceinline__ void dma_group(const char* base, const unsigned (&o)[N], unsigned l) {
    static_assert(4 * PIECE == 4224 && (N == 4 || ((N == 6 || N == 7) && !SC)), "dma_group");
    if constexpr (N == 4 && SC) asm volatile(DMA_G4(" sc1") ::"s"(base), "s"(l), "n"(IMM), "v"(o[0]), "v"(o[1]), "v"(o[2]), "v"(o[3]) : "scc", "memory");
    else if constexpr (N == 4) asm volatile(DMA_G4("") ::"s"(base), "s"(l), "n"(IMM), "v"(o[0]), "v"(o[1]), "v"(o[2]), "v"(o[3]) : "scc", "memory");
    else if constexpr (N == 6) asm volatile(DMA_G4("") DMA_NEXT(7, "") DMA_NEXT(8, "") ::"s"(base), "s"(l), "n"(IMM), "v"(o[0]), "v"(o[1]), "v"(o[2]), "v"(o[3]), "v"(o[4]), "v"(o[5]) : "scc", "memory");
    else asm volatile(DMA_G4("") DMA_NEXT(7, "") DMA_NEXT(8, "") DMA_NEXT(9, "") ::"s"(base), "s"(l), "n"(IMM), "v"(o[0]), "v"(o[1]), "v"(o[2]), "v"(o[3]), "v"(o[4]), "v"(o[5]), "v"(o[6]) : "scc", "memory");
}
__device__ __forceinline__ void dma4(const char* base, unsigned off, unsigned l) {
    asm volatile("s_mov_b32 m0, %2\n\ts_nop 0\n\tglobal_load_lds_dword %0, %1" ::"v"(off), "s"(base), "s"(l) : "memory");
}
__device__ __forceinline__ void dma16x4(const char* base, unsigned off, unsigned l) {
    asm volatile("s_mov_b32 m0, %2\n\ts_nop 0\n\tglobal_load_lds_dwordx4 %0, %1 sc1\n\tglobal_load_lds_dwordx4 %0, %1 offset:1024 sc1\n\t"
                 "global_load_lds_dwordx4 %0, %1 offset:2048 sc1\n\tglobal_load_lds_dwordx4 %0, %1 offset:3072 sc1" ::"v"(off), "s"(base), "s"(l) : "memory");
}

template <int T, int X> struct Addr {
    unsigned a, b;
    static constexpr int NV = (X == 224 || X == 192) ? 2 : 1;
    __device__ __forceinline__ void init(unsigned base, int lane, int w) {
        const int g = lane >> 4;
        b = 0;
        if (T == TR) {
            const int q = (lane & 15) >> 2, p = lane & 3;
            a = base + (unsigned)((q | ((g & 1) << 2)) * PIECE + (g >> 1) * 256 + p * 8 + (X == 128 ? w * BLK : w * 64));
            b = base + (unsigned)(3 * BLK + (g & 1) * PIECE + (g >> 1) * 512 + q * 64 + w * 32 + p * 8);
        } else if (T == ROW) {
            const int r = lane & 15;
            a = base + (unsigned)(w * BLK + (r & 7) * PIECE + (r >> 3) * 256 + g * 16);
        } else {
            a = base + (unsigned)(w * 4 * PIECE + lane * 16);
        }
    }
    __device__ __forceinline__ void step(unsigned d) { a += d; if (NV == 2) b += d; }
    __device__ __forceinline__ bf16x8 read(int n, int kk) const {
        if (T == TR) {
            LAS char* p; int t;
            if (X == 224 && n == 6) { p = (LAS char*)(uintptr_t)b + kk * 2 * PIECE; t = 256; }
            else if (X == 192) { p = (LAS char*)(uintptr_t)((n & 1) ? b : a) + kk * 512 + (n >> 1) * BLK; t = 128; }
            else { p = (LAS char*)(uintptr_t)a + kk * 512 + (X == 128 ? n * 32 : (n >> 1) * BLK + (n & 1) * 32); t = 128; }
            const bf16x4 lo = __builtin_amdgcn_ds_read_tr16_b64_v4bf16((LAS bf16x4*)p);
            const bf16x4 hi = __builtin_amdgcn_ds_read_tr16_b64_v4bf16((LAS bf16x4*)(p + t));
            return __builtin_shufflevector(lo, hi, 0, 1, 2, 3, 4, 5, 6, 7);
        } else if (T == ROW) {
            return *(LAS bf16x8*)((LAS char*)(uintptr_t)a + (n & 1) * 128 + (n >> 1) * 512 + kk * 64);
        } else {
            return *(LAS bf16x8*)((LAS char*)(uintptr_t)a + kk * BLK + n * PIECE);
        }
    }
};
template <int FM, int FN> struct Regs { bf16x8 m[FM], n[FN]; };
#define MFMA16(a, b, c) __builtin_amdgcn_mfma_f32_16x16x32_bf16(a, b, c, 0, 0, 0)

template <int TM, int BM, int TN, int BN, int FM, int FN> struct Reader {
    Addr<TM, BM> m; Addr<TN, BN> n;
    static constexpr int NDS = FM * (TM == TR ? 2 : 1) + FN * (TN == TR ? 2 : 1), NV = Addr<TM, BM>::NV + Addr<TN, BN>::NV;
    __device__ __forceinline__ void init(unsigned slot_base, int lane, int wm, int wn) { m.init(slot_base, lane, wm); n.init(slot_base + BM * 132, lane, wn); }
    __device__ __forceinline__ void read(Regs<FM, FN>& R, int kk) const {
#pragma unroll
        for (int i = 0; i < FM; ++i) R.m[i] = m.read(i, kk);
#pragma unroll
        for (int j = 0; j < FN; ++j) R.n[j] = n.read(j, kk);
    }
    __device__ __forceinline__ void step(unsigned d) { m.step(d); n.step(d); }
};
template <int AM, int FM, int FN> __device__ __forceinline__ void mma(f32x4 (&acc)[AM][FN], const Regs<FM, FN>& R) {
    const u32x4 one = {0x3f803f80u, 0x3f803f80u, 0x3f803f80u, 0x3f803f80u};
#pragma unroll
    for (int j = 0; j < FN; ++j) {
        if (MORD == 1 && AM > FM && !(j & 1)) acc[AM - 1][j] = MFMA16(R.n[j], __builtin_bit_cast(bf16x8, one), acc[AM - 1][j]);
#pragma unroll
        for (int ii = 0; ii < FM; ++ii) { const int i = (MORD == 1 && (j & 1)) ? FM - 1 - ii : ii; acc[i][j] = MFMA16(R.n[j], R.m[i], acc[i][j]); }
        if (MORD == 0 && AM > FM && !(j & 1)) acc[AM - 1][j] = MFMA16(R.n[j], __builtin_bit_cast(bf16x8, one), acc[AM - 1][j]);
    }
}
template <int AM, int FM, int FN> __device__ __forceinline__ void pin_acc(f32x4 (&acc)[AM][FN]) {
#pragma unroll
    for (int j = 0; j < FN; ++j) {
        asm volatile("" : "+v"(acc[0][j]), "+v"(acc[1][j]), "+v"(acc[2][j]), "+v"(acc[3][j]));
        if (AM > FM && !(j & 1)) asm volatile("" : "+v"(acc[AM - 1][j]));
    }
}
template <int NDS, int NV, int NMF> __device__ __forceinline__ void interleave() {
#pragma unroll
    for (int i = 0; i < NMF; ++i) {
        __builtin_amdgcn_sched_group_barrier(0x8, 1, 0);
        if (i < NDS) __builtin_amdgcn_sched_group_barrier(0x80, 1, 0);
        if (i >= NMF - NV) __builtin_amdgcn_sched_group_barrier(0x2, 1, 0);
    }
}
template <int NS, int SB, class RD, class RDN, int AM, int FM, int FN>
__device__ __forceinline__ void stage_step(int& slot, RD& rd, RDN& rn, Regs<FM, FN>& R0, f32x4 (&acc)[AM][FN]) {
    constexpr int NMF = FM * FN + (AM - FM) * (FN / 2);
    Regs<FM, FN> R1;
    const unsigned d = (slot + 1 == NS) ? (unsigned)(-(NS - 1) * SB) : (unsigned)SB;
    slot = (slot + 1 == NS) ? 0 : slot + 1;
    rd.read(R1, 1);
    rn.step(d);
    mma(acc, R0);
    interleave<RD::NDS, RDN::NV, NMF>();
    SCHED0();
    pin_acc<AM, FM, FN>(acc);
    WAIT_LGKM0();
    BARRIER();
    SCHED0();
    rn.read(R0, 0);
    mma(acc, R1);
    interleave<RDN::NDS, 0, NMF>();
    SCHED0();
    pin_acc<AM, FM, FN>(acc);
    SCHED0();
}

template <int NM, int NN> struct Loader {
    static constexpr int NPW = NM + NN;
    unsigned om[NM], on[NN];
    template <int T> __device__ __forceinline__ void init_m(int lw, int lane, unsigned ld) {
#pragma unroll
        for (int i = 0; i < NM; ++i) om[i] = Op<T, NM * 32>::src_off(i >> 1, lw + 4 * (i & 1), lane, ld, (i >> 1) * 128);
    }
    template <int T> __device__ __forceinline__ void init_n(int lw, int lane, unsigned ld, unsigned c0 = 0, unsigned c1 = 128, unsigned c2 = 256, unsigned c3 = 384) {
#pragma unroll
        for (int i = 0; i < NN; ++i) on[i] = Op<T, NN * 32>::src_off(i >> 1, lw + 4 * (i & 1), lane, ld, (i >> 1) == 0 ? c0 : (i >> 1) == 1 ? c1 : (i >> 1) == 2 ? c2 : c3);
    }
    template <bool SC> __device__ __forceinline__ void issue_m(const char* gm, unsigned dst) const { dma_group<SC, 0>(gm, om, dst); }
    template <bool SC> __device__ __forceinline__ void issue_n(const char* gn, unsigned dst) const { dma_group<SC, NM * 32 * 132>(gn, on, dst); }
    template <bool SCM, bool SCN, int W> __device__ __forceinline__ void turn(const char* gm, const char* gn, unsigned dst) const {
        asm volatile("" : "+s"(gm), "+s"(gn), "+s"(dst));
        WAIT_VM(W); BARRIER();
        issue_m<SCM>(gm, dst); issue_n<SCN>(gn, dst);
    }
};
#define LEAD 2
__device__ __forceinline__ int korder(int p, int c, int nk) {
    const int r = (p - c) & 3;
    const int d = ((LEAD == 2 ? 0x1003 : LEAD == 3 ? 0x0004 : 0x1102) >> (4 * r) & 15) - 1;
    return (unsigned)(p - r) < (unsigned)(nk - LEAD) ? p + d : p;
}
__device__ __forceinline__ f32x2 gelu2(f32x2 u) {
    const float c1 = -2.0f * 0.7978845608028654f * 1.4426950408889634f, c2 = c1 * 0.044715f;
    const f32x2 z = u * ((u * u) * c2 + c1);
    f32x2 e; e.x = __builtin_amdgcn_exp2f(z.x); e.y = __builtin_amdgcn_exp2f(z.y);
    e = e + 1.0f;
    f32x2 r; r.x = __builtin_amdgcn_rcpf(e.x); r.y = __builtin_amdgcn_rcpf(e.y);
    return u * r;
}
__device__ __forceinline__ u32x4 pair16(unsigned x0, unsigned x1, unsigned y0, unsigned y1) {
    auto a = __builtin_amdgcn_permlane16_swap(x0, y0, false, false);
    auto b = __builtin_amdgcn_permlane16_swap(x1, y1, false, false);
    return (u32x4){a[0], b[0], a[1], b[1]};
}
__device__ __forceinline__ bool flags_set(u64* flags, int idx, u64 tag) {
    const u64 v = __hip_atomic_load((GAS u64*)flags + (idx < 0 ? 0 : idx), RLX_AGENT);
    const bool ok = __builtin_amdgcn_ballot_w64(idx >= 0 && v - 1ull != tag) == 0ull;
    CFENCE();
    return ok;
}
__device__ __forceinline__ void poll_flags(u64* flags, int idx, u64 tag) {
#pragma unroll 1
    for (unsigned spins = 0; spins < (1u << 20); ++spins) {
        if (flags_set(flags, idx, tag)) break;
        __builtin_amdgcn_s_sleep(2);
    }
}
__device__ __forceinline__ void peek_issue(u64& v, u64* flags, int idx) {
    asm volatile("global_load_dwordx2 %0, %1, off sc1" : "=v"(v) : "v"((GAS u64*)flags + (idx < 0 ? 0 : idx)) : "memory");
}
__device__ __forceinline__ bool peek_ok(u64& v, int idx, u64 tag) {
    asm volatile("" : "+v"(v) :: "memory");
    return __builtin_amdgcn_ballot_w64(idx >= 0 && v - 1ull != tag) == 0ull;
}
__device__ __forceinline__ void set_flag(u64* flags, int idx, u64 tag) {
    asm volatile("" : "+v"(tag));
    __hip_atomic_store((GAS u64*)flags + idx, tag + 1ull, RLX_AGENT);
}

struct Params {
    const u16 *x, *wqkv, *wo, *w1, *b1, *w2, *b2;
    u16* out;
    char* ws;
    __device__ __forceinline__ u16* h() const { return out; }
    __device__ __forceinline__ u16* qkv() const { return (u16*)(ws + WS_QKV); }
    __device__ __forceinline__ u16* U() const { return (u16*)(ws + WS_U); }
    __device__ __forceinline__ u16* o() const { return (u16*)(ws + WS_QKV); }
    __device__ __forceinline__ float* stat() const { return (float*)(ws + WS_STAT); }
    __device__ __forceinline__ u64* flags() const { return (u64*)(ws + WS_FLAGS); }
};

constexpr int SB_M = (128 + 224) * 132, SB_Q = (128 + 192) * 132, SB_F = 256 * 132, NS_F = 4;
constexpr int LDS_QX = 3 * SB_Q;
constexpr int LDS_CTL = LDS_QX + 3072;
constexpr int LDS_AQ = 0, LDS_AK = 2 * SB_Q, LDS_AV = 128 * 1024, LDS_BYTES = 160 * 1024;
constexpr int LDS_MC = LDS_BYTES - 512;
constexpr int LDS_X = 2 * SB_F;
constexpr int LDS_BK = 16 * 1024, LDS_BV = 96 * 1024;
constexpr int LDS_EX = 136 * 1024;
constexpr int LDS_SINK = LDS_BYTES - 256;
static_assert(LDS_CTL + 32 <= LDS_AV && 3 * SB_M <= LDS_SINK && 3 * SB_F <= LDS_AV && NS_F * SB_F <= LDS_EX && LDS_EX + 16384 <= LDS_MC, "LDS map");
#define NA 19
#define ROTQ 4
#define ROTF 1
#define ROTW 4
constexpr int KW = 16, NF_Q = 7 + NA, NF_M = 7 + 42 - NA;

__device__ __forceinline__ int qkv_head(int tn, int blk) { return blk * NH + ((tn & ~3) | ((tn + blk) & 3)) * 2; }

__device__ __forceinline__ f32x4 ln_row(const u32x4 a, const u32x4 b, u32x4& oa, u32x4& ob) {
    f32x2 v[8];
#pragma unroll
    for (int i = 0; i < 4; ++i) { v[i] = (f32x2){lo16(a[i]), hi16(a[i])}; v[4 + i] = (f32x2){lo16(b[i]), hi16(b[i])}; }
    const f32x2 s2 = ((v[0] + v[1]) + (v[2] + v[3])) + ((v[4] + v[5]) + (v[6] + v[7]));
    const float mean = wave_sum(s2.x + s2.y) * (1.0f / D_);
    f32x2 q2 = {0.f, 0.f};
#pragma unroll
    for (int i = 0; i < 8; ++i) { v[i] -= mean; q2 += v[i] * v[i]; }
    const float var = wave_sum(q2.x + q2.y) * (1.0f / D_);
    const float rs = __builtin_amdgcn_rcpf(rbf(__builtin_amdgcn_sqrtf(rbf(rbf(var) + 1e-5f))));
    const float sd2 = rbf(__builtin_amdgcn_sqrtf(rbf(rbf(var * rs * rs) + 1e-5f)));
    const float g = rs * __builtin_amdgcn_rcpf(sd2);
#pragma unroll
    for (int i = 0; i < 8; ++i) {
        const f32x2 t = v[i] * g;
        if (i < 4) oa[i] = pack2(t.x, t.y); else ob[i - 4] = pack2(t.x, t.y);
    }
    return (f32x4){mean, rs, sd2, 0.f};
}

#define MFMA32(a, b, c) __builtin_amdgcn_mfma_f32_32x32x16_bf16(a, b, c, 0, 0, 0)
__device__ __forceinline__ int attn_dep(int hd, int qb, int lane) {
    const int which = lane >> 2, st = 2 * qb - 2 + (lane & 3), p = hd >> 1;
    if (lane >= 12 || st < 0 || (which == 0 && (lane & 3) < 2)) return -1;
    return FL_Q + st * 32 + ((p & ~3) | ((p - which) & 3));
}
template <int WHICH> __device__ __forceinline__ void attn_load(const Params& P, unsigned dst, int hd, int qb, int w, int lane) {
    const int row = WHICH == 0 ? qb * 256 + w * 64 : qb * 256 - ((qb == 0 && w < 2) ? 0 : 256) + w * 128;
    const char* g = (const char*)(P.qkv() + ((size_t)(WHICH * NH + hd) * S_ + row) * 32);
    const unsigned off = (unsigned)((lane >> 2) * 64 + (WHICH == 2 ? lane & 3 : (lane & 3) ^ ((lane >> 4) & 3)) * 16);
    dma16x4(g, off, dst + w * (WHICH == 0 ? 4096 : 8192));
    if (WHICH != 0) dma16x4(g + 4096, off, dst + w * 8192 + 4096);
}
#define PIN(x) asm volatile("" : "+v"(x))
__device__ __forceinline__ float max16(const f32x16& v) {
    float a = __builtin_fmaxf(__builtin_fmaxf(v[0], v[1]), v[2]), b = __builtin_fmaxf(__builtin_fmaxf(v[3], v[4]), v[5]);
    a = __builtin_fmaxf(__builtin_fmaxf(a, v[6]), v[7]); b = __builtin_fmaxf(__builtin_fmaxf(b, v[8]), v[9]);
    a = __builtin_fmaxf(__builtin_fmaxf(a, v[10]), v[11]); b = __builtin_fmaxf(__builtin_fmaxf(b, v[12]), v[13]);
    a = __builtin_fmaxf(__builtin_fmaxf(a, v[14]), v[15]);
    return __builtin_fmaxf(a, b);
}
__device__ __forceinline__ float attn_scores(LAS char* buf, LAS char* qbuf, int nbad, int wid, int lane, unsigned up, f32x16 (&sc)[8]) {
    const int r32 = lane & 31, hh = lane >> 5, sw = (r32 >> 2) & 3;
    LAS char* k0p = buf + (wid * 32 + r32) * 64 + ((hh ^ sw) * 16);
    LAS char* k1p = buf + (wid * 32 + r32) * 64 + (((2 + hh) ^ sw) * 16);
    const bf16x8 qf0 = *(LAS bf16x8*)(qbuf + (wid * 32 + r32) * 64 + ((hh ^ sw) * 16));
    const bf16x8 qf1 = *(LAS bf16x8*)(qbuf + (wid * 32 + r32) * 64 + (((2 + hh) ^ sw) * 16));
    bf16x8 ka[9], kb[9];
#pragma unroll
    for (int i = 0; i < 3; ++i) { const int j = i == 0 ? 8 : i - 1; ka[i] = *(LAS bf16x8*)(k0p + j * 2048); kb[i] = *(LAS bf16x8*)(k1p + j * 2048); SCHED0(); }
    const float NEG = -INFINITY;
    const f32x16 zero = {};
    const float b0 = nbad > 0 ? NEG : 0.f;
    f32x16 s8;
    float m = NEG;
#pragma unroll
    for (int i = 0; i < 11; ++i) {
        if (i + 3 < 9) { ka[i + 3] = *(LAS bf16x8*)(k0p + (i + 2) * 2048); kb[i + 3] = *(LAS bf16x8*)(k1p + (i + 2) * 2048); }
        if (i < 9) {
            const f32x16 t = MFMA32(kb[i], qf1, MFMA32(ka[i], qf0, zero));
            if (i == 0) s8 = t; else sc[i - 1] = t;
        }
        SCHED0();
        const int j = i - 3;
        if (j == 0) {
#pragma unroll
            for (int r = 0; r < 16; ++r) {
                const unsigned mk = (unsigned)__builtin_amdgcn_sbfe((int)up, (r & 3) + 8 * (r >> 2), 1);
                sc[0][r] = __uint_as_float(__builtin_amdgcn_bitop3_b32(mk, __float_as_uint(sc[0][r] + b0), __float_as_uint(s8[r]), 0xca));
            }
        }
        if (j >= 0) { const float mj = max16(sc[j]); m = __builtin_fmaxf(m, (j > 0 && j < nbad) ? NEG : mj); PIN(m); }
        SCHED0();
    }
    auto rr = __builtin_amdgcn_permlane32_swap(__float_as_uint(m), __float_as_uint(m), false, false);
    return __builtin_fmaxf(__uint_as_float(rr[0]), __uint_as_float(rr[1]));
}
struct PvState { f32x2 l0, l1; f32x16 o; bf16x8 vf[2], v8[2]; u32x4 pw[2], p8[2]; };
__device__ __forceinline__ bf16x8 v_read(LAS char* vp, int j, int s) {
    const bf16x4 lo = __builtin_amdgcn_ds_read_tr16_b64_v4bf16((LAS bf16x4*)(vp + j * 2048 + s * 1024));
    const bf16x4 hi = __builtin_amdgcn_ds_read_tr16_b64_v4bf16((LAS bf16x4*)(vp + j * 2048 + s * 1024 + 512));
    return __builtin_shufflevector(lo, hi, 0, 1, 2, 3, 4, 5, 6, 7);
}
template <int J0, int J1> __device__ __forceinline__ void attn_pv(LAS char* buf, int nbad, int wid, int lane, unsigned up, f32x16 (&sc)[8], float m, PvState& S) {
    const int hh = lane >> 5;
    LAS char* vp = buf + (wid * 32 + 4 * hh + ((lane & 15) >> 2)) * 64 + ((lane >> 4) & 1) * 32 + (lane & 3) * 8;
#pragma unroll
    for (int j = J0; j < J1; ++j) {
        bf16x8 vn[2] = {v_read(vp, j, 0), v_read(vp, j, 1)};
        if (j == 0) { S.v8[0] = v_read(vp, 8, 0); S.v8[1] = v_read(vp, 8, 1); }
        const float mj = (j > 0 && j < nbad) ? INFINITY : m;
        f32x2 p[8];
#pragma unroll
        for (int i = 0; i < 8; ++i) p[i] = (f32x2){sc[j][2 * i], sc[j][2 * i + 1]} - mj;
        SCHED0();
#pragma unroll
        for (int g = 0; g < 4; ++g) {
#pragma unroll
            for (int i = 2 * g; i < 2 * g + 2; ++i) { p[i].x = __builtin_amdgcn_exp2f(p[i].x); p[i].y = __builtin_amdgcn_exp2f(p[i].y); PIN(p[i]); }
            SCHED0();
            if (j > 0 && (g & 1) == 0) S.o = MFMA32(S.vf[g >> 1], __builtin_bit_cast(bf16x8, S.pw[g >> 1]), S.o);
            if (j == 1 && (g & 1) == 1) S.o = MFMA32(S.v8[g >> 1], __builtin_bit_cast(bf16x8, S.p8[g >> 1]), S.o);
            SCHED0();
        }
#pragma unroll
        for (int i = 0; i < 8; ++i) {
            S.pw[i >> 2][i & 3] = pack2(p[i].x, p[i].y);
            if (i & 1) S.l1 += p[i]; else S.l0 += p[i];
        }
        if (j == 0) {
#pragma unroll
            for (int i = 0; i < 8; ++i) {
                const int kr = ((2 * i) & 3) + 8 * (i >> 1);
                const unsigned mk = __builtin_amdgcn_bitop3_b32(0xffffu, (unsigned)__builtin_amdgcn_sbfe((int)up, kr, 1), (unsigned)__builtin_amdgcn_sbfe((int)up, kr + 1, 1), 0xca);
                S.p8[i >> 2][i & 3] = S.pw[i >> 2][i & 3] & ~mk; S.pw[i >> 2][i & 3] &= mk;
            }
        }
        S.vf[0] = vn[0]; S.vf[1] = vn[1];
        PIN(S.l0); PIN(S.l1); PIN(S.pw[0]); PIN(S.pw[1]);
        SCHED0();
    }
    if (J1 == 8) {
        S.o = MFMA32(S.vf[0], __builtin_bit_cast(bf16x8, S.pw[0]), S.o);
        S.o = MFMA32(S.vf[1], __builtin_bit_cast(bf16x8, S.pw[1]), S.o);
    }
}
__device__ __forceinline__ void attn_out(const Params& P, int hd, int qblk, int lane, f32x2 l2, const f32x16& oacc) {
    const int r32 = lane & 31, hh = lane >> 5;
    float l = l2.x + l2.y;
    {
        auto rr = __builtin_amdgcn_permlane32_swap(__float_as_uint(l), __float_as_uint(l), false, false);
        l = __uint_as_float(rr[0]) + __uint_as_float(rr[1]);
    }
    const float inv = __builtin_amdgcn_rcpf(l);
    unsigned w[8];
#pragma unroll
    for (int g = 0; g < 4; ++g) { w[2 * g] = pack2(oacc[4 * g] * inv, oacc[4 * g + 1] * inv); w[2 * g + 1] = pack2(oacc[4 * g + 2] * inv, oacc[4 * g + 3] * inv); }
    const __amdgpu_buffer_rsrc_t rs = rsrc(P.o(), (unsigned)S_ * D_ * 2);
    const unsigned off = (unsigned)((hd * (S_ / 16) + qblk * 2 + (r32 >> 4)) * 1024 + (hh * 16 + (r32 & 15)) * 16);
#pragma unroll
    for (int gp = 0; gp < 2; ++gp) {
        auto a = __builtin_amdgcn_permlane32_swap(w[4 * gp], w[4 * gp + 2], false, false);
        auto b = __builtin_amdgcn_permlane32_swap(w[4 * gp + 1], w[4 * gp + 3], false, false);
        __builtin_amdgcn_raw_buffer_store_b128((u32x4){a[0], b[0], a[1], b[1]}, rs, off + gp * 512, 0, SC1);
    }
}

extern "C" __global__ __launch_bounds__(512, 2) void k_main(const Params P) {
    extern __shared__ __attribute__((aligned(16))) char smem[];
    LAS char* lds = (LAS char*)smem;
    const int tid = threadIdx.x, lane = tid & 63, wid = __builtin_amdgcn_readfirstlane(tid >> 6);
    const int grp = blockIdx.x & 7, idx = blockIdx.x >> 3;
    const bool loader = wid >= 4;
    const int lw = wid & 3, wm = (wid >> 1) & 1, wn = wid & 1;
    const unsigned ring0 = (unsigned)(uintptr_t)lds, ringl = ring0 + lw * PIECE;
    const int i16 = idx & 15;
    const bool isM = idx < 16;
    const int fm = (grp >> 1) * 4 + (i16 >> 2), fn = (grp & 1) * 4 + (i16 & 3);
    const int um = (grp & 1) * 4 + (i16 >> 2), un = (grp >> 1) * 4 + (i16 & 3);
    const int hd = (grp & 1) * 8 + (i16 & 7), qb = (grp >> 1) * 2 + (i16 >> 3);
    STAMP_DECL;
    STAMP(1, 0);
#define PFC 1
    if (PFC && wid == 0) {
        u64 pc; unsigned rel, len;
        asm volatile("s_getpc_b64 %0\n1:\n\ts_mov_b32 %1, 1b-k_main\n\ts_mov_b32 %2, 2f-k_main\n\t.subsection 1\n2:\n\t.subsection 0" : "=s"(pc), "=s"(rel), "=s"(len));
        const unsigned off = (unsigned)((idx + 32 * (lane & 15)) * 128);
        dma4((const char*)(pc - rel), off < len ? off : 0u, ring0 + LDS_SINK);
    }
    u64 tag;
    asm volatile("global_load_dwordx2 %0, %1, off sc1" : "=v"(tag) : "v"((GAS u64*)P.flags() + FL_E) : "memory");

    if (tid < 8) *(LAS unsigned*)(lds + (isM ? LDS_MC : LDS_CTL) + tid * 4) = 0u;
    const unsigned eo = (unsigned)(((fm * 128 + wm * 64 + (lane & 15)) * D_ + fn * 128 + wn * 64 + ((lane >> 4) & 1) * 16 + (lane >> 5) * 8) * 2);
    if (isM) {
        constexpr int NK = S_ / 64, SB = SB_M;
        const int pg = (grp >> 1) + 4 * (grp & 1);
        const int tm = um, tn = un, mi = pg * 16 + i16;
        const char* gm = (const char*)P.h() + tm * 256; const char* gn = (const char*)P.w1 + tn * 448;
        const int cls = (tm + tn) & 3;
        auto kof = [&](int u) { const int p0 = u - ((u - cls) & 3); return __builtin_amdgcn_readfirstlane((((p0 == 1 || p0 == 2) ? u : korder(u, cls, NK)) + 4 * pg) & (NK - 1)); };
        Loader<4, 7> L;
        {
            const int row = mi * 16 + wid * 2;
            const u32x4* px = (const u32x4*)(P.x + (size_t)row * D_);
            u32x4 a0 = px[lane], b0 = px[64 + lane], a1 = px[128 + lane], b1 = px[192 + lane];
            SCHED0();
            if (loader) {
                const unsigned c0 = (tn & 1) * 64;
                L.init_m<TR>(lw, lane, D_ * 2);
                L.init_n<TR>(lw, lane, F_ * 2, c0, c0 + 128, c0 + 256, (tn & 1) ? 0 : 384);
            }
            asm volatile("" : "+v"(a0), "+v"(b0), "+v"(a1), "+v"(b1));
            if (loader) L.issue_n<false>(gn + (size_t)kof(0) * (64 * F_ * 2), ringl);
            STAMPL(0, 0);
            const __amdgpu_buffer_rsrc_t rh = rsrc(P.h(), (unsigned)S_ * D_ * 2);
            u32x4 oa0, ob0, oa1, ob1;
            const f32x4 s0 = ln_row(a0, b0, oa0, ob0), s1 = ln_row(a1, b1, oa1, ob1);
            __builtin_amdgcn_raw_buffer_store_b128(oa0, rh, (unsigned)(row * 2048 + lane * 16), 0, SC1);
            __builtin_amdgcn_raw_buffer_store_b128(ob0, rh, (unsigned)(row * 2048 + 1024 + lane * 16), 0, SC1);
            __builtin_amdgcn_raw_buffer_store_b128(oa1, rh, (unsigned)(row * 2048 + 2048 + lane * 16), 0, SC1);
            __builtin_amdgcn_raw_buffer_store_b128(ob1, rh, (unsigned)(row * 2048 + 3072 + lane * 16), 0, SC1);
            if (lane < 2) __builtin_amdgcn_raw_buffer_store_b128(__builtin_bit_cast(u32x4, lane ? s1 : s0), rsrc(P.stat(), S_ * 16), (unsigned)((row + lane) * 16), 0, SC1);
            SCHED0();
            STAMPL(0, 1);
        }
        if (loader) {
            WAIT_VM(0);
            BARRIER();
            STAMPL(0, 2);
            if (tid == 256 + 64) set_flag(P.flags(), FL_L + mi, tag);
#pragma unroll
            for (int u = 1; u < 3; ++u) L.issue_n<false>(gn + (size_t)kof(u) * (64 * F_ * 2), ringl + u * SB);
            if (lw == 0) poll_flags(P.flags(), lane < 16 ? FL_L + pg * 16 + lane : -1, tag);
            STAMPL(0, 3);
            BARRIER();
#pragma unroll
            for (int u = 0; u < 3; ++u) L.issue_m<true>(gm + (size_t)kof(u) * (64 * D_ * 2), ringl + u * SB);
            WAIT_VM(8); BARRIER();
            WAIT_VM(4);
            int slot = 0, t = 0;
            auto issue = [&]() {
                const int k = kof(t + 3);
                L.issue_m<true>(gm + (size_t)k * (64 * D_ * 2), ringl + slot * SB);
                L.issue_n<false>(gn + (size_t)k * (64 * F_ * 2), ringl + slot * SB);
                slot = (slot == 2) ? 0 : slot + 1;
            };
            if (lw == 0) poll_flags(P.flags(), lane < 16 ? FL_L + ((pg + 1) & 7) * 16 + lane : -1, tag);
            BARRIER(); issue(); ++t;
            WAIT_VM(L.NPW); BARRIER(); issue(); ++t;
            WAIT_VM(L.NPW);
            if (lw == 0) { poll_flags(P.flags(), FL_L + lane, tag); poll_flags(P.flags(), FL_L + 64 + lane, tag); }
            BARRIER(); issue(); ++t;
#pragma unroll 1
            for (; t < NK - 3; ++t) {
                const int k = (korder(t + 3, cls, NK) + 4 * pg) & (NK - 1);
                L.turn<true, false, 11>(gm + (size_t)k * (64 * D_ * 2), gn + (size_t)k * (64 * F_ * 2), ringl + slot * SB);
                slot = (slot == 2) ? 0 : slot + 1;
            }
#define PF3 1
            auto warm = [&](int r) {
                const int n = ((r * 16 + i16) * 4 + lw) * 7 + (lane < 56 ? lane >> 3 : 6), row = n * 2 + (grp & 1);
                int f = row + 896 * (grp >> 1); f = f >= F_ ? f - F_ : f;
                if (r < PF3) dma4((const char*)P.w2 + (grp >> 1) * 1024, (unsigned)(f * (S_ * 2) + (lane & 7) * 128), ring0 + LDS_SINK);
            };
            warm(0);
            WAIT_VM(L.NPW + (PF3 > 0)); BARRIER();
            WAIT_VM((PF3 > 0) + 0); BARRIER();
            BARRIER();
#pragma unroll
            for (int i = 0; i < 8; ++i)
                dma16<false, 0>((const char*)P.x + ((size_t)(fm * 128 + i * 16) * D_ + fn * 128 + lw * 32) * 2, (unsigned)((lane >> 2) * (D_ * 2) + (lane & 3) * 16), ring0 + LDS_X + (lw * 8 + i) * 1024);
            dma4((const char*)P.b2 + fm * 256, (unsigned)(lane * 4), ring0 + LDS_X + 32768);
            WAIT_VM(0); warm(1);
            WAIT_VM(0); warm(2);
            WAIT_VM(0); warm(3);
        } else {
            const int fr = lane & 15, fq = lane >> 4;
            const int fb = tn * 224 + ((tn & 1) ? 32 : 0) + wn * 32, fh = tn * 224 + ((tn & 1) ? 0 : 192) + wn * 16;
            WAIT_VM(0);
            WAIT_LGKM0();
            BARRIER();
            f32x4 acc[4][7];
#pragma unroll
            for (int j = 0; j < 7; ++j) {
                const u32x2 bb = *(const u32x2*)(P.b1 + (j < 6 ? fb + (j >> 1) * 64 + (j & 1) * 16 : fh) + fq * 4);
                const f32x4 bv = {lo16(bb[0]), hi16(bb[0]), lo16(bb[1]), hi16(bb[1])};
#pragma unroll
                for (int i = 0; i < 4; ++i) acc[i][j] = bv;
            }
            Reader<TR, 128, TR, 224, 4, 7> rd; rd.init(ring0, lane, wm, wn);
            Regs<4, 7> R0;
            int slot = 0;
            BARRIER();
            __builtin_amdgcn_s_waitcnt(0x0070);
            BARRIER();
            STAMP(1, 1);
            rd.read(R0, 0);
            for (int t = 0; t < NK / 4; ++t) {
                stage_step<3, SB>(slot, rd, rd, R0, acc); stage_step<3, SB>(slot, rd, rd, R0, acc);
                stage_step<3, SB>(slot, rd, rd, R0, acc); stage_step<3, SB>(slot, rd, rd, R0, acc);
            }
            STAMP(1, 2);
            const __amdgpu_buffer_rsrc_t rs = rsrc(P.U(), (unsigned)D_ * F_ * 2);
            const unsigned df = (unsigned)(tm * 8 + wm * 4);
            const unsigned ob = (unsigned)((fb >> 5) * 64 + df) * 1024u + (unsigned)((((fq & 1) * 2 + (fq >> 1)) * 16 + fr) * 16);
            const unsigned oh = (unsigned)((fh >> 5) * 64 + df + (fq & 1)) * 1024u + (unsigned)(((wn * 2 + (fq >> 1)) * 16 + fr) * 16);
            unsigned pk[4][7][2];
#pragma unroll
            for (int i = 0; i < 4; ++i) {
#pragma unroll
                for (int j = 0; j < 7; ++j) {
                    const f32x2 a = gelu2((f32x2){acc[i][j][0], acc[i][j][1]}), b = gelu2((f32x2){acc[i][j][2], acc[i][j][3]});
                    pk[i][j][0] = pack2(a.x, a.y); pk[i][j][1] = pack2(b.x, b.y);
                }
#pragma unroll
                for (int jp = 0; jp < 3; ++jp)
                    __builtin_amdgcn_raw_buffer_store_b128(pair16(pk[i][2 * jp][0], pk[i][2 * jp][1], pk[i][2 * jp + 1][0], pk[i][2 * jp + 1][1]), rs, ob + i * 1024 + jp * 2 * 65536, 0, SC1);
                if (i & 1)
                    __builtin_amdgcn_raw_buffer_store_b128(pair16(pk[i - 1][6][0], pk[i - 1][6][1], pk[i][6][0], pk[i][6][1]), rs, oh + (i - 1) * 1024, 0, SC1);
                SCHED0();
            }
            STAMP(1, 3);
        }
    } else {
        constexpr int NK = D_ / 64, SB = SB_Q;
        const int tm = fm;
        if (loader) {
            Loader<4, 6> L; L.init_m<ROW>(lw, lane, D_ * 2);
            L.init_n<TR>(lw, lane, 3 * D_ * 2, qkv_head(fn, 0) * 64, qkv_head(fn, 1) * 64, qkv_head(fn, 2) * 64);
            const char* gm = (const char*)P.x + (size_t)tm * 128 * D_ * 2; const char* gn = (const char*)P.wqkv;
            const char* gh2 = (const char*)P.h() + (size_t)tm * 128 * D_ * 2;
            int slot = 0;
            auto issue = [&](int u) {
                const int k = (korder(u & (NK - 1), (tm + fn) & 3, NK) + ROTQ * (grp >> 1)) & (NK - 1);
                L.issue_m<false>(gm + k * 128, ringl + slot * SB);
                L.issue_n<false>(gn + (size_t)k * (64 * 3 * D_ * 2) + (u >= NK ? 16 * 64 : 0), ringl + slot * SB);
                slot = (slot == 2) ? 0 : slot + 1;
            };
            issue(0); issue(1);
            WAIT_VM(L.NPW); BARRIER();
            issue(2);
            int t = 0;
            WAIT_VM(L.NPW); BARRIER(); issue(3); ++t;
#pragma unroll 1
            for (int seg = 0; seg < 2; ++seg) {
                const int end = seg == 0 ? 12 : 2 * NK - 3;
#pragma unroll 1
                for (; t < end; ++t) {
                    const int u = t + 3, k = (korder(u & (NK - 1), (tm + fn) & 3, NK) + ROTQ * (grp >> 1)) & (NK - 1);
                    L.turn<true, false, 10>((u >= NK ? gh2 : gm) + k * 128, gn + (size_t)k * (64 * 3 * D_ * 2) + (u & NK) * 64, ringl + slot * SB);
                    slot = (slot == 2) ? 0 : slot + 1;
                }
                if (seg == 0 && lw == 0) {
                    poll_flags(P.flags(), lane < 8 ? FL_L + tm * 8 + lane : -1, tag);
                    dma16<true, LDS_QX>((const char*)P.stat() + tm * 2048, (unsigned)(lane * 16), ring0);
                    dma16<true, LDS_QX + 1024>((const char*)P.stat() + tm * 2048 + 1024, (unsigned)(lane * 16), ring0);
                }
            }
            poll_flags(P.flags(), attn_dep(hd, qb, lane), tag);
            WAIT_VM(0);
            BARRIER();
            attn_load<1>(P, ring0 + LDS_AK, hd, qb, lw, lane);
            attn_load<2>(P, ring0 + LDS_AV, hd, qb, lw, lane);
            BARRIER();
            attn_load<0>(P, ring0 + LDS_AQ, hd, qb, lw, lane);
            BARRIER();
        } else {
            const int fr = lane & 15, fq = lane >> 4;
            Reader<ROW, 128, TR, 192, 4, 6> rd; rd.init(ring0, lane, wm, wn);
            rd.n.b = rd.n.a + 32 - wm * 32; rd.n.a += wm * 32;
            Regs<4, 6> R0;
            int slot = 0;
            __builtin_amdgcn_s_waitcnt(0x0070);
            BARRIER();
            STAMP(1, 1);
            rd.read(R0, 0);
            const __amdgpu_buffer_rsrc_t rs = rsrc(P.qkv(), (unsigned)S_ * 3 * D_ * 2);
            const int s0 = tm * 128 + wm * 64;
            const unsigned so = (unsigned)(((s0 + fr) * 32 + ((fq & 1) ^ wm) * 16 + (fq >> 1) * 8) * 2);
            constexpr float QS = 0.17677669529663687f * 1.4426950408889634f;
            {
                f32x4 acc[5][6] = {};
                for (int t = 0; t < NK / 4; ++t) {
                    stage_step<3, SB>(slot, rd, rd, R0, acc); stage_step<3, SB>(slot, rd, rd, R0, acc);
                    stage_step<3, SB>(slot, rd, rd, R0, acc); stage_step<3, SB>(slot, rd, rd, R0, acc);
                }
                LAS char* ex = lds + LDS_QX + 2048;
                if (fr == 0) {
#pragma unroll
                    for (int jp = 0; jp < 3; ++jp) *(LAS f32x4*)(ex + wid * 256 + jp * 64 + fq * 16) = acc[4][2 * jp];
                }
                WAIT_LGKM0();
                CFENCE();
                __hip_atomic_store((LAS unsigned*)(lds + LDS_CTL + wid * 4), 1u, __ATOMIC_RELAXED, __HIP_MEMORY_SCOPE_WORKGROUP);
                f32x2 st[4];
#pragma unroll
                for (int i = 0; i < 4; ++i) st[i] = *(LAS f32x2*)(lds + LDS_QX + (wm * 64 + i * 16 + fr) * 16);
                while (__builtin_amdgcn_readfirstlane(__hip_atomic_load((LAS unsigned*)(lds + LDS_CTL + (wid ^ 2) * 4), __ATOMIC_RELAXED, __HIP_MEMORY_SCOPE_WORKGROUP)) == 0u) {}
                CFENCE();
#pragma unroll
                for (int jp = 0; jp < 3; ++jp) {
                    const f32x4 ca = acc[4][2 * jp], cb = *(LAS f32x4*)(ex + (wid ^ 2) * 256 + jp * 64 + fq * 16);
#pragma unroll
                    for (int i = 0; i < 4; ++i) {
                        const float r = st[i].y * (jp == 0 ? QS : 1.0f), mr = -st[i].x * r;
                        const f32x4 a = acc[i][2 * jp] * r + ca * mr, b = acc[i][2 * jp + 1] * r + cb * mr;
                        __builtin_amdgcn_raw_buffer_store_b128(pair16(pack2(a[0], a[1]), pack2(a[2], a[3]), pack2(b[0], b[1]), pack2(b[2], b[3])), rs,
                                                               so + (unsigned)((qkv_head(fn, jp) + wn) * (S_ * 64) + i * 1024), 0, SC1);
                    }
                }
            }
            {
                f32x4 acc[4][6] = {};
                for (int t = 0; t < NK / 4; ++t) {
                    if (__builtin_expect(t == 1 || t == 2, 0)) {
                        if (t == 1) WAIT_VM(0);
                        else if (tid == 0) set_flag(P.flags(), FL_Q + tm * 32 + fn, tag);
                    }
                    stage_step<3, SB>(slot, rd, rd, R0, acc); stage_step<3, SB>(slot, rd, rd, R0, acc);
                    stage_step<3, SB>(slot, rd, rd, R0, acc); stage_step<3, SB>(slot, rd, rd, R0, acc);
                }
                STAMP(1, 2);
#pragma unroll
                for (int i = 0; i < 4; ++i) {
                    const float sd = *(LAS float*)(lds + LDS_QX + (wm * 64 + i * 16 + fr) * 16 + 8);
#pragma unroll
                    for (int jp = 0; jp < 3; ++jp) {
                        const float r = sd * (jp == 0 ? QS : 1.0f);
                        const f32x4 a = acc[i][2 * jp] * r, b = acc[i][2 * jp + 1] * r;
                        __builtin_amdgcn_raw_buffer_store_b128(pair16(pack2(a[0], a[1]), pack2(a[2], a[3]), pack2(b[0], b[1]), pack2(b[2], b[3])), rs,
                                                               so + (unsigned)((qkv_head(fn, jp) + 16 + wn) * (S_ * 64) + i * 1024), 0, SC1);
                    }
                }
            }
            STAMP(1, 3);
        }
    }

    constexpr int SB = SB_F, NS = NS_F;
    constexpr size_t OS = 2 * (S_ / 16) * 1024, WOS = 64 * D_ * 2, W2S = 64 * S_ * 2, US = 2 * 65536;
    const int tile = fm * 8 + fn, cls = (fm + fn) & 3, nf = isM ? NF_M : NF_Q;
    auto fof = [&](int i) {
        int r = korder(i - 7, cls, nf - 7) + (ROTF ? (grp & 1) * ((nf - 7) >> 1) : 0);
        r = r >= nf - 7 ? r - (nf - 7) : r;
        const int j = i < 7 ? (isM ? 0 : 7) + korder(i, cls, 7) : (isM ? 14 + NA : 14) + r, k = 14 * (grp >> 1) + j;
        return __builtin_amdgcn_readfirstlane(k >= 56 ? k - 56 : k);
    };
    const char* g2 = (const char*)P.w2 + fm * 256;
    Loader<4, 4> L;
    if (loader) {
        L.init_m<TR>(lw, lane, S_ * 2);
        WAIT_VM(0);
    }
    auto preload_w2 = [&]() {
        int k0 = fof(0), k1 = fof(1);
        asm volatile("" : "+s"(k0), "+s"(k1));
        L.issue_m<false>(g2 + k0 * W2S, ringl); L.issue_m<false>(g2 + k1 * W2S, ringl + SB);
    };
    if (isM) {
        if (loader) preload_w2();
        else {
            WAIT_VM(0);
            if (lane == 0 && __hip_atomic_fetch_add((LAS unsigned*)(lds + LDS_MC), 1u, __ATOMIC_RELAXED, __HIP_MEMORY_SCOPE_WORKGROUP) == 3u)
                set_flag(P.flags(), FL_U + um * 32 + un, tag);
        }
        STAMP(2, 0);
        BARRIER();
    } else {
        if (loader) __builtin_amdgcn_s_setprio(1);
        WAIT_LGKM0();
        BARRIER();
#pragma unroll 1
        for (int it = 0; it < 2; ++it) {
            STAMP(2, 3 * it);
            f32x16 sc[8];
            int ln = lane;
            asm volatile("" : "+v"(ln));
            const int dg = (ln & 31) - 4 * (ln >> 5);
            const unsigned up = dg < 0 ? ~0u : ~1u << dg;
            int nbad = qb == 0 ? 8 - wid : 0;
            asm volatile("" : "+s"(nbad));
            SCHED0();
            const float m = attn_scores(lds + (it ? LDS_BK : LDS_AK), lds + LDS_AQ, nbad, wid, ln, up, sc);
            SCHED0();
            WAIT_VM(0);
            BARRIER();
            if (tid == 0) set_flag(P.flags(), it == 0 ? FL_Q + fm * 32 + fn + 8 : FL_O + qb * 32 + hd, tag);
            if (it == 1 && loader) preload_w2();
            STAMP(2, 3 * it + 1);
            PvState S;
            S.l0 = (f32x2){0.f, 0.f}; S.l1 = S.l0; S.o = (f32x16){};
            attn_pv<0, 8>(lds + (it ? LDS_BV : LDS_AV), nbad, wid, ln, up, sc, m, S);
            attn_out(P, hd + 16 * it, qb * 8 + wid, ln, S.l0 + S.l1, S.o);
            STAMP(2, 3 * it + 2);
            if (it == 0) {
                if (loader) {
                    __builtin_amdgcn_s_setprio(0);
                    int h2 = hd + 16, q2 = qb, w2 = lw;
                    asm volatile("" : "+s"(h2), "+s"(q2), "+s"(w2));
                    poll_flags(P.flags(), attn_dep(h2, q2, ln), tag);
                    attn_load<0>(P, ring0 + LDS_AQ, h2, q2, w2, ln);
                    attn_load<1>(P, ring0 + LDS_BK, h2, q2, w2, ln);
                    attn_load<2>(P, ring0 + LDS_BV, h2, q2, w2, ln);
                    WAIT_VM(8);
                    __builtin_amdgcn_s_setprio(1);
                }
                BARRIER();
            }
        }
        WAIT_LGKM0();
    }
    const int own = isM ? 0 : 1;
    if (loader) {
        const char* go = (const char*)P.o() + fm * 8192 + own * 4096;
        const char* gw = (const char*)P.wo + fn * 256;
        const char* gu = (const char*)P.U() + fn * 8192;
        __builtin_amdgcn_s_setprio(0);
        L.init_n<FRAG>(lw, lane, US / 2);
        auto issue_f = [&](int u) {
            const int k = fof(u);
            L.issue_m<false>(g2 + k * W2S, ringl + (u & (NS - 1)) * SB); L.issue_n<true>(gu + k * US, ringl + (u & (NS - 1)) * SB);
        };
        const int udep = lane < 16 ? FL_U + fn * 32 + lane : -1, odep = lane < 32 ? FL_O + (fm >> 1) * 32 + lane : -1, pdep = lane == 0 ? FL_P + tile * 32 + own : -1;
        u64 pk;
        poll_flags(P.flags(), lane < 2 ? FL_U + fn * 32 + (grp >> 1) * 4 + (isM ? 0 : 2) + lane : -1, tag);
#pragma unroll
        for (int u = 0; u < 2; ++u) L.issue_n<true>(gu + fof(u) * US, ringl + u * SB);
        WAIT_VM(4); BARRIER();
        issue_f(2); issue_f(3);
        WAIT_VM(16); BARRIER(); issue_f(4);
        WAIT_VM(16); BARRIER(); peek_issue(pk, P.flags(), udep); issue_f(5);
        WAIT_VM(16); BARRIER(); issue_f(6);
        WAIT_VM(16);
        int nxt = 7;
        if (!peek_ok(pk, udep, tag)) {
            bool uready = false;
#pragma unroll 1
            for (int t = 3; t < 7; ++t) {
                if (t == 6 && !uready) { poll_flags(P.flags(), udep, tag); uready = true; for (; nxt < 10; ++nxt) issue_f(nxt); }
                if (nxt - (t + 2) >= 2) WAIT_VM(16); else WAIT_VM(0);
                BARRIER();
                if (!uready) uready = flags_set(P.flags(), udep, tag);
                if (uready) for (; nxt <= t + NS; ++nxt) issue_f(nxt);
            }
        }
#pragma unroll 1
        for (; nxt < nf - 2; ++nxt) {
            int r = korder(nxt - 7, cls, nf - 7) + (grp & 1) * ((nf - 7) >> 1);
            r = r >= nf - 7 ? r - (nf - 7) : r;
            int k = 14 * (grp >> 1) + (isM ? 14 + NA : 14) + r;
            k = k >= 56 ? k - 56 : k;
            L.turn<false, true, 16>(g2 + k * W2S, gu + k * US, ringl + (nxt & (NS - 1)) * SB);
        }
        const unsigned xo = (unsigned)(((fm * 128 + (lane >> 2)) * D_ + fn * 128 + lw * 32 + (lane & 3) * 8) * 2);
        constexpr unsigned ROWS16 = 16 * D_ * 2;
        WAIT_VM(16); BARRIER(); peek_issue(pk, P.flags(), odep);
        issue_f(nf - 2);
        WAIT_VM(16); BARRIER(); issue_f(nf - 1);
        L.init_n<TR>(lw, lane, D_ * 2);
        const unsigned oo = (unsigned)(lw * 1024 + lane * 16);
        auto issue_w = [&](int i) {
            const int k = (korder(i, cls, KW) + ROTW * (grp >> 1)) & (KW - 1);
            const unsigned dst = ringl + ((nf + i) & (NS - 1)) * SB;
            dma16<true, 0>(go + k * OS, oo, dst + own * 4 * PIECE); dma16<true, BLK>(go + k * OS, oo + (unsigned)(OS / 2), dst + own * 4 * PIECE);
            L.issue_n<false>(gw + k * WOS, dst);
        };
        WAIT_VM(16);
        if (!peek_ok(pk, odep, tag)) poll_flags(P.flags(), odep, tag);
        STAMPL(0, 4);
        BARRIER(); issue_w(0);
        WAIT_VM(14); BARRIER(); issue_w(1);
#pragma unroll 1
        for (int i = 2; i < 5; ++i) { WAIT_VM(12); BARRIER(); issue_w(i); }
        WAIT_VM(12); BARRIER();
        STAMPL(0, 5);
        issue_w(5);
        {
            u32x4 t0, t1, t2, t3;
            asm volatile("ds_read_b128 %0, %4\n\tds_read_b128 %1, %4 offset:1024\n\tds_read_b128 %2, %4 offset:2048\n\tds_read_b128 %3, %4 offset:3072\n\ts_waitcnt lgkmcnt(0)"
                         : "=&v"(t0), "=&v"(t1), "=&v"(t2), "=&v"(t3) : "v"(ring0 + LDS_EX + lw * 4096 + lane * 16) : "memory");
            const __amdgpu_buffer_rsrc_t ro = rsrc(P.out, (unsigned)S_ * D_ * 2);
            const unsigned so = xo + (1 - own) * 4 * ROWS16;
            __builtin_amdgcn_raw_buffer_store_b128(t0, ro, so, 0, SC1); __builtin_amdgcn_raw_buffer_store_b128(t1, ro, so + ROWS16, 0, SC1);
            __builtin_amdgcn_raw_buffer_store_b128(t2, ro, so + 2 * ROWS16, 0, SC1); __builtin_amdgcn_raw_buffer_store_b128(t3, ro, so + 3 * ROWS16, 0, SC1);
        }
#pragma unroll 1
        for (int i = 6; i < 9; ++i) { WAIT_VM(16); BARRIER(); issue_w(i); }
        WAIT_VM(12); BARRIER();
        STAMPL(0, 6);
        issue_w(9);
        asm volatile("global_store_dwordx2 %0, %1, off sc1" ::"v"((GAS u64*)P.flags() + FL_P + tile * 32 + 1 - own), "v"(tag + 1ull) : "memory");
#pragma unroll 1
        for (int i = 10; i < 13; ++i) { WAIT_VM(13); BARRIER(); issue_w(i); }
        WAIT_VM(12); BARRIER(); issue_w(13);
        WAIT_VM(12); BARRIER(); STAMPL(0, 7); issue_w(14);
        WAIT_VM(12); BARRIER(); issue_w(15);
        peek_issue(pk, P.flags(), pdep);
        WAIT_VM(13); BARRIER();
        WAIT_VM(7); BARRIER();
        WAIT_VM(0);
        auto load_partial = [&]() {
#pragma unroll
            for (int i = 0; i < 4; ++i) dma16<true, 0>((const char*)P.out + (own * 4 + i) * ROWS16, xo, ring0 + LDS_EX + (lw * 4 + i) * 1024);
        };
        const bool early = peek_ok(pk, pdep, tag);
        if (early) load_partial();
        BARRIER();
        BARRIER();
        if (!early) { poll_flags(P.flags(), pdep, tag); load_partial(); }
        WAIT_VM(0); BARRIER();
        if (blockIdx.x == 0 && lw == 0) {
#pragma unroll 1
            for (int i = 0; i < 4; ++i) poll_flags(P.flags(), FL_O + i * 64 + lane, tag);
            if (lane == 0) set_flag(P.flags(), FL_E, tag);
        }
    } else {
        const __amdgpu_buffer_rsrc_t ro = rsrc(P.out, (unsigned)S_ * D_ * 2);
        f32x4 acc[4][4] = {};
        if (isM) {
#pragma unroll
            for (int i = 0; i < 4; ++i)
#pragma unroll
                for (int jp = 0; jp < 2; ++jp) {
                    LAS char* xp = lds + LDS_X + ((wn * 2 + jp) * 8 + wm * 4 + i) * 1024 + (lane & 15) * 64 + (lane >> 4) * 8;
                    const u32x2 e0 = *(LAS u32x2*)xp, e1 = *(LAS u32x2*)(xp + 32);
                    const float bias = bf2f(*(LAS u16*)(lds + LDS_X + 32768 + (wm * 64 + i * 16 + (lane & 15)) * 2));
                    acc[i][2 * jp] = (f32x4){lo16(e0[0]), hi16(e0[0]), lo16(e0[1]), hi16(e0[1])} + bias;
                    acc[i][2 * jp + 1] = (f32x4){lo16(e1[0]), hi16(e1[0]), lo16(e1[1]), hi16(e1[1])} + bias;
                }
        }
        pin_acc<4, 4, 4>(acc);
        Reader<TR, 128, FRAG, 128, 4, 4> rf;
        Reader<FRAG, 128, TR, 128, 4, 4> rw;
        Regs<4, 4> R0;
        LAS char* exl = lds + LDS_EX + wn * 8192 + lane * 16;
        int slot = 0;
#define STEP(a, b) stage_step<NS, SB>(slot, a, b, R0, acc)
        rf.init(ring0, lane, wm, wn);
        WAIT_LGKM0();
        BARRIER();
        STAMP(3, 1);
        rf.read(R0, 0);
        for (int t = 0; t < (nf - 2) / 4; ++t) {
            if (__builtin_expect(!isM && (t == 1 || t == 2), 0)) {
                if (t == 1) WAIT_VM(0);
                else if (tid == 0) set_flag(P.flags(), FL_O + qb * 32 + hd + 16, tag);
            }
            STEP(rf, rf); STEP(rf, rf); STEP(rf, rf); STEP(rf, rf);
        }
        static_assert((NF_M & 3) == (NF_Q & 3), "NA must be odd");
#pragma unroll
        for (int t = 0; t < ((NF_M - 2) & 3); ++t) STEP(rf, rf);
        STEP(rf, rf);
        rw.init(ring0 + slot * SB, lane, wm, wn);
        STEP(rf, rw);
        STAMP(3, 4);
        if (wm != own) {
            BARRIER();
#pragma unroll
            for (int i = 0; i < 4; ++i)
#pragma unroll
                for (int jp = 0; jp < 2; ++jp) {
                    const f32x4 a = acc[i][2 * jp], b = acc[i][2 * jp + 1];
                    *(LAS u32x4*)(exl + jp * 4096 + i * 1024) = (u32x4){pack2(a[0], a[1]), pack2(a[2], a[3]), pack2(b[0], b[1]), pack2(b[2], b[3])};
                }
            WAIT_LGKM0();
        } else {
            for (int t = 0; t < 4; ++t) { STEP(rw, rw); STEP(rw, rw); STEP(rw, rw); STEP(rw, rw); }
            STAMP(3, 2);
            BARRIER();
#pragma unroll
            for (int i = 0; i < 4; ++i)
#pragma unroll
                for (int jp = 0; jp < 2; ++jp) {
                    const u32x4 e = *(LAS u32x4*)(exl + jp * 4096 + i * 1024);
                    const f32x4 a = acc[i][2 * jp] + (f32x4){lo16(e[0]), hi16(e[0]), lo16(e[1]), hi16(e[1])}, b = acc[i][2 * jp + 1] + (f32x4){lo16(e[2]), hi16(e[2]), lo16(e[3]), hi16(e[3])};
                    __builtin_amdgcn_raw_buffer_store_b128(pair16(pack2(a[0], a[1]), pack2(a[2], a[3]), pack2(b[0], b[1]), pack2(b[2], b[3])), ro, eo + i * (16 * D_ * 2) + jp * 64, 0, SC1);
                }
            STAMP(3, 3);
        }
#undef STEP
    }
    STAMP_OUT();
}

extern "C" void kernel_launch(void* const* d_in, const int* in_sizes, int n_in, void* d_out, int out_size, void* d_ws, size_t ws_size, hipStream_t stream) {
    char* ws = (char*)d_ws;
    Params P;
    P.x = (const u16*)d_in[0]; P.wqkv = (const u16*)d_in[1]; P.wo = (const u16*)d_in[2];
    P.w1 = (const u16*)d_in[4]; P.b1 = (const u16*)d_in[5]; P.w2 = (const u16*)d_in[6]; P.b2 = (const u16*)d_in[7];
    P.out = (u16*)d_out; P.ws = ws;
    (void)hipFuncSetAttribute((const void*)k_main, hipFuncAttributeMaxDynamicSharedMemorySize, LDS_BYTES);
    k_main<<<256, 512, LDS_BYTES, stream>>>(P);
}
```

```cpp
#include <hip/hip_runtime.h>
#include <hip/hip_bf16.h>
#include <stdint.h>
#define MORD 1

#define LAS __attribute__((address_space(3)))
#define GAS __attribute__((address_space(1)))
typedef __bf16 bf16x8 __attribute__((ext_vector_type(8)));
typedef __bf16 bf16x4 __attribute__((ext_vector_type(4)));
typedef __bf16 bf16x2 __attribute__((ext_vector_type(2)));
typedef float f32x2 __attribute__((ext_vector_type(2)));
typedef float f32x4 __attribute__((ext_vector_type(4)));
typedef float f32x16 __attribute__((ext_vector_type(16)));
typedef unsigned u32x4 __attribute__((ext_vector_type(4)));
typedef unsigned u32x2 __attribute__((ext_vector_type(2)));
typedef unsigned short u16;
typedef unsigned long long u64;

constexpr int S_ = 2048, D_ = 1024, F_ = 3584, NH = 32;

constexpr size_t WS_FLAGS = 0;
constexpr size_t WS_STAT  = 1 << 16;
constexpr size_t WS_QKV   = (1 << 17) + (size_t)S_ * D_ * 2;
constexpr size_t WS_U     = WS_QKV + (size_t)S_ * 3 * D_ * 2;
constexpr int FL_E = 0;
constexpr int FL_L = 32;
constexpr int FL_U = 8 * 32;
constexpr int FL_Q = 16 * 32;
constexpr int FL_O = 32 * 32;
constexpr int FL_P = 40 * 32;

constexpr size_t WS_DBG = 200u << 20;
#define STAMP_DECL
#define STAMP(kid, i) do {} while (0)
#define STAMPL(kid, i) do {} while (0)
#define STAMP_OUT() do {} while (0)
#define WAIT_VM(n) asm volatile("s_waitcnt vmcnt(%0)" ::"n"(n) : "memory")
#define WAIT_LGKM0() __builtin_amdgcn_s_waitcnt(0xC07F)
#define CFENCE() asm volatile("" ::: "memory")
#define BARRIER() do { CFENCE(); __builtin_amdgcn_s_barrier(); CFENCE(); } while (0)
#define SCHED0() __builtin_amdgcn_sched_barrier(0)
#define RLX_AGENT __ATOMIC_RELAXED, __HIP_MEMORY_SCOPE_AGENT
constexpr int SC1 = 16;

__device__ __forceinline__ float bf2f(u16 v) { return __uint_as_float((unsigned)v << 16); }
__device__ __forceinline__ float lo16(unsigned v) { return __uint_as_float(v << 16); }
__device__ __forceinline__ float hi16(unsigned v) { return __uint_as_float(v & 0xffff0000u); }
__device__ __forceinline__ unsigned pack2(float lo, float hi) {
    f32x2 v = {lo, hi};
    bf16x2 b = __builtin_convertvector(v, bf16x2);
    return __builtin_bit_cast(unsigned, b);
}
__device__ __forceinline__ float rbf(float x) { return __uint_as_float(pack2(x, 0.f) << 16); }
template <int CTRL, int RMASK> __device__ __forceinline__ float dpp_add(float v) {
    return v + __builtin_bit_cast(float, __builtin_amdgcn_update_dpp(0, __builtin_bit_cast(int, v), CTRL, RMASK, 0xf, false));
}
__device__ __forceinline__ float wave_sum(float v) {
    v = dpp_add<0x111, 0xf>(v); v = dpp_add<0x112, 0xf>(v); v = dpp_add<0x114, 0xf>(v); v = dpp_add<0x118, 0xf>(v);
    v = dpp_add<0x142, 0xa>(v); v = dpp_add<0x143, 0xc>(v);
    return __builtin_bit_cast(float, __builtin_amdgcn_readlane(__builtin_bit_cast(int, v), 63));
}
__device__ __forceinline__ __amdgpu_buffer_rsrc_t rsrc(const void* p, unsigned bytes) { return __builtin_amdgcn_make_buffer_rsrc((void*)p, 0, bytes, 0x00020000); }

enum { ROW = 0, TR = 1, FRAG = 2 };
constexpr int PIECE = 1056, BLK = 8 * PIECE;
template <int T, int X> struct Op {
    static constexpr int BYTES = X * 132;
    __device__ static __forceinline__ unsigned src_off(int blk, int p, int lane, unsigned ld, unsigned cb) {
        const int pos = lane >> 3;
        if (T == TR) {
            if (X == 224 && blk == 3) {
                const int ps = lane >> 2, g = (p & 1) | ((ps >> 3) << 1), r = 32 * (p >> 1) + 8 * g + 4 * ((ps >> 2) & 1) + (ps & 3);
                return (unsigned)r * ld + cb + (unsigned)((lane & 3) * 16);
            }
            const int g = (p >> 2) | (pos & 2), r = 32 * (pos >> 2) + 8 * g + 4 * (pos & 1) + (p & 3);
            return (unsigned)r * ld + cb + (unsigned)((lane & 7) * 16);
        } else if (T == ROW) {
            const int j = ((pos >> 1) & 1) | ((pos & 1) << 1) | (pos & 4);
            return (unsigned)(blk * 64 + j * 8 + p) * ld + (unsigned)((lane & 7) * 16);
        } else {
            return (unsigned)blk * ld + (unsigned)(p * 1024 + lane * 16);
        }
    }
};
template <bool SC, int IMM> __device__ __forceinline__ void dma16(const char* base, unsigned off, unsigned l) {
    if (SC) asm volatile("s_add_u32 m0, %2, %3\n\ts_nop 0\n\tglobal_load_lds_dwordx4 %0, %1 sc1" ::"v"(off), "s"(base), "s"(l), "n"(IMM) : "scc", "memory");
    else    asm volatile("s_add_u32 m0, %2, %3\n\ts_nop 0\n\tglobal_load_lds_dwordx4 %0, %1" ::"v"(off), "s"(base), "s"(l), "n"(IMM) : "scc", "memory");
}
#define DMA_FIRST(sc) "s_add_u32 m0, %1, %2\n\ts_nop 0\n\tglobal_load_lds_dwordx4 %3, %0" sc
#define DMA_NEXT(n, sc) "\n\ts_addk_i32 m0, 4224\n\ts_nop 0\n\tglobal_load_lds_dwordx4 %" #n ", %0" sc
#define DMA_G4(sc) DMA_FIRST(sc) DMA_NEXT(4, sc) DMA_NEXT(5, sc) DMA_NEXT(6, sc)
template <bool SC, int IMM, int N> __device__ __forceinline__ void dma_group(const char* base, const unsigned (&o)[N], unsigned l) {
    static_assert(4 * PIECE == 4224 && (N == 4 || ((N == 6 || N == 7) && !SC)), "dma_group");
    if constexpr (N == 4 && SC) asm volatile(DMA_G4(" sc1") ::"s"(base), "s"(l), "n"(IMM), "v"(o[0]), "v"(o[1]), "v"(o[2]), "v"(o[3]) : "scc", "memory");
    else if constexpr (N == 4) asm volatile(DMA_G4("") ::"s"(base), "s"(l), "n"(IMM), "v"(o[0]), "v"(o[1]), "v"(o[2]), "v"(o[3]) : "scc", "memory");
    else if constexpr (N == 6) asm volatile(DMA_G4("") DMA_NEXT(7, "") DMA_NEXT(8, "") ::"s"(base), "s"(l), "n"(IMM), "v"(o[0]), "v"(o[1]), "v"(o[2]), "v"(o[3]), "v"(o[4]), "v"(o[5]) : "scc", "memory");
    else asm volatile(DMA_G4("") DMA_NEXT(7, "") DMA_NEXT(8, "") DMA_NEXT(9, "") ::"s"(base), "s"(l), "n"(IMM), "v"(o[0]), "v"(o[1]), "v"(o[2]), "v"(o[3]), "v"(o[4]), "v"(o[5]), "v"(o[6]) : "scc", "memory");
}
__device__ __forceinline__ void dma4(const char* base, unsigned off, unsigned l) {
    asm volatile("s_mov_b32 m0, %2\n\ts_nop 0\n\tglobal_load_lds_dword %0, %1" ::"v"(off), "s"(base), "s"(l) : "memory");
}
__device__ __forceinline__ void dma16x4(const char* base, unsigned off, unsigned l) {
    asm volatile("s_mov_b32 m0, %2\n\ts_nop 0\n\tglobal_load_lds_dwordx4 %0, %1 sc1\n\tglobal_load_lds_dwordx4 %0, %1 offset:1024 sc1\n\t"
                 "global_load_lds_dwordx4 %0, %1 offset:2048 sc1\n\tglobal_load_lds_dwordx4 %0, %1 offset:3072 sc1" ::"v"(off), "s"(base), "s"(l) : "memory");
}

template <int T, int X> struct Addr {
    unsigned a, b;
    static constexpr int NV = (X == 224 || X == 192) ? 2 : 1;
    __device__ __forceinline__ void init(unsigned base, int lane, int w) {
        const int g = lane >> 4;
        b = 0;
        if (T == TR) {
            const int q = (lane & 15) >> 2, p = lane & 3;
            a = base + (unsigned)((q | ((g & 1) << 2)) * PIECE + (g >> 1) * 256 + p * 8 + (X == 128 ? w * BLK : w * 64));
            b = base + (unsigned)(3 * BLK + (g & 1) * PIECE + (g >> 1) * 512 + q * 64 + w * 32 + p * 8);
        } else if (T == ROW) {
            const int r = lane & 15;
            a = base + (unsigned)(w * BLK + (r & 7) * PIECE + (r >> 3) * 256 + g * 16);
        } else {
            a = base + (unsigned)(w * 4 * PIECE + lane * 16);
        }
    }
    __device__ __forceinline__ void step(unsigned d) { a += d; if (NV == 2) b += d; }
    __device__ __forceinline__ bf16x8 read(int n, int kk) const {
        if (T == TR) {
            LAS char* p; int t;
            if (X == 224 && n == 6) { p = (LAS char*)(uintptr_t)b + kk * 2 * PIECE; t = 256; }
            else if (X == 192) { p = (LAS char*)(uintptr_t)((n & 1) ? b : a) + kk * 512 + (n >> 1) * BLK; t = 128; }
            else { p = (LAS char*)(uintptr_t)a + kk * 512 + (X == 128 ? n * 32 : (n >> 1) * BLK + (n & 1) * 32); t = 128; }
            const bf16x4 lo = __builtin_amdgcn_ds_read_tr16_b64_v4bf16((LAS bf16x4*)p);
            const bf16x4 hi = __builtin_amdgcn_ds_read_tr16_b64_v4bf16((LAS bf16x4*)(p + t));
            return __builtin_shufflevector(lo, hi, 0, 1, 2, 3, 4, 5, 6, 7);
        } else if (T == ROW) {
            return *(LAS bf16x8*)((LAS char*)(uintptr_t)a + (n & 1) * 128 + (n >> 1) * 512 + kk * 64);
        } else {
            return *(LAS bf16x8*)((LAS char*)(uintptr_t)a + kk * BLK + n * PIECE);
        }
    }
};
template <int FM, int FN> struct Regs { bf16x8 m[FM], n[FN]; };
#define MFMA16(a, b, c) __builtin_amdgcn_mfma_f32_16x16x32_bf16(a, b, c, 0, 0, 0)

template <int TM, int BM, int TN, int BN, int FM, int FN> struct Reader {
    Addr<TM, BM> m; Addr<TN, BN> n;
    static constexpr int NDS = FM * (TM == TR ? 2 : 1) + FN * (TN == TR ? 2 : 1), NV = Addr<TM, BM>::NV + Addr<TN, BN>::NV;
    __device__ __forceinline__ void init(unsigned slot_base, int lane, int wm, int wn) { m.init(slot_base, lane, wm); n.init(slot_base + BM * 132, lane, wn); }
    __device__ __forceinline__ void read(Regs<FM, FN>& R, int kk) const {
#pragma unroll
        for (int i = 0; i < FM; ++i) R.m[i] = m.read(i, kk);
#pragma unroll
        for (int j = 0; j < FN; ++j) R.n[j] = n.read(j, kk);
    }
    __device__ __forceinline__ void step(unsigned d) { m.step(d); n.step(d); }
};
template <int AM, int FM, int FN> __device__ __forceinline__ void mma(f32x4 (&acc)[AM][FN], const Regs<FM, FN>& R) {
    const u32x4 one = {0x3f803f80u, 0x3f803f80u, 0x3f803f80u, 0x3f803f80u};
#pragma unroll
    for (int j = 0; j < FN; ++j) {
        if (MORD == 1 && AM > FM && !(j & 1)) acc[AM - 1][j] = MFMA16(R.n[j], __builtin_bit_cast(bf16x8, one), acc[AM - 1][j]);
#pragma unroll
        for (int ii = 0; ii < FM; ++ii) { const int i = (MORD == 1 && (j & 1)) ? FM - 1 - ii : ii; acc[i][j] = MFMA16(R.n[j], R.m[i], acc[i][j]); }
        if (MORD == 0 && AM > FM && !(j & 1)) acc[AM - 1][j] = MFMA16(R.n[j], __builtin_bit_cast(bf16x8, one), acc[AM - 1][j]);
    }
}
template <int AM, int FM, int FN> __device__ __forceinline__ void pin_acc(f32x4 (&acc)[AM][FN]) {
#pragma unroll
    for (int j = 0; j < FN; ++j) {
        asm volatile("" : "+v"(acc[0][j]), "+v"(acc[1][j]), "+v"(acc[2][j]), "+v"(acc[3][j]));
        if (AM > FM && !(j & 1)) asm volatile("" : "+v"(acc[AM - 1][j]));
    }
}
template <int NDS, int NV, int NMF> __device__ __forceinline__ void interleave() {
#pragma unroll
    for (int i = 0; i < NMF; ++i) {
        __builtin_amdgcn_sched_group_barrier(0x8, 1, 0);
        if (i < NDS) __builtin_amdgcn_sched_group_barrier(0x80, 1, 0);
        if (i >= NMF - NV) __builtin_amdgcn_sched_group_barrier(0x2, 1, 0);
    }
}
template <int NS, int SB, class RD, class RDN, int AM, int FM, int FN>
__device__ __forceinline__ void stage_step(int& slot, RD& rd, RDN& rn, Regs<FM, FN>& R0, f32x4 (&acc)[AM][FN]) {
    constexpr int NMF = FM * FN + (AM - FM) * (FN / 2);
    Regs<FM, FN> R1;
    const unsigned d = (slot + 1 == NS) ? (unsigned)(-(NS - 1) * SB) : (unsigned)SB;
    slot = (slot + 1 == NS) ? 0 : slot + 1;
    rd.read(R1, 1);
    rn.step(d);
    mma(acc, R0);
    interleave<RD::NDS, RDN::NV, NMF>();
    SCHED0();
    pin_acc<AM, FM, FN>(acc);
    WAIT_LGKM0();
    BARRIER();
    SCHED0();
    rn.read(R0, 0);
    mma(acc, R1);
    interleave<RDN::NDS, 0, NMF>();
    SCHED0();
    pin_acc<AM, FM, FN>(acc);
    SCHED0();
}

struct HalfReader {
    unsigned m0, m1; Addr<TR, 128> n;
    static constexpr int NDS = 12, NV = 3;
    __device__ __forceinline__ void init(unsigned slot_base, int lane, int own, int kq, int wn) {
        const unsigned a = slot_base + (unsigned)(own * 4 * PIECE + kq * BLK + lane * 16);
        m0 = a + kq * 2 * PIECE; m1 = a - kq * 2 * PIECE;
        n.init(slot_base + 128 * 132 + kq * 512, lane, wn);
    }
    __device__ __forceinline__ void read(Regs<4, 4>& R, int) const {
#pragma unroll
        for (int i = 0; i < 4; ++i) R.m[i] = *(LAS bf16x8*)((LAS char*)(uintptr_t)(i < 2 ? m0 : m1) + i * PIECE);
#pragma unroll
        for (int j = 0; j < 4; ++j) R.n[j] = n.read(j, 0);
    }
    __device__ __forceinline__ void step(unsigned d) { m0 += d; m1 += d; n.step(d); }
};
template <int NS, int SB> __device__ __forceinline__ void half_step(int& slot, HalfReader& rd, Regs<4, 4>& Rc, Regs<4, 4>& Rn, f32x4 (&acc)[4][4]) {
    rd.step((slot + 1 == NS) ? (unsigned)(-(NS - 1) * SB) : (unsigned)SB);
    slot = (slot + 1 == NS) ? 0 : slot + 1;
    SCHED0();
    WAIT_LGKM0();
    BARRIER();
    SCHED0();
    rd.read(Rn, 0);
    mma(acc, Rc);
    interleave<12, 0, 16>();
    SCHED0();
    pin_acc<4, 4, 4>(acc);
    SCHED0();
}

template <int NM, int NN> struct Loader {
    static constexpr int NPW = NM + NN;
    unsigned om[NM], on[NN];
    template <int T> __device__ __forceinline__ void init_m(int lw, int lane, unsigned ld) {
#pragma unroll
        for (int i = 0; i < NM; ++i) om[i] = Op<T, NM * 32>::src_off(i >> 1, lw + 4 * (i & 1), lane, ld, (i >> 1) * 128);
    }
    template <int T> __device__ __forceinline__ void init_n(int lw, int lane, unsigned ld, unsigned c0 = 0, unsigned c1 = 128, unsigned c2 = 256, unsigned c3 = 384) {
#pragma unroll
        for (int i = 0; i < NN; ++i) on[i] = Op<T, NN * 32>::src_off(i >> 1, lw + 4 * (i & 1), lane, ld, (i >> 1) == 0 ? c0 : (i >> 1) == 1 ? c1 : (i >> 1) == 2 ? c2 : c3);
    }
    template <bool SC> __device__ __forceinline__ void issue_m(const char* gm, unsigned dst) const { dma_group<SC, 0>(gm, om, dst); }
    template <bool SC> __device__ __forceinline__ void issue_n(const char* gn, unsigned dst) const { dma_group<SC, NM * 32 * 132>(gn, on, dst); }
    template <bool SCM, bool SCN, int W> __device__ __forceinline__ void turn(const char* gm, const char* gn, unsigned dst) const {
        asm volatile("" : "+s"(gm), "+s"(gn), "+s"(dst));
        WAIT_VM(W); BARRIER();
        issue_m<SCM>(gm, dst); issue_n<SCN>(gn, dst);
    }
};
#define LEAD 2
__device__ __forceinline__ int korder(int p, int c, int nk) {
    const int r = (p - c) & 3;
    const int d = ((LEAD == 2 ? 0x1003 : LEAD == 3 ? 0x0004 : 0x1102) >> (4 * r) & 15) - 1;
    return (unsigned)(p - r) < (unsigned)(nk - LEAD) ? p + d : p;
}
__device__ __forceinline__ f32x2 gelu2(f32x2 u) {
    const float c1 = -2.0f * 0.7978845608028654f * 1.4426950408889634f, c2 = c1 * 0.044715f;
    const f32x2 z = u * ((u * u) * c2 + c1);
    f32x2 e; e.x = __builtin_amdgcn_exp2f(z.x); e.y = __builtin_amdgcn_exp2f(z.y);
    e = e + 1.0f;
    f32x2 r; r.x = __builtin_amdgcn_rcpf(e.x); r.y = __builtin_amdgcn_rcpf(e.y);
    return u * r;
}
__device__ __forceinline__ u32x4 pair16(unsigned x0, unsigned x1, unsigned y0, unsigned y1) {
    auto a = __builtin_amdgcn_permlane16_swap(x0, y0, false, false);
    auto b = __builtin_amdgcn_permlane16_swap(x1, y1, false, false);
    return (u32x4){a[0], b[0], a[1], b[1]};
}
__device__ __forceinline__ bool flags_set(u64* flags, int idx, u64 tag) {
    const u64 v = __hip_atomic_load((GAS u64*)flags + (idx < 0 ? 0 : idx), RLX_AGENT);
    const bool ok = __builtin_amdgcn_ballot_w64(idx >= 0 && v - 1ull != tag) == 0ull;
    CFENCE();
    return ok;
}
__device__ __forceinline__ void poll_flags(u64* flags, int idx, u64 tag) {
#pragma unroll 1
    for (unsigned spins = 0; spins < (1u << 20); ++spins) {
        if (flags_set(flags, idx, tag)) break;
        __builtin_amdgcn_s_sleep(2);
    }
}
__device__ __forceinline__ void peek_issue(u64& v, u64* flags, int idx) {
    asm volatile("global_load_dwordx2 %0, %1, off sc1" : "=v"(v) : "v"((GAS u64*)flags + (idx < 0 ? 0 : idx)) : "memory");
}
__device__ __forceinline__ bool peek_ok(u64& v, int idx, u64 tag) {
    asm volatile("" : "+v"(v) :: "memory");
    return __builtin_amdgcn_ballot_w64(idx >= 0 && v - 1ull != tag) == 0ull;
}
__device__ __forceinline__ void set_flag(u64* flags, int idx, u64 tag) {
    asm volatile("" : "+v"(tag));
    __hip_atomic_store((GAS u64*)flags + idx, tag + 1ull, RLX_AGENT);
}

struct Params {
    const u16 *x, *wqkv, *wo, *w1, *b1, *w2, *b2;
    u16* out;
    char* ws;
    __device__ __forceinline__ u16* h() const { return out; }
    __device__ __forceinline__ u16* qkv() const { return (u16*)(ws + WS_QKV); }
    __device__ __forceinline__ u16* U() const { return (u16*)(ws + WS_U); }
    __device__ __forceinline__ u16* o() const { return (u16*)(ws + WS_QKV); }
    __device__ __forceinline__ float* stat() const { return (float*)(ws + WS_STAT); }
    __device__ __forceinline__ u64* flags() const { return (u64*)(ws + WS_FLAGS); }
};

constexpr int SB_M = (128 + 224) * 132, SB_Q = (128 + 192) * 132, SB_F = 256 * 132, NS_F = 4;
constexpr int LDS_QX = 3 * SB_Q;
constexpr int LDS_CTL = LDS_QX + 3072;
constexpr int LDS_AQ = 0, LDS_AK = 2 * SB_Q, LDS_AV = 128 * 1024, LDS_BYTES = 160 * 1024;
constexpr int LDS_MC = LDS_BYTES - 512;
constexpr int LDS_X = 3 * SB_F;
constexpr int LDS_BK = 16 * 1024, LDS_BV = 96 * 1024;
constexpr int LDS_EX = 136 * 1024;
constexpr int LDS_SINK = LDS_BYTES - 256;
static_assert(LDS_CTL + 32 <= LDS_AV && 3 * SB_M <= LDS_SINK && 3 * SB_F <= LDS_AV && NS_F * SB_F <= LDS_EX && LDS_EX + 16384 <= LDS_MC, "LDS map");
#define NA 19
#define ROTQ 4
#define ROTF 1
#define ROTW 4
constexpr int KW = 16, NF_Q = 7 + NA, NF_M = 7 + 42 - NA;

__device__ __forceinline__ int qkv_head(int tn, int blk) { return blk * NH + ((tn & ~3) | ((tn + blk) & 3)) * 2; }

__device__ __forceinline__ f32x4 ln_row(const u32x4 a, const u32x4 b, u32x4& oa, u32x4& ob) {
    f32x2 v[8];
#pragma unroll
    for (int i = 0; i < 4; ++i) { v[i] = (f32x2){lo16(a[i]), hi16(a[i])}; v[4 + i] = (f32x2){lo16(b[i]), hi16(b[i])}; }
    const f32x2 s2 = ((v[0] + v[1]) + (v[2] + v[3])) + ((v[4] + v[5]) + (v[6] + v[7]));
    const float mean = wave_sum(s2.x + s2.y) * (1.0f / D_);
    f32x2 q2 = {0.f, 0.f};
#pragma unroll
    for (int i = 0; i < 8; ++i) { v[i] -= mean; q2 += v[i] * v[i]; }
    const float var = wave_sum(q2.x + q2.y) * (1.0f / D_);
    const float rs = __builtin_amdgcn_rcpf(rbf(__builtin_amdgcn_sqrtf(rbf(rbf(var) + 1e-5f))));
    const float sd2 = rbf(__builtin_amdgcn_sqrtf(rbf(rbf(var * rs * rs) + 1e-5f)));
    const float g = rs * __builtin_amdgcn_rcpf(sd2);
#pragma unroll
    for (int i = 0; i < 8; ++i) {
        const f32x2 t = v[i] * g;
        if (i < 4) oa[i] = pack2(t.x, t.y); else ob[i - 4] = pack2(t.x, t.y);
    }
    return (f32x4){mean, rs, sd2, 0.f};
}

#define MFMA32(a, b, c) __builtin_amdgcn_mfma_f32_32x32x16_bf16(a, b, c, 0, 0, 0)
__device__ __forceinline__ int attn_dep(int hd, int qb, int lane) {
    const int which = lane >> 2, st = 2 * qb - 2 + (lane & 3), p = hd >> 1;
    if (lane >= 12 || st < 0 || (which == 0 && (lane & 3) < 2)) return -1;
    return FL_Q + st * 32 + ((p & ~3) | ((p - which) & 3));
}
template <int WHICH> __device__ __forceinline__ void attn_load(const Params& P, unsigned dst, int hd, int qb, int w, int lane) {
    const int row = WHICH == 0 ? qb * 256 + w * 64 : qb * 256 - ((qb == 0 && w < 2) ? 0 : 256) + w * 128;
    const char* g = (const char*)(P.qkv() + ((size_t)(WHICH * NH + hd) * S_ + row) * 32);
    const unsigned off = (unsigned)((lane >> 2) * 64 + (WHICH == 2 ? lane & 3 : (lane & 3) ^ ((lane >> 4) & 3)) * 16);
    dma16x4(g, off, dst + w * (WHICH == 0 ? 4096 : 8192));
    if (WHICH != 0) dma16x4(g + 4096, off, dst + w * 8192 + 4096);
}
#define PIN(x) asm volatile("" : "+v"(x))
__device__ __forceinline__ float max16(const f32x16& v) {
    float a = __builtin_fmaxf(__builtin_fmaxf(v[0], v[1]), v[2]), b = __builtin_fmaxf(__builtin_fmaxf(v[3], v[4]), v[5]);
    a = __builtin_fmaxf(__builtin_fmaxf(a, v[6]), v[7]); b = __builtin_fmaxf(__builtin_fmaxf(b, v[8]), v[9]);
    a = __builtin_fmaxf(__builtin_fmaxf(a, v[10]), v[11]); b = __builtin_fmaxf(__builtin_fmaxf(b, v[12]), v[13]);
    a = __builtin_fmaxf(__builtin_fmaxf(a, v[14]), v[15]);
    return __builtin_fmaxf(a, b);
}
__device__ __forceinline__ float attn_scores(LAS char* buf, LAS char* qbuf, int nbad, int wid, int lane, unsigned up, f32x16 (&sc)[8]) {
    const int r32 = lane & 31, hh = lane >> 5, sw = (r32 >> 2) & 3;
    LAS char* k0p = buf + (wid * 32 + r32) * 64 + ((hh ^ sw) * 16);
    LAS char* k1p = buf + (wid * 32 + r32) * 64 + (((2 + hh) ^ sw) * 16);
    const bf16x8 qf0 = *(LAS bf16x8*)(qbuf + (wid * 32 + r32) * 64 + ((hh ^ sw) * 16));
    const bf16x8 qf1 = *(LAS bf16x8*)(qbuf + (wid * 32 + r32) * 64 + (((2 + hh) ^ sw) * 16));
    bf16x8 ka[9], kb[9];
#pragma unroll
    for (int i = 0; i < 3; ++i) { const int j = i == 0 ? 8 : i - 1; ka[i] = *(LAS bf16x8*)(k0p + j * 2048); kb[i] = *(LAS bf16x8*)(k1p + j * 2048); SCHED0(); }
    const float NEG = -INFINITY;
    const f32x16 zero = {};
    const float b0 = nbad > 0 ? NEG : 0.f;
    f32x16 s8;
    float m = NEG;
#pragma unroll
    for (int i = 0; i < 11; ++i) {
        if (i + 3 < 9) { ka[i + 3] = *(LAS bf16x8*)(k0p + (i + 2) * 2048); kb[i + 3] = *(LAS bf16x8*)(k1p + (i + 2) * 2048); }
        if (i < 9) {
            const f32x16 t = MFMA32(kb[i], qf1, MFMA32(ka[i], qf0, zero));
            if (i == 0) s8 = t; else sc[i - 1] = t;
        }
        SCHED0();
        const int j = i - 3;
        if (j == 0) {
#pragma unroll
            for (int r = 0; r < 16; ++r) {
                const unsigned mk = (unsigned)__builtin_amdgcn_sbfe((int)up, (r & 3) + 8 * (r >> 2), 1);
                sc[0][r] = __uint_as_float(__builtin_amdgcn_bitop3_b32(mk, __float_as_uint(sc[0][r] + b0), __float_as_uint(s8[r]), 0xca));
            }
        }
        if (j >= 0) { const float mj = max16(sc[j]); m = __builtin_fmaxf(m, (j > 0 && j < nbad) ? NEG : mj); PIN(m); }
        SCHED0();
    }
    auto rr = __builtin_amdgcn_permlane32_swap(__float_as_uint(m), __float_as_uint(m), false, false);
    return __builtin_fmaxf(__uint_as_float(rr[0]), __uint_as_float(rr[1]));
}
struct PvState { f32x2 l0, l1; f32x16 o; bf16x8 vf[2], v8[2]; u32x4 pw[2], p8[2]; };
__device__ __forceinline__ bf16x8 v_read(LAS char* vp, int j, int s) {
    const bf16x4 lo = __builtin_amdgcn_ds_read_tr16_b64_v4bf16((LAS bf16x4*)(vp + j * 2048 + s * 1024));
    const bf16x4 hi = __builtin_amdgcn_ds_read_tr16_b64_v4bf16((LAS bf16x4*)(vp + j * 2048 + s * 1024 + 512));
    return __builtin_shufflevector(lo, hi, 0, 1, 2, 3, 4, 5, 6, 7);
}
template <int J0, int J1> __device__ __forceinline__ void attn_pv(LAS char* buf, int nbad, int wid, int lane, unsigned up, f32x16 (&sc)[8], float m, PvState& S) {
    const int hh = lane >> 5;
    LAS char* vp = buf + (wid * 32 + 4 * hh + ((lane & 15) >> 2)) * 64 + ((lane >> 4) & 1) * 32 + (lane & 3) * 8;
#pragma unroll
    for (int j = J0; j < J1; ++j) {
        bf16x8 vn[2] = {v_read(vp, j, 0), v_read(vp, j, 1)};
        if (j == 0) { S.v8[0] = v_read(vp, 8, 0); S.v8[1] = v_read(vp, 8, 1); }
        const float mj = (j > 0 && j < nbad) ? INFINITY : m;
        f32x2 p[8];
#pragma unroll
        for (int i = 0; i < 8; ++i) p[i] = (f32x2){sc[j][2 * i], sc[j][2 * i + 1]} - mj;
        SCHED0();
#pragma unroll
        for (int g = 0; g < 4; ++g) {
#pragma unroll
            for (int i = 2 * g; i < 2 * g + 2; ++i) { p[i].x = __builtin_amdgcn_exp2f(p[i].x); p[i].y = __builtin_amdgcn_exp2f(p[i].y); PIN(p[i]); }
            SCHED0();
            if (j > 0 && (g & 1) == 0) S.o = MFMA32(S.vf[g >> 1], __builtin_bit_cast(bf16x8, S.pw[g >> 1]), S.o);
            if (j == 1 && (g & 1) == 1) S.o = MFMA32(S.v8[g >> 1], __builtin_bit_cast(bf16x8, S.p8[g >> 1]), S.o);
            SCHED0();
        }
#pragma unroll
        for (int i = 0; i < 8; ++i) {
            S.pw[i >> 2][i & 3] = pack2(p[i].x, p[i].y);
            if (i & 1) S.l1 += p[i]; else S.l0 += p[i];
        }
        if (j == 0) {
#pragma unroll
            for (int i = 0; i < 8; ++i) {
                const int kr = ((2 * i) & 3) + 8 * (i >> 1);
                const unsigned mk = __builtin_amdgcn_bitop3_b32(0xffffu, (unsigned)__builtin_amdgcn_sbfe((int)up, kr, 1), (unsigned)__builtin_amdgcn_sbfe((int)up, kr + 1, 1), 0xca);
                S.p8[i >> 2][i & 3] = S.pw[i >> 2][i & 3] & ~mk; S.pw[i >> 2][i & 3] &= mk;
            }
        }
        S.vf[0] = vn[0]; S.vf[1] = vn[1];
        PIN(S.l0); PIN(S.l1); PIN(S.pw[0]); PIN(S.pw[1]);
        SCHED0();
    }
    if (J1 == 8) {
        S.o = MFMA32(S.vf[0], __builtin_bit_cast(bf16x8, S.pw[0]), S.o);
        S.o = MFMA32(S.vf[1], __builtin_bit_cast(bf16x8, S.pw[1]), S.o);
    }
}
__device__ __forceinline__ void attn_out(const Params& P, int hd, int qblk, int lane, f32x2 l2, const f32x16& oacc) {
    const int r32 = lane & 31, hh = lane >> 5;
    float l = l2.x + l2.y;
    {
        auto rr = __builtin_amdgcn_permlane32_swap(__float_as_uint(l), __float_as_uint(l), false, false);
        l = __uint_as_float(rr[0]) + __uint_as_float(rr[1]);
    }
    const float inv = __builtin_amdgcn_rcpf(l);
    unsigned w[8];
#pragma unroll
    for (int g = 0; g < 4; ++g) { w[2 * g] = pack2(oacc[4 * g] * inv, oacc[4 * g + 1] * inv); w[2 * g + 1] = pack2(oacc[4 * g + 2] * inv, oacc[4 * g + 3] * inv); }
    const __amdgpu_buffer_rsrc_t rs = rsrc(P.o(), (unsigned)S_ * D_ * 2);
    const unsigned off = (unsigned)((hd * (S_ / 16) + qblk * 2 + (r32 >> 4)) * 1024 + (hh * 16 + (r32 & 15)) * 16);
#pragma unroll
    for (int gp = 0; gp < 2; ++gp) {
        auto a = __builtin_amdgcn_permlane32_swap(w[4 * gp], w[4 * gp + 2], false, false);
        auto b = __builtin_amdgcn_permlane32_swap(w[4 * gp + 1], w[4 * gp + 3], false, false);
        __builtin_amdgcn_raw_buffer_store_b128((u32x4){a[0], b[0], a[1], b[1]}, rs, off + gp * 512, 0, SC1);
    }
}

extern "C" __global__ __launch_bounds__(512, 2) void k_main(const Params P) {
    extern __shared__ __attribute__((aligned(16))) char smem[];
    LAS char* lds = (LAS char*)smem;
    const int tid = threadIdx.x, lane = tid & 63, wid = __builtin_amdgcn_readfirstlane(tid >> 6);
    const int grp = blockIdx.x & 7, idx = blockIdx.x >> 3;
    const bool loader = wid >= 4;
    const int lw = wid & 3, wm = (wid >> 1) & 1, wn = wid & 1;
    const unsigned ring0 = (unsigned)(uintptr_t)lds, ringl = ring0 + lw * PIECE;
    const int i16 = idx & 15;
    const bool isM = idx < 16;
    const int fm = (grp >> 1) * 4 + (isM ? i16 & 3 : i16 >> 2), fn = (grp & 1) * 4 + (isM ? i16 >> 2 : i16 & 3);
    const int um = (grp & 1) * 4 + (i16 >> 2), un = (grp >> 1) * 4 + (i16 & 3);
    const int hd = (grp & 1) * 8 + (i16 & 7), qb = (grp >> 1) * 2 + (i16 >> 3);
    STAMP_DECL;
    STAMP(1, 0);
#define PFC 1
    if (PFC && wid == 0) {
        u64 pc; unsigned rel, len;
        asm volatile("s_getpc_b64 %0\n1:\n\ts_mov_b32 %1, 1b-k_main\n\ts_mov_b32 %2, 2f-k_main\n\t.subsection 1\n2:\n\t.subsection 0" : "=s"(pc), "=s"(rel), "=s"(len));
        const unsigned off = (unsigned)((idx + 32 * (lane & 15)) * 128);
        dma4((const char*)(pc - rel), off < len ? off : 0u, ring0 + LDS_SINK);
    }
    u64 tag;
    asm volatile("global_load_dwordx2 %0, %1, off sc1" : "=v"(tag) : "v"((GAS u64*)P.flags() + FL_E) : "memory");

    if (tid < 8) *(LAS unsigned*)(lds + (isM ? LDS_MC : LDS_CTL) + tid * 4) = 0u;
#define PF3 1
    auto warm = [&](int r) {
        const int n = ((r * 16 + i16) * 4 + lw) * 7 + (lane < 56 ? lane >> 3 : 6), row = n * 2 + (grp & 1);
        int f = row + 896 * (grp >> 1); f = f >= F_ ? f - F_ : f;
        if (r < PF3) dma4((const char*)P.w2 + (grp >> 1) * 1024, (unsigned)(f * (S_ * 2) + (lane & 7) * 128), ring0 + LDS_SINK);
    };
    if (isM) {
        constexpr int NK = S_ / 64, SB = SB_M;
        const int pg = (grp >> 1) + 4 * (grp & 1);
        const int tm = um, tn = un, mi = pg * 16 + i16;
        const char* gm = (const char*)P.h() + tm * 256; const char* gn = (const char*)P.w1 + tn * 448;
        const int cls = (tm + tn) & 3;
        auto kof = [&](int u) { const int p0 = u - ((u - cls) & 3); return __builtin_amdgcn_readfirstlane((((p0 == 1 || p0 == 2) ? u : korder(u, cls, NK)) + 4 * pg) & (NK - 1)); };
        Loader<4, 7> L;
        {
            const int row = mi * 16 + wid * 2;
            const u32x4* px = (const u32x4*)(P.x + (size_t)row * D_);
            u32x4 a0 = px[lane], b0 = px[64 + lane], a1 = px[128 + lane], b1 = px[192 + lane];
            SCHED0();
            if (loader) {
                const unsigned c0 = (tn & 1) * 64;
                L.init_m<TR>(lw, lane, D_ * 2);
                L.init_n<TR>(lw, lane, F_ * 2, c0, c0 + 128, c0 + 256, (tn & 1) ? 0 : 384);
            }
            asm volatile("" : "+v"(a0), "+v"(b0), "+v"(a1), "+v"(b1));
            if (loader) L.issue_n<false>(gn + (size_t)kof(0) * (64 * F_ * 2), ringl);
            STAMPL(0, 0);
            const __amdgpu_buffer_rsrc_t rh = rsrc(P.h(), (unsigned)S_ * D_ * 2);
            u32x4 oa0, ob0, oa1, ob1;
            const f32x4 s0 = ln_row(a0, b0, oa0, ob0), s1 = ln_row(a1, b1, oa1, ob1);
            __builtin_amdgcn_raw_buffer_store_b128(oa0, rh, (unsigned)(row * 2048 + lane * 16), 0, SC1);
            __builtin_amdgcn_raw_buffer_store_b128(ob0, rh, (unsigned)(row * 2048 + 1024 + lane * 16), 0, SC1);
            __builtin_amdgcn_raw_buffer_store_b128(oa1, rh, (unsigned)(row * 2048 + 2048 + lane * 16), 0, SC1);
            __builtin_amdgcn_raw_buffer_store_b128(ob1, rh, (unsigned)(row * 2048 + 3072 + lane * 16), 0, SC1);
            if (lane < 2) __builtin_amdgcn_raw_buffer_store_b128(__builtin_bit_cast(u32x4, lane ? s1 : s0), rsrc(P.stat(), S_ * 16), (unsigned)((row + lane) * 16), 0, SC1);
            SCHED0();
            STAMPL(0, 1);
        }
        if (loader) {
            WAIT_VM(0);
            BARRIER();
            STAMPL(0, 2);
            if (tid == 256 + 64) set_flag(P.flags(), FL_L + mi, tag);
#pragma unroll
            for (int u = 1; u < 3; ++u) L.issue_n<false>(gn + (size_t)kof(u) * (64 * F_ * 2), ringl + u * SB);
            if (lw == 0) poll_flags(P.flags(), lane < 16 ? FL_L + pg * 16 + lane : -1, tag);
            STAMPL(0, 3);
            BARRIER();
#pragma unroll
            for (int u = 0; u < 3; ++u) L.issue_m<true>(gm + (size_t)kof(u) * (64 * D_ * 2), ringl + u * SB);
            WAIT_VM(8); BARRIER();
            WAIT_VM(4);
            int slot = 0, t = 0;
            auto issue = [&]() {
                const int k = kof(t + 3);
                L.issue_m<true>(gm + (size_t)k * (64 * D_ * 2), ringl + slot * SB);
                L.issue_n<false>(gn + (size_t)k * (64 * F_ * 2), ringl + slot * SB);
                slot = (slot == 2) ? 0 : slot + 1;
            };
            if (lw == 0) poll_flags(P.flags(), lane < 16 ? FL_L + ((pg + 1) & 7) * 16 + lane : -1, tag);
            BARRIER(); issue(); ++t;
            WAIT_VM(L.NPW); BARRIER(); issue(); ++t;
            WAIT_VM(L.NPW);
            if (lw == 0) { poll_flags(P.flags(), FL_L + lane, tag); poll_flags(P.flags(), FL_L + 64 + lane, tag); }
            BARRIER(); issue(); ++t;
#pragma unroll 1
            for (; t < NK - 3; ++t) {
                const int k = (korder(t + 3, cls, NK) + 4 * pg) & (NK - 1);
                L.turn<true, false, 11>(gm + (size_t)k * (64 * D_ * 2), gn + (size_t)k * (64 * F_ * 2), ringl + slot * SB);
                slot = (slot == 2) ? 0 : slot + 1;
            }
            warm(0);
            WAIT_VM(L.NPW + (PF3 > 0)); BARRIER();
            WAIT_VM((PF3 > 0) + 0); BARRIER();
            BARRIER();
#pragma unroll
            for (int i = 0; i < 8; ++i)
                dma16<false, 0>((const char*)P.x + ((size_t)(fm * 128 + i * 16) * D_ + fn * 128 + lw * 32) * 2, (unsigned)((lane >> 2) * (D_ * 2) + (lane & 3) * 16), ring0 + LDS_X + (lw * 8 + i) * 1024);
            dma4((const char*)P.b2 + fm * 256, (unsigned)(lane * 4), ring0 + LDS_X + 32768);
        } else {
            const int fr = lane & 15, fq = lane >> 4;
            const int fb = tn * 224 + ((tn & 1) ? 32 : 0) + wn * 32, fh = tn * 224 + ((tn & 1) ? 0 : 192) + wn * 16;
            WAIT_VM(0);
            WAIT_LGKM0();
            BARRIER();
            f32x4 acc[4][7];
#pragma unroll
            for (int j = 0; j < 7; ++j) {
                const u32x2 bb = *(const u32x2*)(P.b1 + (j < 6 ? fb + (j >> 1) * 64 + (j & 1) * 16 : fh) + fq * 4);
                const f32x4 bv = {lo16(bb[0]), hi16(bb[0]), lo16(bb[1]), hi16(bb[1])};
#pragma unroll
                for (int i = 0; i < 4; ++i) acc[i][j] = bv;
            }
            Reader<TR, 128, TR, 224, 4, 7> rd; rd.init(ring0, lane, wm, wn);
            Regs<4, 7> R0;
            int slot = 0;
            BARRIER();
            __builtin_amdgcn_s_waitcnt(0x0070);
            BARRIER();
            STAMP(1, 1);
            rd.read(R0, 0);
            for (int t = 0; t < NK / 4; ++t) {
                stage_step<3, SB>(slot, rd, rd, R0, acc); stage_step<3, SB>(slot, rd, rd, R0, acc);
                stage_step<3, SB>(slot, rd, rd, R0, acc); stage_step<3, SB>(slot, rd, rd, R0, acc);
            }
            STAMP(1, 2);
            const __amdgpu_buffer_rsrc_t rs = rsrc(P.U(), (unsigned)D_ * F_ * 2);
            const unsigned df = (unsigned)(tm * 8 + wm * 4);
            const unsigned ob = (unsigned)((fb >> 5) * 64 + df) * 1024u + (unsigned)((((fq & 1) * 2 + (fq >> 1)) * 16 + fr) * 16);
            const unsigned oh = (unsigned)((fh >> 5) * 64 + df + (fq & 1)) * 1024u + (unsigned)(((wn * 2 + (fq >> 1)) * 16 + fr) * 16);
            LAS char* ul = lds + 128 * 132 + (wn * 8 + wm * 4) * PIECE + (((fq & 1) * 2 + (fq >> 1)) * 16 + fr) * 16;
            unsigned pk[4][7][2];
#pragma unroll
            for (int i = 0; i < 4; ++i) {
#pragma unroll
                for (int j = 0; j < 7; ++j) {
                    const f32x2 a = gelu2((f32x2){acc[i][j][0], acc[i][j][1]}), b = gelu2((f32x2){acc[i][j][2], acc[i][j][3]});
                    pk[i][j][0] = pack2(a.x, a.y); pk[i][j][1] = pack2(b.x, b.y);
                }
#pragma unroll
                for (int jp = 0; jp < 3; ++jp) {
                    const u32x4 w = pair16(pk[i][2 * jp][0], pk[i][2 * jp][1], pk[i][2 * jp + 1][0], pk[i][2 * jp + 1][1]);
                    __builtin_amdgcn_raw_buffer_store_b128(w, rs, ob + i * 1024 + jp * 2 * 65536, 0, SC1);
                    *(LAS u32x4*)(ul + jp * SB_F + i * PIECE) = w;
                }
                if (i & 1)
                    __builtin_amdgcn_raw_buffer_store_b128(pair16(pk[i - 1][6][0], pk[i - 1][6][1], pk[i][6][0], pk[i][6][1]), rs, oh + (i - 1) * 1024, 0, SC1);
                SCHED0();
            }
            STAMP(1, 3);
        }
    } else {
        constexpr int NK = D_ / 64, SB = SB_Q;
        const int tm = fm;
        if (loader) {
            Loader<4, 6> L; L.init_m<ROW>(lw, lane, D_ * 2);
            L.init_n<TR>(lw, lane, 3 * D_ * 2, qkv_head(fn, 0) * 64, qkv_head(fn, 1) * 64, qkv_head(fn, 2) * 64);
            const char* gm = (const char*)P.x + (size_t)tm * 128 * D_ * 2; const char* gn = (const char*)P.wqkv;
            const char* gh2 = (const char*)P.h() + (size_t)tm * 128 * D_ * 2;
            int slot = 0;
            auto issue = [&](int u) {
                const int k = (korder(u & (NK - 1), (tm + fn) & 3, NK) + ROTQ * (grp >> 1)) & (NK - 1);
                L.issue_m<false>(gm + k * 128, ringl + slot * SB);
                L.issue_n<false>(gn + (size_t)k * (64 * 3 * D_ * 2) + (u >= NK ? 16 * 64 : 0), ringl + slot * SB);
                slot = (slot == 2) ? 0 : slot + 1;
            };
            issue(0); issue(1);
            WAIT_VM(L.NPW); BARRIER();
            issue(2);
            int t = 0;
            WAIT_VM(L.NPW); BARRIER(); issue(3); ++t;
#pragma unroll 1
            for (int seg = 0; seg < 2; ++seg) {
                const int end = seg == 0 ? 12 : 2 * NK - 3;
#pragma unroll 1
                for (; t < end; ++t) {
                    const int u = t + 3, k = (korder(u & (NK - 1), (tm + fn) & 3, NK) + ROTQ * (grp >> 1)) & (NK - 1);
                    L.turn<true, false, 10>((u >= NK ? gh2 : gm) + k * 128, gn + (size_t)k * (64 * 3 * D_ * 2) + (u & NK) * 64, ringl + slot * SB);
                    slot = (slot == 2) ? 0 : slot + 1;
                }
                if (seg == 0 && lw == 0) {
                    poll_flags(P.flags(), lane < 8 ? FL_L + tm * 8 + lane : -1, tag);
                    dma16<true, LDS_QX>((const char*)P.stat() + tm * 2048, (unsigned)(lane * 16), ring0);
                    dma16<true, LDS_QX + 1024>((const char*)P.stat() + tm * 2048 + 1024, (unsigned)(lane * 16), ring0);
                }
            }
            poll_flags(P.flags(), attn_dep(hd, qb, lane), tag);
            WAIT_VM(0);
            BARRIER();
            attn_load<1>(P, ring0 + LDS_AK, hd, qb, lw, lane);
            attn_load<2>(P, ring0 + LDS_AV, hd, qb, lw, lane);
            BARRIER();
            attn_load<0>(P, ring0 + LDS_AQ, hd, qb, lw, lane);
            BARRIER();
        } else {
            const int fr = lane & 15, fq = lane >> 4;
            Reader<ROW, 128, TR, 192, 4, 6> rd; rd.init(ring0, lane, wm, wn);
            rd.n.b = rd.n.a + 32 - wm * 32; rd.n.a += wm * 32;
            Regs<4, 6> R0;
            int slot = 0;
            __builtin_amdgcn_s_waitcnt(0x0070);
            BARRIER();
            STAMP(1, 1);
            rd.read(R0, 0);
            const __amdgpu_buffer_rsrc_t rs = rsrc(P.qkv(), (unsigned)S_ * 3 * D_ * 2);
            const int s0 = tm * 128 + wm * 64;
            const unsigned so = (unsigned)(((s0 + fr) * 32 + ((fq & 1) ^ wm) * 16 + (fq >> 1) * 8) * 2);
            constexpr float QS = 0.17677669529663687f * 1.4426950408889634f;
            {
                f32x4 acc[5][6] = {};
                for (int t = 0; t < NK / 4; ++t) {
                    stage_step<3, SB>(slot, rd, rd, R0, acc); stage_step<3, SB>(slot, rd, rd, R0, acc);
                    stage_step<3, SB>(slot, rd, rd, R0, acc); stage_step<3, SB>(slot, rd, rd, R0, acc);
                }
                LAS char* ex = lds + LDS_QX + 2048;
                if (fr == 0) {
#pragma unroll
                    for (int jp = 0; jp < 3; ++jp) *(LAS f32x4*)(ex + wid * 256 + jp * 64 + fq * 16) = acc[4][2 * jp];
                }
                WAIT_LGKM0();
                CFENCE();
                __hip_atomic_store((LAS unsigned*)(lds + LDS_CTL + wid * 4), 1u, __ATOMIC_RELAXED, __HIP_MEMORY_SCOPE_WORKGROUP);
                f32x2 st[4];
#pragma unroll
                for (int i = 0; i < 4; ++i) st[i] = *(LAS f32x2*)(lds + LDS_QX + (wm * 64 + i * 16 + fr) * 16);
                while (__builtin_amdgcn_readfirstlane(__hip_atomic_load((LAS unsigned*)(lds + LDS_CTL + (wid ^ 2) * 4), __ATOMIC_RELAXED, __HIP_MEMORY_SCOPE_WORKGROUP)) == 0u) {}
                CFENCE();
#pragma unroll
                for (int jp = 0; jp < 3; ++jp) {
                    const f32x4 ca = acc[4][2 * jp], cb = *(LAS f32x4*)(ex + (wid ^ 2) * 256 + jp * 64 + fq * 16);
#pragma unroll
                    for (int i = 0; i < 4; ++i) {
                        const float r = st[i].y * (jp == 0 ? QS : 1.0f), mr = -st[i].x * r;
                        const f32x4 a = acc[i][2 * jp] * r + ca * mr, b = acc[i][2 * jp + 1] * r + cb * mr;
                        __builtin_amdgcn_raw_buffer_store_b128(pair16(pack2(a[0], a[1]), pack2(a[2], a[3]), pack2(b[0], b[1]), pack2(b[2], b[3])), rs,
                                                               so + (unsigned)((qkv_head(fn, jp) + wn) * (S_ * 64) + i * 1024), 0, SC1);
                    }
                }
            }
            {
                f32x4 acc[4][6] = {};
                for (int t = 0; t < NK / 4; ++t) {
                    if (__builtin_expect(t == 1 || t == 2, 0)) {
                        if (t == 1) WAIT_VM(0);
                        else if (tid == 0) set_flag(P.flags(), FL_Q + tm * 32 + fn, tag);
                    }
                    stage_step<3, SB>(slot, rd, rd, R0, acc); stage_step<3, SB>(slot, rd, rd, R0, acc);
                    stage_step<3, SB>(slot, rd, rd, R0, acc); stage_step<3, SB>(slot, rd, rd, R0, acc);
                }
                STAMP(1, 2);
#pragma unroll
                for (int i = 0; i < 4; ++i) {
                    const float sd = *(LAS float*)(lds + LDS_QX + (wm * 64 + i * 16 + fr) * 16 + 8);
#pragma unroll
                    for (int jp = 0; jp < 3; ++jp) {
                        const float r = sd * (jp == 0 ? QS : 1.0f);
                        const f32x4 a = acc[i][2 * jp] * r, b = acc[i][2 * jp + 1] * r;
                        __builtin_amdgcn_raw_buffer_store_b128(pair16(pack2(a[0], a[1]), pack2(a[2], a[3]), pack2(b[0], b[1]), pack2(b[2], b[3])), rs,
                                                               so + (unsigned)((qkv_head(fn, jp) + 16 + wn) * (S_ * 64) + i * 1024), 0, SC1);
                    }
                }
            }
            STAMP(1, 3);
        }
    }

    constexpr int SB = SB_F, NS = NS_F;
    constexpr size_t OS = 2 * (S_ / 16) * 1024, WOS = 64 * D_ * 2, W2S = 64 * S_ * 2, US = 2 * 65536;
    const int tile = fm * 8 + fn, cls = (fm + fn) & 3, nf = isM ? NF_M : NF_Q;
    const int uh = ((fm >> 1) & 1) ^ (isM ? 0 : 1);
    auto fof = [&](int i) {
        int r = korder(i - 7, cls, nf - 7) + (ROTF ? (grp & 1) * ((nf - 7) >> 1) : 0);
        r = r >= nf - 7 ? r - (nf - 7) : r;
        const int im = (fm & 1) ? (i < 3 ? i + 4 : i - 3) : i;
        const int j = i < 7 ? 7 * uh + (isM ? im : korder(i, cls, 7)) : (isM ? 14 + NA : 14) + r, k = 14 * (grp >> 1) + j;
        return __builtin_amdgcn_readfirstlane(k >= 56 ? k - 56 : k);
    };
    const char* g2 = (const char*)P.w2 + fm * 256;
    Loader<4, 4> L;
    if (loader) L.init_m<TR>(lw, lane, S_ * 2);
    auto preload_w2 = [&]() {
        int k0 = fof(0), k1 = fof(1);
        asm volatile("" : "+s"(k0), "+s"(k1));
        L.issue_m<false>(g2 + k0 * W2S, ringl); L.issue_m<false>(g2 + k1 * W2S, ringl + SB);
    };
    if (isM) {
        if (loader) { preload_w2(); L.issue_m<false>(g2 + fof(2) * W2S, ringl + 2 * SB); WAIT_VM(0); warm(1); }
        STAMP(2, 0);
        WAIT_LGKM0();
        BARRIER();
        STAMP(2, 1);
    } else {
        if (loader) { WAIT_VM(0); __builtin_amdgcn_s_setprio(1); }
        WAIT_LGKM0();
        BARRIER();
#pragma unroll 1
        for (int it = 0; it < 2; ++it) {
            STAMP(2, 3 * it);
            f32x16 sc[8];
            int ln = lane;
            asm volatile("" : "+v"(ln));
            const int dg = (ln & 31) - 4 * (ln >> 5);
            const unsigned up = dg < 0 ? ~0u : ~1u << dg;
            int nbad = qb == 0 ? 8 - wid : 0;
            asm volatile("" : "+s"(nbad));
            SCHED0();
            const float m = attn_scores(lds + (it ? LDS_BK : LDS_AK), lds + LDS_AQ, nbad, wid, ln, up, sc);
            SCHED0();
            WAIT_VM(0);
            BARRIER();
            if (tid == 0) set_flag(P.flags(), it == 0 ? FL_Q + fm * 32 + fn + 8 : FL_O + qb * 32 + hd, tag);
            if (it == 1 && loader) preload_w2();
            STAMP(2, 3 * it + 1);
            PvState S;
            S.l0 = (f32x2){0.f, 0.f}; S.l1 = S.l0; S.o = (f32x16){};
            attn_pv<0, 8>(lds + (it ? LDS_BV : LDS_AV), nbad, wid, ln, up, sc, m, S);
            attn_out(P, hd + 16 * it, qb * 8 + wid, ln, S.l0 + S.l1, S.o);
            STAMP(2, 3 * it + 2);
            if (it == 0) {
                if (loader) {
                    __builtin_amdgcn_s_setprio(0);
                    int h2 = hd + 16, q2 = qb, w2 = lw;
                    asm volatile("" : "+s"(h2), "+s"(q2), "+s"(w2));
                    poll_flags(P.flags(), attn_dep(h2, q2, ln), tag);
                    attn_load<0>(P, ring0 + LDS_AQ, h2, q2, w2, ln);
                    attn_load<1>(P, ring0 + LDS_BK, h2, q2, w2, ln);
                    attn_load<2>(P, ring0 + LDS_BV, h2, q2, w2, ln);
                    WAIT_VM(8);
                    __builtin_amdgcn_s_setprio(1);
                }
                BARRIER();
            }
        }
        WAIT_LGKM0();
    }
    const int own = isM ? 0 : 1;
    if (loader) {
        const char* go = (const char*)P.o() + fm * 8192 + own * 4096;
        const char* gw = (const char*)P.wo + fn * 256;
        const char* gu = (const char*)P.U() + fn * 8192;
        __builtin_amdgcn_s_setprio(0);
        L.init_n<FRAG>(lw, lane, US / 2);
        auto issue_f = [&](int u) {
            const int k = fof(u);
            L.issue_m<false>(g2 + k * W2S, ringl + (u & (NS - 1)) * SB); L.issue_n<true>(gu + k * US, ringl + (u & (NS - 1)) * SB);
        };
        const int ulo = isM ? ((14 + NA) * 2) / 7 : 4, uhi = isM ? 15 : ((14 + NA) * 2 - 1) / 7;
        const int udep = (lane >= ulo && lane <= uhi) ? FL_U + fn * 32 + (((grp >> 1) * 4 + lane) & 15) : -1;
        const int odep = lane < 32 ? FL_O + (fm >> 1) * 32 + lane : lane < 48 ? FL_U + fn * 32 + lane - 32 : -1, pdep = lane == 0 ? FL_P + tile * 32 + own : -1;
        u64 pk;
        if (isM) { BARRIER(); BARRIER(); }
        poll_flags(P.flags(), lane < 2 ? FL_U + fn * 32 + (grp >> 1) * 4 + 2 * uh + lane : -1, tag);
        if (isM) { issue_f(3); issue_f(4); }
        else {
#pragma unroll
            for (int u = 0; u < 2; ++u) L.issue_n<true>(gu + fof(u) * US, ringl + u * SB);
            WAIT_VM(4); BARRIER();
            issue_f(2); issue_f(3);
            WAIT_VM(16); BARRIER(); issue_f(4);
            WAIT_VM(16); BARRIER();
        }
        peek_issue(pk, P.flags(), udep); issue_f(5);
        WAIT_VM(16); BARRIER(); issue_f(6);
        WAIT_VM(16);
        int nxt = 7;
        if (!peek_ok(pk, udep, tag)) {
            bool uready = false;
#pragma unroll 1
            for (int t = 3; t < 7; ++t) {
                if (t == 6 && !uready) { poll_flags(P.flags(), udep, tag); uready = true; for (; nxt < 10; ++nxt) issue_f(nxt); }
                if (nxt - (t + 2) >= 2) WAIT_VM(16); else WAIT_VM(0);
                BARRIER();
                if (!uready) uready = flags_set(P.flags(), udep, tag);
                if (uready) for (; nxt <= t + NS; ++nxt) issue_f(nxt);
            }
        }
#pragma unroll 1
        for (; nxt < nf - 2; ++nxt) {
            int r = korder(nxt - 7, cls, nf - 7) + (grp & 1) * ((nf - 7) >> 1);
            r = r >= nf - 7 ? r - (nf - 7) : r;
            int k = 14 * (grp >> 1) + (isM ? 14 + NA : 14) + r;
            k = k >= 56 ? k - 56 : k;
            L.turn<false, true, 16>(g2 + k * W2S, gu + k * US, ringl + (nxt & (NS - 1)) * SB);
        }
        const unsigned xo = (unsigned)(((fm * 128 + (lane >> 2)) * D_ + fn * 128 + lw * 32 + (lane & 3) * 8) * 2);
        constexpr unsigned ROWS16 = 16 * D_ * 2;
        WAIT_VM(16); BARRIER(); peek_issue(pk, P.flags(), odep);
        issue_f(nf - 2);
        WAIT_VM(16); BARRIER(); issue_f(nf - 1);
        L.init_n<TR>(lw, lane, D_ * 2);
        const unsigned oo = (unsigned)(lw * 1024 + lane * 16);
        const unsigned oo2 = oo + (unsigned)(OS / 2);
        const char *po, *pw; unsigned pd;
        auto addr_w = [&](int i) {
            const int k = (korder(i, cls, KW) + ROTW * (grp >> 1)) & (KW - 1);
            po = go + k * OS; pw = gw + k * WOS; pd = ringl + ((nf + i) & (NS - 1)) * SB;
            asm volatile("" : "+s"(po), "+s"(pw), "+s"(pd));
        };
        auto issue_w = [&]() {
            asm volatile("s_add_u32 m0, %2, %3\n\ts_nop 0\n\tglobal_load_lds_dwordx4 %4, %0 sc1\n\ts_addk_i32 m0, 8448\n\ts_nop 0\n\tglobal_load_lds_dwordx4 %5, %0 sc1\n\t"
                         "s_add_u32 m0, %2, 16896\n\ts_nop 0\n\tglobal_load_lds_dwordx4 %6, %1\n\ts_addk_i32 m0, 4224\n\ts_nop 0\n\tglobal_load_lds_dwordx4 %7, %1\n\t"
                         "s_addk_i32 m0, 4224\n\ts_nop 0\n\tglobal_load_lds_dwordx4 %8, %1\n\ts_addk_i32 m0, 4224\n\ts_nop 0\n\tglobal_load_lds_dwordx4 %9, %1"
                         ::"s"(po), "s"(pw), "s"(pd), "s"(own * 4 * PIECE), "v"(oo), "v"(oo2), "v"(L.on[0]), "v"(L.on[1]), "v"(L.on[2]), "v"(L.on[3]) : "scc", "memory");
        };
#define TURN_W(i, w) do { addr_w(i); WAIT_VM(w); BARRIER(); issue_w(); } while (0)
        addr_w(0);
        WAIT_VM(16);
        if (!peek_ok(pk, odep, tag)) poll_flags(P.flags(), odep, tag);
        STAMPL(0, 4);
        BARRIER(); issue_w();
        TURN_W(1, 14); TURN_W(2, 12); TURN_W(3, 12);
        TURN_W(4, 12);
        STAMPL(0, 5);
        {
            u32x4 t0, t1, t2, t3;
            asm volatile("ds_read_b128 %0, %4\n\tds_read_b128 %1, %4 offset:1024\n\tds_read_b128 %2, %4 offset:2048\n\tds_read_b128 %3, %4 offset:3072\n\ts_waitcnt lgkmcnt(0)"
                         : "=&v"(t0), "=&v"(t1), "=&v"(t2), "=&v"(t3) : "v"(ring0 + LDS_EX + lw * 4096 + lane * 16) : "memory");
            const __amdgpu_buffer_rsrc_t ro = rsrc(P.out, (unsigned)S_ * D_ * 2);
            const unsigned so = xo + (1 - own) * 4 * ROWS16;
            __builtin_amdgcn_raw_buffer_store_b128(t0, ro, so, 0, SC1); __builtin_amdgcn_raw_buffer_store_b128(t1, ro, so + ROWS16, 0, SC1);
            __builtin_amdgcn_raw_buffer_store_b128(t2, ro, so + 2 * ROWS16, 0, SC1); __builtin_amdgcn_raw_buffer_store_b128(t3, ro, so + 3 * ROWS16, 0, SC1);
        }
        TURN_W(5, 16); TURN_W(6, 16); TURN_W(7, 16);
        TURN_W(8, 12);
        STAMPL(0, 6);
        asm volatile("global_store_dwordx2 %0, %1, off sc1" ::"v"((GAS u64*)P.flags() + FL_P + tile * 32 + 1 - own), "v"(tag + 1ull) : "memory");
        TURN_W(9, 13); TURN_W(10, 13); TURN_W(11, 13);
        TURN_W(12, 12); TURN_W(13, 12); TURN_W(14, 12);
        STAMPL(0, 7);
        TURN_W(15, 12);
#undef TURN_W
        peek_issue(pk, P.flags(), pdep);
        WAIT_VM(13); BARRIER();
        WAIT_VM(7); BARRIER();
        WAIT_VM(0);
        auto load_partial = [&]() {
#pragma unroll
            for (int i = 0; i < 4; ++i) dma16<true, 0>((const char*)P.out + (own * 4 + i) * ROWS16, xo, ring0 + LDS_EX + (lw * 4 + i) * 1024);
        };
        const bool early = peek_ok(pk, pdep, tag);
        if (early) load_partial();
        BARRIER();
        BARRIER();
        if (!early) { poll_flags(P.flags(), pdep, tag); load_partial(); }
        WAIT_VM(0); BARRIER();
        if (blockIdx.x == 0 && lw == 0) {
#pragma unroll 1
            for (int i = 0; i < 4; ++i) poll_flags(P.flags(), FL_O + i * 64 + lane, tag);
            if (lane == 0) set_flag(P.flags(), FL_E, tag);
        }
    } else {
        const __amdgpu_buffer_rsrc_t ro = rsrc(P.out, (unsigned)S_ * D_ * 2);
        f32x4 acc[4][4];
        if (isM) {
            LAS char* xp = lds + LDS_X + (wn * 16 + wm * 4) * 1024 + (lane & 15) * 64 + (((lane >> 4) & 1) * 2 + (lane >> 5)) * 16;
            u32x4 xv[4][2]; unsigned bv[4];
#pragma unroll
            for (int i = 0; i < 4; ++i) {
                xv[i][0] = *(LAS u32x4*)(xp + i * 1024); xv[i][1] = *(LAS u32x4*)(xp + 8192 + i * 1024);
                bv[i] = *(LAS u16*)(lds + LDS_X + 32768 + (wm * 64 + i * 16 + (lane & 15)) * 2);
            }
            SCHED0();
#pragma unroll
            for (int i = 0; i < 4; ++i)
#pragma unroll
                for (int jp = 0; jp < 2; ++jp) {
                    auto e0 = __builtin_amdgcn_permlane16_swap(xv[i][jp][0], xv[i][jp][2], false, false);
                    auto e1 = __builtin_amdgcn_permlane16_swap(xv[i][jp][1], xv[i][jp][3], false, false);
                    acc[i][2 * jp] = (f32x4){lo16(e0[0]), hi16(e0[0]), lo16(e1[0]), hi16(e1[0])} + lo16(bv[i]);
                    acc[i][2 * jp + 1] = (f32x4){lo16(e0[1]), hi16(e0[1]), lo16(e1[1]), hi16(e1[1])} + lo16(bv[i]);
                }
            pin_acc<4, 4, 4>(acc);
            STAMP(2, 2);
            WAIT_VM(0);
            if (lane == 0 && __hip_atomic_fetch_add((LAS unsigned*)(lds + LDS_MC), 1u, __ATOMIC_RELAXED, __HIP_MEMORY_SCOPE_WORKGROUP) == 3u)
                set_flag(P.flags(), FL_U + um * 32 + un, tag);
        } else {
            float z = 0.f;
            asm volatile("" : "+v"(z));
#pragma unroll
            for (int i = 0; i < 4; ++i)
#pragma unroll
                for (int j = 0; j < 4; ++j) acc[i][j] = (f32x4){z, z, z, z};
        }
        pin_acc<4, 4, 4>(acc);
        Reader<TR, 128, FRAG, 128, 4, 4> rf;
        HalfReader rw;
        Regs<4, 4> R0;
        LAS char* exl = lds + LDS_EX + wn * 8192 + lane * 16;
        int slot = 0;
#define STEP(a, b) stage_step<NS, SB>(slot, a, b, R0, acc)
        rf.init(ring0, lane, wm, wn);
        WAIT_LGKM0();
        if (!isM) BARRIER();
        STAMP(3, 1);
        rf.read(R0, 0);
        for (int t = 0; t < (nf - 2) / 4; ++t) {
            if (__builtin_expect(!isM && (t == 1 || t == 2), 0)) {
                if (t == 1) WAIT_VM(0);
                else if (tid == 0) set_flag(P.flags(), FL_O + qb * 32 + hd + 16, tag);
            }
            STEP(rf, rf); STEP(rf, rf); STEP(rf, rf); STEP(rf, rf);
        }
        static_assert((NF_M & 3) == (NF_Q & 3), "NA must be odd");
#pragma unroll
        for (int t = 0; t < ((NF_M - 2) & 3); ++t) STEP(rf, rf);
        STEP(rf, rf);
        const int kq = wm != own ? 1 : 0;
        rw.init(ring0 + slot * SB, lane, own, kq, wn);
        STEP(rf, rw);
        STAMP(3, 4);
        if (kq) {
#pragma unroll
            for (int i = 0; i < 4; ++i)
#pragma unroll
                for (int jp = 0; jp < 2; ++jp) {
                    const f32x4 a = acc[i][2 * jp], b = acc[i][2 * jp + 1];
                    *(LAS u32x4*)(exl + jp * 4096 + i * 1024) = (u32x4){pack2(a[0], a[1]), pack2(a[2], a[3]), pack2(b[0], b[1]), pack2(b[2], b[3])};
                    acc[i][2 * jp] = (f32x4){0.f, 0.f, 0.f, 0.f}; acc[i][2 * jp + 1] = (f32x4){0.f, 0.f, 0.f, 0.f};
                }
        }
        pin_acc<4, 4, 4>(acc);
        Regs<4, 4> R1;
        for (int t = 0; t < 4; ++t) {
            half_step<NS, SB>(slot, rw, R0, R1, acc); half_step<NS, SB>(slot, rw, R1, R0, acc);
            half_step<NS, SB>(slot, rw, R0, R1, acc); half_step<NS, SB>(slot, rw, R1, R0, acc);
        }
        STAMP(3, 2);
#pragma unroll
        for (int i = 0; i < 2; ++i)
#pragma unroll
            for (int j = 0; j < 4; ++j) *(LAS f32x4*)(lds + (wid * 8 + i * 4 + j) * 1024 + lane * 16) = acc[2 + i][j];
        WAIT_LGKM0();
        BARRIER();
        const unsigned fo = (unsigned)(((fm * 128 + own * 64 + kq * 32 + (lane & 15)) * D_ + fn * 128 + wn * 64 + ((lane >> 4) & 1) * 16 + (lane >> 5) * 8) * 2);
#pragma unroll
        for (int i = 0; i < 2; ++i)
#pragma unroll
            for (int jp = 0; jp < 2; ++jp) {
                const u32x4 e = *(LAS u32x4*)(exl + jp * 4096 + (kq * 2 + i) * 1024);
                const f32x4 a = acc[i][2 * jp] + *(LAS f32x4*)(lds + ((wid ^ 2) * 8 + i * 4 + 2 * jp) * 1024 + lane * 16) + (f32x4){lo16(e[0]), hi16(e[0]), lo16(e[1]), hi16(e[1])};
                const f32x4 b = acc[i][2 * jp + 1] + *(LAS f32x4*)(lds + ((wid ^ 2) * 8 + i * 4 + 2 * jp + 1) * 1024 + lane * 16) + (f32x4){lo16(e[2]), hi16(e[2]), lo16(e[3]), hi16(e[3])};
                __builtin_amdgcn_raw_buffer_store_b128(pair16(pack2(a[0], a[1]), pack2(a[2], a[3]), pack2(b[0], b[1]), pack2(b[2], b[3])), ro, fo + i * (16 * D_ * 2) + jp * 64, 0, SC1);
            }
        STAMP(3, 3);
#undef STEP
    }
    STAMP_OUT();
}

extern "C" void kernel_launch(void* const* d_in, const int* in_sizes, int n_in, void* d_out, int out_size, void* d_ws, size_t ws_size, hipStream_t stream) {
    char* ws = (char*)d_ws;
    Params P;
    P.x = (const u16*)d_in[0]; P.wqkv = (const u16*)d_in[1]; P.wo = (const u16*)d_in[2];
    P.w1 = (const u16*)d_in[4]; P.b1 = (const u16*)d_in[5]; P.w2 = (const u16*)d_in[6]; P.b2 = (const u16*)d_in[7];
    P.out = (u16*)d_out; P.ws = ws;
    (void)hipFuncSetAttribute((const void*)k_main, hipFuncAttributeMaxDynamicSharedMemorySize, LDS_BYTES);
    k_main<<<256, 512, LDS_BYTES, stream>>>(P);
}
```

```cpp
#include <hip/hip_runtime.h>
#include <hip/hip_bf16.h>
#include <stdint.h>

#define LAS __attribute__((address_space(3)))
#define GAS __attribute__((address_space(1)))
typedef __bf16 bf16x8 __attribute__((ext_vector_type(8)));
typedef __bf16 bf16x4 __attribute__((ext_vector_type(4)));
typedef __bf16 bf16x2 __attribute__((ext_vector_type(2)));
typedef float f32x2 __attribute__((ext_vector_type(2)));
typedef float f32x4 __attribute__((ext_vector_type(4)));
typedef float f32x16 __attribute__((ext_vector_type(16)));
typedef unsigned u32x4 __attribute__((ext_vector_type(4)));
typedef unsigned u32x2 __attribute__((ext_vector_type(2)));
typedef unsigned short u16;
typedef unsigned long long u64;

constexpr int S_ = 2048, D_ = 1024, F_ = 3584, NH = 32;

constexpr size_t WS_FLAGS = 0;
constexpr size_t WS_STAT  = 1 << 16;
constexpr size_t WS_QKV   = 1 << 17;
constexpr size_t WS_U     = WS_QKV + (size_t)S_ * 3 * D_ * 2;
constexpr int FL_E = 0;
constexpr int FL_L = 32;
constexpr int FL_U = 8 * 32;
constexpr int FL_Q = 16 * 32;
constexpr int FL_O = 32 * 32;
constexpr int FL_P = 40 * 32;

#define WAIT_VM(n) asm volatile("s_waitcnt vmcnt(%0)" ::"n"(n) : "memory")
#define WAIT_LGKM0() __builtin_amdgcn_s_waitcnt(0xC07F)
#define CFENCE() asm volatile("" ::: "memory")
#define BARRIER() do { CFENCE(); __builtin_amdgcn_s_barrier(); CFENCE(); } while (0)
#define SCHED0() __builtin_amdgcn_sched_barrier(0)
#define RLX_AGENT __ATOMIC_RELAXED, __HIP_MEMORY_SCOPE_AGENT
constexpr int SC1 = 16;

__device__ __forceinline__ float bf2f(u16 v) { return __uint_as_float((unsigned)v << 16); }
__device__ __forceinline__ float lo16(unsigned v) { return __uint_as_float(v << 16); }
__device__ __forceinline__ float hi16(unsigned v) { return __uint_as_float(v & 0xffff0000u); }
__device__ __forceinline__ unsigned pack2(float lo, float hi) {
    f32x2 v = {lo, hi};
    bf16x2 b = __builtin_convertvector(v, bf16x2);
    return __builtin_bit_cast(unsigned, b);
}
__device__ __forceinline__ float rbf(float x) { return __uint_as_float(pack2(x, 0.f) << 16); }
template <int CTRL, int RMASK> __device__ __forceinline__ float dpp_add(float v) {
    return v + __builtin_bit_cast(float, __builtin_amdgcn_update_dpp(0, __builtin_bit_cast(int, v), CTRL, RMASK, 0xf, false));
}
__device__ __forceinline__ float wave_sum(float v) {
    v = dpp_add<0x111, 0xf>(v); v = dpp_add<0x112, 0xf>(v); v = dpp_add<0x114, 0xf>(v); v = dpp_add<0x118, 0xf>(v);
    v = dpp_add<0x142, 0xa>(v); v = dpp_add<0x143, 0xc>(v);
    return __builtin_bit_cast(float, __builtin_amdgcn_readlane(__builtin_bit_cast(int, v), 63));
}
__device__ __forceinline__ __amdgpu_buffer_rsrc_t rsrc(const void* p, unsigned bytes) { return __builtin_amdgcn_make_buffer_rsrc((void*)p, 0, bytes, 0x00020000); }

enum { ROW = 0, TR = 1, FRAG = 2 };
constexpr int PIECE = 1056, BLK = 8 * PIECE;
template <int T, int X> struct Op {
    static constexpr int BYTES = X * 132;
    __device__ static __forceinline__ unsigned src_off(int blk, int p, int lane, unsigned ld, unsigned cb) {
        const int pos = lane >> 3;
        if (T == TR) {
            if (X == 224 && blk == 3) {
                const int ps = lane >> 2, g = (p & 1) | ((ps >> 3) << 1), r = 32 * (p >> 1) + 8 * g + 4 * ((ps >> 2) & 1) + (ps & 3);
                return (unsigned)r * ld + cb + (unsigned)((lane & 3) * 16);
            }
            const int g = (p >> 2) | (pos & 2), r = 32 * (pos >> 2) + 8 * g + 4 * (pos & 1) + (p & 3);
            return (unsigned)r * ld + cb + (unsigned)((lane & 7) * 16);
        } else if (T == ROW) {
            const int j = ((pos >> 1) & 1) | ((pos & 1) << 1) | (pos & 4);
            return (unsigned)(blk * 64 + j * 8 + p) * ld + (unsigned)((lane & 7) * 16);
        } else {
            return (unsigned)blk * ld + (unsigned)(p * 1024 + lane * 16);
        }
    }
};
template <bool SC, int IMM> __device__ __forceinline__ void dma16(const char* base, unsigned off, unsigned l) {
    if (SC) asm volatile("s_add_u32 m0, %2, %3\n\ts_nop 0\n\tglobal_load_lds_dwordx4 %0, %1 sc1" ::"v"(off), "s"(base), "s"(l), "n"(IMM) : "scc", "memory");
    else    asm volatile("s_add_u32 m0, %2, %3\n\ts_nop 0\n\tglobal_load_lds_dwordx4 %0, %1" ::"v"(off), "s"(base), "s"(l), "n"(IMM) : "scc", "memory");
}
#define DMA_FIRST(sc) "s_add_u32 m0, %1, %2\n\ts_nop 0\n\tglobal_load_lds_dwordx4 %3, %0" sc
#define DMA_NEXT(n, sc) "\n\ts_addk_i32 m0, 4224\n\ts_nop 0\n\tglobal_load_lds_dwordx4 %" #n ", %0" sc
#define DMA_G4(sc) DMA_FIRST(sc) DMA_NEXT(4, sc) DMA_NEXT(5, sc) DMA_NEXT(6, sc)
template <bool SC, int IMM, int N> __device__ __forceinline__ void dma_group(const char* base, const unsigned (&o)[N], unsigned l) {
    static_assert(4 * PIECE == 4224 && (N == 4 || ((N == 6 || N == 7) && !SC)), "dma_group");
    if constexpr (N == 4 && SC) asm volatile(DMA_G4(" sc1") ::"s"(base), "s"(l), "n"(IMM), "v"(o[0]), "v"(o[1]), "v"(o[2]), "v"(o[3]) : "scc", "memory");
    else if constexpr (N == 4) asm volatile(DMA_G4("") ::"s"(base), "s"(l), "n"(IMM), "v"(o[0]), "v"(o[1]), "v"(o[2]), "v"(o[3]) : "scc", "memory");
    else if constexpr (N == 6) asm volatile(DMA_G4("") DMA_NEXT(7, "") DMA_NEXT(8, "") ::"s"(base), "s"(l), "n"(IMM), "v"(o[0]), "v"(o[1]), "v"(o[2]), "v"(o[3]), "v"(o[4]), "v"(o[5]) : "scc", "memory");
    else asm volatile(DMA_G4("") DMA_NEXT(7, "") DMA_NEXT(8, "") DMA_NEXT(9, "") ::"s"(base), "s"(l), "n"(IMM), "v"(o[0]), "v"(o[1]), "v"(o[2]), "v"(o[3]), "v"(o[4]), "v"(o[5]), "v"(o[6]) : "scc", "memory");
}
__device__ __forceinline__ void dma4(const char* base, unsigned off, unsigned l) {
    asm volatile("s_mov_b32 m0, %2\n\ts_nop 0\n\tglobal_load_lds_dword %0, %1" ::"v"(off), "s"(base), "s"(l) : "memory");
}
__device__ __forceinline__ void dma16x4(const char* base, unsigned off, unsigned l) {
    asm volatile("s_mov_b32 m0, %2\n\ts_nop 0\n\tglobal_load_lds_dwordx4 %0, %1 sc1\n\tglobal_load_lds_dwordx4 %0, %1 offset:1024 sc1\n\t"
                 "global_load_lds_dwordx4 %0, %1 offset:2048 sc1\n\tglobal_load_lds_dwordx4 %0, %1 offset:3072 sc1" ::"v"(off), "s"(base), "s"(l) : "memory");
}

template <int T, int X> struct Addr {
    unsigned a, b;
    static constexpr int NV = (X == 224 || X == 192) ? 2 : 1;
    __device__ __forceinline__ void init(unsigned base, int lane, int w) {
        const int g = lane >> 4;
        b = 0;
        if (T == TR) {
            const int q = (lane & 15) >> 2, p = lane & 3;
            a = base + (unsigned)((q | ((g & 1) << 2)) * PIECE + (g >> 1) * 256 + p * 8 + (X == 128 ? w * BLK : w * 64));
            b = base + (unsigned)(3 * BLK + (g & 1) * PIECE + (g >> 1) * 512 + q * 64 + w * 32 + p * 8);
        } else if (T == ROW) {
            const int r = lane & 15;
            a = base + (unsigned)(w * BLK + (r & 7) * PIECE + (r >> 3) * 256 + g * 16);
        } else {
            a = base + (unsigned)(w * 4 * PIECE + lane * 16);
        }
    }
    __device__ __forceinline__ void step(unsigned d) { a += d; if (NV == 2) b += d; }
    __device__ __forceinline__ bf16x8 read(int n, int kk) const {
        if (T == TR) {
            LAS char* p; int t;
            if (X == 224 && n == 6) { p = (LAS char*)(uintptr_t)b + kk * 2 * PIECE; t = 256; }
            else if (X == 192) { p = (LAS char*)(uintptr_t)((n & 1) ? b : a) + kk * 512 + (n >> 1) * BLK; t = 128; }
            else { p = (LAS char*)(uintptr_t)a + kk * 512 + (X == 128 ? n * 32 : (n >> 1) * BLK + (n & 1) * 32); t = 128; }
            const bf16x4 lo = __builtin_amdgcn_ds_read_tr16_b64_v4bf16((LAS bf16x4*)p);
            const bf16x4 hi = __builtin_amdgcn_ds_read_tr16_b64_v4bf16((LAS bf16x4*)(p + t));
            return __builtin_shufflevector(lo, hi, 0, 1, 2, 3, 4, 5, 6, 7);
        } else if (T == ROW) {
            return *(LAS bf16x8*)((LAS char*)(uintptr_t)a + (n & 1) * 128 + (n >> 1) * 512 + kk * 64);
        } else {
            return *(LAS bf16x8*)((LAS char*)(uintptr_t)a + kk * BLK + n * PIECE);
        }
    }
};
template <int FM, int FN> struct Regs { bf16x8 m[FM], n[FN]; };
#define MFMA16(a, b, c) __builtin_amdgcn_mfma_f32_16x16x32_bf16(a, b, c, 0, 0, 0)

template <int TM, int BM, int TN, int BN, int FM, int FN> struct Reader {
    Addr<TM, BM> m; Addr<TN, BN> n;
    static constexpr int NDS = FM * (TM == TR ? 2 : 1) + FN * (TN == TR ? 2 : 1), NV = Addr<TM, BM>::NV + Addr<TN, BN>::NV;
    __device__ __forceinline__ void init(unsigned slot_base, int lane, int wm, int wn) { m.init(slot_base, lane, wm); n.init(slot_base + BM * 132, lane, wn); }
    __device__ __forceinline__ void read(Regs<FM, FN>& R, int kk) const {
#pragma unroll
        for (int i = 0; i < FM; ++i) R.m[i] = m.read(i, kk);
#pragma unroll
        for (int j = 0; j < FN; ++j) R.n[j] = n.read(j, kk);
    }
    __device__ __forceinline__ void step(unsigned d) { m.step(d); n.step(d); }
};
template <int AM, int FM, int FN> __device__ __forceinline__ void mma(f32x4 (&acc)[AM][FN], const Regs<FM, FN>& R) {
    const u32x4 one = {0x3f803f80u, 0x3f803f80u, 0x3f803f80u, 0x3f803f80u};
#pragma unroll
    for (int j = 0; j < FN; ++j) {
        if (AM > FM && !(j & 1)) acc[AM - 1][j] = MFMA16(R.n[j], __builtin_bit_cast(bf16x8, one), acc[AM - 1][j]);
#pragma unroll
        for (int ii = 0; ii < FM; ++ii) { const int i = (j & 1) ? FM - 1 - ii : ii; acc[i][j] = MFMA16(R.n[j], R.m[i], acc[i][j]); }
    }
}
template <int AM, int FM, int FN> __device__ __forceinline__ void pin_acc(f32x4 (&acc)[AM][FN]) {
#pragma unroll
    for (int j = 0; j < FN; ++j) {
        asm volatile("" : "+v"(acc[0][j]), "+v"(acc[1][j]), "+v"(acc[2][j]), "+v"(acc[3][j]));
        if (AM > FM && !(j & 1)) asm volatile("" : "+v"(acc[AM - 1][j]));
    }
}
template <int NDS, int NV, int NMF> __device__ __forceinline__ void interleave() {
#pragma unroll
    for (int i = 0; i < NMF; ++i) {
        __builtin_amdgcn_sched_group_barrier(0x8, 1, 0);
        if (i < NDS) __builtin_amdgcn_sched_group_barrier(0x80, 1, 0);
        if (i >= NMF - NV) __builtin_amdgcn_sched_group_barrier(0x2, 1, 0);
    }
}
template <int NS, int SB, class RD, class RDN, int AM, int FM, int FN>
__device__ __forceinline__ void stage_step(int& slot, RD& rd, RDN& rn, Regs<FM, FN>& R0, f32x4 (&acc)[AM][FN]) {
    constexpr int NMF = FM * FN + (AM - FM) * (FN / 2);
    Regs<FM, FN> R1;
    const unsigned d = (slot + 1 == NS) ? (unsigned)(-(NS - 1) * SB) : (unsigned)SB;
    slot = (slot + 1 == NS) ? 0 : slot + 1;
    rd.read(R1, 1);
    rn.step(d);
    mma(acc, R0);
    interleave<RD::NDS, RDN::NV, NMF>();
    SCHED0();
    pin_acc<AM, FM, FN>(acc);
    WAIT_LGKM0();
    BARRIER();
    SCHED0();
    rn.read(R0, 0);
    mma(acc, R1);
    interleave<RDN::NDS, 0, NMF>();
    SCHED0();
    pin_acc<AM, FM, FN>(acc);
    SCHED0();
}

struct HalfReader {
    unsigned m0, m1; Addr<TR, 128> n;
    static constexpr int NDS = 12, NV = 3;
    __device__ __forceinline__ void init(unsigned slot_base, int lane, int own, int kq, int wn) {
        const unsigned a = slot_base + (unsigned)(own * 4 * PIECE + kq * BLK + lane * 16);
        m0 = a + kq * 2 * PIECE; m1 = a - kq * 2 * PIECE;
        n.init(slot_base + 128 * 132 + kq * 512, lane, wn);
    }
    __device__ __forceinline__ void read(Regs<4, 4>& R, int) const {
#pragma unroll
        for (int i = 0; i < 4; ++i) R.m[i] = *(LAS bf16x8*)((LAS char*)(uintptr_t)(i < 2 ? m0 : m1) + i * PIECE);
#pragma unroll
        for (int j = 0; j < 4; ++j) R.n[j] = n.read(j, 0);
    }
    __device__ __forceinline__ void step(unsigned d) { m0 += d; m1 += d; n.step(d); }
};
template <int NS, int SB> __device__ __forceinline__ void half_step(int& slot, HalfReader& rd, Regs<4, 4>& Rc, Regs<4, 4>& Rn, f32x4 (&acc)[4][4]) {
    rd.step((slot + 1 == NS) ? (unsigned)(-(NS - 1) * SB) : (unsigned)SB);
    slot = (slot + 1 == NS) ? 0 : slot + 1;
    SCHED0();
    WAIT_LGKM0();
    BARRIER();
    SCHED0();
    rd.read(Rn, 0);
    mma(acc, Rc);
    interleave<12, 0, 16>();
    SCHED0();
    pin_acc<4, 4, 4>(acc);
    SCHED0();
}

template <int NM, int NN> struct Loader {
    static constexpr int NPW = NM + NN;
    unsigned om[NM], on[NN];
    template <int T> __device__ __forceinline__ void init_m(int lw, int lane, unsigned ld) {
#pragma unroll
        for (int i = 0; i < NM; ++i) om[i] = Op<T, NM * 32>::src_off(i >> 1, lw + 4 * (i & 1), lane, ld, (i >> 1) * 128);
    }
    template <int T> __device__ __forceinline__ void init_n(int lw, int lane, unsigned ld, unsigned c0 = 0, unsigned c1 = 128, unsigned c2 = 256, unsigned c3 = 384) {
#pragma unroll
        for (int i = 0; i < NN; ++i) on[i] = Op<T, NN * 32>::src_off(i >> 1, lw + 4 * (i & 1), lane, ld, (i >> 1) == 0 ? c0 : (i >> 1) == 1 ? c1 : (i >> 1) == 2 ? c2 : c3);
    }
    template <bool SC> __device__ __forceinline__ void issue_m(const char* gm, unsigned dst) const { dma_group<SC, 0>(gm, om, dst); }
    template <bool SC> __device__ __forceinline__ void issue_n(const char* gn, unsigned dst) const { dma_group<SC, NM * 32 * 132>(gn, on, dst); }
    template <bool SCM, bool SCN, int W> __device__ __forceinline__ void turn(const char* gm, const char* gn, unsigned dst) const {
        asm volatile("" : "+s"(gm), "+s"(gn), "+s"(dst));
        WAIT_VM(W); BARRIER();
        issue_m<SCM>(gm, dst); issue_n<SCN>(gn, dst);
    }
};
__device__ __forceinline__ int korder(int p, int c, int nk) {
    const int r = (p - c) & 3;
    const int d = (0x1003 >> (4 * r) & 15) - 1;
    return (unsigned)(p - r) < (unsigned)(nk - 2) ? p + d : p;
}
__device__ __forceinline__ f32x2 gelu2(f32x2 u) {
    const float c1 = -2.0f * 0.7978845608028654f * 1.4426950408889634f, c2 = c1 * 0.044715f;
    const f32x2 z = u * ((u * u) * c2 + c1);
    f32x2 e; e.x = __builtin_amdgcn_exp2f(z.x); e.y = __builtin_amdgcn_exp2f(z.y);
    e = e + 1.0f;
    f32x2 r; r.x = __builtin_amdgcn_rcpf(e.x); r.y = __builtin_amdgcn_rcpf(e.y);
    return u * r;
}
__device__ __forceinline__ u32x4 pair16(unsigned x0, unsigned x1, unsigned y0, unsigned y1) {
    auto a = __builtin_amdgcn_permlane16_swap(x0, y0, false, false);
    auto b = __builtin_amdgcn_permlane16_swap(x1, y1, false, false);
    return (u32x4){a[0], b[0], a[1], b[1]};
}
__device__ __forceinline__ bool flags_set(u64* flags, int idx, u64 tag) {
    const u64 v = __hip_atomic_load((GAS u64*)flags + (idx < 0 ? 0 : idx), RLX_AGENT);
    const bool ok = __builtin_amdgcn_ballot_w64(idx >= 0 && v - 1ull != tag) == 0ull;
    CFENCE();
    return ok;
}
__device__ __forceinline__ void poll_flags(u64* flags, int idx, u64 tag) {
#pragma unroll 1
    for (unsigned spins = 0; spins < (1u << 20); ++spins) {
        if (flags_set(flags, idx, tag)) break;
        __builtin_amdgcn_s_sleep(2);
    }
}
__device__ __forceinline__ void peek_issue(u64& v, u64* flags, int idx) {
    asm volatile("global_load_dwordx2 %0, %1, off sc1" : "=v"(v) : "v"((GAS u64*)flags + (idx < 0 ? 0 : idx)) : "memory");
}
__device__ __forceinline__ bool peek_ok(u64& v, int idx, u64 tag) {
    asm volatile("" : "+v"(v) :: "memory");
    return __builtin_amdgcn_ballot_w64(idx >= 0 && v - 1ull != tag) == 0ull;
}
__device__ __forceinline__ void set_flag(u64* flags, int idx, u64 tag) {
    asm volatile("" : "+v"(tag));
    __hip_atomic_store((GAS u64*)flags + idx, tag + 1ull, RLX_AGENT);
}

struct Params {
    const u16 *x, *wqkv, *wo, *w1, *b1, *w2, *b2;
    u16* out;
    char* ws;
    __device__ __forceinline__ u16* h() const { return out; }
    __device__ __forceinline__ u16* qkv() const { return (u16*)(ws + WS_QKV); }
    __device__ __forceinline__ u16* U() const { return (u16*)(ws + WS_U); }
    __device__ __forceinline__ u16* o() const { return (u16*)(ws + WS_QKV); }
    __device__ __forceinline__ float* stat() const { return (float*)(ws + WS_STAT); }
    __device__ __forceinline__ u64* flags() const { return (u64*)(ws + WS_FLAGS); }
};

constexpr int SB_M = (128 + 224) * 132, SB_Q = (128 + 192) * 132, SB_F = 256 * 132, NS_F = 4;
constexpr int LDS_QX = 3 * SB_Q;
constexpr int LDS_CTL = LDS_QX + 3072;
constexpr int LDS_AQ = 0, LDS_AK = 2 * SB_Q, LDS_AV = 128 * 1024, LDS_BYTES = 160 * 1024;
constexpr int LDS_MC = LDS_BYTES - 512;
constexpr int LDS_X = 3 * SB_F;
constexpr int LDS_BK = 16 * 1024, LDS_BV = 96 * 1024;
constexpr int LDS_EX = 136 * 1024;
constexpr int LDS_SINK = LDS_BYTES - 256;
static_assert(LDS_CTL + 32 <= LDS_AV && 3 * SB_M <= LDS_SINK && 3 * SB_F <= LDS_AV && NS_F * SB_F <= LDS_EX && LDS_EX + 16384 <= LDS_MC, "LDS map");
constexpr int NA = 17;
constexpr int KW = 16, NF_Q = 7 + NA, NF_M = 7 + 42 - NA;

__device__ __forceinline__ int qkv_head(int tn, int blk) { return blk * NH + ((tn & ~3) | ((tn + blk) & 3)) * 2; }

__device__ __forceinline__ f32x4 ln_row(const u32x4 a, const u32x4 b, u32x4& oa, u32x4& ob) {
    f32x2 v[8];
#pragma unroll
    for (int i = 0; i < 4; ++i) { v[i] = (f32x2){lo16(a[i]), hi16(a[i])}; v[4 + i] = (f32x2){lo16(b[i]), hi16(b[i])}; }
    const f32x2 s2 = ((v[0] + v[1]) + (v[2] + v[3])) + ((v[4] + v[5]) + (v[6] + v[7]));
    const float mean = wave_sum(s2.x + s2.y) * (1.0f / D_);
    f32x2 q2 = {0.f, 0.f};
#pragma unroll
    for (int i = 0; i < 8; ++i) { v[i] -= mean; q2 += v[i] * v[i]; }
    const float var = wave_sum(q2.x + q2.y) * (1.0f / D_);
    const float rs = __builtin_amdgcn_rcpf(rbf(__builtin_amdgcn_sqrtf(rbf(rbf(var) + 1e-5f))));
    const float sd2 = rbf(__builtin_amdgcn_sqrtf(rbf(rbf(var * rs * rs) + 1e-5f)));
    const float g = rs * __builtin_amdgcn_rcpf(sd2);
#pragma unroll
    for (int i = 0; i < 8; ++i) {
        const f32x2 t = v[i] * g;
        if (i < 4) oa[i] = pack2(t.x, t.y); else ob[i - 4] = pack2(t.x, t.y);
    }
    return (f32x4){mean, rs, sd2, 0.f};
}

#define MFMA32(a, b, c) __builtin_amdgcn_mfma_f32_32x32x16_bf16(a, b, c, 0, 0, 0)
__device__ __forceinline__ int attn_dep(int hd, int qb, int lane) {
    const int which = lane >> 2, st = 2 * qb - 2 + (lane & 3), p = hd >> 1;
    if (lane >= 12 || st < 0 || (which == 0 && (lane & 3) < 2)) return -1;
    return FL_Q + st * 32 + ((p & ~3) | ((p - which) & 3));
}
template <int WHICH> __device__ __forceinline__ void attn_load(const Params& P, unsigned dst, int hd, int qb, int w, int lane) {
    const int row = WHICH == 0 ? qb * 256 + w * 64 : qb * 256 - ((qb == 0 && w < 2) ? 0 : 256) + w * 128;
    const char* g = (const char*)(P.qkv() + ((size_t)(WHICH * NH + hd) * S_ + row) * 32);
    const unsigned off = (unsigned)((lane >> 2) * 64 + (WHICH == 2 ? lane & 3 : (lane & 3) ^ ((lane >> 4) & 3)) * 16);
    dma16x4(g, off, dst + w * (WHICH == 0 ? 4096 : 8192));
    if (WHICH != 0) dma16x4(g + 4096, off, dst + w * 8192 + 4096);
}
#define PIN(x) asm volatile("" : "+v"(x))
__device__ __forceinline__ float attn_scores(LAS char* buf, LAS char* qbuf, int nbad, int wid, int lane, unsigned up, f32x16 (&sc)[8]) {
    const int r32 = lane & 31, hh = lane >> 5, sw = (r32 >> 2) & 3;
    LAS char* k0p = buf + (wid * 32 + r32) * 64 + ((hh ^ sw) * 16);
    LAS char* k1p = buf + (wid * 32 + r32) * 64 + (((2 + hh) ^ sw) * 16);
    const bf16x8 qf0 = *(LAS bf16x8*)(qbuf + (wid * 32 + r32) * 64 + ((hh ^ sw) * 16));
    const bf16x8 qf1 = *(LAS bf16x8*)(qbuf + (wid * 32 + r32) * 64 + (((2 + hh) ^ sw) * 16));
    bf16x8 ka[9], kb[9];
#pragma unroll
    for (int i = 0; i < 3; ++i) { const int j = i == 0 ? 8 : i - 1; ka[i] = *(LAS bf16x8*)(k0p + j * 2048); kb[i] = *(LAS bf16x8*)(k1p + j * 2048); SCHED0(); }
    const float NEG = -INFINITY;
    const f32x16 zero = {};
    f32x16 s8;
    float m = NEG;
#pragma unroll
    for (int i = 0; i < 11; ++i) {
        if (i + 3 < 9) { ka[i + 3] = *(LAS bf16x8*)(k0p + (i + 2) * 2048); kb[i + 3] = *(LAS bf16x8*)(k1p + (i + 2) * 2048); }
        if (i < 9) {
            const f32x16 t = MFMA32(kb[i], qf1, MFMA32(ka[i], qf0, zero));
            if (i == 0) { s8 = t; PIN(s8); } else { sc[i - 1] = t; PIN(sc[i - 1]); }
        }
        SCHED0();
        const int j = i - 3;
        if (j == 0) {
            if (__builtin_expect(nbad > 0, 0)) {
#pragma unroll
                for (int r = 0; r < 16; ++r) sc[0][r] = NEG;
                PIN(sc[0]);
            }
#pragma unroll
            for (int r = 0; r < 16; ++r) {
                const unsigned mk = (unsigned)__builtin_amdgcn_sbfe((int)up, (r & 3) + 8 * (r >> 2), 1);
                sc[0][r] = __uint_as_float(__builtin_amdgcn_bitop3_b32(mk, __float_as_uint(sc[0][r]), __float_as_uint(s8[r]), 0xca));
            }
        }
        if (j >= 0) {
            float mj = m;
#pragma unroll
            for (int r = 0; r < 16; r += 2) mj = __builtin_fmaxf(__builtin_fmaxf(mj, sc[j][r]), sc[j][r + 1]);
            m = (j > 0 && j < nbad) ? m : mj;
            PIN(m);
        }
        SCHED0();
    }
    auto rr = __builtin_amdgcn_permlane32_swap(__float_as_uint(m), __float_as_uint(m), false, false);
    return __builtin_fmaxf(__uint_as_float(rr[0]), __uint_as_float(rr[1]));
}
__device__ __forceinline__ bf16x8 v_read(LAS char* vp, int j, int s) {
    const bf16x4 lo = __builtin_amdgcn_ds_read_tr16_b64_v4bf16((LAS bf16x4*)(vp + j * 2048 + s * 1024));
    const bf16x4 hi = __builtin_amdgcn_ds_read_tr16_b64_v4bf16((LAS bf16x4*)(vp + j * 2048 + s * 1024 + 512));
    return __builtin_shufflevector(lo, hi, 0, 1, 2, 3, 4, 5, 6, 7);
}
__device__ __forceinline__ void attn_pv(LAS char* buf, int nbad, int wid, int lane, unsigned up, f32x16 (&sc)[8], float m, f32x16& o, float& lsum) {
    const int hh = lane >> 5;
    LAS char* vp = buf + (wid * 32 + 4 * hh + ((lane & 15) >> 2)) * 64 + ((lane >> 4) & 1) * 32 + (lane & 3) * 8;
    u32x4 one4 = {0x3f803f80u, 0x3f803f80u, 0x3f803f80u, 0x3f803f80u};
    PIN(one4);
    const bf16x8 ones = __builtin_bit_cast(bf16x8, one4);
    const float h0 = rbf(-m), h1 = rbf(-m - h0);
    const unsigned w0 = hh ? 0u : pack2(h0, h1), w1 = hh ? 0u : pack2((-m - h0) - h1, 0.f);
    auto minus_m = [&](int j) {
        const u32x4 bm = {(j > 0 && j < nbad) ? 0xff80u : w0, w1, 0u, 0u};
        sc[j] = MFMA32(ones, __builtin_bit_cast(bf16x8, bm), sc[j]);
        PIN(sc[j]);
    };
    minus_m(0); minus_m(1);
    f32x16 l = {};
    bf16x8 vf[2], v8[2];
    u32x4 pw[2], p8[2], pl[2];
    SCHED0();
#pragma unroll
    for (int j = 0; j < 8; ++j) {
        bf16x8 vn[2] = {v_read(vp, j, 0), v_read(vp, j, 1)};
        if (j == 0) { v8[0] = v_read(vp, 8, 0); v8[1] = v_read(vp, 8, 1); }
        SCHED0();
#pragma unroll
        for (int g = 0; g < 4; ++g) {
#pragma unroll
            for (int r = 4 * g; r < 4 * g + 4; ++r) sc[j][r] = __builtin_amdgcn_exp2f(sc[j][r]);
            PIN(sc[j]);
            SCHED0();
            if (j > 0) {
                if (g & 1) { l = MFMA32(ones, __builtin_bit_cast(bf16x8, j == 1 ? pl[g >> 1] : pw[g >> 1]), l); PIN(l); }
                else { o = MFMA32(vf[g >> 1], __builtin_bit_cast(bf16x8, pw[g >> 1]), o); PIN(o); }
            }
            if (j == 1 && (g & 1)) { o = MFMA32(v8[g >> 1], __builtin_bit_cast(bf16x8, p8[g >> 1]), o); PIN(o); }
            SCHED0();
        }
#pragma unroll
        for (int i = 0; i < 8; ++i) pw[i >> 2][i & 3] = pack2(sc[j][2 * i], sc[j][2 * i + 1]);
        if (j == 0) {
            pl[0] = pw[0]; pl[1] = pw[1];
#pragma unroll
            for (int i = 0; i < 8; ++i) {
                const int kr = ((2 * i) & 3) + 8 * (i >> 1);
                const unsigned mk = __builtin_amdgcn_bitop3_b32(0xffffu, (unsigned)__builtin_amdgcn_sbfe((int)up, kr, 1), (unsigned)__builtin_amdgcn_sbfe((int)up, kr + 1, 1), 0xca);
                p8[i >> 2][i & 3] = pw[i >> 2][i & 3] & ~mk; pw[i >> 2][i & 3] &= mk;
            }
        }
        vf[0] = vn[0]; vf[1] = vn[1];
        PIN(pw[0]); PIN(pw[1]);
        SCHED0();
        if (j + 2 < 8) minus_m(j + 2);
        SCHED0();
    }
    l = MFMA32(ones, __builtin_bit_cast(bf16x8, pw[0]), l);
    l = MFMA32(ones, __builtin_bit_cast(bf16x8, pw[1]), l);
    o = MFMA32(vf[0], __builtin_bit_cast(bf16x8, pw[0]), o);
    o = MFMA32(vf[1], __builtin_bit_cast(bf16x8, pw[1]), o);
    lsum = l[0];
}
__device__ __forceinline__ void attn_out(const Params& P, int hd, int qblk, int lane, float l, const f32x16& oacc) {
    const int r32 = lane & 31, hh = lane >> 5;
    const float inv = __builtin_amdgcn_rcpf(l);
    unsigned w[8];
#pragma unroll
    for (int g = 0; g < 4; ++g) { w[2 * g] = pack2(oacc[4 * g] * inv, oacc[4 * g + 1] * inv); w[2 * g + 1] = pack2(oacc[4 * g + 2] * inv, oacc[4 * g + 3] * inv); }
    const __amdgpu_buffer_rsrc_t rs = rsrc(P.o(), (unsigned)S_ * D_ * 2);
    const unsigned off = (unsigned)((hd * (S_ / 16) + qblk * 2 + (r32 >> 4)) * 1024 + (hh * 16 + (r32 & 15)) * 16);
#pragma unroll
    for (int gp = 0; gp < 2; ++gp) {
        auto a = __builtin_amdgcn_permlane32_swap(w[4 * gp], w[4 * gp + 2], false, false);
        auto b = __builtin_amdgcn_permlane32_swap(w[4 * gp + 1], w[4 * gp + 3], false, false);
        __builtin_amdgcn_raw_buffer_store_b128((u32x4){a[0], b[0], a[1], b[1]}, rs, off + gp * 512, 0, SC1);
    }
}

extern "C" __global__ __launch_bounds__(512, 2) void k_main(const Params P) {
    extern __shared__ __attribute__((aligned(16))) char smem[];
    LAS char* lds = (LAS char*)smem;
    const int tid = threadIdx.x, lane = tid & 63, wid = __builtin_amdgcn_readfirstlane(tid >> 6);
    const int grp = blockIdx.x & 7, idx = blockIdx.x >> 3;
    const bool loader = wid >= 4;
    const int lw = wid & 3, wm = (wid >> 1) & 1, wn = wid & 1;
    const unsigned ring0 = (unsigned)(uintptr_t)lds, ringl = ring0 + lw * PIECE;
    const int i16 = idx & 15;
    const bool isM = idx < 16;
    const int fm = (grp >> 1) * 4 + (isM ? i16 & 3 : i16 >> 2), fn = (grp & 1) * 4 + (isM ? i16 >> 2 : i16 & 3);
    const int um = (grp & 1) * 4 + (i16 >> 2), un = (grp >> 1) * 4 + (i16 & 3);
    const int hd = (grp & 1) * 8 + (i16 & 7), qb = (grp >> 1) * 2 + (i16 >> 3);
    if (wid == 0) {
        u64 pc; unsigned rel, len;
        asm volatile("s_getpc_b64 %0\n1:\n\ts_mov_b32 %1, 1b-k_main\n\ts_mov_b32 %2, 2f-k_main\n\t.subsection 1\n2:\n\t.subsection 0" : "=s"(pc), "=s"(rel), "=s"(len));
        const unsigned off = (unsigned)((idx + 32 * (lane & 15)) * 128);
        dma4((const char*)(pc - rel), off < len ? off : 0u, ring0 + LDS_SINK);
    }
    u64 tag;
    asm volatile("global_load_dwordx2 %0, %1, off sc1" : "=v"(tag) : "v"((GAS u64*)P.flags() + FL_E) : "memory");

    if (tid < 8) *(LAS unsigned*)(lds + (isM ? LDS_MC : LDS_CTL) + tid * 4) = 0u;
    if (isM) {
        constexpr int NK = S_ / 64, SB = SB_M;
        const int pg = (grp >> 1) + 4 * (grp & 1);
        const int tm = um, tn = un, mi = pg * 16 + i16;
        const char* gm = (const char*)P.h() + tm * 256; const char* gn = (const char*)P.w1 + tn * 448;
        const int cls = (tm + tn) & 3;
        auto kof = [&](int u) { const int p0 = u - ((u - cls) & 3); return __builtin_amdgcn_readfirstlane((((p0 == 1 || p0 == 2) ? u : korder(u, cls, NK)) + 4 * pg) & (NK - 1)); };
        Loader<4, 7> L;
        {
            const int row = mi * 16 + wid * 2;
            const u32x4* px = (const u32x4*)(P.x + (size_t)row * D_);
                        u32x4 a0 = px[lane], b0 = px[64 + lane], a1 = px[128 + lane], b1 = px[192 + lane];
            SCHED0();
            if (loader) {
                const unsigned c0 = (tn & 1) * 64;
                L.init_m<TR>(lw, lane, D_ * 2);
                L.init_n<TR>(lw, lane, F_ * 2, c0, c0 + 128, c0 + 256, (tn & 1) ? 0 : 384);
            }
            asm volatile("" : "+v"(a0), "+v"(b0), "+v"(a1), "+v"(b1));
            if (loader) L.issue_n<false>(gn + (size_t)kof(0) * (64 * F_ * 2), ringl);
            const __amdgpu_buffer_rsrc_t rh = rsrc(P.h(), (unsigned)S_ * D_ * 2);
            u32x4 oa0, ob0, oa1, ob1;
            const f32x4 s0 = ln_row(a0, b0, oa0, ob0), s1 = ln_row(a1, b1, oa1, ob1);
            __builtin_amdgcn_raw_buffer_store_b128(oa0, rh, (unsigned)(row * 2048 + lane * 16), 0, SC1);
            __builtin_amdgcn_raw_buffer_store_b128(ob0, rh, (unsigned)(row * 2048 + 1024 + lane * 16), 0, SC1);
            __builtin_amdgcn_raw_buffer_store_b128(oa1, rh, (unsigned)(row * 2048 + 2048 + lane * 16), 0, SC1);
            __builtin_amdgcn_raw_buffer_store_b128(ob1, rh, (unsigned)(row * 2048 + 3072 + lane * 16), 0, SC1);
            if (lane < 2) __builtin_amdgcn_raw_buffer_store_b128(__builtin_bit_cast(u32x4, lane ? s1 : s0), rsrc(P.stat(), S_ * 16), (unsigned)((row + lane) * 16), 0, SC1);
            SCHED0();
        }
        if (loader) {
            WAIT_VM(0);
            BARRIER();
            if (tid == 256 + 64) set_flag(P.flags(), FL_L + mi, tag);
#pragma unroll
            for (int u = 1; u < 3; ++u) L.issue_n<false>(gn + (size_t)kof(u) * (64 * F_ * 2), ringl + u * SB);
            if (lw == 0) poll_flags(P.flags(), lane < 16 ? FL_L + pg * 16 + lane : -1, tag);
            BARRIER();
#pragma unroll
            for (int u = 0; u < 3; ++u) L.issue_m<true>(gm + (size_t)kof(u) * (64 * D_ * 2), ringl + u * SB);
            WAIT_VM(8); BARRIER();
            WAIT_VM(4);
            int slot = 0, t = 0;
            auto issue = [&]() {
                const int k = kof(t + 3);
                L.issue_m<true>(gm + (size_t)k * (64 * D_ * 2), ringl + slot * SB);
                L.issue_n<false>(gn + (size_t)k * (64 * F_ * 2), ringl + slot * SB);
                slot = (slot == 2) ? 0 : slot + 1;
            };
            if (lw == 0) poll_flags(P.flags(), lane < 16 ? FL_L + ((pg + 1) & 7) * 16 + lane : -1, tag);
            BARRIER(); issue(); ++t;
            WAIT_VM(L.NPW); BARRIER(); issue(); ++t;
            WAIT_VM(L.NPW);
            if (lw == 0) { poll_flags(P.flags(), FL_L + lane, tag); poll_flags(P.flags(), FL_L + 64 + lane, tag); }
            BARRIER(); issue(); ++t;
#pragma unroll 1
            for (; t < NK - 3; ++t) {
                const int k = (korder(t + 3, cls, NK) + 4 * pg) & (NK - 1);
                L.turn<true, false, 11>(gm + (size_t)k * (64 * D_ * 2), gn + (size_t)k * (64 * F_ * 2), ringl + slot * SB);
                slot = (slot == 2) ? 0 : slot + 1;
            }
            {
                const int f = ((i16 * 4 + lw) * 7 + (lane < 56 ? lane >> 3 : 6)) * 2 + (grp & 1) + 896 * (grp >> 1);
                dma4((const char*)P.w2 + (grp >> 1) * 1024, (unsigned)(f * (S_ * 2) + (lane & 7) * 128), ring0 + LDS_SINK);
            }
            WAIT_VM(L.NPW + 1); BARRIER();
            WAIT_VM(1); BARRIER();
            BARRIER();
#pragma unroll
            for (int i = 0; i < 8; ++i)
                dma16<false, 0>((const char*)P.x + ((size_t)(fm * 128 + i * 16) * D_ + fn * 128 + lw * 32) * 2, (unsigned)((lane >> 2) * (D_ * 2) + (lane & 3) * 16), ring0 + LDS_X + (lw * 8 + i) * 1024);
            dma4((const char*)P.b2 + fm * 256, (unsigned)(lane * 4), ring0 + LDS_X + 32768);
        } else {
            const int fr = lane & 15, fq = lane >> 4;
            const int fb = tn * 224 + ((tn & 1) ? 32 : 0) + wn * 32, fh = tn * 224 + ((tn & 1) ? 0 : 192) + wn * 16;
            WAIT_VM(0);
            WAIT_LGKM0();
            BARRIER();
            f32x4 acc[4][7];
#pragma unroll
            for (int j = 0; j < 7; ++j) {
                const u32x2 bb = *(const u32x2*)(P.b1 + (j < 6 ? fb + (j >> 1) * 64 + (j & 1) * 16 : fh) + fq * 4);
                const f32x4 bv = {lo16(bb[0]), hi16(bb[0]), lo16(bb[1]), hi16(bb[1])};
#pragma unroll
                for (int i = 0; i < 4; ++i) acc[i][j] = bv;
            }
            Reader<TR, 128, TR, 224, 4, 7> rd; rd.init(ring0, lane, wm, wn);
            Regs<4, 7> R0;
            int slot = 0;
            BARRIER();
            __builtin_amdgcn_s_waitcnt(0x0070);
            BARRIER();
            rd.read(R0, 0);
            for (int t = 0; t < NK / 4; ++t) {
                stage_step<3, SB>(slot, rd, rd, R0, acc); stage_step<3, SB>(slot, rd, rd, R0, acc);
                stage_step<3, SB>(slot, rd, rd, R0, acc); stage_step<3, SB>(slot, rd, rd, R0, acc);
            }
            const __amdgpu_buffer_rsrc_t rs = rsrc(P.U(), (unsigned)D_ * F_ * 2);
            const unsigned df = (unsigned)(tm * 8 + wm * 4);
            const unsigned ob = (unsigned)((fb >> 5) * 64 + df) * 1024u + (unsigned)((((fq & 1) * 2 + (fq >> 1)) * 16 + fr) * 16);
            const unsigned oh = (unsigned)((fh >> 5) * 64 + df + (fq & 1)) * 1024u + (unsigned)(((wn * 2 + (fq >> 1)) * 16 + fr) * 16);
            LAS char* ul = lds + 128 * 132 + (wn * 8 + wm * 4) * PIECE + (((fq & 1) * 2 + (fq >> 1)) * 16 + fr) * 16;
            unsigned pk[4][7][2];
#pragma unroll
            for (int i = 0; i < 4; ++i) {
#pragma unroll
                for (int j = 0; j < 7; ++j) {
                    const f32x2 a = gelu2((f32x2){acc[i][j][0], acc[i][j][1]}), b = gelu2((f32x2){acc[i][j][2], acc[i][j][3]});
                    pk[i][j][0] = pack2(a.x, a.y); pk[i][j][1] = pack2(b.x, b.y);
                }
#pragma unroll
                for (int jp = 0; jp < 3; ++jp) {
                    const u32x4 w = pair16(pk[i][2 * jp][0], pk[i][2 * jp][1], pk[i][2 * jp + 1][0], pk[i][2 * jp + 1][1]);
                    __builtin_amdgcn_raw_buffer_store_b128(w, rs, ob + i * 1024 + jp * 2 * 65536, 0, SC1);
                    *(LAS u32x4*)(ul + jp * SB_F + i * PIECE) = w;
                }
                if (i & 1)
                    __builtin_amdgcn_raw_buffer_store_b128(pair16(pk[i - 1][6][0], pk[i - 1][6][1], pk[i][6][0], pk[i][6][1]), rs, oh + (i - 1) * 1024, 0, SC1);
                SCHED0();
            }
            WAIT_VM(0);
            if (lane == 0 && __hip_atomic_fetch_add((LAS unsigned*)(lds + LDS_MC), 1u, __ATOMIC_RELAXED, __HIP_MEMORY_SCOPE_WORKGROUP) == 3u)
                set_flag(P.flags(), FL_U + um * 32 + un, tag);
        }
    } else {
        constexpr int NK = D_ / 64, SB = SB_Q;
        const int tm = fm;
        if (loader) {
            Loader<4, 6> L; L.init_m<ROW>(lw, lane, D_ * 2);
            L.init_n<TR>(lw, lane, 3 * D_ * 2, qkv_head(fn, 0) * 64, qkv_head(fn, 1) * 64, qkv_head(fn, 2) * 64);
            const char* gm = (const char*)P.x + (size_t)tm * 128 * D_ * 2; const char* gn = (const char*)P.wqkv;
            const char* gh2 = (const char*)P.h() + (size_t)tm * 128 * D_ * 2;
            int slot = 0;
            auto issue = [&](int u) {
                const int k = (korder(u & (NK - 1), (tm + fn) & 3, NK) + 4 * (grp >> 1)) & (NK - 1);
                L.issue_m<false>(gm + k * 128, ringl + slot * SB);
                L.issue_n<false>(gn + (size_t)k * (64 * 3 * D_ * 2) + (u >= NK ? 16 * 64 : 0), ringl + slot * SB);
                slot = (slot == 2) ? 0 : slot + 1;
            };
            issue(0); issue(1);
            WAIT_VM(L.NPW); BARRIER();
            issue(2);
            int t = 0;
            WAIT_VM(L.NPW); BARRIER(); issue(3); ++t;
#pragma unroll 1
            for (int seg = 0; seg < 2; ++seg) {
                const int end = seg == 0 ? 12 : 2 * NK - 3;
#pragma unroll 1
                for (; t < end; ++t) {
                    const int u = t + 3, k = (korder(u & (NK - 1), (tm + fn) & 3, NK) + 4 * (grp >> 1)) & (NK - 1);
                    L.turn<true, false, 10>((u >= NK ? gh2 : gm) + k * 128, gn + (size_t)k * (64 * 3 * D_ * 2) + (u & NK) * 64, ringl + slot * SB);
                    slot = (slot == 2) ? 0 : slot + 1;
                }
                if (seg == 0 && lw == 0) {
                    poll_flags(P.flags(), lane < 8 ? FL_L + tm * 8 + lane : -1, tag);
                    dma16<true, LDS_QX>((const char*)P.stat() + tm * 2048, (unsigned)(lane * 16), ring0);
                    dma16<true, LDS_QX + 1024>((const char*)P.stat() + tm * 2048 + 1024, (unsigned)(lane * 16), ring0);
                }
            }
            poll_flags(P.flags(), attn_dep(hd, qb, lane), tag);
            WAIT_VM(0);
            BARRIER();
            attn_load<1>(P, ring0 + LDS_AK, hd, qb, lw, lane);
            attn_load<2>(P, ring0 + LDS_AV, hd, qb, lw, lane);
            BARRIER();
            attn_load<0>(P, ring0 + LDS_AQ, hd, qb, lw, lane);
            BARRIER();
        } else {
            const int fr = lane & 15, fq = lane >> 4;
            Reader<ROW, 128, TR, 192, 4, 6> rd; rd.init(ring0, lane, wm, wn);
            rd.n.b = rd.n.a + 32 - wm * 32; rd.n.a += wm * 32;
            Regs<4, 6> R0;
            int slot = 0;
            __builtin_amdgcn_s_waitcnt(0x0070);
            BARRIER();
            rd.read(R0, 0);
            const __amdgpu_buffer_rsrc_t rs = rsrc(P.qkv(), (unsigned)S_ * 3 * D_ * 2);
            const int s0 = tm * 128 + wm * 64;
            const unsigned so = (unsigned)(((s0 + fr) * 32 + ((fq & 1) ^ wm) * 16 + (fq >> 1) * 8) * 2);
            constexpr float QS = 0.17677669529663687f * 1.4426950408889634f;
            {
                f32x4 acc[5][6] = {};
                for (int t = 0; t < NK / 4; ++t) {
                    stage_step<3, SB>(slot, rd, rd, R0, acc); stage_step<3, SB>(slot, rd, rd, R0, acc);
                    stage_step<3, SB>(slot, rd, rd, R0, acc); stage_step<3, SB>(slot, rd, rd, R0, acc);
                }
                LAS char* ex = lds + LDS_QX + 2048;
                if (fr == 0) {
#pragma unroll
                    for (int jp = 0; jp < 3; ++jp) *(LAS f32x4*)(ex + wid * 256 + jp * 64 + fq * 16) = acc[4][2 * jp];
                }
                WAIT_LGKM0();
                CFENCE();
                __hip_atomic_store((LAS unsigned*)(lds + LDS_CTL + wid * 4), 1u, __ATOMIC_RELAXED, __HIP_MEMORY_SCOPE_WORKGROUP);
                f32x2 st[4];
#pragma unroll
                for (int i = 0; i < 4; ++i) st[i] = *(LAS f32x2*)(lds + LDS_QX + (wm * 64 + i * 16 + fr) * 16);
                while (__builtin_amdgcn_readfirstlane(__hip_atomic_load((LAS unsigned*)(lds + LDS_CTL + (wid ^ 2) * 4), __ATOMIC_RELAXED, __HIP_MEMORY_SCOPE_WORKGROUP)) == 0u) {}
                CFENCE();
#pragma unroll
                for (int jp = 0; jp < 3; ++jp) {
                    const f32x4 ca = acc[4][2 * jp], cb = *(LAS f32x4*)(ex + (wid ^ 2) * 256 + jp * 64 + fq * 16);
#pragma unroll
                    for (int i = 0; i < 4; ++i) {
                        const float r = st[i].y * (jp == 0 ? QS : 1.0f), mr = -st[i].x * r;
                        const f32x4 a = acc[i][2 * jp] * r + ca * mr, b = acc[i][2 * jp + 1] * r + cb * mr;
                        __builtin_amdgcn_raw_buffer_store_b128(pair16(pack2(a[0], a[1]), pack2(a[2], a[3]), pack2(b[0], b[1]), pack2(b[2], b[3])), rs,
                                                               so + (unsigned)((qkv_head(fn, jp) + wn) * (S_ * 64) + i * 1024), 0, SC1);
                    }
                }
            }
            {
                f32x4 acc[4][6] = {};
                for (int t = 0; t < NK / 4; ++t) {
                    if (__builtin_expect(t == 1 || t == 2, 0)) {
                        if (t == 1) WAIT_VM(0);
                        else if (tid == 0) set_flag(P.flags(), FL_Q + tm * 32 + fn, tag);
                    }
                    stage_step<3, SB>(slot, rd, rd, R0, acc); stage_step<3, SB>(slot, rd, rd, R0, acc);
                    stage_step<3, SB>(slot, rd, rd, R0, acc); stage_step<3, SB>(slot, rd, rd, R0, acc);
                }
#pragma unroll
                for (int i = 0; i < 4; ++i) {
                    const float sd = *(LAS float*)(lds + LDS_QX + (wm * 64 + i * 16 + fr) * 16 + 8);
#pragma unroll
                    for (int jp = 0; jp < 3; ++jp) {
                        const float r = sd * (jp == 0 ? QS : 1.0f);
                        const f32x4 a = acc[i][2 * jp] * r, b = acc[i][2 * jp + 1] * r;
                        __builtin_amdgcn_raw_buffer_store_b128(pair16(pack2(a[0], a[1]), pack2(a[2], a[3]), pack2(b[0], b[1]), pack2(b[2], b[3])), rs,
                                                               so + (unsigned)((qkv_head(fn, jp) + 16 + wn) * (S_ * 64) + i * 1024), 0, SC1);
                    }
                }
            }
        }
    }

    constexpr int SB = SB_F, NS = NS_F;
    constexpr size_t OS = 2 * (S_ / 16) * 1024, WOS = 64 * D_ * 2, W2S = 64 * S_ * 2, US = 2 * 65536;
    const int tile = fm * 8 + fn, cls = (fm + fn) & 3, nf = isM ? NF_M : NF_Q;
    const int uh = ((fm >> 1) & 1) ^ (isM ? 0 : 1);
    auto fof = [&](int i) {
        int r = korder(i - 7, cls, nf - 7) + (grp & 1) * ((nf - 7) >> 1);
        r = r >= nf - 7 ? r - (nf - 7) : r;
        const int im = (fm & 1) ? (i < 3 ? i + 4 : i - 3) : i;
        const int j = i < 7 ? 7 * uh + (isM ? im : korder(i, cls, 7)) : (isM ? 14 + NA : 14) + r, k = 14 * (grp >> 1) + j;
        return __builtin_amdgcn_readfirstlane(k >= 56 ? k - 56 : k);
    };
    const char* g2 = (const char*)P.w2 + fm * 256;
    Loader<4, 4> L;
    if (loader) L.init_m<TR>(lw, lane, S_ * 2);
    auto preload_w2 = [&]() {
        int k0 = fof(0), k1 = fof(1);
        asm volatile("" : "+s"(k0), "+s"(k1));
        L.issue_m<false>(g2 + k0 * W2S, ringl); L.issue_m<false>(g2 + k1 * W2S, ringl + SB);
    };
    if (isM) {
        if (loader) { preload_w2(); L.issue_m<false>(g2 + fof(2) * W2S, ringl + 2 * SB); WAIT_VM(0); }
        WAIT_LGKM0();
        BARRIER();
    } else {
        if (loader) { WAIT_VM(0); __builtin_amdgcn_s_setprio(1); }
        WAIT_LGKM0();
        BARRIER();
#pragma unroll 1
        for (int it = 0; it < 2; ++it) {
            f32x16 sc[8];
            int ln = lane;
            asm volatile("" : "+v"(ln));
            const int dg = (ln & 31) - 4 * (ln >> 5);
            const unsigned up = dg < 0 ? ~0u : ~1u << dg;
            int nbad = qb == 0 ? 8 - wid : 0;
            asm volatile("" : "+s"(nbad));
            SCHED0();
            const float m = attn_scores(lds + (it ? LDS_BK : LDS_AK), lds + LDS_AQ, nbad, wid, ln, up, sc);
            SCHED0();
            WAIT_VM(0);
            BARRIER();
            if (tid == 0) set_flag(P.flags(), it == 0 ? FL_Q + fm * 32 + fn + 8 : FL_O + qb * 32 + hd, tag);
            if (it == 1 && loader) preload_w2();
            f32x16 oacc = {};
            float lsum;
            attn_pv(lds + (it ? LDS_BV : LDS_AV), nbad, wid, ln, up, sc, m, oacc, lsum);
            attn_out(P, hd + 16 * it, qb * 8 + wid, ln, lsum, oacc);
            if (it == 0) {
                if (loader) {
                    __builtin_amdgcn_s_setprio(0);
                    int h2 = hd + 16, q2 = qb, w2 = lw;
                    asm volatile("" : "+s"(h2), "+s"(q2), "+s"(w2));
                    poll_flags(P.flags(), attn_dep(h2, q2, ln), tag);
                    attn_load<0>(P, ring0 + LDS_AQ, h2, q2, w2, ln);
                    attn_load<1>(P, ring0 + LDS_BK, h2, q2, w2, ln);
                    attn_load<2>(P, ring0 + LDS_BV, h2, q2, w2, ln);
                    WAIT_VM(8);
                    __builtin_amdgcn_s_setprio(1);
                }
                BARRIER();
            }
        }
        WAIT_LGKM0();
    }
    const int own = isM ? 0 : 1;
    if (loader) {
        const char* go = (const char*)P.o() + fm * 8192 + own * 4096;
        const char* gw = (const char*)P.wo + fn * 256;
        const char* gu = (const char*)P.U() + fn * 8192;
        __builtin_amdgcn_s_setprio(0);
        L.init_n<FRAG>(lw, lane, US / 2);
        auto issue_f = [&](int u) {
            const int k = fof(u);
            L.issue_m<false>(g2 + k * W2S, ringl + (u & (NS - 1)) * SB); L.issue_n<true>(gu + k * US, ringl + (u & (NS - 1)) * SB);
        };
        const int ulo = isM ? ((14 + NA) * 2) / 7 : 4, uhi = isM ? 15 : ((14 + NA) * 2 - 1) / 7;
        const int udep = (lane >= ulo && lane <= uhi) ? FL_U + fn * 32 + (((grp >> 1) * 4 + lane) & 15) : -1;
        const int odep = lane < 32 ? FL_O + (fm >> 1) * 32 + lane : lane < 48 ? FL_U + fn * 32 + lane - 32 : -1, pdep = lane == 0 ? FL_P + tile * 32 + own : -1;
        u64 pk;
        if (isM) { BARRIER(); BARRIER(); }
        poll_flags(P.flags(), lane < 2 ? FL_U + fn * 32 + (grp >> 1) * 4 + 2 * uh + lane : -1, tag);
        if (isM) { issue_f(3); issue_f(4); }
        else {
#pragma unroll
            for (int u = 0; u < 2; ++u) L.issue_n<true>(gu + fof(u) * US, ringl + u * SB);
            WAIT_VM(4); BARRIER();
            issue_f(2); issue_f(3);
            WAIT_VM(16); BARRIER(); issue_f(4);
            WAIT_VM(16); BARRIER();
        }
        peek_issue(pk, P.flags(), udep); issue_f(5);
        WAIT_VM(16); BARRIER(); issue_f(6);
        WAIT_VM(16);
        int nxt = 7;
        if (!peek_ok(pk, udep, tag)) {
            bool uready = false;
#pragma unroll 1
            for (int t = 3; t < 7; ++t) {
                if (t == 6 && !uready) { poll_flags(P.flags(), udep, tag); uready = true; for (; nxt < 10; ++nxt) issue_f(nxt); }
                if (nxt - (t + 2) >= 2) WAIT_VM(16); else WAIT_VM(0);
                BARRIER();
                if (!uready) uready = flags_set(P.flags(), udep, tag);
                if (uready) for (; nxt <= t + NS; ++nxt) issue_f(nxt);
            }
        }
#pragma unroll 1
        for (; nxt < nf - 2; ++nxt) {
            int r = korder(nxt - 7, cls, nf - 7) + (grp & 1) * ((nf - 7) >> 1);
            r = r >= nf - 7 ? r - (nf - 7) : r;
            int k = 14 * (grp >> 1) + (isM ? 14 + NA : 14) + r;
            k = k >= 56 ? k - 56 : k;
            L.turn<false, true, 16>(g2 + k * W2S, gu + k * US, ringl + (nxt & (NS - 1)) * SB);
        }
        const unsigned xo = (unsigned)(((fm * 128 + (lane >> 2)) * D_ + fn * 128 + lw * 32 + (lane & 3) * 8) * 2);
        constexpr unsigned ROWS16 = 16 * D_ * 2;
        WAIT_VM(16); BARRIER(); peek_issue(pk, P.flags(), odep);
        issue_f(nf - 2);
        WAIT_VM(16); BARRIER(); issue_f(nf - 1);
        L.init_n<TR>(lw, lane, D_ * 2);
        const unsigned oo = (unsigned)(lw * 1024 + lane * 16);
        const unsigned oo2 = oo + (unsigned)(OS / 2);
        const char *po, *pw; unsigned pd;
        auto addr_w = [&](int i) {
            const int k = (korder(i, cls, KW) + 4 * (grp >> 1)) & (KW - 1);
            po = go + k * OS; pw = gw + k * WOS; pd = ringl + ((nf + i) & (NS - 1)) * SB;
            asm volatile("" : "+s"(po), "+s"(pw), "+s"(pd));
        };
        auto issue_w = [&]() {
            asm volatile("s_add_u32 m0, %2, %3\n\ts_nop 0\n\tglobal_load_lds_dwordx4 %4, %0 sc1\n\ts_addk_i32 m0, 8448\n\ts_nop 0\n\tglobal_load_lds_dwordx4 %5, %0 sc1\n\t"
                         "s_add_u32 m0, %2, 16896\n\ts_nop 0\n\tglobal_load_lds_dwordx4 %6, %1\n\ts_addk_i32 m0, 4224\n\ts_nop 0\n\tglobal_load_lds_dwordx4 %7, %1\n\t"
                         "s_addk_i32 m0, 4224\n\ts_nop 0\n\tglobal_load_lds_dwordx4 %8, %1\n\ts_addk_i32 m0, 4224\n\ts_nop 0\n\tglobal_load_lds_dwordx4 %9, %1"
                         ::"s"(po), "s"(pw), "s"(pd), "s"(own * 4 * PIECE), "v"(oo), "v"(oo2), "v"(L.on[0]), "v"(L.on[1]), "v"(L.on[2]), "v"(L.on[3]) : "scc", "memory");
        };
#define TURN_W(i, w) do { addr_w(i); WAIT_VM(w); BARRIER(); issue_w(); } while (0)
        addr_w(0);
        WAIT_VM(16);
        if (!peek_ok(pk, odep, tag)) poll_flags(P.flags(), odep, tag);
        BARRIER(); issue_w();
        TURN_W(1, 14); TURN_W(2, 12); TURN_W(3, 12);
        TURN_W(4, 12);
        {
            u32x4 t0, t1, t2, t3;
            asm volatile("ds_read_b128 %0, %4\n\tds_read_b128 %1, %4 offset:1024\n\tds_read_b128 %2, %4 offset:2048\n\tds_read_b128 %3, %4 offset:3072\n\ts_waitcnt lgkmcnt(0)"
                         : "=&v"(t0), "=&v"(t1), "=&v"(t2), "=&v"(t3) : "v"(ring0 + LDS_EX + lw * 4096 + lane * 16) : "memory");
            const __amdgpu_buffer_rsrc_t ro = rsrc(P.out, (unsigned)S_ * D_ * 2);
            const unsigned so = xo + (1 - own) * 4 * ROWS16;
            __builtin_amdgcn_raw_buffer_store_b128(t0, ro, so, 0, SC1); __builtin_amdgcn_raw_buffer_store_b128(t1, ro, so + ROWS16, 0, SC1);
            __builtin_amdgcn_raw_buffer_store_b128(t2, ro, so + 2 * ROWS16, 0, SC1); __builtin_amdgcn_raw_buffer_store_b128(t3, ro, so + 3 * ROWS16, 0, SC1);
        }
        TURN_W(5, 16); TURN_W(6, 16); TURN_W(7, 16);
        TURN_W(8, 12);
        asm volatile("global_store_dwordx2 %0, %1, off sc1" ::"v"((GAS u64*)P.flags() + FL_P + tile * 32 + 1 - own), "v"(tag + 1ull) : "memory");
        TURN_W(9, 13); TURN_W(10, 13); TURN_W(11, 13);
        TURN_W(12, 12); TURN_W(13, 12); TURN_W(14, 12);
        TURN_W(15, 12);
#undef TURN_W
        peek_issue(pk, P.flags(), pdep);
        WAIT_VM(13); BARRIER();
        WAIT_VM(7); BARRIER();
        WAIT_VM(0);
        auto load_partial = [&]() {
#pragma unroll
            for (int i = 0; i < 4; ++i) dma16<true, 0>((const char*)P.out + (own * 4 + i) * ROWS16, xo, ring0 + LDS_EX + (lw * 4 + i) * 1024);
        };
        const bool early = peek_ok(pk, pdep, tag);
        if (early) load_partial();
        BARRIER();
        BARRIER();
        if (!early) { poll_flags(P.flags(), pdep, tag); load_partial(); }
        WAIT_VM(0); BARRIER();
        if (blockIdx.x == 0 && lw == 0) {
#pragma unroll 1
            for (int i = 0; i < 4; ++i) poll_flags(P.flags(), FL_O + i * 64 + lane, tag);
            if (lane == 0) set_flag(P.flags(), FL_E, tag);
        }
    } else {
        const __amdgpu_buffer_rsrc_t ro = rsrc(P.out, (unsigned)S_ * D_ * 2);
        f32x4 acc[4][4];
        if (isM) {
            LAS char* xp = lds + LDS_X + (wn * 16 + wm * 4) * 1024 + (lane & 15) * 64 + (((lane >> 4) & 1) * 2 + (lane >> 5)) * 16;
            u32x4 xv[4][2]; unsigned bv[4];
#pragma unroll
            for (int i = 0; i < 4; ++i) {
                xv[i][0] = *(LAS u32x4*)(xp + i * 1024); xv[i][1] = *(LAS u32x4*)(xp + 8192 + i * 1024);
                bv[i] = *(LAS u16*)(lds + LDS_X + 32768 + (wm * 64 + i * 16 + (lane & 15)) * 2);
            }
            SCHED0();
#pragma unroll
            for (int i = 0; i < 4; ++i)
#pragma unroll
                for (int jp = 0; jp < 2; ++jp) {
                    auto e0 = __builtin_amdgcn_permlane16_swap(xv[i][jp][0], xv[i][jp][2], false, false);
                    auto e1 = __builtin_amdgcn_permlane16_swap(xv[i][jp][1], xv[i][jp][3], false, false);
                    acc[i][2 * jp] = (f32x4){lo16(e0[0]), hi16(e0[0]), lo16(e1[0]), hi16(e1[0])} + lo16(bv[i]);
                    acc[i][2 * jp + 1] = (f32x4){lo16(e0[1]), hi16(e0[1]), lo16(e1[1]), hi16(e1[1])} + lo16(bv[i]);
                }
            pin_acc<4, 4, 4>(acc);
        } else {
            float z = 0.f;
            asm volatile("" : "+v"(z));
#pragma unroll
            for (int i = 0; i < 4; ++i)
#pragma unroll
                for (int j = 0; j < 4; ++j) acc[i][j] = (f32x4){z, z, z, z};
        }
        pin_acc<4, 4, 4>(acc);
        Reader<TR, 128, FRAG, 128, 4, 4> rf;
        HalfReader rw;
        Regs<4, 4> R0;
        LAS char* exl = lds + LDS_EX + wn * 8192 + lane * 16;
        int slot = 0;
#define STEP(a, b) stage_step<NS, SB>(slot, a, b, R0, acc)
        rf.init(ring0, lane, wm, wn);
        WAIT_LGKM0();
        if (!isM) BARRIER();
        rf.read(R0, 0);
        for (int t = 0; t < (nf - 2) / 4; ++t) {
            if (__builtin_expect(!isM && (t == 1 || t == 2), 0)) {
                if (t == 1) WAIT_VM(0);
                else if (tid == 0) set_flag(P.flags(), FL_O + qb * 32 + hd + 16, tag);
            }
            STEP(rf, rf); STEP(rf, rf); STEP(rf, rf); STEP(rf, rf);
        }
        static_assert((NF_M & 3) == (NF_Q & 3), "NA must be odd");
#pragma unroll
        for (int t = 0; t < ((NF_M - 2) & 3); ++t) STEP(rf, rf);
        STEP(rf, rf);
        const int kq = wm != own ? 1 : 0;
        rw.init(ring0 + slot * SB, lane, own, kq, wn);
        STEP(rf, rw);
        if (kq) {
#pragma unroll
            for (int i = 0; i < 4; ++i)
#pragma unroll
                for (int jp = 0; jp < 2; ++jp) {
                    const f32x4 a = acc[i][2 * jp], b = acc[i][2 * jp + 1];
                    *(LAS u32x4*)(exl + jp * 4096 + i * 1024) = (u32x4){pack2(a[0], a[1]), pack2(a[2], a[3]), pack2(b[0], b[1]), pack2(b[2], b[3])};
                    acc[i][2 * jp] = (f32x4){0.f, 0.f, 0.f, 0.f}; acc[i][2 * jp + 1] = (f32x4){0.f, 0.f, 0.f, 0.f};
                }
        }
        pin_acc<4, 4, 4>(acc);
        Regs<4, 4> R1;
        for (int t = 0; t < 4; ++t) {
            half_step<NS, SB>(slot, rw, R0, R1, acc); half_step<NS, SB>(slot, rw, R1, R0, acc);
            half_step<NS, SB>(slot, rw, R0, R1, acc); half_step<NS, SB>(slot, rw, R1, R0, acc);
        }
#pragma unroll
        for (int i = 0; i < 2; ++i)
#pragma unroll
            for (int j = 0; j < 4; ++j) *(LAS f32x4*)(lds + (wid * 8 + i * 4 + j) * 1024 + lane * 16) = acc[2 + i][j];
        WAIT_LGKM0();
        BARRIER();
        const unsigned fo = (unsigned)(((fm * 128 + own * 64 + kq * 32 + (lane & 15)) * D_ + fn * 128 + wn * 64 + ((lane >> 4) & 1) * 16 + (lane >> 5) * 8) * 2);
#pragma unroll
        for (int i = 0; i < 2; ++i)
#pragma unroll
            for (int jp = 0; jp < 2; ++jp) {
                const u32x4 e = *(LAS u32x4*)(exl + jp * 4096 + (kq * 2 + i) * 1024);
                const f32x4 a = acc[i][2 * jp] + *(LAS f32x4*)(lds + ((wid ^ 2) * 8 + i * 4 + 2 * jp) * 1024 + lane * 16) + (f32x4){lo16(e[0]), hi16(e[0]), lo16(e[1]), hi16(e[1])};
                const f32x4 b = acc[i][2 * jp + 1] + *(LAS f32x4*)(lds + ((wid ^ 2) * 8 + i * 4 + 2 * jp + 1) * 1024 + lane * 16) + (f32x4){lo16(e[2]), hi16(e[2]), lo16(e[3]), hi16(e[3])};
                __builtin_amdgcn_raw_buffer_store_b128(pair16(pack2(a[0], a[1]), pack2(a[2], a[3]), pack2(b[0], b[1]), pack2(b[2], b[3])), ro, fo + i * (16 * D_ * 2) + jp * 64, 0, SC1);
            }
#undef STEP
    }
}

extern "C" void kernel_launch(void* const* d_in, const int* in_sizes, int n_in, void* d_out, int out_size, void* d_ws, size_t ws_size, hipStream_t stream) {
    char* ws = (char*)d_ws;
    Params P;
    P.x = (const u16*)d_in[0]; P.wqkv = (const u16*)d_in[1]; P.wo = (const u16*)d_in[2];
    P.w1 = (const u16*)d_in[4]; P.b1 = (const u16*)d_in[5]; P.w2 = (const u16*)d_in[6]; P.b2 = (const u16*)d_in[7];
    P.out = (u16*)d_out; P.ws = ws;
    (void)hipFuncSetAttribute((const void*)k_main, hipFuncAttributeMaxDynamicSharedMemorySize, LDS_BYTES);
    k_main<<<256, 512, LDS_BYTES, stream>>>(P);
}
```

```cpp
#include <hip/hip_runtime.h>
#include <hip/hip_bf16.h>
#include <stdint.h>
#define MORD 1
#define PUBFIRST 1

#define LAS __attribute__((address_space(3)))
#define GAS __attribute__((address_space(1)))
typedef __bf16 bf16x8 __attribute__((ext_vector_type(8)));
typedef __bf16 bf16x4 __attribute__((ext_vector_type(4)));
typedef __bf16 bf16x2 __attribute__((ext_vector_type(2)));
typedef float f32x2 __attribute__((ext_vector_type(2)));
typedef float f32x4 __attribute__((ext_vector_type(4)));
typedef float f32x16 __attribute__((ext_vector_type(16)));
typedef unsigned u32x4 __attribute__((ext_vector_type(4)));
typedef unsigned u32x2 __attribute__((ext_vector_type(2)));
typedef unsigned short u16;
typedef unsigned long long u64;

constexpr int S_ = 2048, D_ = 1024, F_ = 3584, NH = 32;

constexpr size_t WS_FLAGS = 0;
constexpr size_t WS_STAT  = 1 << 16;
constexpr size_t WS_QKV   = (1 << 17) + (size_t)S_ * D_ * 2;
constexpr size_t WS_U     = WS_QKV + (size_t)S_ * 3 * D_ * 2;
constexpr int FL_E = 0;
constexpr int FL_L = 32;
constexpr int FL_U = 8 * 32;
constexpr int FL_Q = 16 * 32;
constexpr int FL_O = 32 * 32;
constexpr int FL_P = 40 * 32;

constexpr size_t WS_DBG = 200u << 20;
#define STAMP_DECL unsigned ts_r = 0
#define STAMP_(kid, i) do { if (((kid) == 1 && (i) == 1) || ((kid) == 1 && (i) == 0)) { __builtin_amdgcn_sched_barrier(0); const unsigned r_ = (unsigned)__builtin_amdgcn_s_memrealtime(); __builtin_amdgcn_s_waitcnt(0xC07F); \
    ts_r = lane == (kid) * 8 + (i) ? r_ : ts_r; asm volatile("" : "+v"(ts_r)); __builtin_amdgcn_sched_barrier(0); } } while (0)
#define STAMP(kid, i) STAMP_(kid, i)
#define STAMPL(kid, i) STAMP_(kid, i)
#define STAMP_OUT() do { if (wid == 0 && !isM) { const unsigned t0_ = __builtin_amdgcn_readlane(ts_r, 8), d_ = __builtin_amdgcn_readlane(ts_r, 9) - t0_; \
    while ((unsigned)__builtin_amdgcn_s_memrealtime() - t0_ < 7000u + (d_ < 10000u ? d_ : 0u)) __builtin_amdgcn_s_sleep(1); } } while (0)
#define WAIT_VM(n) asm volatile("s_waitcnt vmcnt(%0)" ::"n"(n) : "memory")
#define WAIT_LGKM0() __builtin_amdgcn_s_waitcnt(0xC07F)
#define CFENCE() asm volatile("" ::: "memory")
#define BARRIER() do { CFENCE(); __builtin_amdgcn_s_barrier(); CFENCE(); } while (0)
#define SCHED0() __builtin_amdgcn_sched_barrier(0)
#define RLX_AGENT __ATOMIC_RELAXED, __HIP_MEMORY_SCOPE_AGENT
constexpr int SC1 = 16;

__device__ __forceinline__ float bf2f(u16 v) { return __uint_as_float((unsigned)v << 16); }
__device__ __forceinline__ float lo16(unsigned v) { return __uint_as_float(v << 16); }
__device__ __forceinline__ float hi16(unsigned v) { return __uint_as_float(v & 0xffff0000u); }
__device__ __forceinline__ unsigned pack2(float lo, float hi) {
    f32x2 v = {lo, hi};
    bf16x2 b = __builtin_convertvector(v, bf16x2);
    return __builtin_bit_cast(unsigned, b);
}
__device__ __forceinline__ float rbf(float x) { return __uint_as_float(pack2(x, 0.f) << 16); }
template <int CTRL, int RMASK> __device__ __forceinline__ float dpp_add(float v) {
    return v + __builtin_bit_cast(float, __builtin_amdgcn_update_dpp(0, __builtin_bit_cast(int, v), CTRL, RMASK, 0xf, false));
}
__device__ __forceinline__ float wave_sum(float v) {
    v = dpp_add<0x111, 0xf>(v); v = dpp_add<0x112, 0xf>(v); v = dpp_add<0x114, 0xf>(v); v = dpp_add<0x118, 0xf>(v);
    v = dpp_add<0x142, 0xa>(v); v = dpp_add<0x143, 0xc>(v);
    return __builtin_bit_cast(float, __builtin_amdgcn_readlane(__builtin_bit_cast(int, v), 63));
}
__device__ __forceinline__ __amdgpu_buffer_rsrc_t rsrc(const void* p, unsigned bytes) { return __builtin_amdgcn_make_buffer_rsrc((void*)p, 0, bytes, 0x00020000); }

enum { ROW = 0, TR = 1, FRAG = 2 };
constexpr int PIECE = 1056, BLK = 8 * PIECE;
template <int T, int X> struct Op {
    static constexpr int BYTES = X * 132;
    __device__ static __forceinline__ unsigned src_off(int blk, int p, int lane, unsigned ld, unsigned cb) {
        const int pos = lane >> 3;
        if (T == TR) {
            if (X == 224 && blk == 3) {
                const int ps = lane >> 2, g = (p & 1) | ((ps >> 3) << 1), r = 32 * (p >> 1) + 8 * g + 4 * ((ps >> 2) & 1) + (ps & 3);
                return (unsigned)r * ld + cb + (unsigned)((lane & 3) * 16);
            }
            const int g = (p >> 2) | (pos & 2), r = 32 * (pos >> 2) + 8 * g + 4 * (pos & 1) + (p & 3);
            return (unsigned)r * ld + cb + (unsigned)((lane & 7) * 16);
        } else if (T == ROW) {
            const int j = ((pos >> 1) & 1) | ((pos & 1) << 1) | (pos & 4);
            return (unsigned)(blk * 64 + j * 8 + p) * ld + (unsigned)((lane & 7) * 16);
        } else {
            return (unsigned)blk * ld + (unsigned)(p * 1024 + lane * 16);
        }
    }
};
template <bool SC, int IMM> __device__ __forceinline__ void dma16(const char* base, unsigned off, unsigned l) {
    if (SC) asm volatile("s_add_u32 m0, %2, %3\n\ts_nop 0\n\tglobal_load_lds_dwordx4 %0, %1 sc1" ::"v"(off), "s"(base), "s"(l), "n"(IMM) : "scc", "memory");
    else    asm volatile("s_add_u32 m0, %2, %3\n\ts_nop 0\n\tglobal_load_lds_dwordx4 %0, %1" ::"v"(off), "s"(base), "s"(l), "n"(IMM) : "scc", "memory");
}
#define DMA_FIRST(sc) "s_add_u32 m0, %1, %2\n\ts_nop 0\n\tglobal_load_lds_dwordx4 %3, %0" sc
#define DMA_NEXT(n, sc) "\n\ts_addk_i32 m0, 4224\n\ts_nop 0\n\tglobal_load_lds_dwordx4 %" #n ", %0" sc
#define DMA_G4(sc) DMA_FIRST(sc) DMA_NEXT(4, sc) DMA_NEXT(5, sc) DMA_NEXT(6, sc)
template <bool SC, int IMM, int N> __device__ __forceinline__ void dma_group(const char* base, const unsigned (&o)[N], unsigned l) {
    static_assert(4 * PIECE == 4224 && (N == 4 || ((N == 6 || N == 7) && !SC)), "dma_group");
    if constexpr (N == 4 && SC) asm volatile(DMA_G4(" sc1") ::"s"(base), "s"(l), "n"(IMM), "v"(o[0]), "v"(o[1]), "v"(o[2]), "v"(o[3]) : "scc", "memory");
    else if constexpr (N == 4) asm volatile(DMA_G4("") ::"s"(base), "s"(l), "n"(IMM), "v"(o[0]), "v"(o[1]), "v"(o[2]), "v"(o[3]) : "scc", "memory");
    else if constexpr (N == 6) asm volatile(DMA_G4("") DMA_NEXT(7, "") DMA_NEXT(8, "") ::"s"(base), "s"(l), "n"(IMM), "v"(o[0]), "v"(o[1]), "v"(o[2]), "v"(o[3]), "v"(o[4]), "v"(o[5]) : "scc", "memory");
    else asm volatile(DMA_G4("") DMA_NEXT(7, "") DMA_NEXT(8, "") DMA_NEXT(9, "") ::"s"(base), "s"(l), "n"(IMM), "v"(o[0]), "v"(o[1]), "v"(o[2]), "v"(o[3]), "v"(o[4]), "v"(o[5]), "v"(o[6]) : "scc", "memory");
}
__device__ __forceinline__ void dma4(const char* base, unsigned off, unsigned l) {
    asm volatile("s_mov_b32 m0, %2\n\ts_nop 0\n\tglobal_load_lds_dword %0, %1" ::"v"(off), "s"(base), "s"(l) : "memory");
}
__device__ __forceinline__ void dma16x4(const char* base, unsigned off, unsigned l) {
    asm volatile("s_mov_b32 m0, %2\n\ts_nop 0\n\tglobal_load_lds_dwordx4 %0, %1 sc1\n\tglobal_load_lds_dwordx4 %0, %1 offset:1024 sc1\n\t"
                 "global_load_lds_dwordx4 %0, %1 offset:2048 sc1\n\tglobal_load_lds_dwordx4 %0, %1 offset:3072 sc1" ::"v"(off), "s"(base), "s"(l) : "memory");
}

template <int T, int X> struct Addr {
    unsigned a, b;
    static constexpr int NV = (X == 224 || X == 192) ? 2 : 1;
    __device__ __forceinline__ void init(unsigned base, int lane, int w) {
        const int g = lane >> 4;
        b = 0;
        if (T == TR) {
            const int q = (lane & 15) >> 2, p = lane & 3;
            a = base + (unsigned)((q | ((g & 1) << 2)) * PIECE + (g >> 1) * 256 + p * 8 + (X == 128 ? w * BLK : w * 64));
            b = base + (unsigned)(3 * BLK + (g & 1) * PIECE + (g >> 1) * 512 + q * 64 + w * 32 + p * 8);
        } else if (T == ROW) {
            const int r = lane & 15;
            a = base + (unsigned)(w * BLK + (r & 7) * PIECE + (r >> 3) * 256 + g * 16);
        } else {
            a = base + (unsigned)(w * 4 * PIECE + lane * 16);
        }
    }
    __device__ __forceinline__ void step(unsigned d) { a += d; if (NV == 2) b += d; }
    __device__ __forceinline__ bf16x8 read(int n, int kk) const {
        if (T == TR) {
            LAS char* p; int t;
            if (X == 224 && n == 6) { p = (LAS char*)(uintptr_t)b + kk * 2 * PIECE; t = 256; }
            else if (X == 192) { p = (LAS char*)(uintptr_t)((n & 1) ? b : a) + kk * 512 + (n >> 1) * BLK; t = 128; }
            else { p = (LAS char*)(uintptr_t)a + kk * 512 + (X == 128 ? n * 32 : (n >> 1) * BLK + (n & 1) * 32); t = 128; }
            const bf16x4 lo = __builtin_amdgcn_ds_read_tr16_b64_v4bf16((LAS bf16x4*)p);
            const bf16x4 hi = __builtin_amdgcn_ds_read_tr16_b64_v4bf16((LAS bf16x4*)(p + t));
            return __builtin_shufflevector(lo, hi, 0, 1, 2, 3, 4, 5, 6, 7);
        } else if (T == ROW) {
            return *(LAS bf16x8*)((LAS char*)(uintptr_t)a + (n & 1) * 128 + (n >> 1) * 512 + kk * 64);
        } else {
            return *(LAS bf16x8*)((LAS char*)(uintptr_t)a + kk * BLK + n * PIECE);
        }
    }
};
template <int FM, int FN> struct Regs { bf16x8 m[FM], n[FN]; };
#define MFMA16(a, b, c) __builtin_amdgcn_mfma_f32_16x16x32_bf16(a, b, c, 0, 0, 0)

template <int TM, int BM, int TN, int BN, int FM, int FN> struct Reader {
    Addr<TM, BM> m; Addr<TN, BN> n;
    static constexpr int NDS = FM * (TM == TR ? 2 : 1) + FN * (TN == TR ? 2 : 1), NV = Addr<TM, BM>::NV + Addr<TN, BN>::NV;
    __device__ __forceinline__ void init(unsigned slot_base, int lane, int wm, int wn) { m.init(slot_base, lane, wm); n.init(slot_base + BM * 132, lane, wn); }
    __device__ __forceinline__ void read(Regs<FM, FN>& R, int kk) const {
#pragma unroll
        for (int i = 0; i < FM; ++i) R.m[i] = m.read(i, kk);
#pragma unroll
        for (int j = 0; j < FN; ++j) R.n[j] = n.read(j, kk);
    }
    __device__ __forceinline__ void step(unsigned d) { m.step(d); n.step(d); }
};
template <int AM, int FM, int FN> __device__ __forceinline__ void mma(f32x4 (&acc)[AM][FN], const Regs<FM, FN>& R) {
    const u32x4 one = {0x3f803f80u, 0x3f803f80u, 0x3f803f80u, 0x3f803f80u};
#pragma unroll
    for (int j = 0; j < FN; ++j) {
        if (MORD == 1 && AM > FM && !(j & 1)) acc[AM - 1][j] = MFMA16(R.n[j], __builtin_bit_cast(bf16x8, one), acc[AM - 1][j]);
#pragma unroll
        for (int ii = 0; ii < FM; ++ii) { const int i = (MORD == 1 && (j & 1)) ? FM - 1 - ii : ii; acc[i][j] = MFMA16(R.n[j], R.m[i], acc[i][j]); }
        if (MORD == 0 && AM > FM && !(j & 1)) acc[AM - 1][j] = MFMA16(R.n[j], __builtin_bit_cast(bf16x8, one), acc[AM - 1][j]);
    }
}
template <int AM, int FM, int FN> __device__ __forceinline__ void pin_acc(f32x4 (&acc)[AM][FN]) {
#pragma unroll
    for (int j = 0; j < FN; ++j) {
        asm volatile("" : "+v"(acc[0][j]), "+v"(acc[1][j]), "+v"(acc[2][j]), "+v"(acc[3][j]));
        if (AM > FM && !(j & 1)) asm volatile("" : "+v"(acc[AM - 1][j]));
    }
}
template <int NDS, int NV, int NMF> __device__ __forceinline__ void interleave() {
#pragma unroll
    for (int i = 0; i < NMF; ++i) {
        __builtin_amdgcn_sched_group_barrier(0x8, 1, 0);
        if (i < NDS) __builtin_amdgcn_sched_group_barrier(0x80, 1, 0);
        if (i >= NMF - NV) __builtin_amdgcn_sched_group_barrier(0x2, 1, 0);
    }
}
template <int NS, int SB, class RD, class RDN, int AM, int FM, int FN>
__device__ __forceinline__ void stage_step(int& slot, RD& rd, RDN& rn, Regs<FM, FN>& R0, f32x4 (&acc)[AM][FN]) {
    constexpr int NMF = FM * FN + (AM - FM) * (FN / 2);
    Regs<FM, FN> R1;
    const unsigned d = (slot + 1 == NS) ? (unsigned)(-(NS - 1) * SB) : (unsigned)SB;
    slot = (slot + 1 == NS) ? 0 : slot + 1;
    rd.read(R1, 1);
    rn.step(d);
    mma(acc, R0);
    interleave<RD::NDS, RDN::NV, NMF>();
    SCHED0();
    pin_acc<AM, FM, FN>(acc);
    WAIT_LGKM0();
    BARRIER();
    SCHED0();
    rn.read(R0, 0);
    mma(acc, R1);
    interleave<RDN::NDS, 0, NMF>();
    SCHED0();
    pin_acc<AM, FM, FN>(acc);
    SCHED0();
}

struct HalfReader {
    unsigned m0, m1; Addr<TR, 128> n;
    static constexpr int NDS = 12, NV = 3;
    __device__ __forceinline__ void init(unsigned slot_base, int lane, int own, int kq, int wn) {
        const unsigned a = slot_base + (unsigned)(own * 4 * PIECE + kq * BLK + lane * 16);
        m0 = a + kq * 2 * PIECE; m1 = a - kq * 2 * PIECE;
        n.init(slot_base + 128 * 132 + kq * 512, lane, wn);
    }
    __device__ __forceinline__ void read(Regs<4, 4>& R, int) const {
#pragma unroll
        for (int i = 0; i < 4; ++i) R.m[i] = *(LAS bf16x8*)((LAS char*)(uintptr_t)(i < 2 ? m0 : m1) + i * PIECE);
#pragma unroll
        for (int j = 0; j < 4; ++j) R.n[j] = n.read(j, 0);
    }
    __device__ __forceinline__ void step(unsigned d) { m0 += d; m1 += d; n.step(d); }
};
template <int NS, int SB> __device__ __forceinline__ void half_step(int& slot, HalfReader& rd, Regs<4, 4>& Rc, Regs<4, 4>& Rn, f32x4 (&acc)[4][4]) {
    rd.step((slot + 1 == NS) ? (unsigned)(-(NS - 1) * SB) : (unsigned)SB);
    slot = (slot + 1 == NS) ? 0 : slot + 1;
    SCHED0();
    WAIT_LGKM0();
    BARRIER();
    SCHED0();
    rd.read(Rn, 0);
    mma(acc, Rc);
    interleave<12, 0, 16>();
    SCHED0();
    pin_acc<4, 4, 4>(acc);
    SCHED0();
}

template <int NM, int NN> struct Loader {
    static constexpr int NPW = NM + NN;
    unsigned om[NM], on[NN];
    template <int T> __device__ __forceinline__ void init_m(int lw, int lane, unsigned ld) {
#pragma unroll
        for (int i = 0; i < NM; ++i) om[i] = Op<T, NM * 32>::src_off(i >> 1, lw + 4 * (i & 1), lane, ld, (i >> 1) * 128);
    }
    template <int T> __device__ __forceinline__ void init_n(int lw, int lane, unsigned ld, unsigned c0 = 0, unsigned c1 = 128, unsigned c2 = 256, unsigned c3 = 384) {
#pragma unroll
        for (int i = 0; i < NN; ++i) on[i] = Op<T, NN * 32>::src_off(i >> 1, lw + 4 * (i & 1), lane, ld, (i >> 1) == 0 ? c0 : (i >> 1) == 1 ? c1 : (i >> 1) == 2 ? c2 : c3);
    }
    template <bool SC> __device__ __forceinline__ void issue_m(const char* gm, unsigned dst) const { dma_group<SC, 0>(gm, om, dst); }
    template <bool SC> __device__ __forceinline__ void issue_n(const char* gn, unsigned dst) const { dma_group<SC, NM * 32 * 132>(gn, on, dst); }
    template <bool SCM, bool SCN, int W> __device__ __forceinline__ void turn(const char* gm, const char* gn, unsigned dst) const {
        asm volatile("" : "+s"(gm), "+s"(gn), "+s"(dst));
        WAIT_VM(W); BARRIER();
#define NFIRST 0
        if (NFIRST && NN > 4) { issue_n<SCN>(gn, dst); issue_m<SCM>(gm, dst); } else { issue_m<SCM>(gm, dst); issue_n<SCN>(gn, dst); }
    }
};
#define LEAD 2
__device__ __forceinline__ int korder(int p, int c, int nk) {
    const int r = (p - c) & 3;
    const int d = ((LEAD == 2 ? 0x1003 : LEAD == 3 ? 0x0004 : 0x1102) >> (4 * r) & 15) - 1;
#define ROT0 0
    if (ROT0) return p < 4 ? (p + c) & 3 : (unsigned)(p - r - 4) < (unsigned)(nk - LEAD - 4) ? p + d : p;
    return (unsigned)(p - r) < (unsigned)(nk - LEAD) ? p + d : p;
}
__device__ __forceinline__ f32x2 gelu2(f32x2 u) {
    const float c1 = -2.0f * 0.7978845608028654f * 1.4426950408889634f, c2 = c1 * 0.044715f;
    const f32x2 z = u * ((u * u) * c2 + c1);
    f32x2 e; e.x = __builtin_amdgcn_exp2f(z.x); e.y = __builtin_amdgcn_exp2f(z.y);
    e = e + 1.0f;
    f32x2 r; r.x = __builtin_amdgcn_rcpf(e.x); r.y = __builtin_amdgcn_rcpf(e.y);
    return u * r;
}
__device__ __forceinline__ u32x4 pair16(unsigned x0, unsigned x1, unsigned y0, unsigned y1) {
    auto a = __builtin_amdgcn_permlane16_swap(x0, y0, false, false);
    auto b = __builtin_amdgcn_permlane16_swap(x1, y1, false, false);
    return (u32x4){a[0], b[0], a[1], b[1]};
}
__device__ __forceinline__ bool flags_set(u64* flags, int idx, u64 tag) {
    const u64 v = __hip_atomic_load((GAS u64*)flags + (idx < 0 ? 0 : idx), RLX_AGENT);
    const bool ok = __builtin_amdgcn_ballot_w64(idx >= 0 && v - 1ull != tag) == 0ull;
    CFENCE();
    return ok;
}
__device__ __forceinline__ void poll_flags(u64* flags, int idx, u64 tag) {
#pragma unroll 1
    for (unsigned spins = 0; spins < (1u << 20); ++spins) {
        if (flags_set(flags, idx, tag)) break;
#define PSLEEP 2
        __builtin_amdgcn_s_sleep(PSLEEP);
    }
}
__device__ __forceinline__ void peek_issue(u64& v, u64* flags, int idx) {
    asm volatile("global_load_dwordx2 %0, %1, off sc1" : "=v"(v) : "v"((GAS u64*)flags + (idx < 0 ? 0 : idx)) : "memory");
}
__device__ __forceinline__ bool peek_ok(u64& v, int idx, u64 tag) {
    asm volatile("" : "+v"(v) :: "memory");
    return __builtin_amdgcn_ballot_w64(idx >= 0 && v - 1ull != tag) == 0ull;
}
__device__ __forceinline__ void set_flag(u64* flags, int idx, u64 tag) {
    asm volatile("" : "+v"(tag));
    __hip_atomic_store((GAS u64*)flags + idx, tag + 1ull, RLX_AGENT);
}

struct Params {
    const u16 *x, *wqkv, *wo, *w1, *b1, *w2, *b2;
    u16* out;
    char* ws;
    __device__ __forceinline__ u16* h() const { return out; }
    __device__ __forceinline__ u16* qkv() const { return (u16*)(ws + WS_QKV); }
    __device__ __forceinline__ u16* U() const { return (u16*)(ws + WS_U); }
    __device__ __forceinline__ u16* o() const { return (u16*)(ws + WS_QKV); }
    __device__ __forceinline__ float* stat() const { return (float*)(ws + WS_STAT); }
    __device__ __forceinline__ u64* flags() const { return (u64*)(ws + WS_FLAGS); }
};

constexpr int SB_M = (128 + 224) * 132, SB_Q = (128 + 192) * 132, SB_F = 256 * 132, NS_F = 4;
constexpr int LDS_QX = 3 * SB_Q;
constexpr int LDS_CTL = LDS_QX + 3072;
constexpr int LDS_AQ = 0, LDS_AK = 2 * SB_Q, LDS_AV = 128 * 1024, LDS_BYTES = 160 * 1024;
constexpr int LDS_MC = LDS_BYTES - 512;
constexpr int LDS_X = 3 * SB_F;
constexpr int LDS_BK = 16 * 1024, LDS_BV = 96 * 1024;
constexpr int LDS_EX = 136 * 1024;
constexpr int LDS_SINK = LDS_BYTES - 256;
static_assert(LDS_CTL + 32 <= LDS_AV && 3 * SB_M <= LDS_SINK && 3 * SB_F <= LDS_AV && NS_F * SB_F <= LDS_EX && LDS_EX + 16384 <= LDS_MC, "LDS map");
#define NA 19
#define ROTQ 4
#define ROTF 1
#define ROTW 4
constexpr int KW = 16, NF_Q = 7 + NA, NF_M = 7 + 42 - NA;

__device__ __forceinline__ int qkv_head(int tn, int blk) { return blk * NH + ((tn & ~3) | ((tn + blk) & 3)) * 2; }

__device__ __forceinline__ f32x4 ln_row(const u32x4 a, const u32x4 b, u32x4& oa, u32x4& ob) {
    f32x2 v[8];
#pragma unroll
    for (int i = 0; i < 4; ++i) { v[i] = (f32x2){lo16(a[i]), hi16(a[i])}; v[4 + i] = (f32x2){lo16(b[i]), hi16(b[i])}; }
    const f32x2 s2 = ((v[0] + v[1]) + (v[2] + v[3])) + ((v[4] + v[5]) + (v[6] + v[7]));
    const float mean = wave_sum(s2.x + s2.y) * (1.0f / D_);
    f32x2 q2 = {0.f, 0.f};
#pragma unroll
    for (int i = 0; i < 8; ++i) { v[i] -= mean; q2 += v[i] * v[i]; }
    const float var = wave_sum(q2.x + q2.y) * (1.0f / D_);
    const float rs = __builtin_amdgcn_rcpf(rbf(__builtin_amdgcn_sqrtf(rbf(rbf(var) + 1e-5f))));
    const float sd2 = rbf(__builtin_amdgcn_sqrtf(rbf(rbf(var * rs * rs) + 1e-5f)));
    const float g = rs * __builtin_amdgcn_rcpf(sd2);
#pragma unroll
    for (int i = 0; i < 8; ++i) {
        const f32x2 t = v[i] * g;
        if (i < 4) oa[i] = pack2(t.x, t.y); else ob[i - 4] = pack2(t.x, t.y);
    }
    return (f32x4){mean, rs, sd2, 0.f};
}

#define MFMA32(a, b, c) __builtin_amdgcn_mfma_f32_32x32x16_bf16(a, b, c, 0, 0, 0)
__device__ __forceinline__ int attn_dep(int hd, int qb, int lane) {
    const int which = lane >> 2, st = 2 * qb - 2 + (lane & 3), p = hd >> 1;
    if (lane >= 12 || st < 0 || (which == 0 && (lane & 3) < 2)) return -1;
    return FL_Q + st * 32 + ((p & ~3) | ((p - which) & 3));
}
template <int WHICH> __device__ __forceinline__ void attn_load(const Params& P, unsigned dst, int hd, int qb, int w, int lane) {
    const int row = WHICH == 0 ? qb * 256 + w * 64 : qb * 256 - ((qb == 0 && w < 2) ? 0 : 256) + w * 128;
    const char* g = (const char*)(P.qkv() + ((size_t)(WHICH * NH + hd) * S_ + row) * 32);
    const unsigned off = (unsigned)((lane >> 2) * 64 + (WHICH == 2 ? lane & 3 : (lane & 3) ^ ((lane >> 4) & 3)) * 16);
    dma16x4(g, off, dst + w * (WHICH == 0 ? 4096 : 8192));
    if (WHICH != 0) dma16x4(g + 4096, off, dst + w * 8192 + 4096);
}
#define PIN(x) asm volatile("" : "+v"(x))
__device__ __forceinline__ float attn_scores(LAS char* buf, LAS char* qbuf, int nbad, int wid, int lane, unsigned up, f32x16 (&sc)[8]) {
    const int r32 = lane & 31, hh = lane >> 5, sw = (r32 >> 2) & 3;
    LAS char* k0p = buf + (wid * 32 + r32) * 64 + ((hh ^ sw) * 16);
    LAS char* k1p = buf + (wid * 32 + r32) * 64 + (((2 + hh) ^ sw) * 16);
    const bf16x8 qf0 = *(LAS bf16x8*)(qbuf + (wid * 32 + r32) * 64 + ((hh ^ sw) * 16));
    const bf16x8 qf1 = *(LAS bf16x8*)(qbuf + (wid * 32 + r32) * 64 + (((2 + hh) ^ sw) * 16));
    bf16x8 ka[9], kb[9];
#pragma unroll
    for (int i = 0; i < 3; ++i) { const int j = i == 0 ? 8 : i - 1; ka[i] = *(LAS bf16x8*)(k0p + j * 2048); kb[i] = *(LAS bf16x8*)(k1p + j * 2048); SCHED0(); }
    const float NEG = -INFINITY;
    const f32x16 zero = {};
    f32x16 s8;
    float m = NEG;
#pragma unroll
    for (int i = 0; i < 11; ++i) {
        if (i + 3 < 9) { ka[i + 3] = *(LAS bf16x8*)(k0p + (i + 2) * 2048); kb[i + 3] = *(LAS bf16x8*)(k1p + (i + 2) * 2048); }
        if (i < 9) {
            const f32x16 t = MFMA32(kb[i], qf1, MFMA32(ka[i], qf0, zero));
            if (i == 0) { s8 = t; PIN(s8); } else { sc[i - 1] = t; PIN(sc[i - 1]); }
        }
        SCHED0();
        const int j = i - 3;
        if (j == 0) {
            if (__builtin_expect(nbad > 0, 0)) {
#pragma unroll
                for (int r = 0; r < 16; ++r) sc[0][r] = NEG;
                PIN(sc[0]);
            }
#pragma unroll
            for (int r = 0; r < 16; ++r) {
                const unsigned mk = (unsigned)__builtin_amdgcn_sbfe((int)up, (r & 3) + 8 * (r >> 2), 1);
                sc[0][r] = __uint_as_float(__builtin_amdgcn_bitop3_b32(mk, __float_as_uint(sc[0][r]), __float_as_uint(s8[r]), 0xca));
            }
        }
        if (j >= 0) {
            float mj = m;
#pragma unroll
            for (int r = 0; r < 16; r += 2) mj = __builtin_fmaxf(__builtin_fmaxf(mj, sc[j][r]), sc[j][r + 1]);
            m = (j > 0 && j < nbad) ? m : mj;
            PIN(m);
        }
        SCHED0();
    }
    auto rr = __builtin_amdgcn_permlane32_swap(__float_as_uint(m), __float_as_uint(m), false, false);
    return __builtin_fmaxf(__uint_as_float(rr[0]), __uint_as_float(rr[1]));
}
__device__ __forceinline__ bf16x8 v_read(LAS char* vp, int j, int s) {
    const bf16x4 lo = __builtin_amdgcn_ds_read_tr16_b64_v4bf16((LAS bf16x4*)(vp + j * 2048 + s * 1024));
    const bf16x4 hi = __builtin_amdgcn_ds_read_tr16_b64_v4bf16((LAS bf16x4*)(vp + j * 2048 + s * 1024 + 512));
    return __builtin_shufflevector(lo, hi, 0, 1, 2, 3, 4, 5, 6, 7);
}
__device__ __forceinline__ void attn_pv(LAS char* buf, int nbad, int wid, int lane, unsigned up, f32x16 (&sc)[8], float m, f32x16& o, float& lsum) {
    const int hh = lane >> 5;
    LAS char* vp = buf + (wid * 32 + 4 * hh + ((lane & 15) >> 2)) * 64 + ((lane >> 4) & 1) * 32 + (lane & 3) * 8;
    u32x4 one4 = {0x3f803f80u, 0x3f803f80u, 0x3f803f80u, 0x3f803f80u};
    PIN(one4);
    const bf16x8 ones = __builtin_bit_cast(bf16x8, one4);
    const float h0 = rbf(-m), h1 = rbf(-m - h0);
    const unsigned w0 = hh ? 0u : pack2(h0, h1), w1 = hh ? 0u : pack2((-m - h0) - h1, 0.f);
    auto minus_m = [&](int j) {
        const u32x4 bm = {(j > 0 && j < nbad) ? 0xff80u : w0, w1, 0u, 0u};
        sc[j] = MFMA32(ones, __builtin_bit_cast(bf16x8, bm), sc[j]);
        PIN(sc[j]);
    };
    minus_m(0); minus_m(1);
    f32x16 l = {};
    bf16x8 vf[2], v8[2];
    u32x4 pw[2], p8[2], pl[2];
    SCHED0();
#pragma unroll
    for (int j = 0; j < 8; ++j) {
        bf16x8 vn[2] = {v_read(vp, j, 0), v_read(vp, j, 1)};
        if (j == 0) { v8[0] = v_read(vp, 8, 0); v8[1] = v_read(vp, 8, 1); }
        SCHED0();
#pragma unroll
        for (int g = 0; g < 4; ++g) {
#pragma unroll
            for (int r = 4 * g; r < 4 * g + 4; ++r) sc[j][r] = __builtin_amdgcn_exp2f(sc[j][r]);
            PIN(sc[j]);
            SCHED0();
            if (j > 0) {
                if (g & 1) { l = MFMA32(ones, __builtin_bit_cast(bf16x8, j == 1 ? pl[g >> 1] : pw[g >> 1]), l); PIN(l); }
                else { o = MFMA32(vf[g >> 1], __builtin_bit_cast(bf16x8, pw[g >> 1]), o); PIN(o); }
            }
            if (j == 1 && (g & 1)) { o = MFMA32(v8[g >> 1], __builtin_bit_cast(bf16x8, p8[g >> 1]), o); PIN(o); }
            SCHED0();
        }
#pragma unroll
        for (int i = 0; i < 8; ++i) pw[i >> 2][i & 3] = pack2(sc[j][2 * i], sc[j][2 * i + 1]);
        if (j == 0) {
            pl[0] = pw[0]; pl[1] = pw[1];
#pragma unroll
            for (int i = 0; i < 8; ++i) {
                const int kr = ((2 * i) & 3) + 8 * (i >> 1);
                const unsigned mk = __builtin_amdgcn_bitop3_b32(0xffffu, (unsigned)__builtin_amdgcn_sbfe((int)up, kr, 1), (unsigned)__builtin_amdgcn_sbfe((int)up, kr + 1, 1), 0xca);
                p8[i >> 2][i & 3] = pw[i >> 2][i & 3] & ~mk; pw[i >> 2][i & 3] &= mk;
            }
        }
        vf[0] = vn[0]; vf[1] = vn[1];
        PIN(pw[0]); PIN(pw[1]);
        SCHED0();
        if (j + 2 < 8) minus_m(j + 2);
        SCHED0();
    }
    l = MFMA32(ones, __builtin_bit_cast(bf16x8, pw[0]), l);
    l = MFMA32(ones, __builtin_bit_cast(bf16x8, pw[1]), l);
    o = MFMA32(vf[0], __builtin_bit_cast(bf16x8, pw[0]), o);
    o = MFMA32(vf[1], __builtin_bit_cast(bf16x8, pw[1]), o);
    lsum = l[0];
}
__device__ __forceinline__ void attn_out(const Params& P, int hd, int qblk, int lane, float l, const f32x16& oacc) {
    const int r32 = lane & 31, hh = lane >> 5;
    const float inv = __builtin_amdgcn_rcpf(l);
    unsigned w[8];
#pragma unroll
    for (int g = 0; g < 4; ++g) { w[2 * g] = pack2(oacc[4 * g] * inv, oacc[4 * g + 1] * inv); w[2 * g + 1] = pack2(oacc[4 * g + 2] * inv, oacc[4 * g + 3] * inv); }
    const __amdgpu_buffer_rsrc_t rs = rsrc(P.o(), (unsigned)S_ * D_ * 2);
    const unsigned off = (unsigned)((hd * (S_ / 16) + qblk * 2 + (r32 >> 4)) * 1024 + (hh * 16 + (r32 & 15)) * 16);
#pragma unroll
    for (int gp = 0; gp < 2; ++gp) {
        auto a = __builtin_amdgcn_permlane32_swap(w[4 * gp], w[4 * gp + 2], false, false);
        auto b = __builtin_amdgcn_permlane32_swap(w[4 * gp + 1], w[4 * gp + 3], false, false);
        __builtin_amdgcn_raw_buffer_store_b128((u32x4){a[0], b[0], a[1], b[1]}, rs, off + gp * 512, 0, SC1);
    }
}

extern "C" __global__ __launch_bounds__(512, 2) void k_main(const Params P) {
    extern __shared__ __attribute__((aligned(16))) char smem[];
    LAS char* lds = (LAS char*)smem;
    const int tid = threadIdx.x, lane = tid & 63, wid = __builtin_amdgcn_readfirstlane(tid >> 6);
    const int grp = blockIdx.x & 7, idx = blockIdx.x >> 3;
    const bool loader = wid >= 4;
    const int lw = wid & 3, wm = (wid >> 1) & 1, wn = wid & 1;
    const unsigned ring0 = (unsigned)(uintptr_t)lds, ringl = ring0 + lw * PIECE;
    const int i16 = idx & 15;
    const bool isM = idx < 16;
    const int fm = (grp >> 1) * 4 + (isM ? i16 & 3 : i16 >> 2), fn = (grp & 1) * 4 + (isM ? i16 >> 2 : i16 & 3);
    const int um = (grp & 1) * 4 + (i16 >> 2), un = (grp >> 1) * 4 + (i16 & 3);
    const int hd = (grp & 1) * 8 + (i16 & 7), qb = (grp >> 1) * 2 + (i16 >> 3);
    STAMP_DECL;
    STAMP(1, 0);
#define PFC 1
    if (PFC && wid == 0) {
        u64 pc; unsigned rel, len;
        asm volatile("s_getpc_b64 %0\n1:\n\ts_mov_b32 %1, 1b-k_main\n\ts_mov_b32 %2, 2f-k_main\n\t.subsection 1\n2:\n\t.subsection 0" : "=s"(pc), "=s"(rel), "=s"(len));
        const unsigned off = (unsigned)((idx + 32 * (lane & 15)) * 128);
        dma4((const char*)(pc - rel), off < len ? off : 0u, ring0 + LDS_SINK);
    }
    u64 tag;
    asm volatile("global_load_dwordx2 %0, %1, off sc1" : "=v"(tag) : "v"((GAS u64*)P.flags() + FL_E) : "memory");

    if (tid < 8) *(LAS unsigned*)(lds + (isM ? LDS_MC : LDS_CTL) + tid * 4) = 0u;
#define PF3 1
    auto warm = [&](int r) {
        const int n = ((r * 16 + i16) * 4 + lw) * 7 + (lane < 56 ? lane >> 3 : 6), row = n * 2 + (grp & 1);
        int f = row + 896 * (grp >> 1); f = f >= F_ ? f - F_ : f;
        if (r < PF3) dma4((const char*)P.w2 + (grp >> 1) * 1024, (unsigned)(f * (S_ * 2) + (lane & 7) * 128), ring0 + LDS_SINK);
    };
    if (isM) {
        constexpr int NK = S_ / 64, SB = SB_M;
        const int pg = (grp >> 1) + 4 * (grp & 1);
        const int tm = um, tn = un, mi = pg * 16 + i16;
        const char* gm = (const char*)P.h() + tm * 256; const char* gn = (const char*)P.w1 + tn * 448;
        const int cls = (tm + tn) & 3;
        auto kof = [&](int u) { const int p0 = u - ((u - cls) & 3); return __builtin_amdgcn_readfirstlane((((!ROT0 && (p0 == 1 || p0 == 2)) ? u : korder(u, cls, NK)) + 4 * pg) & (NK - 1)); };
        Loader<4, 7> L;
        {
            const int row = mi * 16 + wid * 2;
            const u32x4* px = (const u32x4*)(P.x + (size_t)row * D_);
            u32x4 a0 = px[lane], b0 = px[64 + lane], a1 = px[128 + lane], b1 = px[192 + lane];
            SCHED0();
            if (loader) {
                const unsigned c0 = (tn & 1) * 64;
                L.init_m<TR>(lw, lane, D_ * 2);
                L.init_n<TR>(lw, lane, F_ * 2, c0, c0 + 128, c0 + 256, (tn & 1) ? 0 : 384);
            }
            asm volatile("" : "+v"(a0), "+v"(b0), "+v"(a1), "+v"(b1));
#define W1EARLY 1
            if (W1EARLY && loader) L.issue_n<false>(gn + (size_t)kof(0) * (64 * F_ * 2), ringl);
            STAMPL(0, 0);
            const __amdgpu_buffer_rsrc_t rh = rsrc(P.h(), (unsigned)S_ * D_ * 2);
            u32x4 oa0, ob0, oa1, ob1;
            const f32x4 s0 = ln_row(a0, b0, oa0, ob0), s1 = ln_row(a1, b1, oa1, ob1);
            __builtin_amdgcn_raw_buffer_store_b128(oa0, rh, (unsigned)(row * 2048 + lane * 16), 0, SC1);
            __builtin_amdgcn_raw_buffer_store_b128(ob0, rh, (unsigned)(row * 2048 + 1024 + lane * 16), 0, SC1);
            __builtin_amdgcn_raw_buffer_store_b128(oa1, rh, (unsigned)(row * 2048 + 2048 + lane * 16), 0, SC1);
            __builtin_amdgcn_raw_buffer_store_b128(ob1, rh, (unsigned)(row * 2048 + 3072 + lane * 16), 0, SC1);
            if (lane < 2) __builtin_amdgcn_raw_buffer_store_b128(__builtin_bit_cast(u32x4, lane ? s1 : s0), rsrc(P.stat(), S_ * 16), (unsigned)((row + lane) * 16), 0, SC1);
            SCHED0();
            STAMPL(0, 1);
        }
        if (loader) {
            WAIT_VM(0);
            BARRIER();
            STAMPL(0, 2);
            if (tid == 256 + 64) set_flag(P.flags(), FL_L + mi, tag);
#define SEQ 0
#pragma unroll
            for (int u = W1EARLY; u < (SEQ ? 1 : 3); ++u) L.issue_n<false>(gn + (size_t)kof(u) * (64 * F_ * 2), ringl + u * SB);
            if (lw == 0) poll_flags(P.flags(), lane < 16 ? FL_L + pg * 16 + lane : -1, tag);
            STAMPL(0, 3);
            BARRIER();
            if (SEQ) {
                L.issue_m<true>(gm + (size_t)kof(0) * (64 * D_ * 2), ringl);
                L.issue_m<true>(gm + (size_t)kof(1) * (64 * D_ * 2), ringl + SB); L.issue_n<false>(gn + (size_t)kof(1) * (64 * F_ * 2), ringl + SB);
                WAIT_VM(11); BARRIER();
                L.issue_m<true>(gm + (size_t)kof(2) * (64 * D_ * 2), ringl + 2 * SB); L.issue_n<false>(gn + (size_t)kof(2) * (64 * F_ * 2), ringl + 2 * SB);
                WAIT_VM(11);
            } else {
#pragma unroll
                for (int u = 0; u < 3; ++u) L.issue_m<true>(gm + (size_t)kof(u) * (64 * D_ * 2), ringl + u * SB);
                WAIT_VM(8); BARRIER();
                WAIT_VM(4);
            }
            int slot = 0, t = 0;
            auto issue = [&]() {
                const int k = kof(t + 3);
                L.issue_m<true>(gm + (size_t)k * (64 * D_ * 2), ringl + slot * SB);
                L.issue_n<false>(gn + (size_t)k * (64 * F_ * 2), ringl + slot * SB);
                slot = (slot == 2) ? 0 : slot + 1;
            };
            if (lw == 0) poll_flags(P.flags(), lane < 16 ? FL_L + ((pg + 1) & 7) * 16 + lane : -1, tag);
            BARRIER(); issue(); ++t;
            WAIT_VM(L.NPW); BARRIER(); issue(); ++t;
            WAIT_VM(L.NPW);
            if (lw == 0) { poll_flags(P.flags(), FL_L + lane, tag); poll_flags(P.flags(), FL_L + 64 + lane, tag); }
            BARRIER(); issue(); ++t;
#pragma unroll 1
            for (; t < NK - 3; ++t) {
                const int k = (korder(t + 3, cls, NK) + 4 * pg) & (NK - 1);
                L.turn<true, false, 11>(gm + (size_t)k * (64 * D_ * 2), gn + (size_t)k * (64 * F_ * 2), ringl + slot * SB);
                slot = (slot == 2) ? 0 : slot + 1;
            }
            warm(0);
            WAIT_VM(L.NPW + (PF3 > 0)); BARRIER();
            WAIT_VM((PF3 > 0) + 0); BARRIER();
            BARRIER();
#pragma unroll
            for (int i = 0; i < 8; ++i)
                dma16<false, 0>((const char*)P.x + ((size_t)(fm * 128 + i * 16) * D_ + fn * 128 + lw * 32) * 2, (unsigned)((lane >> 2) * (D_ * 2) + (lane & 3) * 16), ring0 + LDS_X + (lw * 8 + i) * 1024);
            dma4((const char*)P.b2 + fm * 256, (unsigned)(lane * 4), ring0 + LDS_X + 32768);
        } else {
            const int fr = lane & 15, fq = lane >> 4;
            const int fb = tn * 224 + ((tn & 1) ? 32 : 0) + wn * 32, fh = tn * 224 + ((tn & 1) ? 0 : 192) + wn * 16;
            WAIT_VM(0);
            WAIT_LGKM0();
            BARRIER();
            f32x4 acc[4][7];
#pragma unroll
            for (int j = 0; j < 7; ++j) {
                const u32x2 bb = *(const u32x2*)(P.b1 + (j < 6 ? fb + (j >> 1) * 64 + (j & 1) * 16 : fh) + fq * 4);
                const f32x4 bv = {lo16(bb[0]), hi16(bb[0]), lo16(bb[1]), hi16(bb[1])};
#pragma unroll
                for (int i = 0; i < 4; ++i) acc[i][j] = bv;
            }
            Reader<TR, 128, TR, 224, 4, 7> rd; rd.init(ring0, lane, wm, wn);
            Regs<4, 7> R0;
            int slot = 0;
            BARRIER();
            __builtin_amdgcn_s_waitcnt(0x0070);
            BARRIER();
            STAMP(1, 1);
            rd.read(R0, 0);
            for (int t = 0; t < NK / 4; ++t) {
                stage_step<3, SB>(slot, rd, rd, R0, acc); stage_step<3, SB>(slot, rd, rd, R0, acc);
                stage_step<3, SB>(slot, rd, rd, R0, acc); stage_step<3, SB>(slot, rd, rd, R0, acc);
            }
            STAMP(1, 2);
            const __amdgpu_buffer_rsrc_t rs = rsrc(P.U(), (unsigned)D_ * F_ * 2);
            const unsigned df = (unsigned)(tm * 8 + wm * 4);
            const unsigned ob = (unsigned)((fb >> 5) * 64 + df) * 1024u + (unsigned)((((fq & 1) * 2 + (fq >> 1)) * 16 + fr) * 16);
            const unsigned oh = (unsigned)((fh >> 5) * 64 + df + (fq & 1)) * 1024u + (unsigned)(((wn * 2 + (fq >> 1)) * 16 + fr) * 16);
            LAS char* ul = lds + 128 * 132 + (wn * 8 + wm * 4) * PIECE + (((fq & 1) * 2 + (fq >> 1)) * 16 + fr) * 16;
            unsigned pk[4][7][2];
#pragma unroll
            for (int i = 0; i < 4; ++i) {
#pragma unroll
                for (int j = 0; j < 7; ++j) {
                    const f32x2 a = gelu2((f32x2){acc[i][j][0], acc[i][j][1]}), b = gelu2((f32x2){acc[i][j][2], acc[i][j][3]});
                    pk[i][j][0] = pack2(a.x, a.y); pk[i][j][1] = pack2(b.x, b.y);
                }
#pragma unroll
                for (int jp = 0; jp < 3; ++jp) {
                    const u32x4 w = pair16(pk[i][2 * jp][0], pk[i][2 * jp][1], pk[i][2 * jp + 1][0], pk[i][2 * jp + 1][1]);
                    __builtin_amdgcn_raw_buffer_store_b128(w, rs, ob + i * 1024 + jp * 2 * 65536, 0, SC1);
                    *(LAS u32x4*)(ul + jp * SB_F + i * PIECE) = w;
                }
                if (i & 1)
                    __builtin_amdgcn_raw_buffer_store_b128(pair16(pk[i - 1][6][0], pk[i - 1][6][1], pk[i][6][0], pk[i][6][1]), rs, oh + (i - 1) * 1024, 0, SC1);
                SCHED0();
            }
            STAMP(1, 3);
            WAIT_VM(0);
            if (lane == 0 && __hip_atomic_fetch_add((LAS unsigned*)(lds + LDS_MC), 1u, __ATOMIC_RELAXED, __HIP_MEMORY_SCOPE_WORKGROUP) == 3u)
                set_flag(P.flags(), FL_U + um * 32 + un, tag);
        }
    } else {
        constexpr int NK = D_ / 64, SB = SB_Q;
        const int tm = fm;
        if (loader) {
            Loader<4, 6> L; L.init_m<ROW>(lw, lane, D_ * 2);
            L.init_n<TR>(lw, lane, 3 * D_ * 2, qkv_head(fn, 0) * 64, qkv_head(fn, 1) * 64, qkv_head(fn, 2) * 64);
            const char* gm = (const char*)P.x + (size_t)tm * 128 * D_ * 2; const char* gn = (const char*)P.wqkv;
            const char* gh2 = (const char*)P.h() + (size_t)tm * 128 * D_ * 2;
            int slot = 0;
            auto issue = [&](int u) {
                const int k = (korder(u & (NK - 1), (tm + fn) & 3, NK) + ROTQ * (grp >> 1)) & (NK - 1);
                L.issue_m<false>(gm + k * 128, ringl + slot * SB);
                L.issue_n<false>(gn + (size_t)k * (64 * 3 * D_ * 2) + (u >= NK ? 16 * 64 : 0), ringl + slot * SB);
                slot = (slot == 2) ? 0 : slot + 1;
            };
#define QDELAY 0
            if (QDELAY) __builtin_amdgcn_s_sleep(QDELAY);
            issue(0); issue(1);
            WAIT_VM(L.NPW); BARRIER();
            issue(2);
            int t = 0;
            WAIT_VM(L.NPW); BARRIER(); issue(3); ++t;
#pragma unroll 1
            for (int seg = 0; seg < 2; ++seg) {
                const int end = seg == 0 ? 12 : 2 * NK - 3;
#pragma unroll 1
                for (; t < end; ++t) {
                    const int u = t + 3, k = (korder(u & (NK - 1), (tm + fn) & 3, NK) + ROTQ * (grp >> 1)) & (NK - 1);
                    L.turn<true, false, 10>((u >= NK ? gh2 : gm) + k * 128, gn + (size_t)k * (64 * 3 * D_ * 2) + (u & NK) * 64, ringl + slot * SB);
                    slot = (slot == 2) ? 0 : slot + 1;
                }
                if (seg == 0 && lw == 0) {
                    poll_flags(P.flags(), lane < 8 ? FL_L + tm * 8 + lane : -1, tag);
                    dma16<true, LDS_QX>((const char*)P.stat() + tm * 2048, (unsigned)(lane * 16), ring0);
                    dma16<true, LDS_QX + 1024>((const char*)P.stat() + tm * 2048 + 1024, (unsigned)(lane * 16), ring0);
                }
            }
            poll_flags(P.flags(), attn_dep(hd, qb, lane), tag);
            WAIT_VM(0);
            BARRIER();
            attn_load<1>(P, ring0 + LDS_AK, hd, qb, lw, lane);
            attn_load<2>(P, ring0 + LDS_AV, hd, qb, lw, lane);
            BARRIER();
            attn_load<0>(P, ring0 + LDS_AQ, hd, qb, lw, lane);
            BARRIER();
        } else {
            const int fr = lane & 15, fq = lane >> 4;
            Reader<ROW, 128, TR, 192, 4, 6> rd; rd.init(ring0, lane, wm, wn);
            rd.n.b = rd.n.a + 32 - wm * 32; rd.n.a += wm * 32;
            Regs<4, 6> R0;
            int slot = 0;
            __builtin_amdgcn_s_waitcnt(0x0070);
            BARRIER();
            STAMP(1, 1);
            rd.read(R0, 0);
            const __amdgpu_buffer_rsrc_t rs = rsrc(P.qkv(), (unsigned)S_ * 3 * D_ * 2);
            const int s0 = tm * 128 + wm * 64;
            const unsigned so = (unsigned)(((s0 + fr) * 32 + ((fq & 1) ^ wm) * 16 + (fq >> 1) * 8) * 2);
            constexpr float QS = 0.17677669529663687f * 1.4426950408889634f;
            {
                f32x4 acc[5][6] = {};
                for (int t = 0; t < NK / 4; ++t) {
                    stage_step<3, SB>(slot, rd, rd, R0, acc); stage_step<3, SB>(slot, rd, rd, R0, acc);
                    stage_step<3, SB>(slot, rd, rd, R0, acc); stage_step<3, SB>(slot, rd, rd, R0, acc);
                }
                LAS char* ex = lds + LDS_QX + 2048;
                if (fr == 0) {
#pragma unroll
                    for (int jp = 0; jp < 3; ++jp) *(LAS f32x4*)(ex + wid * 256 + jp * 64 + fq * 16) = acc[4][2 * jp];
                }
                WAIT_LGKM0();
                CFENCE();
                __hip_atomic_store((LAS unsigned*)(lds + LDS_CTL + wid * 4), 1u, __ATOMIC_RELAXED, __HIP_MEMORY_SCOPE_WORKGROUP);
                f32x2 st[4];
#pragma unroll
                for (int i = 0; i < 4; ++i) st[i] = *(LAS f32x2*)(lds + LDS_QX + (wm * 64 + i * 16 + fr) * 16);
                while (__builtin_amdgcn_readfirstlane(__hip_atomic_load((LAS unsigned*)(lds + LDS_CTL + (wid ^ 2) * 4), __ATOMIC_RELAXED, __HIP_MEMORY_SCOPE_WORKGROUP)) == 0u) {}
                CFENCE();
#pragma unroll
                for (int jp = 0; jp < 3; ++jp) {
                    const f32x4 ca = acc[4][2 * jp], cb = *(LAS f32x4*)(ex + (wid ^ 2) * 256 + jp * 64 + fq * 16);
#pragma unroll
                    for (int i = 0; i < 4; ++i) {
                        const float r = st[i].y * (jp == 0 ? QS : 1.0f), mr = -st[i].x * r;
                        const f32x4 a = acc[i][2 * jp] * r + ca * mr, b = acc[i][2 * jp + 1] * r + cb * mr;
                        __builtin_amdgcn_raw_buffer_store_b128(pair16(pack2(a[0], a[1]), pack2(a[2], a[3]), pack2(b[0], b[1]), pack2(b[2], b[3])), rs,
                                                               so + (unsigned)((qkv_head(fn, jp) + wn) * (S_ * 64) + i * 1024), 0, SC1);
                    }
                }
            }
            {
                f32x4 acc[4][6] = {};
                for (int t = 0; t < NK / 4; ++t) {
                    if (__builtin_expect(t == 1 || t == 2, 0)) {
                        if (t == 1) WAIT_VM(0);
                        else if (tid == 0) set_flag(P.flags(), FL_Q + tm * 32 + fn, tag);
                    }
                    stage_step<3, SB>(slot, rd, rd, R0, acc); stage_step<3, SB>(slot, rd, rd, R0, acc);
                    stage_step<3, SB>(slot, rd, rd, R0, acc); stage_step<3, SB>(slot, rd, rd, R0, acc);
                }
                STAMP(1, 2);
#pragma unroll
                for (int i = 0; i < 4; ++i) {
                    const float sd = *(LAS float*)(lds + LDS_QX + (wm * 64 + i * 16 + fr) * 16 + 8);
#pragma unroll
                    for (int jp = 0; jp < 3; ++jp) {
                        const float r = sd * (jp == 0 ? QS : 1.0f);
                        const f32x4 a = acc[i][2 * jp] * r, b = acc[i][2 * jp + 1] * r;
                        __builtin_amdgcn_raw_buffer_store_b128(pair16(pack2(a[0], a[1]), pack2(a[2], a[3]), pack2(b[0], b[1]), pack2(b[2], b[3])), rs,
                                                               so + (unsigned)((qkv_head(fn, jp) + 16 + wn) * (S_ * 64) + i * 1024), 0, SC1);
                    }
                }
            }
            STAMP(1, 3);
        }
    }

    constexpr int SB = SB_F, NS = NS_F;
    constexpr size_t OS = 2 * (S_ / 16) * 1024, WOS = 64 * D_ * 2, W2S = 64 * S_ * 2, US = 2 * 65536;
    const int tile = fm * 8 + fn, cls = (fm + fn) & 3, nf = isM ? NF_M : NF_Q;
    const int uh = ((fm >> 1) & 1) ^ (isM ? 0 : 1);
    auto fof = [&](int i) {
        int r = korder(i - 7, cls, nf - 7) + (ROTF ? (grp & 1) * ((nf - 7) >> 1) : 0);
        r = r >= nf - 7 ? r - (nf - 7) : r;
        const int im = (fm & 1) ? (i < 3 ? i + 4 : i - 3) : i;
        const int j = i < 7 ? 7 * uh + (isM ? im : korder(i, cls, 7)) : (isM ? 14 + NA : 14) + r, k = 14 * (grp >> 1) + j;
        return __builtin_amdgcn_readfirstlane(k >= 56 ? k - 56 : k);
    };
    const char* g2 = (const char*)P.w2 + fm * 256;
    Loader<4, 4> L;
    if (loader) L.init_m<TR>(lw, lane, S_ * 2);
    auto preload_w2 = [&]() {
        int k0 = fof(0), k1 = fof(1);
        asm volatile("" : "+s"(k0), "+s"(k1));
        L.issue_m<false>(g2 + k0 * W2S, ringl); L.issue_m<false>(g2 + k1 * W2S, ringl + SB);
    };
    if (isM) {
        if (loader) { preload_w2(); L.issue_m<false>(g2 + fof(2) * W2S, ringl + 2 * SB); WAIT_VM(0); warm(1); }
        STAMP(2, 0);
        WAIT_LGKM0();
        BARRIER();
        STAMP(2, 1);
    } else {
        if (loader) { WAIT_VM(0); __builtin_amdgcn_s_setprio(1); }
        WAIT_LGKM0();
        BARRIER();
#pragma unroll 1
        for (int it = 0; it < 2; ++it) {
            STAMP(2, 3 * it);
            f32x16 sc[8];
            int ln = lane;
            asm volatile("" : "+v"(ln));
            const int dg = (ln & 31) - 4 * (ln >> 5);
            const unsigned up = dg < 0 ? ~0u : ~1u << dg;
            int nbad = qb == 0 ? 8 - wid : 0;
            asm volatile("" : "+s"(nbad));
            SCHED0();
            const float m = attn_scores(lds + (it ? LDS_BK : LDS_AK), lds + LDS_AQ, nbad, wid, ln, up, sc);
            SCHED0();
            WAIT_VM(0);
            BARRIER();
            if (tid == 0) set_flag(P.flags(), it == 0 ? FL_Q + fm * 32 + fn + 8 : FL_O + qb * 32 + hd, tag);
            if (it == 1 && loader) preload_w2();
            STAMP(2, 3 * it + 1);
            f32x16 oacc = {};
            float lsum;
            attn_pv(lds + (it ? LDS_BV : LDS_AV), nbad, wid, ln, up, sc, m, oacc, lsum);
            attn_out(P, hd + 16 * it, qb * 8 + wid, ln, lsum, oacc);
            STAMP(2, 3 * it + 2);
            if (it == 0) {
                if (loader) {
                    __builtin_amdgcn_s_setprio(0);
                    int h2 = hd + 16, q2 = qb, w2 = lw;
                    asm volatile("" : "+s"(h2), "+s"(q2), "+s"(w2));
                    poll_flags(P.flags(), attn_dep(h2, q2, ln), tag);
                    attn_load<0>(P, ring0 + LDS_AQ, h2, q2, w2, ln);
                    attn_load<1>(P, ring0 + LDS_BK, h2, q2, w2, ln);
                    attn_load<2>(P, ring0 + LDS_BV, h2, q2, w2, ln);
                    WAIT_VM(8);
                    __builtin_amdgcn_s_setprio(1);
                }
                BARRIER();
            }
        }
        WAIT_LGKM0();
    }
    const int own = isM ? 0 : 1;
    if (loader) {
        const char* go = (const char*)P.o() + fm * 8192 + own * 4096;
        const char* gw = (const char*)P.wo + fn * 256;
        const char* gu = (const char*)P.U() + fn * 8192;
        __builtin_amdgcn_s_setprio(0);
        L.init_n<FRAG>(lw, lane, US / 2);
        auto issue_f = [&](int u) {
            const int k = fof(u);
            L.issue_m<false>(g2 + k * W2S, ringl + (u & (NS - 1)) * SB); L.issue_n<true>(gu + k * US, ringl + (u & (NS - 1)) * SB);
        };
        const int ulo = isM ? ((14 + NA) * 2) / 7 : 4, uhi = isM ? 15 : ((14 + NA) * 2 - 1) / 7;
        const int udep = (lane >= ulo && lane <= uhi) ? FL_U + fn * 32 + (((grp >> 1) * 4 + lane) & 15) : -1;
        const int odep = lane < 32 ? FL_O + (fm >> 1) * 32 + lane : lane < 48 ? FL_U + fn * 32 + lane - 32 : -1, pdep = lane == 0 ? FL_P + tile * 32 + own : -1;
        u64 pk;
        if (isM) { BARRIER(); BARRIER(); }
        poll_flags(P.flags(), lane < 2 ? FL_U + fn * 32 + (grp >> 1) * 4 + 2 * uh + lane : -1, tag);
        if (isM) { issue_f(3); issue_f(4); }
        else {
#pragma unroll
            for (int u = 0; u < 2; ++u) L.issue_n<true>(gu + fof(u) * US, ringl + u * SB);
            WAIT_VM(4); BARRIER();
            issue_f(2); issue_f(3);
            WAIT_VM(16); BARRIER(); issue_f(4);
            WAIT_VM(16); BARRIER();
        }
        peek_issue(pk, P.flags(), udep); issue_f(5);
        WAIT_VM(16); BARRIER(); issue_f(6);
        WAIT_VM(16);
        int nxt = 7;
        if (!peek_ok(pk, udep, tag)) {
            bool uready = false;
#pragma unroll 1
            for (int t = 3; t < 7; ++t) {
                if (t == 6 && !uready) { poll_flags(P.flags(), udep, tag); uready = true; for (; nxt < 10; ++nxt) issue_f(nxt); }
                if (nxt - (t + 2) >= 2) WAIT_VM(16); else WAIT_VM(0);
                BARRIER();
                if (!uready) uready = flags_set(P.flags(), udep, tag);
                if (uready) for (; nxt <= t + NS; ++nxt) issue_f(nxt);
            }
        }
#pragma unroll 1
        for (; nxt < nf - 2; ++nxt) {
            int r = korder(nxt - 7, cls, nf - 7) + (grp & 1) * ((nf - 7) >> 1);
            r = r >= nf - 7 ? r - (nf - 7) : r;
            int k = 14 * (grp >> 1) + (isM ? 14 + NA : 14) + r;
            k = k >= 56 ? k - 56 : k;
            L.turn<false, true, 16>(g2 + k * W2S, gu + k * US, ringl + (nxt & (NS - 1)) * SB);
        }
        const unsigned xo = (unsigned)(((fm * 128 + (lane >> 2)) * D_ + fn * 128 + lw * 32 + (lane & 3) * 8) * 2);
        constexpr unsigned ROWS16 = 16 * D_ * 2;
        WAIT_VM(16); BARRIER(); peek_issue(pk, P.flags(), odep);
        issue_f(nf - 2);
        WAIT_VM(16); BARRIER(); issue_f(nf - 1);
        L.init_n<TR>(lw, lane, D_ * 2);
        const unsigned oo = (unsigned)(lw * 1024 + lane * 16);
        const unsigned oo2 = oo + (unsigned)(OS / 2);
        const char *po, *pw; unsigned pd;
        auto addr_w = [&](int i) {
            const int k = (korder(i, cls, KW) + ROTW * (grp >> 1)) & (KW - 1);
            po = go + k * OS; pw = gw + k * WOS; pd = ringl + ((nf + i) & (NS - 1)) * SB;
            asm volatile("" : "+s"(po), "+s"(pw), "+s"(pd));
        };
        auto issue_w = [&]() {
            asm volatile("s_add_u32 m0, %2, %3\n\ts_nop 0\n\tglobal_load_lds_dwordx4 %4, %0 sc1\n\ts_addk_i32 m0, 8448\n\ts_nop 0\n\tglobal_load_lds_dwordx4 %5, %0 sc1\n\t"
                         "s_add_u32 m0, %2, 16896\n\ts_nop 0\n\tglobal_load_lds_dwordx4 %6, %1\n\ts_addk_i32 m0, 4224\n\ts_nop 0\n\tglobal_load_lds_dwordx4 %7, %1\n\t"
                         "s_addk_i32 m0, 4224\n\ts_nop 0\n\tglobal_load_lds_dwordx4 %8, %1\n\ts_addk_i32 m0, 4224\n\ts_nop 0\n\tglobal_load_lds_dwordx4 %9, %1"
                         ::"s"(po), "s"(pw), "s"(pd), "s"(own * 4 * PIECE), "v"(oo), "v"(oo2), "v"(L.on[0]), "v"(L.on[1]), "v"(L.on[2]), "v"(L.on[3]) : "scc", "memory");
        };
#define TURN_W(i, w) do { addr_w(i); WAIT_VM(w); BARRIER(); issue_w(); } while (0)
        addr_w(0);
        WAIT_VM(16);
        if (!peek_ok(pk, odep, tag)) poll_flags(P.flags(), odep, tag);
        STAMPL(0, 4);
        BARRIER(); issue_w();
        TURN_W(1, 14); TURN_W(2, 12); TURN_W(3, 12);
        TURN_W(4, 12);
        STAMPL(0, 5);
        {
            u32x4 t0, t1, t2, t3;
            asm volatile("ds_read_b128 %0, %4\n\tds_read_b128 %1, %4 offset:1024\n\tds_read_b128 %2, %4 offset:2048\n\tds_read_b128 %3, %4 offset:3072\n\ts_waitcnt lgkmcnt(0)"
                         : "=&v"(t0), "=&v"(t1), "=&v"(t2), "=&v"(t3) : "v"(ring0 + LDS_EX + lw * 4096 + lane * 16) : "memory");
            const __amdgpu_buffer_rsrc_t ro = rsrc(P.out, (unsigned)S_ * D_ * 2);
            const unsigned so = xo + (1 - own) * 4 * ROWS16;
            __builtin_amdgcn_raw_buffer_store_b128(t0, ro, so, 0, SC1); __builtin_amdgcn_raw_buffer_store_b128(t1, ro, so + ROWS16, 0, SC1);
            __builtin_amdgcn_raw_buffer_store_b128(t2, ro, so + 2 * ROWS16, 0, SC1); __builtin_amdgcn_raw_buffer_store_b128(t3, ro, so + 3 * ROWS16, 0, SC1);
        }
        TURN_W(5, 16); TURN_W(6, 16); TURN_W(7, 16);
        TURN_W(8, 12);
        STAMPL(0, 6);
        asm volatile("global_store_dwordx2 %0, %1, off sc1" ::"v"((GAS u64*)P.flags() + FL_P + tile * 32 + 1 - own), "v"(tag + 1ull) : "memory");
        TURN_W(9, 13); TURN_W(10, 13); TURN_W(11, 13);
        TURN_W(12, 12); TURN_W(13, 12); TURN_W(14, 12);
        STAMPL(0, 7);
        TURN_W(15, 12);
#undef TURN_W
        peek_issue(pk, P.flags(), pdep);
        WAIT_VM(13); BARRIER();
        WAIT_VM(7); BARRIER();
        WAIT_VM(0);
        auto load_partial = [&]() {
#pragma unroll
            for (int i = 0; i < 4; ++i) dma16<true, 0>((const char*)P.out + (own * 4 + i) * ROWS16, xo, ring0 + LDS_EX + (lw * 4 + i) * 1024);
        };
        const bool early = peek_ok(pk, pdep, tag);
        if (early) load_partial();
        BARRIER();
        BARRIER();
        if (!early) { poll_flags(P.flags(), pdep, tag); load_partial(); }
        WAIT_VM(0); BARRIER();
        if (blockIdx.x == 0 && lw == 0) {
#pragma unroll 1
            for (int i = 0; i < 4; ++i) poll_flags(P.flags(), FL_O + i * 64 + lane, tag);
            if (lane == 0) set_flag(P.flags(), FL_E, tag);
        }
    } else {
        const __amdgpu_buffer_rsrc_t ro = rsrc(P.out, (unsigned)S_ * D_ * 2);
        f32x4 acc[4][4];
        if (isM) {
            LAS char* xp = lds + LDS_X + (wn * 16 + wm * 4) * 1024 + (lane & 15) * 64 + (((lane >> 4) & 1) * 2 + (lane >> 5)) * 16;
            u32x4 xv[4][2]; unsigned bv[4];
#pragma unroll
            for (int i = 0; i < 4; ++i) {
                xv[i][0] = *(LAS u32x4*)(xp + i * 1024); xv[i][1] = *(LAS u32x4*)(xp + 8192 + i * 1024);
                bv[i] = *(LAS u16*)(lds + LDS_X + 32768 + (wm * 64 + i * 16 + (lane & 15)) * 2);
            }
            SCHED0();
#pragma unroll
            for (int i = 0; i < 4; ++i)
#pragma unroll
                for (int jp = 0; jp < 2; ++jp) {
                    auto e0 = __builtin_amdgcn_permlane16_swap(xv[i][jp][0], xv[i][jp][2], false, false);
                    auto e1 = __builtin_amdgcn_permlane16_swap(xv[i][jp][1], xv[i][jp][3], false, false);
                    acc[i][2 * jp] = (f32x4){lo16(e0[0]), hi16(e0[0]), lo16(e1[0]), hi16(e1[0])} + lo16(bv[i]);
                    acc[i][2 * jp + 1] = (f32x4){lo16(e0[1]), hi16(e0[1]), lo16(e1[1]), hi16(e1[1])} + lo16(bv[i]);
                }
            pin_acc<4, 4, 4>(acc);
            STAMP(2, 2);
        } else {
            float z = 0.f;
            asm volatile("" : "+v"(z));
#pragma unroll
            for (int i = 0; i < 4; ++i)
#pragma unroll
                for (int j = 0; j < 4; ++j) acc[i][j] = (f32x4){z, z, z, z};
        }
        pin_acc<4, 4, 4>(acc);
        Reader<TR, 128, FRAG, 128, 4, 4> rf;
        HalfReader rw;
        Regs<4, 4> R0;
        LAS char* exl = lds + LDS_EX + wn * 8192 + lane * 16;
        int slot = 0;
#define STEP(a, b) stage_step<NS, SB>(slot, a, b, R0, acc)
        rf.init(ring0, lane, wm, wn);
        WAIT_LGKM0();
        if (!isM) BARRIER();
        STAMP(3, 1);
        rf.read(R0, 0);
        for (int t = 0; t < (nf - 2) / 4; ++t) {
            if (__builtin_expect(!isM && (t == 1 || t == 2), 0)) {
                if (t == 1) WAIT_VM(0);
                else if (tid == 0) set_flag(P.flags(), FL_O + qb * 32 + hd + 16, tag);
            }
            STEP(rf, rf); STEP(rf, rf); STEP(rf, rf); STEP(rf, rf);
        }
        static_assert((NF_M & 3) == (NF_Q & 3), "NA must be odd");
#pragma unroll
        for (int t = 0; t < ((NF_M - 2) & 3); ++t) STEP(rf, rf);
        STEP(rf, rf);
        const int kq = wm != own ? 1 : 0;
        rw.init(ring0 + slot * SB, lane, own, kq, wn);
        STEP(rf, rw);
        STAMP(3, 4);
        if (kq) {
#pragma unroll
            for (int i = 0; i < 4; ++i)
#pragma unroll
                for (int jp = 0; jp < 2; ++jp) {
                    const f32x4 a = acc[i][2 * jp], b = acc[i][2 * jp + 1];
                    *(LAS u32x4*)(exl + jp * 4096 + i * 1024) = (u32x4){pack2(a[0], a[1]), pack2(a[2], a[3]), pack2(b[0], b[1]), pack2(b[2], b[3])};
                    acc[i][2 * jp] = (f32x4){0.f, 0.f, 0.f, 0.f}; acc[i][2 * jp + 1] = (f32x4){0.f, 0.f, 0.f, 0.f};
                }
        }
        pin_acc<4, 4, 4>(acc);
        Regs<4, 4> R1;
        for (int t = 0; t < 4; ++t) {
            half_step<NS, SB>(slot, rw, R0, R1, acc); half_step<NS, SB>(slot, rw, R1, R0, acc);
            half_step<NS, SB>(slot, rw, R0, R1, acc); half_step<NS, SB>(slot, rw, R1, R0, acc);
        }
        STAMP(3, 2);
#pragma unroll
        for (int i = 0; i < 2; ++i)
#pragma unroll
            for (int j = 0; j < 4; ++j) *(LAS f32x4*)(lds + (wid * 8 + i * 4 + j) * 1024 + lane * 16) = acc[2 + i][j];
        WAIT_LGKM0();
        BARRIER();
        const unsigned fo = (unsigned)(((fm * 128 + own * 64 + kq * 32 + (lane & 15)) * D_ + fn * 128 + wn * 64 + ((lane >> 4) & 1) * 16 + (lane >> 5) * 8) * 2);
#pragma unroll
        for (int i = 0; i < 2; ++i)
#pragma unroll
            for (int jp = 0; jp < 2; ++jp) {
                const u32x4 e = *(LAS u32x4*)(exl + jp * 4096 + (kq * 2 + i) * 1024);
                const f32x4 a = acc[i][2 * jp] + *(LAS f32x4*)(lds + ((wid ^ 2) * 8 + i * 4 + 2 * jp) * 1024 + lane * 16) + (f32x4){lo16(e[0]), hi16(e[0]), lo16(e[1]), hi16(e[1])};
                const f32x4 b = acc[i][2 * jp + 1] + *(LAS f32x4*)(lds + ((wid ^ 2) * 8 + i * 4 + 2 * jp + 1) * 1024 + lane * 16) + (f32x4){lo16(e[2]), hi16(e[2]), lo16(e[3]), hi16(e[3])};
                __builtin_amdgcn_raw_buffer_store_b128(pair16(pack2(a[0], a[1]), pack2(a[2], a[3]), pack2(b[0], b[1]), pack2(b[2], b[3])), ro, fo + i * (16 * D_ * 2) + jp * 64, 0, SC1);
            }
        STAMP(3, 3);
#undef STEP
    }
    STAMP_OUT();
}

extern "C" void kernel_launch(void* const* d_in, const int* in_sizes, int n_in, void* d_out, int out_size, void* d_ws, size_t ws_size, hipStream_t stream) {
    char* ws = (char*)d_ws;
    Params P;
    P.x = (const u16*)d_in[0]; P.wqkv = (const u16*)d_in[1]; P.wo = (const u16*)d_in[2];
    P.w1 = (const u16*)d_in[4]; P.b1 = (const u16*)d_in[5]; P.w2 = (const u16*)d_in[6]; P.b2 = (const u16*)d_in[7];
    P.out = (u16*)d_out; P.ws = ws;
    (void)hipFuncSetAttribute((const void*)k_main, hipFuncAttributeMaxDynamicSharedMemorySize, LDS_BYTES);
    k_main<<<256, 512, LDS_BYTES, stream>>>(P);
}
```

```cpp
#include <hip/hip_runtime.h>
#include <hip/hip_bf16.h>
#include <stdint.h>

#define LAS __attribute__((address_space(3)))
#define GAS __attribute__((address_space(1)))
typedef __bf16 bf16x8 __attribute__((ext_vector_type(8)));
typedef __bf16 bf16x4 __attribute__((ext_vector_type(4)));
typedef __bf16 bf16x2 __attribute__((ext_vector_type(2)));
typedef float f32x2 __attribute__((ext_vector_type(2)));
typedef float f32x4 __attribute__((ext_vector_type(4)));
typedef float f32x16 __attribute__((ext_vector_type(16)));
typedef unsigned u32x4 __attribute__((ext_vector_type(4)));
typedef unsigned u32x2 __attribute__((ext_vector_type(2)));
typedef unsigned short u16;
typedef unsigned long long u64;

constexpr int S_ = 2048, D_ = 1024, F_ = 3584, NH = 32;

constexpr size_t WS_FLAGS = 0;
constexpr size_t WS_STAT  = 1 << 16;
constexpr size_t WS_QKV   = 1 << 17;
constexpr size_t WS_U     = WS_QKV + (size_t)S_ * 3 * D_ * 2;
constexpr int FL_E = 0;
constexpr int FL_L = 32;
constexpr int FL_U = 8 * 32;
constexpr int FL_Q = 16 * 32;
constexpr int FL_O = 32 * 32;
constexpr int FL_P = 40 * 32;

#define WAIT_VM(n) asm volatile("s_waitcnt vmcnt(%0)" ::"n"(n) : "memory")
#define WAIT_LGKM0() __builtin_amdgcn_s_waitcnt(0xC07F)
#define CFENCE() asm volatile("" ::: "memory")
#define BARRIER() do { CFENCE(); __builtin_amdgcn_s_barrier(); CFENCE(); } while (0)
#define SCHED0() __builtin_amdgcn_sched_barrier(0)
#define RLX_AGENT __ATOMIC_RELAXED, __HIP_MEMORY_SCOPE_AGENT
constexpr int SC1 = 16;

__device__ __forceinline__ float bf2f(u16 v) { return __uint_as_float((unsigned)v << 16); }
__device__ __forceinline__ float lo16(unsigned v) { return __uint_as_float(v << 16); }
__device__ __forceinline__ float hi16(unsigned v) { return __uint_as_float(v & 0xffff0000u); }
__device__ __forceinline__ unsigned pack2(float lo, float hi) {
    f32x2 v = {lo, hi};
    bf16x2 b = __builtin_convertvector(v, bf16x2);
    return __builtin_bit_cast(unsigned, b);
}
__device__ __forceinline__ float rbf(float x) { return __uint_as_float(pack2(x, 0.f) << 16); }
template <int CTRL, int RMASK> __device__ __forceinline__ float dpp_add(float v) {
    return v + __builtin_bit_cast(float, __builtin_amdgcn_update_dpp(0, __builtin_bit_cast(int, v), CTRL, RMASK, 0xf, false));
}
__device__ __forceinline__ float wave_sum(float v) {
    v = dpp_add<0x111, 0xf>(v); v = dpp_add<0x112, 0xf>(v); v = dpp_add<0x114, 0xf>(v); v = dpp_add<0x118, 0xf>(v);
    v = dpp_add<0x142, 0xa>(v); v = dpp_add<0x143, 0xc>(v);
    return __builtin_bit_cast(float, __builtin_amdgcn_readlane(__builtin_bit_cast(int, v), 63));
}
__device__ __forceinline__ __amdgpu_buffer_rsrc_t rsrc(const void* p, unsigned bytes) { return __builtin_amdgcn_make_buffer_rsrc((void*)p, 0, bytes, 0x00020000); }

enum { ROW = 0, TR = 1, FRAG = 2 };
constexpr int PIECE = 1056, BLK = 8 * PIECE;
template <int T, int X> struct Op {
    static constexpr int BYTES = X * 132;
    __device__ static __forceinline__ unsigned src_off(int blk, int p, int lane, unsigned ld, unsigned cb) {
        const int pos = lane >> 3;
        if (T == TR) {
            if (X == 224 && blk == 3) {
                const int ps = lane >> 2, g = (p & 1) | ((ps >> 3) << 1), r = 32 * (p >> 1) + 8 * g + 4 * ((ps >> 2) & 1) + (ps & 3);
                return (unsigned)r * ld + cb + (unsigned)((lane & 3) * 16);
            }
            const int g = (p >> 2) | (pos & 2), r = 32 * (pos >> 2) + 8 * g + 4 * (pos & 1) + (p & 3);
            return (unsigned)r * ld + cb + (unsigned)((lane & 7) * 16);
        } else if (T == ROW) {
            const int j = ((pos >> 1) & 1) | ((pos & 1) << 1) | (pos & 4);
            return (unsigned)(blk * 64 + j * 8 + p) * ld + (unsigned)((lane & 7) * 16);
        } else {
            return (unsigned)blk * ld + (unsigned)(p * 1024 + lane * 16);
        }
    }
};
template <bool SC, int IMM> __device__ __forceinline__ void dma16(const char* base, unsigned off, unsigned l) {
    if (SC) asm volatile("s_add_u32 m0, %2, %3\n\ts_nop 0\n\tglobal_load_lds_dwordx4 %0, %1 sc1" ::"v"(off), "s"(base), "s"(l), "n"(IMM) : "scc", "memory");
    else    asm volatile("s_add_u32 m0, %2, %3\n\ts_nop 0\n\tglobal_load_lds_dwordx4 %0, %1" ::"v"(off), "s"(base), "s"(l), "n"(IMM) : "scc", "memory");
}
#define DMA_FIRST(sc) "s_add_u32 m0, %1, %2\n\ts_nop 0\n\tglobal_load_lds_dwordx4 %3, %0" sc
#define DMA_NEXT(n, sc) "\n\ts_addk_i32 m0, 4224\n\ts_nop 0\n\tglobal_load_lds_dwordx4 %" #n ", %0" sc
#define DMA_G4(sc) DMA_FIRST(sc) DMA_NEXT(4, sc) DMA_NEXT(5, sc) DMA_NEXT(6, sc)
template <bool SC, int IMM, int N> __device__ __forceinline__ void dma_group(const char* base, const unsigned (&o)[N], unsigned l) {
    static_assert(4 * PIECE == 4224 && (N == 4 || ((N == 6 || N == 7) && !SC)), "dma_group");
    if constexpr (N == 4 && SC) asm volatile(DMA_G4(" sc1") ::"s"(base), "s"(l), "n"(IMM), "v"(o[0]), "v"(o[1]), "v"(o[2]), "v"(o[3]) : "scc", "memory");
    else if constexpr (N == 4) asm volatile(DMA_G4("") ::"s"(base), "s"(l), "n"(IMM), "v"(o[0]), "v"(o[1]), "v"(o[2]), "v"(o[3]) : "scc", "memory");
    else if constexpr (N == 6) asm volatile(DMA_G4("") DMA_NEXT(7, "") DMA_NEXT(8, "") ::"s"(base), "s"(l), "n"(IMM), "v"(o[0]), "v"(o[1]), "v"(o[2]), "v"(o[3]), "v"(o[4]), "v"(o[5]) : "scc", "memory");
    else asm volatile(DMA_G4("") DMA_NEXT(7, "") DMA_NEXT(8, "") DMA_NEXT(9, "") ::"s"(base), "s"(l), "n"(IMM), "v"(o[0]), "v"(o[1]), "v"(o[2]), "v"(o[3]), "v"(o[4]), "v"(o[5]), "v"(o[6]) : "scc", "memory");
}
__device__ __forceinline__ void dma4(const char* base, unsigned off, unsigned l) {
    asm volatile("s_mov_b32 m0, %2\n\ts_nop 0\n\tglobal_load_lds_dword %0, %1" ::"v"(off), "s"(base), "s"(l) : "memory");
}
__device__ __forceinline__ void dma16x4(const char* base, unsigned off, unsigned l) {
    asm volatile("s_mov_b32 m0, %2\n\ts_nop 0\n\tglobal_load_lds_dwordx4 %0, %1 sc1\n\tglobal_load_lds_dwordx4 %0, %1 offset:1024 sc1\n\t"
                 "global_load_lds_dwordx4 %0, %1 offset:2048 sc1\n\tglobal_load_lds_dwordx4 %0, %1 offset:3072 sc1" ::"v"(off), "s"(base), "s"(l) : "memory");
}

template <int T, int X> struct Addr {
    unsigned a, b;
    static constexpr int NV = (X == 224 || X == 192) ? 2 : 1;
    __device__ __forceinline__ void init(unsigned base, int lane, int w) {
        const int g = lane >> 4;
        b = 0;
        if (T == TR) {
            const int q = (lane & 15) >> 2, p = lane & 3;
            a = base + (unsigned)((q | ((g & 1) << 2)) * PIECE + (g >> 1) * 256 + p * 8 + (X == 128 ? w * BLK : w * 64));
            b = base + (unsigned)(3 * BLK + (g & 1) * PIECE + (g >> 1) * 512 + q * 64 + w * 32 + p * 8);
        } else if (T == ROW) {
            const int r = lane & 15;
            a = base + (unsigned)(w * BLK + (r & 7) * PIECE + (r >> 3) * 256 + g * 16);
        } else {
            a = base + (unsigned)(w * 4 * PIECE + lane * 16);
        }
    }
    __device__ __forceinline__ void step(unsigned d) { a += d; if (NV == 2) b += d; }
    __device__ __forceinline__ bf16x8 read(int n, int kk) const {
        if (T == TR) {
            LAS char* p; int t;
            if (X == 224 && n == 6) { p = (LAS char*)(uintptr_t)b + kk * 2 * PIECE; t = 256; }
            else if (X == 192) { p = (LAS char*)(uintptr_t)((n & 1) ? b : a) + kk * 512 + (n >> 1) * BLK; t = 128; }
            else { p = (LAS char*)(uintptr_t)a + kk * 512 + (X == 128 ? n * 32 : (n >> 1) * BLK + (n & 1) * 32); t = 128; }
            const bf16x4 lo = __builtin_amdgcn_ds_read_tr16_b64_v4bf16((LAS bf16x4*)p);
            const bf16x4 hi = __builtin_amdgcn_ds_read_tr16_b64_v4bf16((LAS bf16x4*)(p + t));
            return __builtin_shufflevector(lo, hi, 0, 1, 2, 3, 4, 5, 6, 7);
        } else if (T == ROW) {
            return *(LAS bf16x8*)((LAS char*)(uintptr_t)a + (n & 1) * 128 + (n >> 1) * 512 + kk * 64);
        } else {
            return *(LAS bf16x8*)((LAS char*)(uintptr_t)a + kk * BLK + n * PIECE);
        }
    }
};
template <int FM, int FN> struct Regs { bf16x8 m[FM], n[FN]; };
#define MFMA16(a, b, c) __builtin_amdgcn_mfma_f32_16x16x32_bf16(a, b, c, 0, 0, 0)

template <int TM, int BM, int TN, int BN, int FM, int FN> struct Reader {
    Addr<TM, BM> m; Addr<TN, BN> n;
    static constexpr int NDS = FM * (TM == TR ? 2 : 1) + FN * (TN == TR ? 2 : 1), NV = Addr<TM, BM>::NV + Addr<TN, BN>::NV;
    __device__ __forceinline__ void init(unsigned slot_base, int lane, int wm, int wn) { m.init(slot_base, lane, wm); n.init(slot_base + BM * 132, lane, wn); }
    __device__ __forceinline__ void read(Regs<FM, FN>& R, int kk) const {
#pragma unroll
        for (int i = 0; i < FM; ++i) R.m[i] = m.read(i, kk);
#pragma unroll
        for (int j = 0; j < FN; ++j) R.n[j] = n.read(j, kk);
    }
    __device__ __forceinline__ void step(unsigned d) { m.step(d); n.step(d); }
};
template <int AM, int FM, int FN> __device__ __forceinline__ void mma(f32x4 (&acc)[AM][FN], const Regs<FM, FN>& R) {
    const u32x4 one = {0x3f803f80u, 0x3f803f80u, 0x3f803f80u, 0x3f803f80u};
#pragma unroll
    for (int j = 0; j < FN; ++j) {
        if (AM > FM && !(j & 1)) acc[AM - 1][j] = MFMA16(R.n[j], __builtin_bit_cast(bf16x8, one), acc[AM - 1][j]);
#pragma unroll
        for (int ii = 0; ii < FM; ++ii) { const int i = (j & 1) ? FM - 1 - ii : ii; acc[i][j] = MFMA16(R.n[j], R.m[i], acc[i][j]); }
    }
}
template <int AM, int FM, int FN> __device__ __forceinline__ void pin_acc(f32x4 (&acc)[AM][FN]) {
#pragma unroll
    for (int j = 0; j < FN; ++j) {
        asm volatile("" : "+v"(acc[0][j]), "+v"(acc[1][j]), "+v"(acc[2][j]), "+v"(acc[3][j]));
        if (AM > FM && !(j & 1)) asm volatile("" : "+v"(acc[AM - 1][j]));
    }
}
template <int NDS, int NV, int NMF> __device__ __forceinline__ void interleave() {
#pragma unroll
    for (int i = 0; i < NMF; ++i) {
        __builtin_amdgcn_sched_group_barrier(0x8, 1, 0);
        if (i < NDS) __builtin_amdgcn_sched_group_barrier(0x80, 1, 0);
        if (i >= NMF - NV) __builtin_amdgcn_sched_group_barrier(0x2, 1, 0);
    }
}
template <int NS, int SB, class RD, class RDN, int AM, int FM, int FN>
__device__ __forceinline__ void stage_step(int& slot, RD& rd, RDN& rn, Regs<FM, FN>& R0, f32x4 (&acc)[AM][FN]) {
    constexpr int NMF = FM * FN + (AM - FM) * (FN / 2);
    Regs<FM, FN> R1;
    const unsigned d = (slot + 1 == NS) ? (unsigned)(-(NS - 1) * SB) : (unsigned)SB;
    slot = (slot + 1 == NS) ? 0 : slot + 1;
    rd.read(R1, 1);
    rn.step(d);
    mma(acc, R0);
    interleave<RD::NDS, RDN::NV, NMF>();
    SCHED0();
    pin_acc<AM, FM, FN>(acc);
    WAIT_LGKM0();
    BARRIER();
    SCHED0();
    rn.read(R0, 0);
    mma(acc, R1);
    interleave<RDN::NDS, 0, NMF>();
    SCHED0();
    pin_acc<AM, FM, FN>(acc);
    SCHED0();
}

struct HalfReader {
    unsigned m0, m1; Addr<TR, 128> n;
    static constexpr int NDS = 12, NV = 3;
    __device__ __forceinline__ void init(unsigned slot_base, int lane, int own, int kq, int wn) {
        const unsigned a = slot_base + (unsigned)(own * 4 * PIECE + kq * BLK + lane * 16);
        m0 = a + kq * 2 * PIECE; m1 = a - kq * 2 * PIECE;
        n.init(slot_base + 128 * 132 + kq * 512, lane, wn);
    }
    __device__ __forceinline__ void read(Regs<4, 4>& R, int) const {
#pragma unroll
        for (int i = 0; i < 4; ++i) R.m[i] = *(LAS bf16x8*)((LAS char*)(uintptr_t)(i < 2 ? m0 : m1) + i * PIECE);
#pragma unroll
        for (int j = 0; j < 4; ++j) R.n[j] = n.read(j, 0);
    }
    __device__ __forceinline__ void step(unsigned d) { m0 += d; m1 += d; n.step(d); }
};
template <int NS, int SB> __device__ __forceinline__ void half_step(int& slot, HalfReader& rd, Regs<4, 4>& Rc, Regs<4, 4>& Rn, f32x4 (&acc)[4][4]) {
    rd.step((slot + 1 == NS) ? (unsigned)(-(NS - 1) * SB) : (unsigned)SB);
    slot = (slot + 1 == NS) ? 0 : slot + 1;
    SCHED0();
    WAIT_LGKM0();
    BARRIER();
    SCHED0();
    rd.read(Rn, 0);
    mma(acc, Rc);
    interleave<12, 0, 16>();
    SCHED0();
    pin_acc<4, 4, 4>(acc);
    SCHED0();
}

template <int NM, int NN> struct Loader {
    static constexpr int NPW = NM + NN;
    unsigned om[NM], on[NN];
    template <int T> __device__ __forceinline__ void init_m(int lw, int lane, unsigned ld) {
#pragma unroll
        for (int i = 0; i < NM; ++i) om[i] = Op<T, NM * 32>::src_off(i >> 1, lw + 4 * (i & 1), lane, ld, (i >> 1) * 128);
    }
    template <int T> __device__ __forceinline__ void init_n(int lw, int lane, unsigned ld, unsigned c0 = 0, unsigned c1 = 128, unsigned c2 = 256, unsigned c3 = 384) {
#pragma unroll
        for (int i = 0; i < NN; ++i) on[i] = Op<T, NN * 32>::src_off(i >> 1, lw + 4 * (i & 1), lane, ld, (i >> 1) == 0 ? c0 : (i >> 1) == 1 ? c1 : (i >> 1) == 2 ? c2 : c3);
    }
    template <bool SC> __device__ __forceinline__ void issue_m(const char* gm, unsigned dst) const { dma_group<SC, 0>(gm, om, dst); }
    template <bool SC> __device__ __forceinline__ void issue_n(const char* gn, unsigned dst) const { dma_group<SC, NM * 32 * 132>(gn, on, dst); }
    template <bool SCM, bool SCN, int W> __device__ __forceinline__ void turn(const char* gm, const char* gn, unsigned dst) const {
        asm volatile("" : "+s"(gm), "+s"(gn), "+s"(dst));
        WAIT_VM(W); BARRIER();
        issue_m<SCM>(gm, dst); issue_n<SCN>(gn, dst);
    }
};
__device__ __forceinline__ int korder(int p, int c, int nk) {
    const int r = (p - c) & 3;
    const int d = (0x1003 >> (4 * r) & 15) - 1;
    return (unsigned)(p - r) < (unsigned)(nk - 2) ? p + d : p;
}
__device__ __forceinline__ f32x2 gelu2(f32x2 u) {
    const float c1 = -2.0f * 0.7978845608028654f * 1.4426950408889634f, c2 = c1 * 0.044715f;
    const f32x2 z = u * ((u * u) * c2 + c1);
    f32x2 e; e.x = __builtin_amdgcn_exp2f(z.x); e.y = __builtin_amdgcn_exp2f(z.y);
    e = e + 1.0f;
    f32x2 r; r.x = __builtin_amdgcn_rcpf(e.x); r.y = __builtin_amdgcn_rcpf(e.y);
    return u * r;
}
__device__ __forceinline__ u32x4 pair16(unsigned x0, unsigned x1, unsigned y0, unsigned y1) {
    auto a = __builtin_amdgcn_permlane16_swap(x0, y0, false, false);
    auto b = __builtin_amdgcn_permlane16_swap(x1, y1, false, false);
    return (u32x4){a[0], b[0], a[1], b[1]};
}
__device__ __forceinline__ bool flags_set(u64* flags, int idx, u64 tag) {
    const u64 v = __hip_atomic_load((GAS u64*)flags + (idx < 0 ? 0 : idx), RLX_AGENT);
    const bool ok = __builtin_amdgcn_ballot_w64(idx >= 0 && v - 1ull != tag) == 0ull;
    CFENCE();
    return ok;
}
__device__ __forceinline__ void poll_flags(u64* flags, int idx, u64 tag) {
#pragma unroll 1
    for (unsigned spins = 0; spins < (1u << 20); ++spins) {
        if (flags_set(flags, idx, tag)) break;
        __builtin_amdgcn_s_sleep(2);
    }
}
__device__ __forceinline__ void peek_issue(u64& v, u64* flags, int idx) {
    asm volatile("global_load_dwordx2 %0, %1, off sc1" : "=v"(v) : "v"((GAS u64*)flags + (idx < 0 ? 0 : idx)) : "memory");
}
__device__ __forceinline__ bool peek_ok(u64& v, int idx, u64 tag) {
    asm volatile("" : "+v"(v) :: "memory");
    return __builtin_amdgcn_ballot_w64(idx >= 0 && v - 1ull != tag) == 0ull;
}
__device__ __forceinline__ void set_flag(u64* flags, int idx, u64 tag) {
    asm volatile("" : "+v"(tag));
    __hip_atomic_store((GAS u64*)flags + idx, tag + 1ull, RLX_AGENT);
}

struct Params {
    const u16 *x, *wqkv, *wo, *w1, *b1, *w2, *b2;
    u16* out;
    char* ws;
    __device__ __forceinline__ u16* h() const { return out; }
    __device__ __forceinline__ u16* qkv() const { return (u16*)(ws + WS_QKV); }
    __device__ __forceinline__ u16* U() const { return (u16*)(ws + WS_U); }
    __device__ __forceinline__ u16* o() const { return (u16*)(ws + WS_QKV); }
    __device__ __forceinline__ float* stat() const { return (float*)(ws + WS_STAT); }
    __device__ __forceinline__ u64* flags() const { return (u64*)(ws + WS_FLAGS); }
};

constexpr int SB_M = (128 + 224) * 132, SB_Q = (128 + 192) * 132, SB_F = 256 * 132, NS_F = 4;
constexpr int LDS_QX = 3 * SB_Q;
constexpr int LDS_CTL = LDS_QX + 3072;
constexpr int LDS_AQ = 0, LDS_AK = 2 * SB_Q, LDS_AV = 128 * 1024, LDS_BYTES = 160 * 1024;
constexpr int LDS_MC = LDS_BYTES - 512;
constexpr int LDS_X = 3 * SB_F;
constexpr int LDS_BK = 16 * 1024, LDS_BV = 96 * 1024;
constexpr int LDS_EX = 136 * 1024;
constexpr int LDS_SINK = LDS_BYTES - 256;
static_assert(LDS_CTL + 32 <= LDS_AV && 3 * SB_M <= LDS_SINK && 3 * SB_F <= LDS_AV && NS_F * SB_F <= LDS_EX && LDS_EX + 16384 <= LDS_MC, "LDS map");
constexpr int NA = 19;
constexpr int KW = 16, NF_Q = 7 + NA, NF_M = 7 + 42 - NA;

__device__ __forceinline__ int qkv_head(int tn, int blk) { return blk * NH + ((tn & ~3) | ((tn + blk) & 3)) * 2; }

__device__ __forceinline__ f32x4 ln_row(const u32x4 a, const u32x4 b, u32x4& oa, u32x4& ob) {
    f32x2 v[8];
#pragma unroll
    for (int i = 0; i < 4; ++i) { v[i] = (f32x2){lo16(a[i]), hi16(a[i])}; v[4 + i] = (f32x2){lo16(b[i]), hi16(b[i])}; }
    const f32x2 s2 = ((v[0] + v[1]) + (v[2] + v[3])) + ((v[4] + v[5]) + (v[6] + v[7]));
    const float mean = wave_sum(s2.x + s2.y) * (1.0f / D_);
    f32x2 q2 = {0.f, 0.f};
#pragma unroll
    for (int i = 0; i < 8; ++i) { v[i] -= mean; q2 += v[i] * v[i]; }
    const float var = wave_sum(q2.x + q2.y) * (1.0f / D_);
    const float rs = __builtin_amdgcn_rcpf(rbf(__builtin_amdgcn_sqrtf(rbf(rbf(var) + 1e-5f))));
    const float sd2 = rbf(__builtin_amdgcn_sqrtf(rbf(rbf(var * rs * rs) + 1e-5f)));
    const float g = rs * __builtin_amdgcn_rcpf(sd2);
#pragma unroll
    for (int i = 0; i < 8; ++i) {
        const f32x2 t = v[i] * g;
        if (i < 4) oa[i] = pack2(t.x, t.y); else ob[i - 4] = pack2(t.x, t.y);
    }
    return (f32x4){mean, rs, sd2, 0.f};
}

#define MFMA32(a, b, c) __builtin_amdgcn_mfma_f32_32x32x16_bf16(a, b, c, 0, 0, 0)
__device__ __forceinline__ int attn_dep(int hd, int qb, int lane) {
    const int which = lane >> 2, st = 2 * qb - 2 + (lane & 3), p = hd >> 1;
    if (lane >= 12 || st < 0 || (which == 0 && (lane & 3) < 2)) return -1;
    return FL_Q + st * 32 + ((p & ~3) | ((p - which) & 3));
}
template <int WHICH> __device__ __forceinline__ void attn_load(const Params& P, unsigned dst, int hd, int qb, int w, int lane) {
    const int row = WHICH == 0 ? qb * 256 + w * 64 : qb * 256 - ((qb == 0 && w < 2) ? 0 : 256) + w * 128;
    const char* g = (const char*)(P.qkv() + ((size_t)(WHICH * NH + hd) * S_ + row) * 32);
    const unsigned off = (unsigned)((lane >> 2) * 64 + (WHICH == 2 ? lane & 3 : (lane & 3) ^ ((lane >> 4) & 3)) * 16);
    dma16x4(g, off, dst + w * (WHICH == 0 ? 4096 : 8192));
    if (WHICH != 0) dma16x4(g + 4096, off, dst + w * 8192 + 4096);
}
#define PIN(x) asm volatile("" : "+v"(x))
__device__ __forceinline__ float attn_scores(LAS char* buf, LAS char* qbuf, int nbad, int wid, int lane, unsigned up, f32x16 (&sc)[8]) {
    const int r32 = lane & 31, hh = lane >> 5, sw = (r32 >> 2) & 3;
    LAS char* k0p = buf + (wid * 32 + r32) * 64 + ((hh ^ sw) * 16);
    LAS char* k1p = buf + (wid * 32 + r32) * 64 + (((2 + hh) ^ sw) * 16);
    const bf16x8 qf0 = *(LAS bf16x8*)(qbuf + (wid * 32 + r32) * 64 + ((hh ^ sw) * 16));
    const bf16x8 qf1 = *(LAS bf16x8*)(qbuf + (wid * 32 + r32) * 64 + (((2 + hh) ^ sw) * 16));
    bf16x8 ka[9], kb[9];
#pragma unroll
    for (int i = 0; i < 3; ++i) { const int j = i == 0 ? 8 : i - 1; ka[i] = *(LAS bf16x8*)(k0p + j * 2048); kb[i] = *(LAS bf16x8*)(k1p + j * 2048); SCHED0(); }
    const float NEG = -INFINITY;
    const f32x16 zero = {};
    f32x16 s8;
    float m = NEG;
#pragma unroll
    for (int i = 0; i < 11; ++i) {
        if (i + 3 < 9) { ka[i + 3] = *(LAS bf16x8*)(k0p + (i + 2) * 2048); kb[i + 3] = *(LAS bf16x8*)(k1p + (i + 2) * 2048); }
        if (i < 9) {
            const f32x16 t = MFMA32(kb[i], qf1, MFMA32(ka[i], qf0, zero));
            if (i == 0) { s8 = t; PIN(s8); } else { sc[i - 1] = t; PIN(sc[i - 1]); }
        }
        SCHED0();
        const int j = i - 3;
        if (j == 0) {
            if (__builtin_expect(nbad > 0, 0)) {
#pragma unroll
                for (int r = 0; r < 16; ++r) sc[0][r] = NEG;
                PIN(sc[0]);
            }
#pragma unroll
            for (int r = 0; r < 16; ++r) {
                const unsigned mk = (unsigned)__builtin_amdgcn_sbfe((int)up, (r & 3) + 8 * (r >> 2), 1);
                sc[0][r] = __uint_as_float(__builtin_amdgcn_bitop3_b32(mk, __float_as_uint(sc[0][r]), __float_as_uint(s8[r]), 0xca));
            }
        }
        if (j >= 0) {
            float mj = m;
#pragma unroll
            for (int r = 0; r < 16; r += 2) mj = __builtin_fmaxf(__builtin_fmaxf(mj, sc[j][r]), sc[j][r + 1]);
            m = (j > 0 && j < nbad) ? m : mj;
            PIN(m);
        }
        SCHED0();
    }
    auto rr = __builtin_amdgcn_permlane32_swap(__float_as_uint(m), __float_as_uint(m), false, false);
    return __builtin_fmaxf(__uint_as_float(rr[0]), __uint_as_float(rr[1]));
}
__device__ __forceinline__ bf16x8 v_read(LAS char* vp, int j, int s) {
    const bf16x4 lo = __builtin_amdgcn_ds_read_tr16_b64_v4bf16((LAS bf16x4*)(vp + j * 2048 + s * 1024));
    const bf16x4 hi = __builtin_amdgcn_ds_read_tr16_b64_v4bf16((LAS bf16x4*)(vp + j * 2048 + s * 1024 + 512));
    return __builtin_shufflevector(lo, hi, 0, 1, 2, 3, 4, 5, 6, 7);
}
__device__ __forceinline__ void attn_pv(LAS char* buf, int nbad, int wid, int lane, unsigned up, f32x16 (&sc)[8], float m, f32x16& o, float& lsum) {
    const int hh = lane >> 5;
    LAS char* vp = buf + (wid * 32 + 4 * hh + ((lane & 15) >> 2)) * 64 + ((lane >> 4) & 1) * 32 + (lane & 3) * 8;
    u32x4 one4 = {0x3f803f80u, 0x3f803f80u, 0x3f803f80u, 0x3f803f80u};
    PIN(one4);
    const bf16x8 ones = __builtin_bit_cast(bf16x8, one4);
    const float h0 = rbf(-m), h1 = rbf(-m - h0);
    const unsigned w0 = hh ? 0u : pack2(h0, h1), w1 = hh ? 0u : pack2((-m - h0) - h1, 0.f);
    auto minus_m = [&](int j) {
        const u32x4 bm = {(j > 0 && j < nbad) ? 0xff80u : w0, w1, 0u, 0u};
        sc[j] = MFMA32(ones, __builtin_bit_cast(bf16x8, bm), sc[j]);
        PIN(sc[j]);
    };
    minus_m(0); minus_m(1);
    f32x16 l = {};
    bf16x8 vf[2], v8[2];
    u32x4 pw[2], p8[2], pl[2];
    SCHED0();
#pragma unroll
    for (int j = 0; j < 8; ++j) {
        bf16x8 vn[2] = {v_read(vp, j, 0), v_read(vp, j, 1)};
        if (j == 0) { v8[0] = v_read(vp, 8, 0); v8[1] = v_read(vp, 8, 1); }
        SCHED0();
#pragma unroll
        for (int g = 0; g < 4; ++g) {
#pragma unroll
            for (int r = 4 * g; r < 4 * g + 4; ++r) sc[j][r] = __builtin_amdgcn_exp2f(sc[j][r]);
            PIN(sc[j]);
            SCHED0();
            if (j > 0) {
                if (g & 1) { l = MFMA32(ones, __builtin_bit_cast(bf16x8, j == 1 ? pl[g >> 1] : pw[g >> 1]), l); PIN(l); }
                else { o = MFMA32(vf[g >> 1], __builtin_bit_cast(bf16x8, pw[g >> 1]), o); PIN(o); }
            }
            if (j == 1 && (g & 1)) { o = MFMA32(v8[g >> 1], __builtin_bit_cast(bf16x8, p8[g >> 1]), o); PIN(o); }
            SCHED0();
        }
#pragma unroll
        for (int i = 0; i < 8; ++i) pw[i >> 2][i & 3] = pack2(sc[j][2 * i], sc[j][2 * i + 1]);
        if (j == 0) {
            pl[0] = pw[0]; pl[1] = pw[1];
#pragma unroll
            for (int i = 0; i < 8; ++i) {
                const int kr = ((2 * i) & 3) + 8 * (i >> 1);
                const unsigned mk = __builtin_amdgcn_bitop3_b32(0xffffu, (unsigned)__builtin_amdgcn_sbfe((int)up, kr, 1), (unsigned)__builtin_amdgcn_sbfe((int)up, kr + 1, 1), 0xca);
                p8[i >> 2][i & 3] = pw[i >> 2][i & 3] & ~mk; pw[i >> 2][i & 3] &= mk;
            }
        }
        vf[0] = vn[0]; vf[1] = vn[1];
        PIN(pw[0]); PIN(pw[1]);
        SCHED0();
        if (j + 2 < 8) minus_m(j + 2);
        SCHED0();
    }
    l = MFMA32(ones, __builtin_bit_cast(bf16x8, pw[0]), l);
    l = MFMA32(ones, __builtin_bit_cast(bf16x8, pw[1]), l);
    o = MFMA32(vf[0], __builtin_bit_cast(bf16x8, pw[0]), o);
    o = MFMA32(vf[1], __builtin_bit_cast(bf16x8, pw[1]), o);
    lsum = l[0];
}
__device__ __forceinline__ void attn_out(const Params& P, int hd, int qblk, int lane, float l, const f32x16& oacc) {
    const int r32 = lane & 31, hh = lane >> 5;
    const float inv = __builtin_amdgcn_rcpf(l);
    unsigned w[8];
#pragma unroll
    for (int g = 0; g < 4; ++g) { w[2 * g] = pack2(oacc[4 * g] * inv, oacc[4 * g + 1] * inv); w[2 * g + 1] = pack2(oacc[4 * g + 2] * inv, oacc[4 * g + 3] * inv); }
    const __amdgpu_buffer_rsrc_t rs = rsrc(P.o(), (unsigned)S_ * D_ * 2);
    const unsigned off = (unsigned)((hd * (S_ / 16) + qblk * 2 + (r32 >> 4)) * 1024 + (hh * 16 + (r32 & 15)) * 16);
#pragma unroll
    for (int gp = 0; gp < 2; ++gp) {
        auto a = __builtin_amdgcn_permlane32_swap(w[4 * gp], w[4 * gp + 2], false, false);
        auto b = __builtin_amdgcn_permlane32_swap(w[4 * gp + 1], w[4 * gp + 3], false, false);
        __builtin_amdgcn_raw_buffer_store_b128((u32x4){a[0], b[0], a[1], b[1]}, rs, off + gp * 512, 0, SC1);
    }
}

extern "C" __global__ __launch_bounds__(512, 2) void k_main(const Params P) {
    extern __shared__ __attribute__((aligned(16))) char smem[];
    LAS char* lds = (LAS char*)smem;
    const int tid = threadIdx.x, lane = tid & 63, wid = __builtin_amdgcn_readfirstlane(tid >> 6);
    const int grp = blockIdx.x & 7, idx = blockIdx.x >> 3;
    const bool loader = wid >= 4;
    const int lw = wid & 3, wm = (wid >> 1) & 1, wn = wid & 1;
    const unsigned ring0 = (unsigned)(uintptr_t)lds, ringl = ring0 + lw * PIECE;
    const int i16 = idx & 15;
    const bool isM = idx < 16;
    const int fm = (grp >> 1) * 4 + (isM ? i16 & 3 : i16 >> 2), fn = (grp & 1) * 4 + (isM ? i16 >> 2 : i16 & 3);
    const int um = (grp & 1) * 4 + (i16 >> 2), un = (grp >> 1) * 4 + (i16 & 3);
    const int hd = (grp & 1) * 8 + (i16 & 7), qb = (grp >> 1) * 2 + (i16 >> 3);
    if (wid == 0) {
        u64 pc; unsigned rel, len;
        asm volatile("s_getpc_b64 %0\n1:\n\ts_mov_b32 %1, 1b-k_main\n\ts_mov_b32 %2, 2f-k_main\n\t.subsection 1\n2:\n\t.subsection 0" : "=s"(pc), "=s"(rel), "=s"(len));
        const unsigned off = (unsigned)((idx + 32 * (lane & 15)) * 128);
        dma4((const char*)(pc - rel), off < len ? off : 0u, ring0 + LDS_SINK);
    }
    u64 tag;
    asm volatile("global_load_dwordx2 %0, %1, off sc1" : "=v"(tag) : "v"((GAS u64*)P.flags() + FL_E) : "memory");

    if (tid < 8) *(LAS unsigned*)(lds + (isM ? LDS_MC : LDS_CTL) + tid * 4) = 0u;
    if (isM) {
        constexpr int NK = S_ / 64, SB = SB_M;
        const int pg = (grp >> 1) + 4 * (grp & 1);
        const int tm = um, tn = un, mi = pg * 16 + i16;
        const char* gm = (const char*)P.h() + tm * 256; const char* gn = (const char*)P.w1 + tn * 448;
        const int cls = (tm + tn) & 3;
        auto kof = [&](int u) { const int p0 = u - ((u - cls) & 3); return __builtin_amdgcn_readfirstlane((((p0 == 1 || p0 == 2) ? u : korder(u, cls, NK)) + 4 * pg) & (NK - 1)); };
        Loader<4, 7> L;
        {
            const int row = mi * 16 + wid * 2;
            const u32x4* px = (const u32x4*)(P.x + (size_t)row * D_);
                        u32x4 a0 = px[lane], b0 = px[64 + lane], a1 = px[128 + lane], b1 = px[192 + lane];
            SCHED0();
            if (loader) {
                const unsigned c0 = (tn & 1) * 64;
                L.init_m<TR>(lw, lane, D_ * 2);
                L.init_n<TR>(lw, lane, F_ * 2, c0, c0 + 128, c0 + 256, (tn & 1) ? 0 : 384);
            }
            asm volatile("" : "+v"(a0), "+v"(b0), "+v"(a1), "+v"(b1));
            if (loader) L.issue_n<false>(gn + (size_t)kof(0) * (64 * F_ * 2), ringl);
            const __amdgpu_buffer_rsrc_t rh = rsrc(P.h(), (unsigned)S_ * D_ * 2);
            u32x4 oa0, ob0, oa1, ob1;
            const f32x4 s0 = ln_row(a0, b0, oa0, ob0), s1 = ln_row(a1, b1, oa1, ob1);
            __builtin_amdgcn_raw_buffer_store_b128(oa0, rh, (unsigned)(row * 2048 + lane * 16), 0, SC1);
            __builtin_amdgcn_raw_buffer_store_b128(ob0, rh, (unsigned)(row * 2048 + 1024 + lane * 16), 0, SC1);
            __builtin_amdgcn_raw_buffer_store_b128(oa1, rh, (unsigned)(row * 2048 + 2048 + lane * 16), 0, SC1);
            __builtin_amdgcn_raw_buffer_store_b128(ob1, rh, (unsigned)(row * 2048 + 3072 + lane * 16), 0, SC1);
            if (lane < 2) __builtin_amdgcn_raw_buffer_store_b128(__builtin_bit_cast(u32x4, lane ? s1 : s0), rsrc(P.stat(), S_ * 16), (unsigned)((row + lane) * 16), 0, SC1);
            SCHED0();
        }
        if (loader) {
            WAIT_VM(0);
            BARRIER();
            if (tid == 256 + 64) set_flag(P.flags(), FL_L + mi, tag);
#pragma unroll
            for (int u = 1; u < 3; ++u) L.issue_n<false>(gn + (size_t)kof(u) * (64 * F_ * 2), ringl + u * SB);
            if (lw == 0) poll_flags(P.flags(), lane < 16 ? FL_L + pg * 16 + lane : -1, tag);
            BARRIER();
#pragma unroll
            for (int u = 0; u < 3; ++u) L.issue_m<true>(gm + (size_t)kof(u) * (64 * D_ * 2), ringl + u * SB);
            WAIT_VM(8); BARRIER();
            WAIT_VM(4);
            int slot = 0, t = 0;
            auto issue = [&]() {
                const int k = kof(t + 3);
                L.issue_m<true>(gm + (size_t)k * (64 * D_ * 2), ringl + slot * SB);
                L.issue_n<false>(gn + (size_t)k * (64 * F_ * 2), ringl + slot * SB);
                slot = (slot == 2) ? 0 : slot + 1;
            };
            if (lw == 0) poll_flags(P.flags(), lane < 16 ? FL_L + ((pg + 1) & 7) * 16 + lane : -1, tag);
            BARRIER(); issue(); ++t;
            WAIT_VM(L.NPW); BARRIER(); issue(); ++t;
            WAIT_VM(L.NPW);
            if (lw == 0) { poll_flags(P.flags(), FL_L + lane, tag); poll_flags(P.flags(), FL_L + 64 + lane, tag); }
            BARRIER(); issue(); ++t;
#pragma unroll 1
            for (; t < NK - 3; ++t) {
                const int k = (korder(t + 3, cls, NK) + 4 * pg) & (NK - 1);
                L.turn<true, false, 11>(gm + (size_t)k * (64 * D_ * 2), gn + (size_t)k * (64 * F_ * 2), ringl + slot * SB);
                slot = (slot == 2) ? 0 : slot + 1;
            }
            {
                const int f = ((i16 * 4 + lw) * 7 + (lane < 56 ? lane >> 3 : 6)) * 2 + (grp & 1) + 896 * (grp >> 1);
                dma4((const char*)P.w2 + (grp >> 1) * 1024, (unsigned)(f * (S_ * 2) + (lane & 7) * 128), ring0 + LDS_SINK);
            }
            WAIT_VM(L.NPW + 1); BARRIER();
            WAIT_VM(1); BARRIER();
            BARRIER();
#pragma unroll
            for (int i = 0; i < 8; ++i)
                dma16<false, 0>((const char*)P.x + ((size_t)(fm * 128 + i * 16) * D_ + fn * 128 + lw * 32) * 2, (unsigned)((lane >> 2) * (D_ * 2) + (lane & 3) * 16), ring0 + LDS_X + (lw * 8 + i) * 1024);
            dma4((const char*)P.b2 + fm * 256, (unsigned)(lane * 4), ring0 + LDS_X + 32768);
        } else {
            const int fr = lane & 15, fq = lane >> 4;
            const int fb = tn * 224 + ((tn & 1) ? 32 : 0) + wn * 32, fh = tn * 224 + ((tn & 1) ? 0 : 192) + wn * 16;
            WAIT_VM(0);
            WAIT_LGKM0();
            BARRIER();
            f32x4 acc[4][7];
#pragma unroll
            for (int j = 0; j < 7; ++j) {
                const u32x2 bb = *(const u32x2*)(P.b1 + (j < 6 ? fb + (j >> 1) * 64 + (j & 1) * 16 : fh) + fq * 4);
                const f32x4 bv = {lo16(bb[0]), hi16(bb[0]), lo16(bb[1]), hi16(bb[1])};
#pragma unroll
                for (int i = 0; i < 4; ++i) acc[i][j] = bv;
            }
            Reader<TR, 128, TR, 224, 4, 7> rd; rd.init(ring0, lane, wm, wn);
            Regs<4, 7> R0;
            int slot = 0;
            BARRIER();
            __builtin_amdgcn_s_waitcnt(0x0070);
            BARRIER();
            rd.read(R0, 0);
            for (int t = 0; t < NK / 4; ++t) {
                stage_step<3, SB>(slot, rd, rd, R0, acc); stage_step<3, SB>(slot, rd, rd, R0, acc);
                stage_step<3, SB>(slot, rd, rd, R0, acc); stage_step<3, SB>(slot, rd, rd, R0, acc);
            }
            const __amdgpu_buffer_rsrc_t rs = rsrc(P.U(), (unsigned)D_ * F_ * 2);
            const unsigned df = (unsigned)(tm * 8 + wm * 4);
            const unsigned ob = (unsigned)((fb >> 5) * 64 + df) * 1024u + (unsigned)((((fq & 1) * 2 + (fq >> 1)) * 16 + fr) * 16);
            const unsigned oh = (unsigned)((fh >> 5) * 64 + df + (fq & 1)) * 1024u + (unsigned)(((wn * 2 + (fq >> 1)) * 16 + fr) * 16);
            LAS char* ul = lds + 128 * 132 + (wn * 8 + wm * 4) * PIECE + (((fq & 1) * 2 + (fq >> 1)) * 16 + fr) * 16;
            unsigned pk[4][7][2];
#pragma unroll
            for (int i = 0; i < 4; ++i) {
#pragma unroll
                for (int j = 0; j < 7; ++j) {
                    const f32x2 a = gelu2((f32x2){acc[i][j][0], acc[i][j][1]}), b = gelu2((f32x2){acc[i][j][2], acc[i][j][3]});
                    pk[i][j][0] = pack2(a.x, a.y); pk[i][j][1] = pack2(b.x, b.y);
                }
#pragma unroll
                for (int jp = 0; jp < 3; ++jp) {
                    const u32x4 w = pair16(pk[i][2 * jp][0], pk[i][2 * jp][1], pk[i][2 * jp + 1][0], pk[i][2 * jp + 1][1]);
                    __builtin_amdgcn_raw_buffer_store_b128(w, rs, ob + i * 1024 + jp * 2 * 65536, 0, SC1);
                    *(LAS u32x4*)(ul + jp * SB_F + i * PIECE) = w;
                }
                if (i & 1)
                    __builtin_amdgcn_raw_buffer_store_b128(pair16(pk[i - 1][6][0], pk[i - 1][6][1], pk[i][6][0], pk[i][6][1]), rs, oh + (i - 1) * 1024, 0, SC1);
                SCHED0();
            }
            WAIT_VM(0);
            if (lane == 0 && __hip_atomic_fetch_add((LAS unsigned*)(lds + LDS_MC), 1u, __ATOMIC_RELAXED, __HIP_MEMORY_SCOPE_WORKGROUP) == 3u)
                set_flag(P.flags(), FL_U + um * 32 + un, tag);
        }
    } else {
        constexpr int NK = D_ / 64, SB = SB_Q;
        const int tm = fm;
        if (loader) {
            Loader<4, 6> L; L.init_m<ROW>(lw, lane, D_ * 2);
            L.init_n<TR>(lw, lane, 3 * D_ * 2, qkv_head(fn, 0) * 64, qkv_head(fn, 1) * 64, qkv_head(fn, 2) * 64);
            const char* gm = (const char*)P.x + (size_t)tm * 128 * D_ * 2; const char* gn = (const char*)P.wqkv;
            const char* gh2 = (const char*)P.h() + (size_t)tm * 128 * D_ * 2;
            int slot = 0;
            auto issue = [&](int u) {
                const int k = (korder(u & (NK - 1), (tm + fn) & 3, NK) + 4 * (grp >> 1)) & (NK - 1);
                L.issue_m<false>(gm + k * 128, ringl + slot * SB);
                L.issue_n<false>(gn + (size_t)k * (64 * 3 * D_ * 2) + (u >= NK ? 16 * 64 : 0), ringl + slot * SB);
                slot = (slot == 2) ? 0 : slot + 1;
            };
            issue(0); issue(1);
            WAIT_VM(L.NPW); BARRIER();
            issue(2);
            int t = 0;
            WAIT_VM(L.NPW); BARRIER(); issue(3); ++t;
#pragma unroll 1
            for (int seg = 0; seg < 2; ++seg) {
                const int end = seg == 0 ? 12 : 2 * NK - 3;
#pragma unroll 1
                for (; t < end; ++t) {
                    const int u = t + 3, k = (korder(u & (NK - 1), (tm + fn) & 3, NK) + 4 * (grp >> 1)) & (NK - 1);
                    L.turn<true, false, 10>((u >= NK ? gh2 : gm) + k * 128, gn + (size_t)k * (64 * 3 * D_ * 2) + (u & NK) * 64, ringl + slot * SB);
                    slot = (slot == 2) ? 0 : slot + 1;
                }
                if (seg == 0 && lw == 0) {
                    poll_flags(P.flags(), lane < 8 ? FL_L + tm * 8 + lane : -1, tag);
                    dma16<true, LDS_QX>((const char*)P.stat() + tm * 2048, (unsigned)(lane * 16), ring0);
                    dma16<true, LDS_QX + 1024>((const char*)P.stat() + tm * 2048 + 1024, (unsigned)(lane * 16), ring0);
                }
            }
            poll_flags(P.flags(), attn_dep(hd, qb, lane), tag);
            WAIT_VM(0);
            BARRIER();
            attn_load<1>(P, ring0 + LDS_AK, hd, qb, lw, lane);
            attn_load<2>(P, ring0 + LDS_AV, hd, qb, lw, lane);
            BARRIER();
            attn_load<0>(P, ring0 + LDS_AQ, hd, qb, lw, lane);
            BARRIER();
        } else {
            const int fr = lane & 15, fq = lane >> 4;
            Reader<ROW, 128, TR, 192, 4, 6> rd; rd.init(ring0, lane, wm, wn);
            rd.n.b = rd.n.a + 32 - wm * 32; rd.n.a += wm * 32;
            Regs<4, 6> R0;
            int slot = 0;
            __builtin_amdgcn_s_waitcnt(0x0070);
            BARRIER();
            rd.read(R0, 0);
            const __amdgpu_buffer_rsrc_t rs = rsrc(P.qkv(), (unsigned)S_ * 3 * D_ * 2);
            const int s0 = tm * 128 + wm * 64;
            const unsigned so = (unsigned)(((s0 + fr) * 32 + ((fq & 1) ^ wm) * 16 + (fq >> 1) * 8) * 2);
            constexpr float QS = 0.17677669529663687f * 1.4426950408889634f;
            {
                f32x4 acc[5][6] = {};
                for (int t = 0; t < NK / 4; ++t) {
                    stage_step<3, SB>(slot, rd, rd, R0, acc); stage_step<3, SB>(slot, rd, rd, R0, acc);
                    stage_step<3, SB>(slot, rd, rd, R0, acc); stage_step<3, SB>(slot, rd, rd, R0, acc);
                }
                LAS char* ex = lds + LDS_QX + 2048;
                if (fr == 0) {
#pragma unroll
                    for (int jp = 0; jp < 3; ++jp) *(LAS f32x4*)(ex + wid * 256 + jp * 64 + fq * 16) = acc[4][2 * jp];
                }
                WAIT_LGKM0();
                CFENCE();
                __hip_atomic_store((LAS unsigned*)(lds + LDS_CTL + wid * 4), 1u, __ATOMIC_RELAXED, __HIP_MEMORY_SCOPE_WORKGROUP);
                f32x2 st[4];
#pragma unroll
                for (int i = 0; i < 4; ++i) st[i] = *(LAS f32x2*)(lds + LDS_QX + (wm * 64 + i * 16 + fr) * 16);
                while (__builtin_amdgcn_readfirstlane(__hip_atomic_load((LAS unsigned*)(lds + LDS_CTL + (wid ^ 2) * 4), __ATOMIC_RELAXED, __HIP_MEMORY_SCOPE_WORKGROUP)) == 0u) {}
                CFENCE();
#pragma unroll
                for (int jp = 0; jp < 3; ++jp) {
                    const f32x4 ca = acc[4][2 * jp], cb = *(LAS f32x4*)(ex + (wid ^ 2) * 256 + jp * 64 + fq * 16);
#pragma unroll
                    for (int i = 0; i < 4; ++i) {
                        const float r = st[i].y * (jp == 0 ? QS : 1.0f), mr = -st[i].x * r;
                        const f32x4 a = acc[i][2 * jp] * r + ca * mr, b = acc[i][2 * jp + 1] * r + cb * mr;
                        __builtin_amdgcn_raw_buffer_store_b128(pair16(pack2(a[0], a[1]), pack2(a[2], a[3]), pack2(b[0], b[1]), pack2(b[2], b[3])), rs,
                                                               so + (unsigned)((qkv_head(fn, jp) + wn) * (S_ * 64) + i * 1024), 0, SC1);
                    }
                }
            }
            {
                f32x4 acc[4][6] = {};
                for (int t = 0; t < NK / 4; ++t) {
                    if (__builtin_expect(t == 1 || t == 2, 0)) {
                        if (t == 1) WAIT_VM(0);
                        else if (tid == 0) set_flag(P.flags(), FL_Q + tm * 32 + fn, tag);
                    }
                    stage_step<3, SB>(slot, rd, rd, R0, acc); stage_step<3, SB>(slot, rd, rd, R0, acc);
                    stage_step<3, SB>(slot, rd, rd, R0, acc); stage_step<3, SB>(slot, rd, rd, R0, acc);
                }
#pragma unroll
                for (int i = 0; i < 4; ++i) {
                    const float sd = *(LAS float*)(lds + LDS_QX + (wm * 64 + i * 16 + fr) * 16 + 8);
#pragma unroll
                    for (int jp = 0; jp < 3; ++jp) {
                        const float r = sd * (jp == 0 ? QS : 1.0f);
                        const f32x4 a = acc[i][2 * jp] * r, b = acc[i][2 * jp + 1] * r;
                        __builtin_amdgcn_raw_buffer_store_b128(pair16(pack2(a[0], a[1]), pack2(a[2], a[3]), pack2(b[0], b[1]), pack2(b[2], b[3])), rs,
                                                               so + (unsigned)((qkv_head(fn, jp) + 16 + wn) * (S_ * 64) + i * 1024), 0, SC1);
                    }
                }
            }
        }
    }

    constexpr int SB = SB_F, NS = NS_F;
    constexpr size_t OS = 2 * (S_ / 16) * 1024, WOS = 64 * D_ * 2, W2S = 64 * S_ * 2, US = 2 * 65536;
    const int tile = fm * 8 + fn, cls = (fm + fn) & 3, nf = isM ? NF_M : NF_Q;
    const int uh = ((fm >> 1) & 1) ^ (isM ? 0 : 1);
    auto fof = [&](int i) {
        int r = korder(i - 7, cls, nf - 7) + (grp & 1) * ((nf - 7) >> 1);
        r = r >= nf - 7 ? r - (nf - 7) : r;
        const int im = (fm & 1) ? (i < 3 ? i + 4 : i - 3) : i;
        const int j = i < 7 ? 7 * uh + (isM ? im : korder(i, cls, 7)) : (isM ? 14 + NA : 14) + r, k = 14 * (grp >> 1) + j;
        return __builtin_amdgcn_readfirstlane(k >= 56 ? k - 56 : k);
    };
    const char* g2 = (const char*)P.w2 + fm * 256;
    Loader<4, 4> L;
    if (loader) L.init_m<TR>(lw, lane, S_ * 2);
    auto preload_w2 = [&]() {
        int k0 = fof(0), k1 = fof(1);
        asm volatile("" : "+s"(k0), "+s"(k1));
        L.issue_m<false>(g2 + k0 * W2S, ringl); L.issue_m<false>(g2 + k1 * W2S, ringl + SB);
    };
    if (isM) {
        if (loader) { preload_w2(); L.issue_m<false>(g2 + fof(2) * W2S, ringl + 2 * SB); WAIT_VM(0); }
        WAIT_LGKM0();
        BARRIER();
    } else {
        if (loader) { WAIT_VM(0); __builtin_amdgcn_s_setprio(1); }
        WAIT_LGKM0();
        BARRIER();
#pragma unroll 1
        for (int it = 0; it < 2; ++it) {
            f32x16 sc[8];
            int ln = lane;
            asm volatile("" : "+v"(ln));
            const int dg = (ln & 31) - 4 * (ln >> 5);
            const unsigned up = dg < 0 ? ~0u : ~1u << dg;
            int nbad = qb == 0 ? 8 - wid : 0;
            asm volatile("" : "+s"(nbad));
            SCHED0();
            const float m = attn_scores(lds + (it ? LDS_BK : LDS_AK), lds + LDS_AQ, nbad, wid, ln, up, sc);
            SCHED0();
            WAIT_VM(0);
            BARRIER();
            if (tid == 0) set_flag(P.flags(), it == 0 ? FL_Q + fm * 32 + fn + 8 : FL_O + qb * 32 + hd, tag);
            if (it == 1 && loader) preload_w2();
            f32x16 oacc = {};
            float lsum;
            attn_pv(lds + (it ? LDS_BV : LDS_AV), nbad, wid, ln, up, sc, m, oacc, lsum);
            attn_out(P, hd + 16 * it, qb * 8 + wid, ln, lsum, oacc);
            if (it == 0) {
                if (loader) {
                    __builtin_amdgcn_s_setprio(0);
                    int h2 = hd + 16, q2 = qb, w2 = lw;
                    asm volatile("" : "+s"(h2), "+s"(q2), "+s"(w2));
                    poll_flags(P.flags(), attn_dep(h2, q2, ln), tag);
                    attn_load<0>(P, ring0 + LDS_AQ, h2, q2, w2, ln);
                    attn_load<1>(P, ring0 + LDS_BK, h2, q2, w2, ln);
                    attn_load<2>(P, ring0 + LDS_BV, h2, q2, w2, ln);
                    WAIT_VM(8);
                    __builtin_amdgcn_s_setprio(1);
                }
                BARRIER();
            }
        }
        WAIT_LGKM0();
    }
    const int own = isM ? 0 : 1;
    if (loader) {
        const char* go = (const char*)P.o() + fm * 8192 + own * 4096;
        const char* gw = (const char*)P.wo + fn * 256;
        const char* gu = (const char*)P.U() + fn * 8192;
        __builtin_amdgcn_s_setprio(0);
        L.init_n<FRAG>(lw, lane, US / 2);
        auto issue_f = [&](int u) {
            const int k = fof(u);
            L.issue_m<false>(g2 + k * W2S, ringl + (u & (NS - 1)) * SB); L.issue_n<true>(gu + k * US, ringl + (u & (NS - 1)) * SB);
        };
        const int ulo = isM ? ((14 + NA) * 2) / 7 : 4, uhi = isM ? 15 : ((14 + NA) * 2 - 1) / 7;
        const int udep = (lane >= ulo && lane <= uhi) ? FL_U + fn * 32 + (((grp >> 1) * 4 + lane) & 15) : -1;
        const int odep = lane < 32 ? FL_O + (fm >> 1) * 32 + lane : lane < 48 ? FL_U + fn * 32 + lane - 32 : -1, pdep = lane == 0 ? FL_P + tile * 32 + own : -1;
        u64 pk;
        if (isM) { BARRIER(); BARRIER(); }
        poll_flags(P.flags(), lane < 2 ? FL_U + fn * 32 + (grp >> 1) * 4 + 2 * uh + lane : -1, tag);
        if (isM) { issue_f(3); issue_f(4); }
        else {
#pragma unroll
            for (int u = 0; u < 2; ++u) L.issue_n<true>(gu + fof(u) * US, ringl + u * SB);
            WAIT_VM(4); BARRIER();
            issue_f(2); issue_f(3);
            WAIT_VM(16); BARRIER(); issue_f(4);
            WAIT_VM(16); BARRIER();
        }
        peek_issue(pk, P.flags(), udep); issue_f(5);
        WAIT_VM(16); BARRIER(); issue_f(6);
        WAIT_VM(16);
        int nxt = 7;
        if (!peek_ok(pk, udep, tag)) {
            bool uready = false;
#pragma unroll 1
            for (int t = 3; t < 7; ++t) {
                if (t == 6 && !uready) { poll_flags(P.flags(), udep, tag); uready = true; for (; nxt < 10; ++nxt) issue_f(nxt); }
                if (nxt - (t + 2) >= 2) WAIT_VM(16); else WAIT_VM(0);
                BARRIER();
                if (!uready) uready = flags_set(P.flags(), udep, tag);
                if (uready) for (; nxt <= t + NS; ++nxt) issue_f(nxt);
            }
        }
#pragma unroll 1
        for (; nxt < nf - 2; ++nxt) {
            int r = korder(nxt - 7, cls, nf - 7) + (grp & 1) * ((nf - 7) >> 1);
            r = r >= nf - 7 ? r - (nf - 7) : r;
            int k = 14 * (grp >> 1) + (isM ? 14 + NA : 14) + r;
            k = k >= 56 ? k - 56 : k;
            L.turn<false, true, 16>(g2 + k * W2S, gu + k * US, ringl + (nxt & (NS - 1)) * SB);
        }
        const unsigned xo = (unsigned)(((fm * 128 + (lane >> 2)) * D_ + fn * 128 + lw * 32 + (lane & 3) * 8) * 2);
        constexpr unsigned ROWS16 = 16 * D_ * 2;
        WAIT_VM(16); BARRIER(); peek_issue(pk, P.flags(), odep);
        issue_f(nf - 2);
        WAIT_VM(16); BARRIER(); issue_f(nf - 1);
        L.init_n<TR>(lw, lane, D_ * 2);
        const unsigned oo = (unsigned)(lw * 1024 + lane * 16);
        const unsigned oo2 = oo + (unsigned)(OS / 2);
        const char *po, *pw; unsigned pd;
        auto addr_w = [&](int i) {
            const int k = (korder(i, cls, KW) + 4 * (grp >> 1)) & (KW - 1);
            po = go + k * OS; pw = gw + k * WOS; pd = ringl + ((nf + i) & (NS - 1)) * SB;
            asm volatile("" : "+s"(po), "+s"(pw), "+s"(pd));
        };
        auto issue_w = [&]() {
            asm volatile("s_add_u32 m0, %2, %3\n\ts_nop 0\n\tglobal_load_lds_dwordx4 %4, %0 sc1\n\ts_addk_i32 m0, 8448\n\ts_nop 0\n\tglobal_load_lds_dwordx4 %5, %0 sc1\n\t"
                         "s_add_u32 m0, %2, 16896\n\ts_nop 0\n\tglobal_load_lds_dwordx4 %6, %1\n\ts_addk_i32 m0, 4224\n\ts_nop 0\n\tglobal_load_lds_dwordx4 %7, %1\n\t"
                         "s_addk_i32 m0, 4224\n\ts_nop 0\n\tglobal_load_lds_dwordx4 %8, %1\n\ts_addk_i32 m0, 4224\n\ts_nop 0\n\tglobal_load_lds_dwordx4 %9, %1"
                         ::"s"(po), "s"(pw), "s"(pd), "s"(own * 4 * PIECE), "v"(oo), "v"(oo2), "v"(L.on[0]), "v"(L.on[1]), "v"(L.on[2]), "v"(L.on[3]) : "scc", "memory");
        };
#define TURN_W(i, w) do { addr_w(i); WAIT_VM(w); BARRIER(); issue_w(); } while (0)
        addr_w(0);
        WAIT_VM(16);
        if (!peek_ok(pk, odep, tag)) poll_flags(P.flags(), odep, tag);
        BARRIER(); issue_w();
        TURN_W(1, 14); TURN_W(2, 12); TURN_W(3, 12);
        TURN_W(4, 12);
        {
            u32x4 t0, t1, t2, t3;
            asm volatile("ds_read_b128 %0, %4\n\tds_read_b128 %1, %4 offset:1024\n\tds_read_b128 %2, %4 offset:2048\n\tds_read_b128 %3, %4 offset:3072\n\ts_waitcnt lgkmcnt(0)"
                         : "=&v"(t0), "=&v"(t1), "=&v"(t2), "=&v"(t3) : "v"(ring0 + LDS_EX + lw * 4096 + lane * 16) : "memory");
            const __amdgpu_buffer_rsrc_t ro = rsrc(P.out, (unsigned)S_ * D_ * 2);
            const unsigned so = xo + (1 - own) * 4 * ROWS16;
            __builtin_amdgcn_raw_buffer_store_b128(t0, ro, so, 0, SC1); __builtin_amdgcn_raw_buffer_store_b128(t1, ro, so + ROWS16, 0, SC1);
            __builtin_amdgcn_raw_buffer_store_b128(t2, ro, so + 2 * ROWS16, 0, SC1); __builtin_amdgcn_raw_buffer_store_b128(t3, ro, so + 3 * ROWS16, 0, SC1);
        }
        TURN_W(5, 16); TURN_W(6, 16); TURN_W(7, 16);
        TURN_W(8, 12);
        asm volatile("global_store_dwordx2 %0, %1, off sc1" ::"v"((GAS u64*)P.flags() + FL_P + tile * 32 + 1 - own), "v"(tag + 1ull) : "memory");
        TURN_W(9, 13); TURN_W(10, 13); TURN_W(11, 13);
        TURN_W(12, 12); TURN_W(13, 12); TURN_W(14, 12);
        TURN_W(15, 12);
#undef TURN_W
        peek_issue(pk, P.flags(), pdep);
        WAIT_VM(13); BARRIER();
        WAIT_VM(7); BARRIER();
        WAIT_VM(0);
        auto load_partial = [&]() {
#pragma unroll
            for (int i = 0; i < 4; ++i) dma16<true, 0>((const char*)P.out + (own * 4 + i) * ROWS16, xo, ring0 + LDS_EX + (lw * 4 + i) * 1024);
        };
        const bool early = peek_ok(pk, pdep, tag);
        if (early) load_partial();
        BARRIER();
        BARRIER();
        if (!early) { poll_flags(P.flags(), pdep, tag); load_partial(); }
        WAIT_VM(0); BARRIER();
        if (blockIdx.x == 0 && lw == 0) {
#pragma unroll 1
            for (int i = 0; i < 4; ++i) poll_flags(P.flags(), FL_O + i * 64 + lane, tag);
            if (lane == 0) set_flag(P.flags(), FL_E, tag);
        }
    } else {
        const __amdgpu_buffer_rsrc_t ro = rsrc(P.out, (unsigned)S_ * D_ * 2);
        f32x4 acc[4][4];
        if (isM) {
            LAS char* xp = lds + LDS_X + (wn * 16 + wm * 4) * 1024 + (lane & 15) * 64 + (((lane >> 4) & 1) * 2 + (lane >> 5)) * 16;
            u32x4 xv[4][2]; unsigned bv[4];
#pragma unroll
            for (int i = 0; i < 4; ++i) {
                xv[i][0] = *(LAS u32x4*)(xp + i * 1024); xv[i][1] = *(LAS u32x4*)(xp + 8192 + i * 1024);
                bv[i] = *(LAS u16*)(lds + LDS_X + 32768 + (wm * 64 + i * 16 + (lane & 15)) * 2);
            }
            SCHED0();
#pragma unroll
            for (int i = 0; i < 4; ++i)
#pragma unroll
                for (int jp = 0; jp < 2; ++jp) {
                    auto e0 = __builtin_amdgcn_permlane16_swap(xv[i][jp][0], xv[i][jp][2], false, false);
                    auto e1 = __builtin_amdgcn_permlane16_swap(xv[i][jp][1], xv[i][jp][3], false, false);
                    acc[i][2 * jp] = (f32x4){lo16(e0[0]), hi16(e0[0]), lo16(e1[0]), hi16(e1[0])} + lo16(bv[i]);
                    acc[i][2 * jp + 1] = (f32x4){lo16(e0[1]), hi16(e0[1]), lo16(e1[1]), hi16(e1[1])} + lo16(bv[i]);
                }
            pin_acc<4, 4, 4>(acc);
        } else {
            float z = 0.f;
            asm volatile("" : "+v"(z));
#pragma unroll
            for (int i = 0; i < 4; ++i)
#pragma unroll
                for (int j = 0; j < 4; ++j) acc[i][j] = (f32x4){z, z, z, z};
        }
        pin_acc<4, 4, 4>(acc);
        Reader<TR, 128, FRAG, 128, 4, 4> rf;
        HalfReader rw;
        Regs<4, 4> R0;
        LAS char* exl = lds + LDS_EX + wn * 8192 + lane * 16;
        int slot = 0;
#define STEP(a, b) stage_step<NS, SB>(slot, a, b, R0, acc)
        rf.init(ring0, lane, wm, wn);
        WAIT_LGKM0();
        if (!isM) BARRIER();
        rf.read(R0, 0);
        for (int t = 0; t < (nf - 2) / 4; ++t) {
            if (__builtin_expect(!isM && (t == 1 || t == 2), 0)) {
                if (t == 1) WAIT_VM(0);
                else if (tid == 0) set_flag(P.flags(), FL_O + qb * 32 + hd + 16, tag);
            }
            STEP(rf, rf); STEP(rf, rf); STEP(rf, rf); STEP(rf, rf);
        }
        static_assert((NF_M & 3) == (NF_Q & 3), "NA must be odd");
#pragma unroll
        for (int t = 0; t < ((NF_M - 2) & 3); ++t) STEP(rf, rf);
        STEP(rf, rf);
        const int kq = wm != own ? 1 : 0;
        rw.init(ring0 + slot * SB, lane, own, kq, wn);
        STEP(rf, rw);
        if (kq) {
#pragma unroll
            for (int i = 0; i < 4; ++i)
#pragma unroll
                for (int jp = 0; jp < 2; ++jp) {
                    const f32x4 a = acc[i][2 * jp], b = acc[i][2 * jp + 1];
                    *(LAS u32x4*)(exl + jp * 4096 + i * 1024) = (u32x4){pack2(a[0], a[1]), pack2(a[2], a[3]), pack2(b[0], b[1]), pack2(b[2], b[3])};
                    acc[i][2 * jp] = (f32x4){0.f, 0.f, 0.f, 0.f}; acc[i][2 * jp + 1] = (f32x4){0.f, 0.f, 0.f, 0.f};
                }
        }
        pin_acc<4, 4, 4>(acc);
        Regs<4, 4> R1;
        for (int t = 0; t < 4; ++t) {
            half_step<NS, SB>(slot, rw, R0, R1, acc); half_step<NS, SB>(slot, rw, R1, R0, acc);
            half_step<NS, SB>(slot, rw, R0, R1, acc); half_step<NS, SB>(slot, rw, R1, R0, acc);
        }
#pragma unroll
        for (int i = 0; i < 2; ++i)
#pragma unroll
            for (int j = 0; j < 4; ++j) *(LAS f32x4*)(lds + (wid * 8 + i * 4 + j) * 1024 + lane * 16) = acc[2 + i][j];
        WAIT_LGKM0();
        BARRIER();
        const unsigned fo = (unsigned)(((fm * 128 + own * 64 + kq * 32 + (lane & 15)) * D_ + fn * 128 + wn * 64 + ((lane >> 4) & 1) * 16 + (lane >> 5) * 8) * 2);
#pragma unroll
        for (int i = 0; i < 2; ++i)
#pragma unroll
            for (int jp = 0; jp < 2; ++jp) {
                const u32x4 e = *(LAS u32x4*)(exl + jp * 4096 + (kq * 2 + i) * 1024);
                const f32x4 a = acc[i][2 * jp] + *(LAS f32x4*)(lds + ((wid ^ 2) * 8 + i * 4 + 2 * jp) * 1024 + lane * 16) + (f32x4){lo16(e[0]), hi16(e[0]), lo16(e[1]), hi16(e[1])};
                const f32x4 b = acc[i][2 * jp + 1] + *(LAS f32x4*)(lds + ((wid ^ 2) * 8 + i * 4 + 2 * jp + 1) * 1024 + lane * 16) + (f32x4){lo16(e[2]), hi16(e[2]), lo16(e[3]), hi16(e[3])};
                __builtin_amdgcn_raw_buffer_store_b128(pair16(pack2(a[0], a[1]), pack2(a[2], a[3]), pack2(b[0], b[1]), pack2(b[2], b[3])), ro, fo + i * (16 * D_ * 2) + jp * 64, 0, SC1);
            }
#undef STEP
    }
}

extern "C" void kernel_launch(void* const* d_in, const int* in_sizes, int n_in, void* d_out, int out_size, void* d_ws, size_t ws_size, hipStream_t stream) {
    char* ws = (char*)d_ws;
    Params P;
    P.x = (const u16*)d_in[0]; P.wqkv = (const u16*)d_in[1]; P.wo = (const u16*)d_in[2];
    P.w1 = (const u16*)d_in[4]; P.b1 = (const u16*)d_in[5]; P.w2 = (const u16*)d_in[6]; P.b2 = (const u16*)d_in[7];
    P.out = (u16*)d_out; P.ws = ws;
    (void)hipFuncSetAttribute((const void*)k_main, hipFuncAttributeMaxDynamicSharedMemorySize, LDS_BYTES);
    k_main<<<256, 512, LDS_BYTES, stream>>>(P);
}
```

```cpp
#include <hip/hip_runtime.h>
#include <hip/hip_bf16.h>
#include <stdint.h>

#define LAS __attribute__((address_space(3)))
#define GAS __attribute__((address_space(1)))
typedef __bf16 bf16x8 __attribute__((ext_vector_type(8)));
typedef __bf16 bf16x4 __attribute__((ext_vector_type(4)));
typedef __bf16 bf16x2 __attribute__((ext_vector_type(2)));
typedef float f32x2 __attribute__((ext_vector_type(2)));
typedef float f32x4 __attribute__((ext_vector_type(4)));
typedef float f32x16 __attribute__((ext_vector_type(16)));
typedef unsigned u32x4 __attribute__((ext_vector_type(4)));
typedef unsigned u32x2 __attribute__((ext_vector_type(2)));
typedef unsigned short u16;
typedef unsigned long long u64;

constexpr int S_ = 2048, D_ = 1024, F_ = 3584, NH = 32;

constexpr size_t WS_FLAGS = 0;
constexpr size_t WS_STAT  = 1 << 16;
constexpr size_t WS_QKV   = 1 << 17;
constexpr size_t WS_U     = WS_QKV + (size_t)S_ * 3 * D_ * 2;
constexpr int FL_E = 0;
constexpr int FL_L = 32;
constexpr int FL_U = 8 * 32;
constexpr int FL_Q = 16 * 32;
constexpr int FL_O = 32 * 32;
constexpr int FL_P = 40 * 32;

#define WAIT_VM(n) asm volatile("s_waitcnt vmcnt(%0)" ::"n"(n) : "memory")
#define WAIT_LGKM0() __builtin_amdgcn_s_waitcnt(0xC07F)
#define CFENCE() asm volatile("" ::: "memory")
#define BARRIER() do { CFENCE(); __builtin_amdgcn_s_barrier(); CFENCE(); } while (0)
#define SCHED0() __builtin_amdgcn_sched_barrier(0)
#define RLX_AGENT __ATOMIC_RELAXED, __HIP_MEMORY_SCOPE_AGENT
constexpr int SC1 = 16;

__device__ __forceinline__ float bf2f(u16 v) { return __uint_as_float((unsigned)v << 16); }
__device__ __forceinline__ float lo16(unsigned v) { return __uint_as_float(v << 16); }
__device__ __forceinline__ float hi16(unsigned v) { return __uint_as_float(v & 0xffff0000u); }
__device__ __forceinline__ unsigned pack2(float lo, float hi) {
    f32x2 v = {lo, hi};
    bf16x2 b = __builtin_convertvector(v, bf16x2);
    return __builtin_bit_cast(unsigned, b);
}
__device__ __forceinline__ float rbf(float x) { return __uint_as_float(pack2(x, 0.f) << 16); }
template <int CTRL, int RMASK> __device__ __forceinline__ float dpp_add(float v) {
    return v + __builtin_bit_cast(float, __builtin_amdgcn_update_dpp(0, __builtin_bit_cast(int, v), CTRL, RMASK, 0xf, false));
}
__device__ __forceinline__ float wave_sum(float v) {
    v = dpp_add<0x111, 0xf>(v); v = dpp_add<0x112, 0xf>(v); v = dpp_add<0x114, 0xf>(v); v = dpp_add<0x118, 0xf>(v);
    v = dpp_add<0x142, 0xa>(v); v = dpp_add<0x143, 0xc>(v);
    return __builtin_bit_cast(float, __builtin_amdgcn_readlane(__builtin_bit_cast(int, v), 63));
}
__device__ __forceinline__ __amdgpu_buffer_rsrc_t rsrc(const void* p, unsigned bytes) { return __builtin_amdgcn_make_buffer_rsrc((void*)p, 0, bytes, 0x00020000); }

enum { ROW = 0, TR = 1, FRAG = 2 };
constexpr int PIECE = 1056, BLK = 8 * PIECE;
template <int T, int X> struct Op {
    static constexpr int BYTES = X * 132;
    __device__ static __forceinline__ unsigned src_off(int blk, int p, int lane, unsigned ld, unsigned cb) {
        const int pos = lane >> 3;
        if (T == TR) {
            if (X == 224 && blk == 3) {
                const int ps = lane >> 2, g = (p & 1) | ((ps >> 3) << 1), r = 32 * (p >> 1) + 8 * g + 4 * ((ps >> 2) & 1) + (ps & 3);
                return (unsigned)r * ld + cb + (unsigned)((lane & 3) * 16);
            }
            const int g = (p >> 2) | (pos & 2), r = 32 * (pos >> 2) + 8 * g + 4 * (pos & 1) + (p & 3);
            return (unsigned)r * ld + cb + (unsigned)((lane & 7) * 16);
        } else if (T == ROW) {
            const int j = ((pos >> 1) & 1) | ((pos & 1) << 1) | (pos & 4);
            return (unsigned)(blk * 64 + j * 8 + p) * ld + (unsigned)((lane & 7) * 16);
        } else {
            return (unsigned)blk * ld + (unsigned)(p * 1024 + lane * 16);
        }
    }
};
template <bool SC, int IMM> __device__ __forceinline__ void dma16(const char* base, unsigned off, unsigned l) {
    if (SC) asm volatile("s_add_u32 m0, %2, %3\n\ts_nop 0\n\tglobal_load_lds_dwordx4 %0, %1 sc1" ::"v"(off), "s"(base), "s"(l), "n"(IMM) : "scc", "memory");
    else    asm volatile("s_add_u32 m0, %2, %3\n\ts_nop 0\n\tglobal_load_lds_dwordx4 %0, %1" ::"v"(off), "s"(base), "s"(l), "n"(IMM) : "scc", "memory");
}
#define DMA_FIRST(sc) "s_add_u32 m0, %1, %2\n\ts_nop 0\n\tglobal_load_lds_dwordx4 %3, %0" sc
#define DMA_NEXT(n, sc) "\n\ts_addk_i32 m0, 4224\n\ts_nop 0\n\tglobal_load_lds_dwordx4 %" #n ", %0" sc
#define DMA_G4(sc) DMA_FIRST(sc) DMA_NEXT(4, sc) DMA_NEXT(5, sc) DMA_NEXT(6, sc)
template <bool SC, int IMM, int N> __device__ __forceinline__ void dma_group(const char* base, const unsigned (&o)[N], unsigned l) {
    static_assert(4 * PIECE == 4224 && (N == 4 || ((N == 6 || N == 7) && !SC)), "dma_group");
    if constexpr (N == 4 && SC) asm volatile(DMA_G4(" sc1") ::"s"(base), "s"(l), "n"(IMM), "v"(o[0]), "v"(o[1]), "v"(o[2]), "v"(o[3]) : "scc", "memory");
    else if constexpr (N == 4) asm volatile(DMA_G4("") ::"s"(base), "s"(l), "n"(IMM), "v"(o[0]), "v"(o[1]), "v"(o[2]), "v"(o[3]) : "scc", "memory");
    else if constexpr (N == 6) asm volatile(DMA_G4("") DMA_NEXT(7, "") DMA_NEXT(8, "") ::"s"(base), "s"(l), "n"(IMM), "v"(o[0]), "v"(o[1]), "v"(o[2]), "v"(o[3]), "v"(o[4]), "v"(o[5]) : "scc", "memory");
    else asm volatile(DMA_G4("") DMA_NEXT(7, "") DMA_NEXT(8, "") DMA_NEXT(9, "") ::"s"(base), "s"(l), "n"(IMM), "v"(o[0]), "v"(o[1]), "v"(o[2]), "v"(o[3]), "v"(o[4]), "v"(o[5]), "v"(o[6]) : "scc", "memory");
}
__device__ __forceinline__ void dma4(const char* base, unsigned off, unsigned l) {
    asm volatile("s_mov_b32 m0, %2\n\ts_nop 0\n\tglobal_load_lds_dword %0, %1" ::"v"(off), "s"(base), "s"(l) : "memory");
}
__device__ __forceinline__ void dma16x4(const char* base, unsigned off, unsigned l) {
    asm volatile("s_mov_b32 m0, %2\n\ts_nop 0\n\tglobal_load_lds_dwordx4 %0, %1 sc1\n\tglobal_load_lds_dwordx4 %0, %1 offset:1024 sc1\n\t"
                 "global_load_lds_dwordx4 %0, %1 offset:2048 sc1\n\tglobal_load_lds_dwordx4 %0, %1 offset:3072 sc1" ::"v"(off), "s"(base), "s"(l) : "memory");
}

template <int T, int X> struct Addr {
    unsigned a, b;
    static constexpr int NV = (X == 224 || X == 192) ? 2 : 1;
    __device__ __forceinline__ void init(unsigned base, int lane, int w) {
        const int g = lane >> 4;
        b = 0;
        if (T == TR) {
            const int q = (lane & 15) >> 2, p = lane & 3;
            a = base + (unsigned)((q | ((g & 1) << 2)) * PIECE + (g >> 1) * 256 + p * 8 + (X == 128 ? w * BLK : w * 64));
            b = base + (unsigned)(3 * BLK + (g & 1) * PIECE + (g >> 1) * 512 + q * 64 + w * 32 + p * 8);
        } else if (T == ROW) {
            const int r = lane & 15;
            a = base + (unsigned)(w * BLK + (r & 7) * PIECE + (r >> 3) * 256 + g * 16);
        } else {
            a = base + (unsigned)(w * 4 * PIECE + lane * 16);
        }
    }
    __device__ __forceinline__ void step(unsigned d) { a += d; if (NV == 2) b += d; }
    __device__ __forceinline__ bf16x8 read(int n, int kk) const {
        if (T == TR) {
            LAS char* p; int t;
            if (X == 224 && n == 6) { p = (LAS char*)(uintptr_t)b + kk * 2 * PIECE; t = 256; }
            else if (X == 192) { p = (LAS char*)(uintptr_t)((n & 1) ? b : a) + kk * 512 + (n >> 1) * BLK; t = 128; }
            else { p = (LAS char*)(uintptr_t)a + kk * 512 + (X == 128 ? n * 32 : (n >> 1) * BLK + (n & 1) * 32); t = 128; }
            const bf16x4 lo = __builtin_amdgcn_ds_read_tr16_b64_v4bf16((LAS bf16x4*)p);
            const bf16x4 hi = __builtin_amdgcn_ds_read_tr16_b64_v4bf16((LAS bf16x4*)(p + t));
            return __builtin_shufflevector(lo, hi, 0, 1, 2, 3, 4, 5, 6, 7);
        } else if (T == ROW) {
            return *(LAS bf16x8*)((LAS char*)(uintptr_t)a + (n & 1) * 128 + (n >> 1) * 512 + kk * 64);
        } else {
            return *(LAS bf16x8*)((LAS char*)(uintptr_t)a + kk * BLK + n * PIECE);
        }
    }
};
template <int FM, int FN> struct Regs { bf16x8 m[FM], n[FN]; };
#define MFMA16(a, b, c) __builtin_amdgcn_mfma_f32_16x16x32_bf16(a, b, c, 0, 0, 0)

template <int TM, int BM, int TN, int BN, int FM, int FN> struct Reader {
    Addr<TM, BM> m; Addr<TN, BN> n;
    static constexpr int NDS = FM * (TM == TR ? 2 : 1) + FN * (TN == TR ? 2 : 1), NV = Addr<TM, BM>::NV + Addr<TN, BN>::NV;
    __device__ __forceinline__ void init(unsigned slot_base, int lane, int wm, int wn) { m.init(slot_base, lane, wm); n.init(slot_base + BM * 132, lane, wn); }
    __device__ __forceinline__ void read(Regs<FM, FN>& R, int kk) const {
#pragma unroll
        for (int i = 0; i < FM; ++i) R.m[i] = m.read(i, kk);
#pragma unroll
        for (int j = 0; j < FN; ++j) R.n[j] = n.read(j, kk);
    }
    __device__ __forceinline__ void step(unsigned d) { m.step(d); n.step(d); }
};
template <int AM, int FM, int FN> __device__ __forceinline__ void mma(f32x4 (&acc)[AM][FN], const Regs<FM, FN>& R) {
    const u32x4 one = {0x3f803f80u, 0x3f803f80u, 0x3f803f80u, 0x3f803f80u};
#pragma unroll
    for (int j = 0; j < FN; ++j) {
        if (AM > FM && !(j & 1)) acc[AM - 1][j] = MFMA16(R.n[j], __builtin_bit_cast(bf16x8, one), acc[AM - 1][j]);
#pragma unroll
        for (int ii = 0; ii < FM; ++ii) { const int i = (j & 1) ? FM - 1 - ii : ii; acc[i][j] = MFMA16(R.n[j], R.m[i], acc[i][j]); }
    }
}
template <int AM, int FM, int FN> __device__ __forceinline__ void pin_acc(f32x4 (&acc)[AM][FN]) {
#pragma unroll
    for (int j = 0; j < FN; ++j) {
        asm volatile("" : "+v"(acc[0][j]), "+v"(acc[1][j]), "+v"(acc[2][j]), "+v"(acc[3][j]));
        if (AM > FM && !(j & 1)) asm volatile("" : "+v"(acc[AM - 1][j]));
    }
}
template <int NDS, int NV, int NMF> __device__ __forceinline__ void interleave() {
#pragma unroll
    for (int i = 0; i < NMF; ++i) {
        __builtin_amdgcn_sched_group_barrier(0x8, 1, 0);
        if (i < NDS) __builtin_amdgcn_sched_group_barrier(0x80, 1, 0);
        if (i >= NMF - NV) __builtin_amdgcn_sched_group_barrier(0x2, 1, 0);
    }
}
template <int NS, int SB, class RD, class RDN, int AM, int FM, int FN>
__device__ __forceinline__ void stage_step(int& slot, RD& rd, RDN& rn, Regs<FM, FN>& R0, f32x4 (&acc)[AM][FN]) {
    constexpr int NMF = FM * FN + (AM - FM) * (FN / 2);
    Regs<FM, FN> R1;
    const unsigned d = (slot + 1 == NS) ? (unsigned)(-(NS - 1) * SB) : (unsigned)SB;
    slot = (slot + 1 == NS) ? 0 : slot + 1;
    rd.read(R1, 1);
    rn.step(d);
    mma(acc, R0);
    interleave<RD::NDS, RDN::NV, NMF>();
    SCHED0();
    pin_acc<AM, FM, FN>(acc);
    WAIT_LGKM0();
    BARRIER();
    SCHED0();
    rn.read(R0, 0);
    mma(acc, R1);
    interleave<RDN::NDS, 0, NMF>();
    SCHED0();
    pin_acc<AM, FM, FN>(acc);
    SCHED0();
}

struct HalfReader {
    unsigned m0, m1; Addr<TR, 128> n;
    static constexpr int NDS = 12, NV = 3;
    __device__ __forceinline__ void init(unsigned slot_base, int lane, int own, int kq, int wn) {
        const unsigned a = slot_base + (unsigned)(own * 4 * PIECE + kq * BLK + lane * 16);
        m0 = a + kq * 2 * PIECE; m1 = a - kq * 2 * PIECE;
        n.init(slot_base + 128 * 132 + kq * 512, lane, wn);
    }
    __device__ __forceinline__ void read(Regs<4, 4>& R, int) const {
#pragma unroll
        for (int i = 0; i < 4; ++i) R.m[i] = *(LAS bf16x8*)((LAS char*)(uintptr_t)(i < 2 ? m0 : m1) + i * PIECE);
#pragma unroll
        for (int j = 0; j < 4; ++j) R.n[j] = n.read(j, 0);
    }
    __device__ __forceinline__ void step(unsigned d) { m0 += d; m1 += d; n.step(d); }
};
template <int NS, int SB> __device__ __forceinline__ void half_step(int& slot, HalfReader& rd, Regs<4, 4>& Rc, Regs<4, 4>& Rn, f32x4 (&acc)[4][4]) {
    rd.step((slot + 1 == NS) ? (unsigned)(-(NS - 1) * SB) : (unsigned)SB);
    slot = (slot + 1 == NS) ? 0 : slot + 1;
    SCHED0();
    WAIT_LGKM0();
    BARRIER();
    SCHED0();
    rd.read(Rn, 0);
    mma(acc, Rc);
    interleave<12, 0, 16>();
    SCHED0();
    pin_acc<4, 4, 4>(acc);
    SCHED0();
}

template <int NM, int NN> struct Loader {
    static constexpr int NPW = NM + NN;
    unsigned om[NM], on[NN];
    template <int T> __device__ __forceinline__ void init_m(int lw, int lane, unsigned ld) {
#pragma unroll
        for (int i = 0; i < NM; ++i) om[i] = Op<T, NM * 32>::src_off(i >> 1, lw + 4 * (i & 1), lane, ld, (i >> 1) * 128);
    }
    template <int T> __device__ __forceinline__ void init_n(int lw, int lane, unsigned ld, unsigned c0 = 0, unsigned c1 = 128, unsigned c2 = 256, unsigned c3 = 384) {
#pragma unroll
        for (int i = 0; i < NN; ++i) on[i] = Op<T, NN * 32>::src_off(i >> 1, lw + 4 * (i & 1), lane, ld, (i >> 1) == 0 ? c0 : (i >> 1) == 1 ? c1 : (i >> 1) == 2 ? c2 : c3);
    }
    template <bool SC> __device__ __forceinline__ void issue_m(const char* gm, unsigned dst) const { dma_group<SC, 0>(gm, om, dst); }
    template <bool SC> __device__ __forceinline__ void issue_n(const char* gn, unsigned dst) const { dma_group<SC, NM * 32 * 132>(gn, on, dst); }
    template <bool SCM, bool SCN, int W> __device__ __forceinline__ void turn(const char* gm, const char* gn, unsigned dst) const {
        asm volatile("" : "+s"(gm), "+s"(gn), "+s"(dst));
        WAIT_VM(W); BARRIER();
        issue_m<SCM>(gm, dst); issue_n<SCN>(gn, dst);
    }
};
__device__ __forceinline__ int korder(int p, int c, int nk) {
    const int r = (p - c) & 3;
    const int d = (0x1003 >> (4 * r) & 15) - 1;
    return (unsigned)(p - r) < (unsigned)(nk - 2) ? p + d : p;
}
__device__ __forceinline__ f32x2 gelu2(f32x2 u) {
    const float c1 = -2.0f * 0.7978845608028654f * 1.4426950408889634f, c2 = c1 * 0.044715f;
    const f32x2 z = u * ((u * u) * c2 + c1);
    f32x2 e; e.x = __builtin_amdgcn_exp2f(z.x); e.y = __builtin_amdgcn_exp2f(z.y);
    e = e + 1.0f;
    f32x2 r; r.x = __builtin_amdgcn_rcpf(e.x); r.y = __builtin_amdgcn_rcpf(e.y);
    return u * r;
}
__device__ __forceinline__ u32x4 pair16(unsigned x0, unsigned x1, unsigned y0, unsigned y1) {
    auto a = __builtin_amdgcn_permlane16_swap(x0, y0, false, false);
    auto b = __builtin_amdgcn_permlane16_swap(x1, y1, false, false);
    return (u32x4){a[0], b[0], a[1], b[1]};
}
__device__ __forceinline__ bool flags_set(u64* flags, int idx, u64 tag) {
    const u64 v = __hip_atomic_load((GAS u64*)flags + (idx < 0 ? 0 : idx), RLX_AGENT);
    const bool ok = __builtin_amdgcn_ballot_w64(idx >= 0 && v - 1ull != tag) == 0ull;
    CFENCE();
    return ok;
}
__device__ __forceinline__ void poll_flags(u64* flags, int idx, u64 tag) {
#pragma unroll 1
    for (unsigned spins = 0; spins < (1u << 20); ++spins) {
        if (flags_set(flags, idx, tag)) break;
        __builtin_amdgcn_s_sleep(2);
    }
}
__device__ __forceinline__ void peek_issue(u64& v, u64* flags, int idx) {
    asm volatile("global_load_dwordx2 %0, %1, off sc1" : "=v"(v) : "v"((GAS u64*)flags + (idx < 0 ? 0 : idx)) : "memory");
}
__device__ __forceinline__ bool peek_ok(u64& v, int idx, u64 tag) {
    asm volatile("" : "+v"(v) :: "memory");
    return __builtin_amdgcn_ballot_w64(idx >= 0 && v - 1ull != tag) == 0ull;
}
__device__ __forceinline__ void set_flag(u64* flags, int idx, u64 tag) {
    asm volatile("" : "+v"(tag));
    __hip_atomic_store((GAS u64*)flags + idx, tag + 1ull, RLX_AGENT);
}

struct Params {
    const u16 *x, *wqkv, *wo, *w1, *b1, *w2, *b2;
    u16* out;
    char* ws;
    __device__ __forceinline__ u16* h() const { return out; }
    __device__ __forceinline__ u16* qkv() const { return (u16*)(ws + WS_QKV); }
    __device__ __forceinline__ u16* U() const { return (u16*)(ws + WS_U); }
    __device__ __forceinline__ u16* o() const { return (u16*)(ws + WS_QKV); }
    __device__ __forceinline__ float* stat() const { return (float*)(ws + WS_STAT); }
    __device__ __forceinline__ u64* flags() const { return (u64*)(ws + WS_FLAGS); }
};

constexpr int SB_M = (128 + 224) * 132, SB_Q = (128 + 192) * 132, SB_F = 256 * 132, NS_F = 4;
constexpr int LDS_QX = 3 * SB_Q;
constexpr int LDS_CTL = LDS_QX + 3072;
constexpr int LDS_AQ = 0, LDS_AK = 2 * SB_Q, LDS_AV = 128 * 1024, LDS_BYTES = 160 * 1024;
constexpr int LDS_MC = LDS_BYTES - 512;
constexpr int LDS_X = 3 * SB_F;
constexpr int LDS_BK = 16 * 1024, LDS_BV = 96 * 1024;
constexpr int LDS_EX = 136 * 1024;
constexpr int LDS_SINK = LDS_BYTES - 256;
static_assert(LDS_CTL + 32 <= LDS_AV && 3 * SB_M <= LDS_SINK && 3 * SB_F <= LDS_AV && NS_F * SB_F <= LDS_EX && LDS_EX + 16384 <= LDS_MC, "LDS map");
constexpr int NA = 19;
constexpr int KW = 16, NF_Q = 7 + NA, NF_M = 7 + 42 - NA;

__device__ __forceinline__ int qkv_head(int tn, int blk) { return blk * NH + ((tn & ~3) | ((tn + blk) & 3)) * 2; }

__device__ __forceinline__ f32x4 ln_row(const u32x4 a, const u32x4 b, u32x4& oa, u32x4& ob) {
    f32x2 v[8];
#pragma unroll
    for (int i = 0; i < 4; ++i) { v[i] = (f32x2){lo16(a[i]), hi16(a[i])}; v[4 + i] = (f32x2){lo16(b[i]), hi16(b[i])}; }
    const f32x2 s2 = ((v[0] + v[1]) + (v[2] + v[3])) + ((v[4] + v[5]) + (v[6] + v[7]));
    const float mean = wave_sum(s2.x + s2.y) * (1.0f / D_);
    f32x2 q2 = {0.f, 0.f};
#pragma unroll
    for (int i = 0; i < 8; ++i) { v[i] -= mean; q2 += v[i] * v[i]; }
    const float var = wave_sum(q2.x + q2.y) * (1.0f / D_);
    const float rs = __builtin_amdgcn_rcpf(rbf(__builtin_amdgcn_sqrtf(rbf(rbf(var) + 1e-5f))));
    const float sd2 = rbf(__builtin_amdgcn_sqrtf(rbf(rbf(var * rs * rs) + 1e-5f)));
    const float g = rs * __builtin_amdgcn_rcpf(sd2);
#pragma unroll
    for (int i = 0; i < 8; ++i) {
        const f32x2 t = v[i] * g;
        if (i < 4) oa[i] = pack2(t.x, t.y); else ob[i - 4] = pack2(t.x, t.y);
    }
    return (f32x4){mean, rs, sd2, 0.f};
}

#define MFMA32(a, b, c) __builtin_amdgcn_mfma_f32_32x32x16_bf16(a, b, c, 0, 0, 0)
__device__ __forceinline__ int attn_dep(int hd, int qb, int lane) {
    const int which = lane >> 2, st = 2 * qb - 2 + (lane & 3), p = hd >> 1;
    if (lane >= 12 || st < 0 || (which == 0 && (lane & 3) < 2)) return -1;
    return FL_Q + st * 32 + ((p & ~3) | ((p - which) & 3));
}
template <int WHICH> __device__ __forceinline__ void attn_load(const Params& P, unsigned dst, int hd, int qb, int w, int lane) {
    const int row = WHICH == 0 ? qb * 256 + w * 64 : qb * 256 - ((qb == 0 && w < 2) ? 0 : 256) + w * 128;
    const char* g = (const char*)(P.qkv() + ((size_t)(WHICH * NH + hd) * S_ + row) * 32);
    const unsigned off = (unsigned)((lane >> 2) * 64 + (WHICH == 2 ? lane & 3 : (lane & 3) ^ ((lane >> 4) & 3)) * 16);
    dma16x4(g, off, dst + w * (WHICH == 0 ? 4096 : 8192));
    if (WHICH != 0) dma16x4(g + 4096, off, dst + w * 8192 + 4096);
}
#define PIN(x) asm volatile("" : "+v"(x))
__device__ __forceinline__ float attn_scores(LAS char* buf, LAS char* qbuf, int nbad, int wid, int lane, unsigned up, f32x16 (&sc)[8]) {
    const int r32 = lane & 31, hh = lane >> 5, sw = (r32 >> 2) & 3;
    LAS char* k0p = buf + (wid * 32 + r32) * 64 + ((hh ^ sw) * 16);
    LAS char* k1p = buf + (wid * 32 + r32) * 64 + (((2 + hh) ^ sw) * 16);
    const bf16x8 qf0 = *(LAS bf16x8*)(qbuf + (wid * 32 + r32) * 64 + ((hh ^ sw) * 16));
    const bf16x8 qf1 = *(LAS bf16x8*)(qbuf + (wid * 32 + r32) * 64 + (((2 + hh) ^ sw) * 16));
    bf16x8 ka[9], kb[9];
#pragma unroll
    for (int i = 0; i < 3; ++i) { const int j = i == 0 ? 8 : i - 1; ka[i] = *(LAS bf16x8*)(k0p + j * 2048); kb[i] = *(LAS bf16x8*)(k1p + j * 2048); SCHED0(); }
    const float NEG = -INFINITY;
    const f32x16 zero = {};
    f32x16 s8;
    float m = NEG;
#pragma unroll
    for (int i = 0; i < 11; ++i) {
        if (i + 3 < 9) { ka[i + 3] = *(LAS bf16x8*)(k0p + (i + 2) * 2048); kb[i + 3] = *(LAS bf16x8*)(k1p + (i + 2) * 2048); }
        if (i < 9) {
            const f32x16 t = MFMA32(kb[i], qf1, MFMA32(ka[i], qf0, zero));
            if (i == 0) { s8 = t; PIN(s8); } else { sc[i - 1] = t; PIN(sc[i - 1]); }
        }
        SCHED0();
        const int j = i - 3;
        if (j == 0) {
            if (__builtin_expect(nbad > 0, 0)) {
#pragma unroll
                for (int r = 0; r < 16; ++r) sc[0][r] = NEG;
                PIN(sc[0]);
            }
#pragma unroll
            for (int r = 0; r < 16; ++r) {
                const unsigned mk = (unsigned)__builtin_amdgcn_sbfe((int)up, (r & 3) + 8 * (r >> 2), 1);
                sc[0][r] = __uint_as_float(__builtin_amdgcn_bitop3_b32(mk, __float_as_uint(sc[0][r]), __float_as_uint(s8[r]), 0xca));
            }
        }
        if (j >= 0) {
            float mj = m;
#pragma unroll
            for (int r = 0; r < 16; r += 2) mj = __builtin_fmaxf(__builtin_fmaxf(mj, sc[j][r]), sc[j][r + 1]);
            m = (j > 0 && j < nbad) ? m : mj;
            PIN(m);
        }
        SCHED0();
    }
    auto rr = __builtin_amdgcn_permlane32_swap(__float_as_uint(m), __float_as_uint(m), false, false);
    return __builtin_fmaxf(__uint_as_float(rr[0]), __uint_as_float(rr[1]));
}
__device__ __forceinline__ bf16x8 v_read(LAS char* vp, int j, int s) {
    const bf16x4 lo = __builtin_amdgcn_ds_read_tr16_b64_v4bf16((LAS bf16x4*)(vp + j * 2048 + s * 1024));
    const bf16x4 hi = __builtin_amdgcn_ds_read_tr16_b64_v4bf16((LAS bf16x4*)(vp + j * 2048 + s * 1024 + 512));
    return __builtin_shufflevector(lo, hi, 0, 1, 2, 3, 4, 5, 6, 7);
}
__device__ __forceinline__ void attn_pv(LAS char* buf, int nbad, int wid, int lane, unsigned up, f32x16 (&sc)[8], float m, f32x16& o, float& lsum) {
    const int hh = lane >> 5;
    LAS char* vp = buf + (wid * 32 + 4 * hh + ((lane & 15) >> 2)) * 64 + ((lane >> 4) & 1) * 32 + (lane & 3) * 8;
    u32x4 one4 = {0x3f803f80u, 0x3f803f80u, 0x3f803f80u, 0x3f803f80u};
    PIN(one4);
    const bf16x8 ones = __builtin_bit_cast(bf16x8, one4);
    const float h0 = rbf(-m), h1 = rbf(-m - h0);
    const unsigned w0 = hh ? 0u : pack2(h0, h1), w1 = hh ? 0u : pack2((-m - h0) - h1, 0.f);
    auto minus_m = [&](int j) {
        const u32x4 bm = {(j > 0 && j < nbad) ? 0xff80u : w0, w1, 0u, 0u};
        sc[j] = MFMA32(ones, __builtin_bit_cast(bf16x8, bm), sc[j]);
        PIN(sc[j]);
    };
    minus_m(0); minus_m(1);
    f32x16 l = {};
    bf16x8 vf[2], v8[2];
    u32x4 pw[2], p8[2], pl[2];
    SCHED0();
#pragma unroll
    for (int j = 0; j < 8; ++j) {
        bf16x8 vn[2] = {v_read(vp, j, 0), v_read(vp, j, 1)};
        if (j == 0) { v8[0] = v_read(vp, 8, 0); v8[1] = v_read(vp, 8, 1); }
        SCHED0();
#pragma unroll
        for (int g = 0; g < 4; ++g) {
#pragma unroll
            for (int r = 4 * g; r < 4 * g + 4; ++r) sc[j][r] = __builtin_amdgcn_exp2f(sc[j][r]);
            PIN(sc[j]);
            SCHED0();
            if (j > 0) {
                if (g & 1) { l = MFMA32(ones, __builtin_bit_cast(bf16x8, j == 1 ? pl[g >> 1] : pw[g >> 1]), l); PIN(l); }
                else { o = MFMA32(vf[g >> 1], __builtin_bit_cast(bf16x8, pw[g >> 1]), o); PIN(o); }
            }
            if (j == 1 && (g & 1)) { o = MFMA32(v8[g >> 1], __builtin_bit_cast(bf16x8, p8[g >> 1]), o); PIN(o); }
            SCHED0();
        }
#pragma unroll
        for (int i = 0; i < 8; ++i) pw[i >> 2][i & 3] = pack2(sc[j][2 * i], sc[j][2 * i + 1]);
        if (j == 0) {
            pl[0] = pw[0]; pl[1] = pw[1];
#pragma unroll
            for (int i = 0; i < 8; ++i) {
                const int kr = ((2 * i) & 3) + 8 * (i >> 1);
                const unsigned mk = __builtin_amdgcn_bitop3_b32(0xffffu, (unsigned)__builtin_amdgcn_sbfe((int)up, kr, 1), (unsigned)__builtin_amdgcn_sbfe((int)up, kr + 1, 1), 0xca);
                p8[i >> 2][i & 3] = pw[i >> 2][i & 3] & ~mk; pw[i >> 2][i & 3] &= mk;
            }
        }
        vf[0] = vn[0]; vf[1] = vn[1];
        PIN(pw[0]); PIN(pw[1]);
        SCHED0();
        if (j + 2 < 8) minus_m(j + 2);
        SCHED0();
    }
    l = MFMA32(ones, __builtin_bit_cast(bf16x8, pw[0]), l);
    l = MFMA32(ones, __builtin_bit_cast(bf16x8, pw[1]), l);
    o = MFMA32(vf[0], __builtin_bit_cast(bf16x8, pw[0]), o);
    o = MFMA32(vf[1], __builtin_bit_cast(bf16x8, pw[1]), o);
    lsum = l[0];
}
__device__ __forceinline__ void attn_out(const Params& P, int hd, int qblk, int lane, float l, const f32x16& oacc) {
    const int r32 = lane & 31, hh = lane >> 5;
    const float inv = __builtin_amdgcn_rcpf(l);
    unsigned w[8];
#pragma unroll
    for (int g = 0; g < 4; ++g) { w[2 * g] = pack2(oacc[4 * g] * inv, oacc[4 * g + 1] * inv); w[2 * g + 1] = pack2(oacc[4 * g + 2] * inv, oacc[4 * g + 3] * inv); }
    const __amdgpu_buffer_rsrc_t rs = rsrc(P.o(), (unsigned)S_ * D_ * 2);
    const unsigned off = (unsigned)((hd * (S_ / 16) + qblk * 2 + (r32 >> 4)) * 1024 + (hh * 16 + (r32 & 15)) * 16);
#pragma unroll
    for (int gp = 0; gp < 2; ++gp) {
        auto a = __builtin_amdgcn_permlane32_swap(w[4 * gp], w[4 * gp + 2], false, false);
        auto b = __builtin_amdgcn_permlane32_swap(w[4 * gp + 1], w[4 * gp + 3], false, false);
        __builtin_amdgcn_raw_buffer_store_b128((u32x4){a[0], b[0], a[1], b[1]}, rs, off + gp * 512, 0, SC1);
    }
}

extern "C" __global__ __launch_bounds__(512, 2) void k_main(const Params P) {
    extern __shared__ __attribute__((aligned(16))) char smem[];
    LAS char* lds = (LAS char*)smem;
    const int tid = threadIdx.x, lane = tid & 63, wid = __builtin_amdgcn_readfirstlane(tid >> 6);
    const int grp = blockIdx.x & 7, idx = blockIdx.x >> 3;
    const bool loader = wid >= 4;
    const int lw = wid & 3, wm = (wid >> 1) & 1, wn = wid & 1;
    const unsigned ring0 = (unsigned)(uintptr_t)lds, ringl = ring0 + lw * PIECE;
    const int i16 = idx & 15;
    const bool isM = idx < 16;
    const int fm = (grp >> 1) * 4 + (isM ? i16 & 3 : i16 >> 2), fn = (grp & 1) * 4 + (isM ? i16 >> 2 : i16 & 3);
    const int um = (grp & 1) * 4 + (i16 >> 2), un = (grp >> 1) * 4 + (i16 & 3);
    const int hd = (grp & 1) * 8 + (i16 & 7), qb = (grp >> 1) * 2 + (i16 >> 3);
    const char* code; unsigned len;
    {
        u64 pc; unsigned rel;
        asm volatile("s_getpc_b64 %0\n1:\n\ts_mov_b32 %1, 1b-k_main\n\ts_mov_b32 %2, 2f-k_main\n\t.subsection 1\n2:\n\t.subsection 0" : "=s"(pc), "=s"(rel), "=s"(len));
        code = (const char*)(pc - rel);
    }
    if (wid == 0) {
        const unsigned off = (unsigned)((idx + 32 * (lane & 15)) * 128);
        dma4(code, off < len ? off : 0u, ring0 + LDS_SINK);
    }
    auto warm_code = [&]() {
        const unsigned off = (unsigned)((i16 * 4 + lw + 64 * (lane & 7)) * 128);
        dma4(code, off < len ? off : 0u, ring0 + LDS_SINK);
    };
    u64 tag;
    asm volatile("global_load_dwordx2 %0, %1, off sc1" : "=v"(tag) : "v"((GAS u64*)P.flags() + FL_E) : "memory");

    if (tid < 8) *(LAS unsigned*)(lds + (isM ? LDS_MC : LDS_CTL) + tid * 4) = 0u;
    if (isM) {
        constexpr int NK = S_ / 64, SB = SB_M;
        const int pg = (grp >> 1) + 4 * (grp & 1);
        const int tm = um, tn = un, mi = pg * 16 + i16;
        const char* gm = (const char*)P.h() + tm * 256; const char* gn = (const char*)P.w1 + tn * 448;
        const int cls = (tm + tn) & 3;
        auto kof = [&](int u) { const int p0 = u - ((u - cls) & 3); return __builtin_amdgcn_readfirstlane((((p0 == 1 || p0 == 2) ? u : korder(u, cls, NK)) + 4 * pg) & (NK - 1)); };
        Loader<4, 7> L;
        {
            const int row = mi * 16 + wid * 2;
            const u32x4* px = (const u32x4*)(P.x + (size_t)row * D_);
            u32x4 a0 = px[lane], b0 = px[64 + lane], a1 = px[128 + lane], b1 = px[192 + lane];
            SCHED0();
            if (loader) {
                const unsigned c0 = (tn & 1) * 64;
                L.init_m<TR>(lw, lane, D_ * 2);
                L.init_n<TR>(lw, lane, F_ * 2, c0, c0 + 128, c0 + 256, (tn & 1) ? 0 : 384);
            }
            asm volatile("" : "+v"(a0), "+v"(b0), "+v"(a1), "+v"(b1));
            if (loader) L.issue_n<false>(gn + (size_t)kof(0) * (64 * F_ * 2), ringl);
            const __amdgpu_buffer_rsrc_t rh = rsrc(P.h(), (unsigned)S_ * D_ * 2);
            u32x4 oa0, ob0, oa1, ob1;
            const f32x4 s0 = ln_row(a0, b0, oa0, ob0), s1 = ln_row(a1, b1, oa1, ob1);
            __builtin_amdgcn_raw_buffer_store_b128(oa0, rh, (unsigned)(row * 2048 + lane * 16), 0, SC1);
            __builtin_amdgcn_raw_buffer_store_b128(ob0, rh, (unsigned)(row * 2048 + 1024 + lane * 16), 0, SC1);
            __builtin_amdgcn_raw_buffer_store_b128(oa1, rh, (unsigned)(row * 2048 + 2048 + lane * 16), 0, SC1);
            __builtin_amdgcn_raw_buffer_store_b128(ob1, rh, (unsigned)(row * 2048 + 3072 + lane * 16), 0, SC1);
            if (lane < 2) __builtin_amdgcn_raw_buffer_store_b128(__builtin_bit_cast(u32x4, lane ? s1 : s0), rsrc(P.stat(), S_ * 16), (unsigned)((row + lane) * 16), 0, SC1);
            SCHED0();
        }
        if (loader) {
            WAIT_VM(0);
            BARRIER();
            if (tid == 256 + 64) set_flag(P.flags(), FL_L + mi, tag);
#pragma unroll
            for (int u = 1; u < 3; ++u) L.issue_n<false>(gn + (size_t)kof(u) * (64 * F_ * 2), ringl + u * SB);
            if (lw == 0) poll_flags(P.flags(), lane < 16 ? FL_L + pg * 16 + lane : -1, tag);
            BARRIER();
#pragma unroll
            for (int u = 0; u < 3; ++u) L.issue_m<true>(gm + (size_t)kof(u) * (64 * D_ * 2), ringl + u * SB);
            WAIT_VM(8); BARRIER();
            WAIT_VM(4);
            int slot = 0, t = 0;
            auto issue = [&]() {
                const int k = kof(t + 3);
                L.issue_m<true>(gm + (size_t)k * (64 * D_ * 2), ringl + slot * SB);
                L.issue_n<false>(gn + (size_t)k * (64 * F_ * 2), ringl + slot * SB);
                slot = (slot == 2) ? 0 : slot + 1;
            };
            if (lw == 0) poll_flags(P.flags(), lane < 16 ? FL_L + ((pg + 1) & 7) * 16 + lane : -1, tag);
            BARRIER(); issue(); ++t;
            WAIT_VM(L.NPW); BARRIER(); issue(); ++t;
            WAIT_VM(L.NPW);
            if (lw == 0) { poll_flags(P.flags(), FL_L + lane, tag); poll_flags(P.flags(), FL_L + 64 + lane, tag); }
            BARRIER(); issue(); ++t;
#pragma unroll 1
            for (; t < NK - 3; ++t) {
                const int k = (korder(t + 3, cls, NK) + 4 * pg) & (NK - 1);
                L.turn<true, false, 11>(gm + (size_t)k * (64 * D_ * 2), gn + (size_t)k * (64 * F_ * 2), ringl + slot * SB);
                slot = (slot == 2) ? 0 : slot + 1;
            }
            warm_code();
            {
                const int f = ((i16 * 4 + lw) * 7 + (lane < 56 ? lane >> 3 : 6)) * 2 + (grp & 1) + 896 * (grp >> 1);
                dma4((const char*)P.w2 + (grp >> 1) * 1024, (unsigned)(f * (S_ * 2) + (lane & 7) * 128), ring0 + LDS_SINK);
            }
            WAIT_VM(L.NPW + 2); BARRIER();
            WAIT_VM(2); BARRIER();
            BARRIER();
#pragma unroll
            for (int i = 0; i < 8; ++i)
                dma16<false, 0>((const char*)P.x + ((size_t)(fm * 128 + i * 16) * D_ + fn * 128 + lw * 32) * 2, (unsigned)((lane >> 2) * (D_ * 2) + (lane & 3) * 16), ring0 + LDS_X + (lw * 8 + i) * 1024);
            dma4((const char*)P.b2 + fm * 256, (unsigned)(lane * 4), ring0 + LDS_X + 32768);
        } else {
            const int fr = lane & 15, fq = lane >> 4;
            const int fb = tn * 224 + ((tn & 1) ? 32 : 0) + wn * 32, fh = tn * 224 + ((tn & 1) ? 0 : 192) + wn * 16;
            WAIT_VM(0);
            WAIT_LGKM0();
            BARRIER();
            f32x4 acc[4][7];
#pragma unroll
            for (int j = 0; j < 7; ++j) {
                const u32x2 bb = *(const u32x2*)(P.b1 + (j < 6 ? fb + (j >> 1) * 64 + (j & 1) * 16 : fh) + fq * 4);
                const f32x4 bv = {lo16(bb[0]), hi16(bb[0]), lo16(bb[1]), hi16(bb[1])};
#pragma unroll
                for (int i = 0; i < 4; ++i) acc[i][j] = bv;
            }
            Reader<TR, 128, TR, 224, 4, 7> rd; rd.init(ring0, lane, wm, wn);
            Regs<4, 7> R0;
            int slot = 0;
            BARRIER();
            __builtin_amdgcn_s_waitcnt(0x0070);
            BARRIER();
            rd.read(R0, 0);
            for (int t = 0; t < NK / 4; ++t) {
                stage_step<3, SB>(slot, rd, rd, R0, acc); stage_step<3, SB>(slot, rd, rd, R0, acc);
                stage_step<3, SB>(slot, rd, rd, R0, acc); stage_step<3, SB>(slot, rd, rd, R0, acc);
            }
            const __amdgpu_buffer_rsrc_t rs = rsrc(P.U(), (unsigned)D_ * F_ * 2);
            const unsigned df = (unsigned)(tm * 8 + wm * 4);
            const unsigned ob = (unsigned)((fb >> 5) * 64 + df) * 1024u + (unsigned)((((fq & 1) * 2 + (fq >> 1)) * 16 + fr) * 16);
            const unsigned oh = (unsigned)((fh >> 5) * 64 + df + (fq & 1)) * 1024u + (unsigned)(((wn * 2 + (fq >> 1)) * 16 + fr) * 16);
            LAS char* ul = lds + 128 * 132 + (wn * 8 + wm * 4) * PIECE + (((fq & 1) * 2 + (fq >> 1)) * 16 + fr) * 16;
            unsigned pk[4][7][2];
#pragma unroll
            for (int i = 0; i < 4; ++i) {
#pragma unroll
                for (int j = 0; j < 7; ++j) {
                    const f32x2 a = gelu2((f32x2){acc[i][j][0], acc[i][j][1]}), b = gelu2((f32x2){acc[i][j][2], acc[i][j][3]});
                    pk[i][j][0] = pack2(a.x, a.y); pk[i][j][1] = pack2(b.x, b.y);
                }
#pragma unroll
                for (int jp = 0; jp < 3; ++jp) {
                    const u32x4 w = pair16(pk[i][2 * jp][0], pk[i][2 * jp][1], pk[i][2 * jp + 1][0], pk[i][2 * jp + 1][1]);
                    __builtin_amdgcn_raw_buffer_store_b128(w, rs, ob + i * 1024 + jp * 2 * 65536, 0, SC1);
                    *(LAS u32x4*)(ul + jp * SB_F + i * PIECE) = w;
                }
                if (i & 1)
                    __builtin_amdgcn_raw_buffer_store_b128(pair16(pk[i - 1][6][0], pk[i - 1][6][1], pk[i][6][0], pk[i][6][1]), rs, oh + (i - 1) * 1024, 0, SC1);
                SCHED0();
            }
            WAIT_VM(0);
            if (lane == 0 && __hip_atomic_fetch_add((LAS unsigned*)(lds + LDS_MC), 1u, __ATOMIC_RELAXED, __HIP_MEMORY_SCOPE_WORKGROUP) == 3u)
                set_flag(P.flags(), FL_U + um * 32 + un, tag);
        }
    } else {
        constexpr int NK = D_ / 64, SB = SB_Q;
        const int tm = fm;
        if (loader) {
            Loader<4, 6> L; L.init_m<ROW>(lw, lane, D_ * 2);
            L.init_n<TR>(lw, lane, 3 * D_ * 2, qkv_head(fn, 0) * 64, qkv_head(fn, 1) * 64, qkv_head(fn, 2) * 64);
            const char* gm = (const char*)P.x + (size_t)tm * 128 * D_ * 2; const char* gn = (const char*)P.wqkv;
            const char* gh2 = (const char*)P.h() + (size_t)tm * 128 * D_ * 2;
            int slot = 0;
            auto issue = [&](int u) {
                const int k = (korder(u & (NK - 1), (tm + fn) & 3, NK) + 4 * (grp >> 1)) & (NK - 1);
                L.issue_m<false>(gm + k * 128, ringl + slot * SB);
                L.issue_n<false>(gn + (size_t)k * (64 * 3 * D_ * 2) + (u >= NK ? 16 * 64 : 0), ringl + slot * SB);
                slot = (slot == 2) ? 0 : slot + 1;
            };
            issue(0); issue(1);
            WAIT_VM(L.NPW); BARRIER();
            issue(2);
            int t = 0;
            WAIT_VM(L.NPW); BARRIER(); issue(3); ++t;
#pragma unroll 1
            for (int seg = 0; seg < 2; ++seg) {
                const int end = seg == 0 ? 12 : 2 * NK - 3;
#pragma unroll 1
                for (; t < end; ++t) {
                    const int u = t + 3, k = (korder(u & (NK - 1), (tm + fn) & 3, NK) + 4 * (grp >> 1)) & (NK - 1);
                    L.turn<true, false, 10>((u >= NK ? gh2 : gm) + k * 128, gn + (size_t)k * (64 * 3 * D_ * 2) + (u & NK) * 64, ringl + slot * SB);
                    slot = (slot == 2) ? 0 : slot + 1;
                }
                if (seg == 0 && lw == 0) {
                    poll_flags(P.flags(), lane < 8 ? FL_L + tm * 8 + lane : -1, tag);
                    dma16<true, LDS_QX>((const char*)P.stat() + tm * 2048, (unsigned)(lane * 16), ring0);
                    dma16<true, LDS_QX + 1024>((const char*)P.stat() + tm * 2048 + 1024, (unsigned)(lane * 16), ring0);
                }
            }
            warm_code();
            poll_flags(P.flags(), attn_dep(hd, qb, lane), tag);
            WAIT_VM(0);
            BARRIER();
            attn_load<1>(P, ring0 + LDS_AK, hd, qb, lw, lane);
            attn_load<2>(P, ring0 + LDS_AV, hd, qb, lw, lane);
            BARRIER();
            attn_load<0>(P, ring0 + LDS_AQ, hd, qb, lw, lane);
            BARRIER();
        } else {
            const int fr = lane & 15, fq = lane >> 4;
            Reader<ROW, 128, TR, 192, 4, 6> rd; rd.init(ring0, lane, wm, wn);
            rd.n.b = rd.n.a + 32 - wm * 32; rd.n.a += wm * 32;
            Regs<4, 6> R0;
            int slot = 0;
            __builtin_amdgcn_s_waitcnt(0x0070);
            BARRIER();
            rd.read(R0, 0);
            const __amdgpu_buffer_rsrc_t rs = rsrc(P.qkv(), (unsigned)S_ * 3 * D_ * 2);
            const int s0 = tm * 128 + wm * 64;
            const unsigned so = (unsigned)(((s0 + fr) * 32 + ((fq & 1) ^ wm) * 16 + (fq >> 1) * 8) * 2);
            constexpr float QS = 0.17677669529663687f * 1.4426950408889634f;
            {
                f32x4 acc[5][6] = {};
                for (int t = 0; t < NK / 4; ++t) {
                    stage_step<3, SB>(slot, rd, rd, R0, acc); stage_step<3, SB>(slot, rd, rd, R0, acc);
                    stage_step<3, SB>(slot, rd, rd, R0, acc); stage_step<3, SB>(slot, rd, rd, R0, acc);
                }
                LAS char* ex = lds + LDS_QX + 2048;
                if (fr == 0) {
#pragma unroll
                    for (int jp = 0; jp < 3; ++jp) *(LAS f32x4*)(ex + wid * 256 + jp * 64 + fq * 16) = acc[4][2 * jp];
                }
                WAIT_LGKM0();
                CFENCE();
                __hip_atomic_store((LAS unsigned*)(lds + LDS_CTL + wid * 4), 1u, __ATOMIC_RELAXED, __HIP_MEMORY_SCOPE_WORKGROUP);
                f32x2 st[4];
#pragma unroll
                for (int i = 0; i < 4; ++i) st[i] = *(LAS f32x2*)(lds + LDS_QX + (wm * 64 + i * 16 + fr) * 16);
                while (__builtin_amdgcn_readfirstlane(__hip_atomic_load((LAS unsigned*)(lds + LDS_CTL + (wid ^ 2) * 4), __ATOMIC_RELAXED, __HIP_MEMORY_SCOPE_WORKGROUP)) == 0u) {}
                CFENCE();
#pragma unroll
                for (int jp = 0; jp < 3; ++jp) {
                    const f32x4 ca = acc[4][2 * jp], cb = *(LAS f32x4*)(ex + (wid ^ 2) * 256 + jp * 64 + fq * 16);
#pragma unroll
                    for (int i = 0; i < 4; ++i) {
                        const float r = st[i].y * (jp == 0 ? QS : 1.0f), mr = -st[i].x * r;
                        const f32x4 a = acc[i][2 * jp] * r + ca * mr, b = acc[i][2 * jp + 1] * r + cb * mr;
                        __builtin_amdgcn_raw_buffer_store_b128(pair16(pack2(a[0], a[1]), pack2(a[2], a[3]), pack2(b[0], b[1]), pack2(b[2], b[3])), rs,
                                                               so + (unsigned)((qkv_head(fn, jp) + wn) * (S_ * 64) + i * 1024), 0, SC1);
                    }
                }
            }
            {
                f32x4 acc[4][6] = {};
                for (int t = 0; t < NK / 4; ++t) {
                    if (__builtin_expect(t == 1 || t == 2, 0)) {
                        if (t == 1) WAIT_VM(0);
                        else if (tid == 0) set_flag(P.flags(), FL_Q + tm * 32 + fn, tag);
                    }
                    stage_step<3, SB>(slot, rd, rd, R0, acc); stage_step<3, SB>(slot, rd, rd, R0, acc);
                    stage_step<3, SB>(slot, rd, rd, R0, acc); stage_step<3, SB>(slot, rd, rd, R0, acc);
                }
#pragma unroll
                for (int i = 0; i < 4; ++i) {
                    const float sd = *(LAS float*)(lds + LDS_QX + (wm * 64 + i * 16 + fr) * 16 + 8);
#pragma unroll
                    for (int jp = 0; jp < 3; ++jp) {
                        const float r = sd * (jp == 0 ? QS : 1.0f);
                        const f32x4 a = acc[i][2 * jp] * r, b = acc[i][2 * jp + 1] * r;
                        __builtin_amdgcn_raw_buffer_store_b128(pair16(pack2(a[0], a[1]), pack2(a[2], a[3]), pack2(b[0], b[1]), pack2(b[2], b[3])), rs,
                                                               so + (unsigned)((qkv_head(fn, jp) + 16 + wn) * (S_ * 64) + i * 1024), 0, SC1);
                    }
                }
            }
        }
    }

    constexpr int SB = SB_F, NS = NS_F;
    constexpr size_t OS = 2 * (S_ / 16) * 1024, WOS = 64 * D_ * 2, W2S = 64 * S_ * 2, US = 2 * 65536;
    const int tile = fm * 8 + fn, cls = (fm + fn) & 3, nf = isM ? NF_M : NF_Q;
    const int uh = ((fm >> 1) & 1) ^ (isM ? 0 : 1);
    auto fof = [&](int i) {
        int r = korder(i - 7, cls, nf - 7) + (grp & 1) * ((nf - 7) >> 1);
        r = r >= nf - 7 ? r - (nf - 7) : r;
        const int im = (fm & 1) ? (i < 3 ? i + 4 : i - 3) : i;
        const int j = i < 7 ? 7 * uh + (isM ? im : korder(i, cls, 7)) : (isM ? 14 + NA : 14) + r, k = 14 * (grp >> 1) + j;
        return __builtin_amdgcn_readfirstlane(k >= 56 ? k - 56 : k);
    };
    const char* g2 = (const char*)P.w2 + fm * 256;
    Loader<4, 4> L;
    if (loader) L.init_m<TR>(lw, lane, S_ * 2);
    auto preload_w2 = [&]() {
        int k0 = fof(0), k1 = fof(1);
        asm volatile("" : "+s"(k0), "+s"(k1));
        L.issue_m<false>(g2 + k0 * W2S, ringl); L.issue_m<false>(g2 + k1 * W2S, ringl + SB);
    };
    if (isM) {
        if (loader) { preload_w2(); L.issue_m<false>(g2 + fof(2) * W2S, ringl + 2 * SB); WAIT_VM(0); }
        WAIT_LGKM0();
        BARRIER();
    } else {
        if (loader) { WAIT_VM(0); __builtin_amdgcn_s_setprio(1); }
        WAIT_LGKM0();
        BARRIER();
#pragma unroll 1
        for (int it = 0; it < 2; ++it) {
            f32x16 sc[8];
            int ln = lane;
            asm volatile("" : "+v"(ln));
            const int dg = (ln & 31) - 4 * (ln >> 5);
            const unsigned up = dg < 0 ? ~0u : ~1u << dg;
            int nbad = qb == 0 ? 8 - wid : 0;
            asm volatile("" : "+s"(nbad));
            SCHED0();
            const float m = attn_scores(lds + (it ? LDS_BK : LDS_AK), lds + LDS_AQ, nbad, wid, ln, up, sc);
            SCHED0();
            WAIT_VM(0);
            BARRIER();
            if (tid == 0) set_flag(P.flags(), it == 0 ? FL_Q + fm * 32 + fn + 8 : FL_O + qb * 32 + hd, tag);
            if (it == 1 && loader) preload_w2();
            f32x16 oacc = {};
            float lsum;
            attn_pv(lds + (it ? LDS_BV : LDS_AV), nbad, wid, ln, up, sc, m, oacc, lsum);
            attn_out(P, hd + 16 * it, qb * 8 + wid, ln, lsum, oacc);
            if (it == 0) {
                if (loader) {
                    __builtin_amdgcn_s_setprio(0);
                    int h2 = hd + 16, q2 = qb, w2 = lw;
                    asm volatile("" : "+s"(h2), "+s"(q2), "+s"(w2));
                    poll_flags(P.flags(), attn_dep(h2, q2, ln), tag);
                    attn_load<0>(P, ring0 + LDS_AQ, h2, q2, w2, ln);
                    attn_load<1>(P, ring0 + LDS_BK, h2, q2, w2, ln);
                    attn_load<2>(P, ring0 + LDS_BV, h2, q2, w2, ln);
                    WAIT_VM(8);
                    __builtin_amdgcn_s_setprio(1);
                }
                BARRIER();
            }
        }
        WAIT_LGKM0();
    }
    const int own = isM ? 0 : 1;
    if (loader) {
        const char* go = (const char*)P.o() + fm * 8192 + own * 4096;
        const char* gw = (const char*)P.wo + fn * 256;
        const char* gu = (const char*)P.U() + fn * 8192;
        __builtin_amdgcn_s_setprio(0);
        L.init_n<FRAG>(lw, lane, US / 2);
        auto issue_f = [&](int u) {
            const int k = fof(u);
            L.issue_m<false>(g2 + k * W2S, ringl + (u & (NS - 1)) * SB); L.issue_n<true>(gu + k * US, ringl + (u & (NS - 1)) * SB);
        };
        const int ulo = isM ? ((14 + NA) * 2) / 7 : 4, uhi = isM ? 15 : ((14 + NA) * 2 - 1) / 7;
        const int udep = (lane >= ulo && lane <= uhi) ? FL_U + fn * 32 + (((grp >> 1) * 4 + lane) & 15) : -1;
        const int odep = lane < 32 ? FL_O + (fm >> 1) * 32 + lane : lane < 48 ? FL_U + fn * 32 + lane - 32 : -1, pdep = lane == 0 ? FL_P + tile * 32 + own : -1;
        u64 pk;
        if (isM) { BARRIER(); BARRIER(); }
        poll_flags(P.flags(), lane < 2 ? FL_U + fn * 32 + (grp >> 1) * 4 + 2 * uh + lane : -1, tag);
        if (isM) { issue_f(3); issue_f(4); }
        else {
#pragma unroll
            for (int u = 0; u < 2; ++u) L.issue_n<true>(gu + fof(u) * US, ringl + u * SB);
            WAIT_VM(4); BARRIER();
            issue_f(2); issue_f(3);
            WAIT_VM(16); BARRIER(); issue_f(4);
            WAIT_VM(16); BARRIER();
        }
        peek_issue(pk, P.flags(), udep); issue_f(5);
        WAIT_VM(16); BARRIER(); issue_f(6);
        WAIT_VM(16);
        int nxt = 7;
        if (!peek_ok(pk, udep, tag)) {
            bool uready = false;
#pragma unroll 1
            for (int t = 3; t < 7; ++t) {
                if (t == 6 && !uready) { poll_flags(P.flags(), udep, tag); uready = true; for (; nxt < 10; ++nxt) issue_f(nxt); }
                if (nxt - (t + 2) >= 2) WAIT_VM(16); else WAIT_VM(0);
                BARRIER();
                if (!uready) uready = flags_set(P.flags(), udep, tag);
                if (uready) for (; nxt <= t + NS; ++nxt) issue_f(nxt);
            }
        }
#pragma unroll 1
        for (; nxt < nf - 2; ++nxt) {
            int r = korder(nxt - 7, cls, nf - 7) + (grp & 1) * ((nf - 7) >> 1);
            r = r >= nf - 7 ? r - (nf - 7) : r;
            int k = 14 * (grp >> 1) + (isM ? 14 + NA : 14) + r;
            k = k >= 56 ? k - 56 : k;
            L.turn<false, true, 16>(g2 + k * W2S, gu + k * US, ringl + (nxt & (NS - 1)) * SB);
        }
        const unsigned xo = (unsigned)(((fm * 128 + (lane >> 2)) * D_ + fn * 128 + lw * 32 + (lane & 3) * 8) * 2);
        constexpr unsigned ROWS16 = 16 * D_ * 2;
        WAIT_VM(16); BARRIER(); peek_issue(pk, P.flags(), odep);
        issue_f(nf - 2);
        WAIT_VM(16); BARRIER(); issue_f(nf - 1);
        L.init_n<TR>(lw, lane, D_ * 2);
        const unsigned oo = (unsigned)(lw * 1024 + lane * 16);
        const unsigned oo2 = oo + (unsigned)(OS / 2);
        const char *po, *pw; unsigned pd;
        auto addr_w = [&](int i) {
            const int k = (korder(i, cls, KW) + 4 * (grp >> 1)) & (KW - 1);
            po = go + k * OS; pw = gw + k * WOS; pd = ringl + ((nf + i) & (NS - 1)) * SB;
            asm volatile("" : "+s"(po), "+s"(pw), "+s"(pd));
        };
        auto issue_w = [&]() {
            asm volatile("s_add_u32 m0, %2, %3\n\ts_nop 0\n\tglobal_load_lds_dwordx4 %4, %0 sc1\n\ts_addk_i32 m0, 8448\n\ts_nop 0\n\tglobal_load_lds_dwordx4 %5, %0 sc1\n\t"
                         "s_add_u32 m0, %2, 16896\n\ts_nop 0\n\tglobal_load_lds_dwordx4 %6, %1\n\ts_addk_i32 m0, 4224\n\ts_nop 0\n\tglobal_load_lds_dwordx4 %7, %1\n\t"
                         "s_addk_i32 m0, 4224\n\ts_nop 0\n\tglobal_load_lds_dwordx4 %8, %1\n\ts_addk_i32 m0, 4224\n\ts_nop 0\n\tglobal_load_lds_dwordx4 %9, %1"
                         ::"s"(po), "s"(pw), "s"(pd), "s"(own * 4 * PIECE), "v"(oo), "v"(oo2), "v"(L.on[0]), "v"(L.on[1]), "v"(L.on[2]), "v"(L.on[3]) : "scc", "memory");
        };
#define TURN_W(i, w) do { addr_w(i); WAIT_VM(w); BARRIER(); issue_w(); } while (0)
        addr_w(0);
        WAIT_VM(16);
        if (!peek_ok(pk, odep, tag)) poll_flags(P.flags(), odep, tag);
        BARRIER(); issue_w();
        TURN_W(1, 14); TURN_W(2, 12); TURN_W(3, 12);
        TURN_W(4, 12);
        {
            u32x4 t0, t1, t2, t3;
            asm volatile("ds_read_b128 %0, %4\n\tds_read_b128 %1, %4 offset:1024\n\tds_read_b128 %2, %4 offset:2048\n\tds_read_b128 %3, %4 offset:3072\n\ts_waitcnt lgkmcnt(0)"
                         : "=&v"(t0), "=&v"(t1), "=&v"(t2), "=&v"(t3) : "v"(ring0 + LDS_EX + lw * 4096 + lane * 16) : "memory");
            const __amdgpu_buffer_rsrc_t ro = rsrc(P.out, (unsigned)S_ * D_ * 2);
            const unsigned so = xo + (1 - own) * 4 * ROWS16;
            __builtin_amdgcn_raw_buffer_store_b128(t0, ro, so, 0, SC1); __builtin_amdgcn_raw_buffer_store_b128(t1, ro, so + ROWS16, 0, SC1);
            __builtin_amdgcn_raw_buffer_store_b128(t2, ro, so + 2 * ROWS16, 0, SC1); __builtin_amdgcn_raw_buffer_store_b128(t3, ro, so + 3 * ROWS16, 0, SC1);
        }
        TURN_W(5, 12); TURN_W(6, 12);
        TURN_W(7, 0);
        if (tid == 256) set_flag(P.flags(), FL_P + tile * 32 + 1 - own, tag);
        TURN_W(8, 12); TURN_W(9, 12); TURN_W(10, 12); TURN_W(11, 12);
        TURN_W(12, 12); TURN_W(13, 12); TURN_W(14, 12);
        TURN_W(15, 12);
#undef TURN_W
        peek_issue(pk, P.flags(), pdep);
        WAIT_VM(13); BARRIER();
        WAIT_VM(7); BARRIER();
        WAIT_VM(0);
        auto load_partial = [&]() {
#pragma unroll
            for (int i = 0; i < 4; ++i) dma16<true, 0>((const char*)P.out + (own * 4 + i) * ROWS16, xo, ring0 + LDS_EX + (lw * 4 + i) * 1024);
        };
        const bool early = peek_ok(pk, pdep, tag);
        if (early) load_partial();
        BARRIER();
        BARRIER();
        if (!early) { poll_flags(P.flags(), pdep, tag); load_partial(); }
        WAIT_VM(0); BARRIER();
        if (blockIdx.x == 0 && lw == 0) {
#pragma unroll 1
            for (int i = 0; i < 4; ++i) poll_flags(P.flags(), FL_O + i * 64 + lane, tag);
            if (lane == 0) set_flag(P.flags(), FL_E, tag);
        }
    } else {
        const __amdgpu_buffer_rsrc_t ro = rsrc(P.out, (unsigned)S_ * D_ * 2);
        f32x4 acc[4][4];
        if (isM) {
            LAS char* xp = lds + LDS_X + (wn * 16 + wm * 4) * 1024 + (lane & 15) * 64 + (((lane >> 4) & 1) * 2 + (lane >> 5)) * 16;
            u32x4 xv[4][2]; unsigned bv[4];
#pragma unroll
            for (int i = 0; i < 4; ++i) {
                xv[i][0] = *(LAS u32x4*)(xp + i * 1024); xv[i][1] = *(LAS u32x4*)(xp + 8192 + i * 1024);
                bv[i] = *(LAS u16*)(lds + LDS_X + 32768 + (wm * 64 + i * 16 + (lane & 15)) * 2);
            }
            SCHED0();
#pragma unroll
            for (int i = 0; i < 4; ++i)
#pragma unroll
                for (int jp = 0; jp < 2; ++jp) {
                    auto e0 = __builtin_amdgcn_permlane16_swap(xv[i][jp][0], xv[i][jp][2], false, false);
                    auto e1 = __builtin_amdgcn_permlane16_swap(xv[i][jp][1], xv[i][jp][3], false, false);
                    acc[i][2 * jp] = (f32x4){lo16(e0[0]), hi16(e0[0]), lo16(e1[0]), hi16(e1[0])} + lo16(bv[i]);
                    acc[i][2 * jp + 1] = (f32x4){lo16(e0[1]), hi16(e0[1]), lo16(e1[1]), hi16(e1[1])} + lo16(bv[i]);
                }
            pin_acc<4, 4, 4>(acc);
        } else {
            float z = 0.f;
            asm volatile("" : "+v"(z));
#pragma unroll
            for (int i = 0; i < 4; ++i)
#pragma unroll
                for (int j = 0; j < 4; ++j) acc[i][j] = (f32x4){z, z, z, z};
        }
        pin_acc<4, 4, 4>(acc);
        Reader<TR, 128, FRAG, 128, 4, 4> rf;
        HalfReader rw;
        Regs<4, 4> R0;
        LAS char* exl = lds + LDS_EX + wn * 8192 + lane * 16;
        int slot = 0;
#define STEP(a, b) stage_step<NS, SB>(slot, a, b, R0, acc)
        rf.init(ring0, lane, wm, wn);
        WAIT_LGKM0();
        if (!isM) BARRIER();
        rf.read(R0, 0);
        for (int t = 0; t < (nf - 2) / 4; ++t) {
            if (__builtin_expect(!isM && (t == 1 || t == 2), 0)) {
                if (t == 1) WAIT_VM(0);
                else if (tid == 0) set_flag(P.flags(), FL_O + qb * 32 + hd + 16, tag);
            }
            STEP(rf, rf); STEP(rf, rf); STEP(rf, rf); STEP(rf, rf);
        }
        static_assert((NF_M & 3) == (NF_Q & 3), "NA must be odd");
#pragma unroll
        for (int t = 0; t < ((NF_M - 2) & 3); ++t) STEP(rf, rf);
        STEP(rf, rf);
        const int kq = wm != own ? 1 : 0;
        rw.init(ring0 + slot * SB, lane, own, kq, wn);
        STEP(rf, rw);
        if (kq) {
#pragma unroll
            for (int i = 0; i < 4; ++i)
#pragma unroll
                for (int jp = 0; jp < 2; ++jp) {
                    const f32x4 a = acc[i][2 * jp], b = acc[i][2 * jp + 1];
                    *(LAS u32x4*)(exl + jp * 4096 + i * 1024) = (u32x4){pack2(a[0], a[1]), pack2(a[2], a[3]), pack2(b[0], b[1]), pack2(b[2], b[3])};
                    acc[i][2 * jp] = (f32x4){0.f, 0.f, 0.f, 0.f}; acc[i][2 * jp + 1] = (f32x4){0.f, 0.f, 0.f, 0.f};
                }
        }
        pin_acc<4, 4, 4>(acc);
        Regs<4, 4> R1;
        for (int t = 0; t < 4; ++t) {
            half_step<NS, SB>(slot, rw, R0, R1, acc); half_step<NS, SB>(slot, rw, R1, R0, acc);
            half_step<NS, SB>(slot, rw, R0, R1, acc); half_step<NS, SB>(slot, rw, R1, R0, acc);
        }
#pragma unroll
        for (int i = 0; i < 2; ++i)
#pragma unroll
            for (int j = 0; j < 4; ++j) *(LAS f32x4*)(lds + (wid * 8 + i * 4 + j) * 1024 + lane * 16) = acc[2 + i][j];
        WAIT_LGKM0();
        BARRIER();
        const unsigned fo = (unsigned)(((fm * 128 + own * 64 + kq * 32 + (lane & 15)) * D_ + fn * 128 + wn * 64 + ((lane >> 4) & 1) * 16 + (lane >> 5) * 8) * 2);
#pragma unroll
        for (int i = 0; i < 2; ++i)
#pragma unroll
            for (int jp = 0; jp < 2; ++jp) {
                const u32x4 e = *(LAS u32x4*)(exl + jp * 4096 + (kq * 2 + i) * 1024);
                const f32x4 a = acc[i][2 * jp] + *(LAS f32x4*)(lds + ((wid ^ 2) * 8 + i * 4 + 2 * jp) * 1024 + lane * 16) + (f32x4){lo16(e[0]), hi16(e[0]), lo16(e[1]), hi16(e[1])};
                const f32x4 b = acc[i][2 * jp + 1] + *(LAS f32x4*)(lds + ((wid ^ 2) * 8 + i * 4 + 2 * jp + 1) * 1024 + lane * 16) + (f32x4){lo16(e[2]), hi16(e[2]), lo16(e[3]), hi16(e[3])};
                __builtin_amdgcn_raw_buffer_store_b128(pair16(pack2(a[0], a[1]), pack2(a[2], a[3]), pack2(b[0], b[1]), pack2(b[2], b[3])), ro, fo + i * (16 * D_ * 2) + jp * 64, 0, SC1);
            }
#undef STEP
    }
}

extern "C" void kernel_launch(void* const* d_in, const int* in_sizes, int n_in, void* d_out, int out_size, void* d_ws, size_t ws_size, hipStream_t stream) {
    char* ws = (char*)d_ws;
    Params P;
    P.x = (const u16*)d_in[0]; P.wqkv = (const u16*)d_in[1]; P.wo = (const u16*)d_in[2];
    P.w1 = (const u16*)d_in[4]; P.b1 = (const u16*)d_in[5]; P.w2 = (const u16*)d_in[6]; P.b2 = (const u16*)d_in[7];
    P.out = (u16*)d_out; P.ws = ws;
    (void)hipFuncSetAttribute((const void*)k_main, hipFuncAttributeMaxDynamicSharedMemorySize, LDS_BYTES);
    k_main<<<256, 512, LDS_BYTES, stream>>>(P);
}
```

```cpp
#define TLBD 0
#include <hip/hip_runtime.h>
#include <hip/hip_bf16.h>
#include <stdint.h>

#define LAS __attribute__((address_space(3)))
#define GAS __attribute__((address_space(1)))
typedef __bf16 bf16x8 __attribute__((ext_vector_type(8)));
typedef __bf16 bf16x4 __attribute__((ext_vector_type(4)));
typedef __bf16 bf16x2 __attribute__((ext_vector_type(2)));
typedef float f32x2 __attribute__((ext_vector_type(2)));
typedef float f32x4 __attribute__((ext_vector_type(4)));
typedef float f32x16 __attribute__((ext_vector_type(16)));
typedef unsigned u32x4 __attribute__((ext_vector_type(4)));
typedef unsigned u32x2 __attribute__((ext_vector_type(2)));
typedef unsigned short u16;
typedef unsigned long long u64;

constexpr int S_ = 2048, D_ = 1024, F_ = 3584, NH = 32;

constexpr size_t WS_FLAGS = 0;
constexpr size_t WS_STAT  = 1 << 16;
constexpr size_t WS_QKV   = 1 << 17;
constexpr size_t WS_U     = WS_QKV + (size_t)S_ * 3 * D_ * 2;
constexpr int FL_E = 0;
constexpr int FL_L = 32;
constexpr int FL_U = 8 * 32;
constexpr int FL_Q = 16 * 32;
constexpr int FL_O = 32 * 32;
constexpr int FL_P = 40 * 32;

#define WAIT_VM(n) asm volatile("s_waitcnt vmcnt(%0)" ::"n"(n) : "memory")
#define WAIT_LGKM0() __builtin_amdgcn_s_waitcnt(0xC07F)
#define CFENCE() asm volatile("" ::: "memory")
#define BARRIER() do { CFENCE(); __builtin_amdgcn_s_barrier(); CFENCE(); } while (0)
#define SCHED0() __builtin_amdgcn_sched_barrier(0)
#define RLX_AGENT __ATOMIC_RELAXED, __HIP_MEMORY_SCOPE_AGENT
constexpr int SC1 = 16;

__device__ __forceinline__ float bf2f(u16 v) { return __uint_as_float((unsigned)v << 16); }
__device__ __forceinline__ float lo16(unsigned v) { return __uint_as_float(v << 16); }
__device__ __forceinline__ float hi16(unsigned v) { return __uint_as_float(v & 0xffff0000u); }
__device__ __forceinline__ unsigned pack2(float lo, float hi) {
    f32x2 v = {lo, hi};
    bf16x2 b = __builtin_convertvector(v, bf16x2);
    return __builtin_bit_cast(unsigned, b);
}
__device__ __forceinline__ float rbf(float x) { return __uint_as_float(pack2(x, 0.f) << 16); }
template <int CTRL, int RMASK> __device__ __forceinline__ float dpp_add(float v) {
    return v + __builtin_bit_cast(float, __builtin_amdgcn_update_dpp(0, __builtin_bit_cast(int, v), CTRL, RMASK, 0xf, false));
}
__device__ __forceinline__ float wave_sum(float v) {
    v = dpp_add<0x111, 0xf>(v); v = dpp_add<0x112, 0xf>(v); v = dpp_add<0x114, 0xf>(v); v = dpp_add<0x118, 0xf>(v);
    v = dpp_add<0x142, 0xa>(v); v = dpp_add<0x143, 0xc>(v);
    return __builtin_bit_cast(float, __builtin_amdgcn_readlane(__builtin_bit_cast(int, v), 63));
}
__device__ __forceinline__ __amdgpu_buffer_rsrc_t rsrc(const void* p, unsigned bytes) { return __builtin_amdgcn_make_buffer_rsrc((void*)p, 0, bytes, 0x00020000); }

enum { ROW = 0, TR = 1, FRAG = 2 };
constexpr int PIECE = 1056, BLK = 8 * PIECE;
template <int T, int X> struct Op {
    static constexpr int BYTES = X * 132;
    __device__ static __forceinline__ unsigned src_off(int blk, int p, int lane, unsigned ld, unsigned cb) {
        const int pos = lane >> 3;
        if (T == TR) {
            if (X == 224 && blk == 3) {
                const int ps = lane >> 2, g = (p & 1) | ((ps >> 3) << 1), r = 32 * (p >> 1) + 8 * g + 4 * ((ps >> 2) & 1) + (ps & 3);
                return (unsigned)r * ld + cb + (unsigned)((lane & 3) * 16);
            }
            const int g = (p >> 2) | (pos & 2), r = 32 * (pos >> 2) + 8 * g + 4 * (pos & 1) + (p & 3);
            return (unsigned)r * ld + cb + (unsigned)((lane & 7) * 16);
        } else if (T == ROW) {
            const int j = ((pos >> 1) & 1) | ((pos & 1) << 1) | (pos & 4);
            return (unsigned)(blk * 64 + j * 8 + p) * ld + (unsigned)((lane & 7) * 16);
        } else {
            return (unsigned)blk * ld + (unsigned)(p * 1024 + lane * 16);
        }
    }
};
template <bool SC, int IMM> __device__ __forceinline__ void dma16(const char* base, unsigned off, unsigned l) {
    if (SC) asm volatile("s_add_u32 m0, %2, %3\n\ts_nop 0\n\tglobal_load_lds_dwordx4 %0, %1 sc1" ::"v"(off), "s"(base), "s"(l), "n"(IMM) : "scc", "memory");
    else    asm volatile("s_add_u32 m0, %2, %3\n\ts_nop 0\n\tglobal_load_lds_dwordx4 %0, %1" ::"v"(off), "s"(base), "s"(l), "n"(IMM) : "scc", "memory");
}
#define DMA_FIRST(sc) "s_add_u32 m0, %1, %2\n\ts_nop 0\n\tglobal_load_lds_dwordx4 %3, %0" sc
#define DMA_NEXT(n, sc) "\n\ts_addk_i32 m0, 4224\n\ts_nop 0\n\tglobal_load_lds_dwordx4 %" #n ", %0" sc
#define DMA_G4(sc) DMA_FIRST(sc) DMA_NEXT(4, sc) DMA_NEXT(5, sc) DMA_NEXT(6, sc)
template <bool SC, int IMM, int N> __device__ __forceinline__ void dma_group(const char* base, const unsigned (&o)[N], unsigned l) {
    static_assert(4 * PIECE == 4224 && (N == 4 || ((N == 6 || N == 7) && !SC)), "dma_group");
    if constexpr (N == 4 && SC) asm volatile(DMA_G4(" sc1") ::"s"(base), "s"(l), "n"(IMM), "v"(o[0]), "v"(o[1]), "v"(o[2]), "v"(o[3]) : "scc", "memory");
    else if constexpr (N == 4) asm volatile(DMA_G4("") ::"s"(base), "s"(l), "n"(IMM), "v"(o[0]), "v"(o[1]), "v"(o[2]), "v"(o[3]) : "scc", "memory");
    else if constexpr (N == 6) asm volatile(DMA_G4("") DMA_NEXT(7, "") DMA_NEXT(8, "") ::"s"(base), "s"(l), "n"(IMM), "v"(o[0]), "v"(o[1]), "v"(o[2]), "v"(o[3]), "v"(o[4]), "v"(o[5]) : "scc", "memory");
    else asm volatile(DMA_G4("") DMA_NEXT(7, "") DMA_NEXT(8, "") DMA_NEXT(9, "") ::"s"(base), "s"(l), "n"(IMM), "v"(o[0]), "v"(o[1]), "v"(o[2]), "v"(o[3]), "v"(o[4]), "v"(o[5]), "v"(o[6]) : "scc", "memory");
}
__device__ __forceinline__ void dma4(const char* base, unsigned off, unsigned l) {
    asm volatile("s_mov_b32 m0, %2\n\ts_nop 0\n\tglobal_load_lds_dword %0, %1" ::"v"(off), "s"(base), "s"(l) : "memory");
}
__device__ __forceinline__ void dma16x4(const char* base, unsigned off, unsigned l) {
    asm volatile("s_mov_b32 m0, %2\n\ts_nop 0\n\tglobal_load_lds_dwordx4 %0, %1 sc1\n\tglobal_load_lds_dwordx4 %0, %1 offset:1024 sc1\n\t"
                 "global_load_lds_dwordx4 %0, %1 offset:2048 sc1\n\tglobal_load_lds_dwordx4 %0, %1 offset:3072 sc1" ::"v"(off), "s"(base), "s"(l) : "memory");
}

template <int T, int X> struct Addr {
    unsigned a, b;
    static constexpr int NV = (X == 224 || X == 192) ? 2 : 1;
    __device__ __forceinline__ void init(unsigned base, int lane, int w) {
        const int g = lane >> 4;
        b = 0;
        if (T == TR) {
            const int q = (lane & 15) >> 2, p = lane & 3;
            a = base + (unsigned)((q | ((g & 1) << 2)) * PIECE + (g >> 1) * 256 + p * 8 + (X == 128 ? w * BLK : w * 64));
            b = base + (unsigned)(3 * BLK + (g & 1) * PIECE + (g >> 1) * 512 + q * 64 + w * 32 + p * 8);
        } else if (T == ROW) {
            const int r = lane & 15;
            a = base + (unsigned)(w * BLK + (r & 7) * PIECE + (r >> 3) * 256 + g * 16);
        } else {
            a = base + (unsigned)(w * 4 * PIECE + lane * 16);
        }
    }
    __device__ __forceinline__ void step(unsigned d) { a += d; if (NV == 2) b += d; }
    __device__ __forceinline__ bf16x8 read(int n, int kk) const {
        if (T == TR) {
            LAS char* p; int t;
            if (X == 224 && n == 6) { p = (LAS char*)(uintptr_t)b + kk * 2 * PIECE; t = 256; }
            else if (X == 192) { p = (LAS char*)(uintptr_t)((n & 1) ? b : a) + kk * 512 + (n >> 1) * BLK; t = 128; }
            else { p = (LAS char*)(uintptr_t)a + kk * 512 + (X == 128 ? n * 32 : (n >> 1) * BLK + (n & 1) * 32); t = 128; }
            const bf16x4 lo = __builtin_amdgcn_ds_read_tr16_b64_v4bf16((LAS bf16x4*)p);
            const bf16x4 hi = __builtin_amdgcn_ds_read_tr16_b64_v4bf16((LAS bf16x4*)(p + t));
            return __builtin_shufflevector(lo, hi, 0, 1, 2, 3, 4, 5, 6, 7);
        } else if (T == ROW) {
            return *(LAS bf16x8*)((LAS char*)(uintptr_t)a + (n & 1) * 128 + (n >> 1) * 512 + kk * 64);
        } else {
            return *(LAS bf16x8*)((LAS char*)(uintptr_t)a + kk * BLK + n * PIECE);
        }
    }
};
template <int FM, int FN> struct Regs { bf16x8 m[FM], n[FN]; };
#define MFMA16(a, b, c) __builtin_amdgcn_mfma_f32_16x16x32_bf16(a, b, c, 0, 0, 0)

template <int TM, int BM, int TN, int BN, int FM, int FN> struct Reader {
    Addr<TM, BM> m; Addr<TN, BN> n;
    static constexpr int NDS = FM * (TM == TR ? 2 : 1) + FN * (TN == TR ? 2 : 1), NV = Addr<TM, BM>::NV + Addr<TN, BN>::NV;
    __device__ __forceinline__ void init(unsigned slot_base, int lane, int wm, int wn) { m.init(slot_base, lane, wm); n.init(slot_base + BM * 132, lane, wn); }
    __device__ __forceinline__ void read(Regs<FM, FN>& R, int kk) const {
#pragma unroll
        for (int i = 0; i < FM; ++i) R.m[i] = m.read(i, kk);
#pragma unroll
        for (int j = 0; j < FN; ++j) R.n[j] = n.read(j, kk);
    }
    __device__ __forceinline__ void step(unsigned d) { m.step(d); n.step(d); }
};
template <int AM, int FM, int FN> __device__ __forceinline__ void mma(f32x4 (&acc)[AM][FN], const Regs<FM, FN>& R) {
    const u32x4 one = {0x3f803f80u, 0x3f803f80u, 0x3f803f80u, 0x3f803f80u};
#pragma unroll
    for (int j = 0; j < FN; ++j) {
        if (AM > FM && !(j & 1)) acc[AM - 1][j] = MFMA16(R.n[j], __builtin_bit_cast(bf16x8, one), acc[AM - 1][j]);
#pragma unroll
        for (int ii = 0; ii < FM; ++ii) { const int i = (j & 1) ? FM - 1 - ii : ii; acc[i][j] = MFMA16(R.n[j], R.m[i], acc[i][j]); }
    }
}
template <int AM, int FM, int FN> __device__ __forceinline__ void pin_acc(f32x4 (&acc)[AM][FN]) {
#pragma unroll
    for (int j = 0; j < FN; ++j) {
        asm volatile("" : "+v"(acc[0][j]), "+v"(acc[1][j]), "+v"(acc[2][j]), "+v"(acc[3][j]));
        if (AM > FM && !(j & 1)) asm volatile("" : "+v"(acc[AM - 1][j]));
    }
}
template <int NDS, int NV, int NMF> __device__ __forceinline__ void interleave() {
#pragma unroll
    for (int i = 0; i < NMF; ++i) {
        __builtin_amdgcn_sched_group_barrier(0x8, 1, 0);
        if (i < NDS) __builtin_amdgcn_sched_group_barrier(0x80, 1, 0);
        if (i >= NMF - NV) __builtin_amdgcn_sched_group_barrier(0x2, 1, 0);
    }
}
template <int NS, int SB, class RD, class RDN, int AM, int FM, int FN>
__device__ __forceinline__ void stage_step(int& slot, RD& rd, RDN& rn, Regs<FM, FN>& R0, f32x4 (&acc)[AM][FN]) {
    constexpr int NMF = FM * FN + (AM - FM) * (FN / 2);
    Regs<FM, FN> R1;
    const unsigned d = (slot + 1 == NS) ? (unsigned)(-(NS - 1) * SB) : (unsigned)SB;
    slot = (slot + 1 == NS) ? 0 : slot + 1;
    rd.read(R1, 1);
    rn.step(d);
    mma(acc, R0);
    interleave<RD::NDS, RDN::NV, NMF>();
    SCHED0();
    pin_acc<AM, FM, FN>(acc);
    WAIT_LGKM0();
    BARRIER();
    SCHED0();
    rn.read(R0, 0);
    mma(acc, R1);
    interleave<RDN::NDS, 0, NMF>();
    SCHED0();
    pin_acc<AM, FM, FN>(acc);
    SCHED0();
}

struct HalfReader {
    unsigned m0, m1; Addr<TR, 128> n;
    static constexpr int NDS = 12, NV = 3;
    __device__ __forceinline__ void init(unsigned slot_base, int lane, int own, int kq, int wn) {
        const unsigned a = slot_base + (unsigned)(own * 4 * PIECE + kq * BLK + lane * 16);
        m0 = a + kq * 2 * PIECE; m1 = a - kq * 2 * PIECE;
        n.init(slot_base + 128 * 132 + kq * 512, lane, wn);
    }
    __device__ __forceinline__ void read(Regs<4, 4>& R, int) const {
#pragma unroll
        for (int i = 0; i < 4; ++i) R.m[i] = *(LAS bf16x8*)((LAS char*)(uintptr_t)(i < 2 ? m0 : m1) + i * PIECE);
#pragma unroll
        for (int j = 0; j < 4; ++j) R.n[j] = n.read(j, 0);
    }
    __device__ __forceinline__ void step(unsigned d) { m0 += d; m1 += d; n.step(d); }
};
template <int NS, int SB> __device__ __forceinline__ void half_step(int& slot, HalfReader& rd, Regs<4, 4>& Rc, Regs<4, 4>& Rn, f32x4 (&acc)[4][4]) {
    rd.step((slot + 1 == NS) ? (unsigned)(-(NS - 1) * SB) : (unsigned)SB);
    slot = (slot + 1 == NS) ? 0 : slot + 1;
    SCHED0();
    WAIT_LGKM0();
    BARRIER();
    SCHED0();
    rd.read(Rn, 0);
    mma(acc, Rc);
    interleave<12, 0, 16>();
    SCHED0();
    pin_acc<4, 4, 4>(acc);
    SCHED0();
}

template <int NM, int NN> struct Loader {
    static constexpr int NPW = NM + NN;
    unsigned om[NM], on[NN];
    template <int T> __device__ __forceinline__ void init_m(int lw, int lane, unsigned ld) {
#pragma unroll
        for (int i = 0; i < NM; ++i) om[i] = Op<T, NM * 32>::src_off(i >> 1, lw + 4 * (i & 1), lane, ld, (i >> 1) * 128);
    }
    template <int T> __device__ __forceinline__ void init_n(int lw, int lane, unsigned ld, unsigned c0 = 0, unsigned c1 = 128, unsigned c2 = 256, unsigned c3 = 384) {
#pragma unroll
        for (int i = 0; i < NN; ++i) on[i] = Op<T, NN * 32>::src_off(i >> 1, lw + 4 * (i & 1), lane, ld, (i >> 1) == 0 ? c0 : (i >> 1) == 1 ? c1 : (i >> 1) == 2 ? c2 : c3);
    }
    template <bool SC> __device__ __forceinline__ void issue_m(const char* gm, unsigned dst) const { dma_group<SC, 0>(gm, om, dst); }
    template <bool SC> __device__ __forceinline__ void issue_n(const char* gn, unsigned dst) const { dma_group<SC, NM * 32 * 132>(gn, on, dst); }
    template <bool SCM, bool SCN, int W> __device__ __forceinline__ void turn(const char* gm, const char* gn, unsigned dst) const {
        asm volatile("" : "+s"(gm), "+s"(gn), "+s"(dst));
        WAIT_VM(W); BARRIER();
        issue_m<SCM>(gm, dst); issue_n<SCN>(gn, dst);
    }
};
__device__ __forceinline__ int korder(int p, int c, int nk) {
    const int r = (p - c) & 3;
    const int d = (0x1003 >> (4 * r) & 15) - 1;
    return (unsigned)(p - r) < (unsigned)(nk - 2) ? p + d : p;
}
__device__ __forceinline__ f32x2 gelu2(f32x2 u) {
    const float c1 = -2.0f * 0.7978845608028654f * 1.4426950408889634f, c2 = c1 * 0.044715f;
    const f32x2 z = u * ((u * u) * c2 + c1);
    f32x2 e; e.x = __builtin_amdgcn_exp2f(z.x); e.y = __builtin_amdgcn_exp2f(z.y);
    e = e + 1.0f;
    f32x2 r; r.x = __builtin_amdgcn_rcpf(e.x); r.y = __builtin_amdgcn_rcpf(e.y);
    return u * r;
}
__device__ __forceinline__ u32x4 pair16(unsigned x0, unsigned x1, unsigned y0, unsigned y1) {
    auto a = __builtin_amdgcn_permlane16_swap(x0, y0, false, false);
    auto b = __builtin_amdgcn_permlane16_swap(x1, y1, false, false);
    return (u32x4){a[0], b[0], a[1], b[1]};
}
__device__ __forceinline__ bool flags_set(u64* flags, int idx, u64 tag) {
    const u64 v = __hip_atomic_load((GAS u64*)flags + (idx < 0 ? 0 : idx), RLX_AGENT);
    const bool ok = __builtin_amdgcn_ballot_w64(idx >= 0 && v - 1ull != tag) == 0ull;
    CFENCE();
    return ok;
}
__device__ __forceinline__ void poll_flags(u64* flags, int idx, u64 tag) {
#pragma unroll 1
    for (unsigned spins = 0; spins < (1u << 20); ++spins) {
        if (flags_set(flags, idx, tag)) break;
        __builtin_amdgcn_s_sleep(2);
    }
}
__device__ __forceinline__ void peek_issue(u64& v, u64* flags, int idx) {
    asm volatile("global_load_dwordx2 %0, %1, off sc1" : "=v"(v) : "v"((GAS u64*)flags + (idx < 0 ? 0 : idx)) : "memory");
}
__device__ __forceinline__ bool peek_ok(u64& v, int idx, u64 tag) {
    asm volatile("" : "+v"(v) :: "memory");
    return __builtin_amdgcn_ballot_w64(idx >= 0 && v - 1ull != tag) == 0ull;
}
__device__ __forceinline__ void set_flag(u64* flags, int idx, u64 tag) {
    asm volatile("" : "+v"(tag));
    __hip_atomic_store((GAS u64*)flags + idx, tag + 1ull, RLX_AGENT);
}

struct Params {
    const u16 *x, *wqkv, *wo, *w1, *b1, *w2, *b2;
    u16* out;
    char* ws;
    __device__ __forceinline__ u16* h() const { return out; }
    __device__ __forceinline__ u16* qkv() const { return (u16*)(ws + WS_QKV); }
    __device__ __forceinline__ u16* U() const { return (u16*)(ws + WS_U); }
    __device__ __forceinline__ u16* o() const { return (u16*)(ws + WS_QKV); }
    __device__ __forceinline__ float* stat() const { return (float*)(ws + WS_STAT); }
    __device__ __forceinline__ u64* flags() const { return (u64*)(ws + WS_FLAGS); }
};

constexpr int SB_M = (128 + 224) * 132, SB_Q = (128 + 192) * 132, SB_F = 256 * 132, NS_F = 4;
constexpr int LDS_QX = 3 * SB_Q;
constexpr int LDS_CTL = LDS_QX + 3072;
constexpr int LDS_AQ = 0, LDS_AK = 2 * SB_Q, LDS_AV = 128 * 1024, LDS_BYTES = 160 * 1024;
constexpr int LDS_MC = LDS_BYTES - 512;
constexpr int LDS_X = 3 * SB_F;
constexpr int LDS_BK = 16 * 1024, LDS_BV = 96 * 1024;
constexpr int LDS_EX = 136 * 1024;
constexpr int LDS_SINK = LDS_BYTES - 256;
static_assert(LDS_CTL + 32 <= LDS_AV && 3 * SB_M <= LDS_SINK && 3 * SB_F <= LDS_AV && NS_F * SB_F <= LDS_EX && LDS_EX + 16384 <= LDS_MC, "LDS map");
constexpr int NA = 19;
constexpr int KW = 16, NF_Q = 7 + NA, NF_M = 7 + 42 - NA;

__device__ __forceinline__ int qkv_head(int tn, int blk) { return blk * NH + ((tn & ~3) | ((tn + blk) & 3)) * 2; }

__device__ __forceinline__ f32x4 ln_row(const u32x4 a, const u32x4 b, u32x4& oa, u32x4& ob) {
    f32x2 v[8];
#pragma unroll
    for (int i = 0; i < 4; ++i) { v[i] = (f32x2){lo16(a[i]), hi16(a[i])}; v[4 + i] = (f32x2){lo16(b[i]), hi16(b[i])}; }
    const f32x2 s2 = ((v[0] + v[1]) + (v[2] + v[3])) + ((v[4] + v[5]) + (v[6] + v[7]));
    const float mean = wave_sum(s2.x + s2.y) * (1.0f / D_);
    f32x2 q2 = {0.f, 0.f};
#pragma unroll
    for (int i = 0; i < 8; ++i) { v[i] -= mean; q2 += v[i] * v[i]; }
    const float var = wave_sum(q2.x + q2.y) * (1.0f / D_);
    const float rs = __builtin_amdgcn_rcpf(rbf(__builtin_amdgcn_sqrtf(rbf(rbf(var) + 1e-5f))));
    const float sd2 = rbf(__builtin_amdgcn_sqrtf(rbf(rbf(var * rs * rs) + 1e-5f)));
    const float g = rs * __builtin_amdgcn_rcpf(sd2);
#pragma unroll
    for (int i = 0; i < 8; ++i) {
        const f32x2 t = v[i] * g;
        if (i < 4) oa[i] = pack2(t.x, t.y); else ob[i - 4] = pack2(t.x, t.y);
    }
    return (f32x4){mean, rs, sd2, 0.f};
}

#define MFMA32(a, b, c) __builtin_amdgcn_mfma_f32_32x32x16_bf16(a, b, c, 0, 0, 0)
__device__ __forceinline__ int attn_dep(int hd, int qb, int lane) {
    const int which = lane >> 2, st = 2 * qb - 2 + (lane & 3), p = hd >> 1;
    if (lane >= 12 || st < 0 || (which == 0 && (lane & 3) < 2)) return -1;
    return FL_Q + st * 32 + ((p & ~3) | ((p - which) & 3));
}
template <int WHICH> __device__ __forceinline__ void attn_load(const Params& P, unsigned dst, int hd, int qb, int w, int lane) {
    const int row = WHICH == 0 ? qb * 256 + w * 64 : qb * 256 - ((qb == 0 && w < 2) ? 0 : 256) + w * 128;
    const char* g = (const char*)(P.qkv() + ((size_t)(WHICH * NH + hd) * S_ + row) * 32);
    const unsigned off = (unsigned)((lane >> 2) * 64 + (WHICH == 2 ? lane & 3 : (lane & 3) ^ ((lane >> 4) & 3)) * 16);
    dma16x4(g, off, dst + w * (WHICH == 0 ? 4096 : 8192));
    if (WHICH != 0) dma16x4(g + 4096, off, dst + w * 8192 + 4096);
}
#define PIN(x) asm volatile("" : "+v"(x))
__device__ __forceinline__ float attn_scores(LAS char* buf, LAS char* qbuf, int nbad, int wid, int lane, unsigned up, f32x16 (&sc)[8]) {
    const int r32 = lane & 31, hh = lane >> 5, sw = (r32 >> 2) & 3;
    LAS char* k0p = buf + (wid * 32 + r32) * 64 + ((hh ^ sw) * 16);
    LAS char* k1p = buf + (wid * 32 + r32) * 64 + (((2 + hh) ^ sw) * 16);
    const bf16x8 qf0 = *(LAS bf16x8*)(qbuf + (wid * 32 + r32) * 64 + ((hh ^ sw) * 16));
    const bf16x8 qf1 = *(LAS bf16x8*)(qbuf + (wid * 32 + r32) * 64 + (((2 + hh) ^ sw) * 16));
    bf16x8 ka[9], kb[9];
#pragma unroll
    for (int i = 0; i < 3; ++i) { const int j = i == 0 ? 8 : i - 1; ka[i] = *(LAS bf16x8*)(k0p + j * 2048); kb[i] = *(LAS bf16x8*)(k1p + j * 2048); SCHED0(); }
    const float NEG = -INFINITY;
    const f32x16 zero = {};
    f32x16 s8;
    float m = NEG;
#pragma unroll
    for (int i = 0; i < 11; ++i) {
        if (i + 3 < 9) { ka[i + 3] = *(LAS bf16x8*)(k0p + (i + 2) * 2048); kb[i + 3] = *(LAS bf16x8*)(k1p + (i + 2) * 2048); }
        if (i < 9) {
            const f32x16 t = MFMA32(kb[i], qf1, MFMA32(ka[i], qf0, zero));
            if (i == 0) { s8 = t; PIN(s8); } else { sc[i - 1] = t; PIN(sc[i - 1]); }
        }
        SCHED0();
        const int j = i - 3;
        if (j == 0) {
            if (__builtin_expect(nbad > 0, 0)) {
#pragma unroll
                for (int r = 0; r < 16; ++r) sc[0][r] = NEG;
                PIN(sc[0]);
            }
#pragma unroll
            for (int r = 0; r < 16; ++r) {
                const unsigned mk = (unsigned)__builtin_amdgcn_sbfe((int)up, (r & 3) + 8 * (r >> 2), 1);
                sc[0][r] = __uint_as_float(__builtin_amdgcn_bitop3_b32(mk, __float_as_uint(sc[0][r]), __float_as_uint(s8[r]), 0xca));
            }
        }
        if (j >= 0) {
            float mj = m;
#pragma unroll
            for (int r = 0; r < 16; r += 2) mj = __builtin_fmaxf(__builtin_fmaxf(mj, sc[j][r]), sc[j][r + 1]);
            m = (j > 0 && j < nbad) ? m : mj;
            PIN(m);
        }
        SCHED0();
    }
    auto rr = __builtin_amdgcn_permlane32_swap(__float_as_uint(m), __float_as_uint(m), false, false);
    return __builtin_fmaxf(__uint_as_float(rr[0]), __uint_as_float(rr[1]));
}
__device__ __forceinline__ bf16x8 v_read(LAS char* vp, int j, int s) {
    const bf16x4 lo = __builtin_amdgcn_ds_read_tr16_b64_v4bf16((LAS bf16x4*)(vp + j * 2048 + s * 1024));
    const bf16x4 hi = __builtin_amdgcn_ds_read_tr16_b64_v4bf16((LAS bf16x4*)(vp + j * 2048 + s * 1024 + 512));
    return __builtin_shufflevector(lo, hi, 0, 1, 2, 3, 4, 5, 6, 7);
}
__device__ __forceinline__ void attn_pv(LAS char* buf, int nbad, int wid, int lane, unsigned up, f32x16 (&sc)[8], float m, f32x16& o, float& lsum) {
    const int hh = lane >> 5;
    LAS char* vp = buf + (wid * 32 + 4 * hh + ((lane & 15) >> 2)) * 64 + ((lane >> 4) & 1) * 32 + (lane & 3) * 8;
    u32x4 one4 = {0x3f803f80u, 0x3f803f80u, 0x3f803f80u, 0x3f803f80u};
    PIN(one4);
    const bf16x8 ones = __builtin_bit_cast(bf16x8, one4);
    const float h0 = rbf(-m), h1 = rbf(-m - h0);
    const unsigned w0 = hh ? 0u : pack2(h0, h1), w1 = hh ? 0u : pack2((-m - h0) - h1, 0.f);
    auto minus_m = [&](int j) {
        const u32x4 bm = {(j > 0 && j < nbad) ? 0xff80u : w0, w1, 0u, 0u};
        sc[j] = MFMA32(ones, __builtin_bit_cast(bf16x8, bm), sc[j]);
        PIN(sc[j]);
    };
    minus_m(0); minus_m(1);
    f32x16 l = {};
    bf16x8 vf[2], v8[2];
    u32x4 pw[2], p8[2], pl[2];
    SCHED0();
#pragma unroll
    for (int j = 0; j < 8; ++j) {
        bf16x8 vn[2] = {v_read(vp, j, 0), v_read(vp, j, 1)};
        if (j == 0) { v8[0] = v_read(vp, 8, 0); v8[1] = v_read(vp, 8, 1); }
        SCHED0();
#pragma unroll
        for (int g = 0; g < 4; ++g) {
#pragma unroll
            for (int r = 4 * g; r < 4 * g + 4; ++r) sc[j][r] = __builtin_amdgcn_exp2f(sc[j][r]);
            PIN(sc[j]);
            SCHED0();
            if (j > 0) {
                if (g & 1) { l = MFMA32(ones, __builtin_bit_cast(bf16x8, j == 1 ? pl[g >> 1] : pw[g >> 1]), l); PIN(l); }
                else { o = MFMA32(vf[g >> 1], __builtin_bit_cast(bf16x8, pw[g >> 1]), o); PIN(o); }
            }
            if (j == 1 && (g & 1)) { o = MFMA32(v8[g >> 1], __builtin_bit_cast(bf16x8, p8[g >> 1]), o); PIN(o); }
            SCHED0();
        }
#pragma unroll
        for (int i = 0; i < 8; ++i) pw[i >> 2][i & 3] = pack2(sc[j][2 * i], sc[j][2 * i + 1]);
        if (j == 0) {
            pl[0] = pw[0]; pl[1] = pw[1];
#pragma unroll
            for (int i = 0; i < 8; ++i) {
                const int kr = ((2 * i) & 3) + 8 * (i >> 1);
                const unsigned mk = __builtin_amdgcn_bitop3_b32(0xffffu, (unsigned)__builtin_amdgcn_sbfe((int)up, kr, 1), (unsigned)__builtin_amdgcn_sbfe((int)up, kr + 1, 1), 0xca);
                p8[i >> 2][i & 3] = pw[i >> 2][i & 3] & ~mk; pw[i >> 2][i & 3] &= mk;
            }
        }
        vf[0] = vn[0]; vf[1] = vn[1];
        PIN(pw[0]); PIN(pw[1]);
        SCHED0();
        if (j + 2 < 8) minus_m(j + 2);
        SCHED0();
    }
    l = MFMA32(ones, __builtin_bit_cast(bf16x8, pw[0]), l);
    l = MFMA32(ones, __builtin_bit_cast(bf16x8, pw[1]), l);
    o = MFMA32(vf[0], __builtin_bit_cast(bf16x8, pw[0]), o);
    o = MFMA32(vf[1], __builtin_bit_cast(bf16x8, pw[1]), o);
    lsum = l[0];
}
__device__ __forceinline__ void attn_out(const Params& P, int hd, int qblk, int lane, float l, const f32x16& oacc) {
    const int r32 = lane & 31, hh = lane >> 5;
    const float inv = __builtin_amdgcn_rcpf(l);
    unsigned w[8];
#pragma unroll
    for (int g = 0; g < 4; ++g) { w[2 * g] = pack2(oacc[4 * g] * inv, oacc[4 * g + 1] * inv); w[2 * g + 1] = pack2(oacc[4 * g + 2] * inv, oacc[4 * g + 3] * inv); }
    const __amdgpu_buffer_rsrc_t rs = rsrc(P.o(), (unsigned)S_ * D_ * 2);
    const unsigned off = (unsigned)((hd * (S_ / 16) + qblk * 2 + (r32 >> 4)) * 1024 + (hh * 16 + (r32 & 15)) * 16);
#pragma unroll
    for (int gp = 0; gp < 2; ++gp) {
        auto a = __builtin_amdgcn_permlane32_swap(w[4 * gp], w[4 * gp + 2], false, false);
        auto b = __builtin_amdgcn_permlane32_swap(w[4 * gp + 1], w[4 * gp + 3], false, false);
        __builtin_amdgcn_raw_buffer_store_b128((u32x4){a[0], b[0], a[1], b[1]}, rs, off + gp * 512, 0, SC1);
    }
}

extern "C" __global__ __launch_bounds__(512, 2) void k_main(const Params P) {
    extern __shared__ __attribute__((aligned(16))) char smem[];
    LAS char* lds = (LAS char*)smem;
    const int tid = threadIdx.x, lane = tid & 63, wid = __builtin_amdgcn_readfirstlane(tid >> 6);
    const int grp = blockIdx.x & 7, idx = blockIdx.x >> 3;
    const bool loader = wid >= 4;
    const int lw = wid & 3, wm = (wid >> 1) & 1, wn = wid & 1;
    const unsigned ring0 = (unsigned)(uintptr_t)lds, ringl = ring0 + lw * PIECE;
    const int i16 = idx & 15;
    const bool isM = idx < 16;
    const int fm = (grp >> 1) * 4 + (isM ? i16 & 3 : i16 >> 2), fn = (grp & 1) * 4 + (isM ? i16 >> 2 : i16 & 3);
    const int um = (grp & 1) * 4 + (i16 >> 2), un = (grp >> 1) * 4 + (i16 & 3);
    const int hd = (grp & 1) * 8 + (i16 & 7), qb = (grp >> 1) * 2 + (i16 >> 3);
    if (wid == 0) {
        u64 pc; unsigned rel, len;
        asm volatile("s_getpc_b64 %0\n1:\n\ts_mov_b32 %1, 1b-k_main\n\ts_mov_b32 %2, 2f-k_main\n\t.subsection 1\n2:\n\t.subsection 0" : "=s"(pc), "=s"(rel), "=s"(len));
        const unsigned off = (unsigned)((idx + 32 * (lane & 15)) * 128);
        dma4((const char*)(pc - rel), off < len ? off : 0u, ring0 + LDS_SINK);
    }
    u64 tag;
    asm volatile("global_load_dwordx2 %0, %1, off sc1" : "=v"(tag) : "v"((GAS u64*)P.flags() + FL_E) : "memory");

    if (tid < 8) *(LAS unsigned*)(lds + (isM ? LDS_MC : LDS_CTL) + tid * 4) = 0u;
    if (isM) {
        constexpr int NK = S_ / 64, SB = SB_M;
        const int pg = (grp >> 1) + 4 * (grp & 1);
        const int tm = um, tn = un, mi = pg * 16 + i16;
        const char* gm = (const char*)P.h() + tm * 256; const char* gn = (const char*)P.w1 + tn * 448;
        const int cls = (tm + tn) & 3;
        auto kof = [&](int u) { const int p0 = u - ((u - cls) & 3); return __builtin_amdgcn_readfirstlane((((p0 == 1 || p0 == 2) ? u : korder(u, cls, NK)) + 4 * pg) & (NK - 1)); };
        Loader<4, 7> L;
        {
            const int row = mi * 16 + wid * 2;
            const u32x4* px = (const u32x4*)(P.x + (size_t)row * D_);
            u32x4 a0 = px[lane], b0 = px[64 + lane], a1 = px[128 + lane], b1 = px[192 + lane];
            SCHED0();
            if (loader) {
                const unsigned c0 = (tn & 1) * 64;
                L.init_m<TR>(lw, lane, D_ * 2);
                L.init_n<TR>(lw, lane, F_ * 2, c0, c0 + 128, c0 + 256, (tn & 1) ? 0 : 384);
            }
            asm volatile("" : "+v"(a0), "+v"(b0), "+v"(a1), "+v"(b1));
            if (loader) L.issue_n<false>(gn + (size_t)kof(0) * (64 * F_ * 2), ringl);
            const __amdgpu_buffer_rsrc_t rh = rsrc(P.h(), (unsigned)S_ * D_ * 2);
            u32x4 oa0, ob0, oa1, ob1;
            const f32x4 s0 = ln_row(a0, b0, oa0, ob0), s1 = ln_row(a1, b1, oa1, ob1);
            __builtin_amdgcn_raw_buffer_store_b128(oa0, rh, (unsigned)(row * 2048 + lane * 16), 0, SC1);
            __builtin_amdgcn_raw_buffer_store_b128(ob0, rh, (unsigned)(row * 2048 + 1024 + lane * 16), 0, SC1);
            __builtin_amdgcn_raw_buffer_store_b128(oa1, rh, (unsigned)(row * 2048 + 2048 + lane * 16), 0, SC1);
            __builtin_amdgcn_raw_buffer_store_b128(ob1, rh, (unsigned)(row * 2048 + 3072 + lane * 16), 0, SC1);
            if (lane < 2) __builtin_amdgcn_raw_buffer_store_b128(__builtin_bit_cast(u32x4, lane ? s1 : s0), rsrc(P.stat(), S_ * 16), (unsigned)((row + lane) * 16), 0, SC1);
            SCHED0();
        }
        if (loader) {
            WAIT_VM(0);
            BARRIER();
            if (tid == 256 + 64) set_flag(P.flags(), FL_L + mi, tag);
#pragma unroll
            for (int u = 1; u < 3; ++u) L.issue_n<false>(gn + (size_t)kof(u) * (64 * F_ * 2), ringl + u * SB);
            if (lw == 0) poll_flags(P.flags(), lane < 16 ? FL_L + pg * 16 + lane : -1, tag);
            BARRIER();
#pragma unroll
            for (int u = 0; u < 3; ++u) L.issue_m<true>(gm + (size_t)kof(u) * (64 * D_ * 2), ringl + u * SB);
            WAIT_VM(8); BARRIER();
            WAIT_VM(4);
            int slot = 0, t = 0;
            auto issue = [&]() {
                const int k = kof(t + 3);
                L.issue_m<true>(gm + (size_t)k * (64 * D_ * 2), ringl + slot * SB);
                L.issue_n<false>(gn + (size_t)k * (64 * F_ * 2), ringl + slot * SB);
                slot = (slot == 2) ? 0 : slot + 1;
            };
            if (lw == 0) poll_flags(P.flags(), lane < 16 ? FL_L + ((pg + 1) & 7) * 16 + lane : -1, tag);
            BARRIER(); issue(); ++t;
            WAIT_VM(L.NPW); BARRIER(); issue(); ++t;
            WAIT_VM(L.NPW);
            if (lw == 0) { poll_flags(P.flags(), FL_L + lane, tag); poll_flags(P.flags(), FL_L + 64 + lane, tag); }
            BARRIER(); issue(); ++t;
#pragma unroll 1
            for (; t < NK - 3; ++t) {
                const int k = (korder(t + 3, cls, NK) + 4 * pg) & (NK - 1);
                L.turn<true, false, 11>(gm + (size_t)k * (64 * D_ * 2), gn + (size_t)k * (64 * F_ * 2), ringl + slot * SB);
                slot = (slot == 2) ? 0 : slot + 1;
            }
            {
                const int f = ((i16 * 4 + lw) * 7 + (lane < 56 ? lane >> 3 : 6)) * 2 + (grp & 1) + 896 * (grp >> 1);
                dma4((const char*)P.w2 + (grp >> 1) * 1024, (unsigned)(f * (S_ * 2) + (lane & 7) * 128), ring0 + LDS_SINK);
            }
            WAIT_VM(L.NPW + 1); BARRIER();
            WAIT_VM(1); BARRIER();
            BARRIER();
#pragma unroll
            for (int i = 0; i < 8; ++i)
                dma16<false, 0>((const char*)P.x + ((size_t)(fm * 128 + i * 16) * D_ + fn * 128 + lw * 32) * 2, (unsigned)((lane >> 2) * (D_ * 2) + (lane & 3) * 16), ring0 + LDS_X + (lw * 8 + i) * 1024);
            dma4((const char*)P.b2 + fm * 256, (unsigned)(lane * 4), ring0 + LDS_X + 32768);
        } else {
            const int fr = lane & 15, fq = lane >> 4;
            const int fb = tn * 224 + ((tn & 1) ? 32 : 0) + wn * 32, fh = tn * 224 + ((tn & 1) ? 0 : 192) + wn * 16;
            WAIT_VM(0);
            WAIT_LGKM0();
            BARRIER();
            f32x4 acc[4][7];
#pragma unroll
            for (int j = 0; j < 7; ++j) {
                const u32x2 bb = *(const u32x2*)(P.b1 + (j < 6 ? fb + (j >> 1) * 64 + (j & 1) * 16 : fh) + fq * 4);
                const f32x4 bv = {lo16(bb[0]), hi16(bb[0]), lo16(bb[1]), hi16(bb[1])};
#pragma unroll
                for (int i = 0; i < 4; ++i) acc[i][j] = bv;
            }
            Reader<TR, 128, TR, 224, 4, 7> rd; rd.init(ring0, lane, wm, wn);
            Regs<4, 7> R0;
            int slot = 0;
            BARRIER();
            __builtin_amdgcn_s_waitcnt(0x0070);
            BARRIER();
            rd.read(R0, 0);
            for (int t = 0; t < NK / 4; ++t) {
                stage_step<3, SB>(slot, rd, rd, R0, acc); stage_step<3, SB>(slot, rd, rd, R0, acc);
                stage_step<3, SB>(slot, rd, rd, R0, acc); stage_step<3, SB>(slot, rd, rd, R0, acc);
            }
            const __amdgpu_buffer_rsrc_t rs = rsrc(P.U(), (unsigned)D_ * F_ * 2);
            const unsigned df = (unsigned)(tm * 8 + wm * 4);
            const unsigned ob = (unsigned)((fb >> 5) * 64 + df) * 1024u + (unsigned)((((fq & 1) * 2 + (fq >> 1)) * 16 + fr) * 16);
            const unsigned oh = (unsigned)((fh >> 5) * 64 + df + (fq & 1)) * 1024u + (unsigned)(((wn * 2 + (fq >> 1)) * 16 + fr) * 16);
            LAS char* ul = lds + 128 * 132 + (wn * 8 + wm * 4) * PIECE + (((fq & 1) * 2 + (fq >> 1)) * 16 + fr) * 16;
            unsigned pk[4][7][2];
#pragma unroll
            for (int i = 0; i < 4; ++i) {
#pragma unroll
                for (int j = 0; j < 7; ++j) {
                    const f32x2 a = gelu2((f32x2){acc[i][j][0], acc[i][j][1]}), b = gelu2((f32x2){acc[i][j][2], acc[i][j][3]});
                    pk[i][j][0] = pack2(a.x, a.y); pk[i][j][1] = pack2(b.x, b.y);
                }
#pragma unroll
                for (int jp = 0; jp < 3; ++jp) {
                    const u32x4 w = pair16(pk[i][2 * jp][0], pk[i][2 * jp][1], pk[i][2 * jp + 1][0], pk[i][2 * jp + 1][1]);
                    __builtin_amdgcn_raw_buffer_store_b128(w, rs, ob + i * 1024 + jp * 2 * 65536, 0, SC1);
                    *(LAS u32x4*)(ul + jp * SB_F + i * PIECE) = w;
                }
                if (i & 1)
                    __builtin_amdgcn_raw_buffer_store_b128(pair16(pk[i - 1][6][0], pk[i - 1][6][1], pk[i][6][0], pk[i][6][1]), rs, oh + (i - 1) * 1024, 0, SC1);
                SCHED0();
            }
            WAIT_VM(0);
            if (lane == 0 && __hip_atomic_fetch_add((LAS unsigned*)(lds + LDS_MC), 1u, __ATOMIC_RELAXED, __HIP_MEMORY_SCOPE_WORKGROUP) == 3u)
                set_flag(P.flags(), FL_U + um * 32 + un, tag);
        }
    } else {
        constexpr int NK = D_ / 64, SB = SB_Q;
        const int tm = fm;
        if (loader) {
            Loader<4, 6> L; L.init_m<ROW>(lw, lane, D_ * 2);
            L.init_n<TR>(lw, lane, 3 * D_ * 2, qkv_head(fn, 0) * 64, qkv_head(fn, 1) * 64, qkv_head(fn, 2) * 64);
            const char* gm = (const char*)P.x + (size_t)tm * 128 * D_ * 2; const char* gn = (const char*)P.wqkv;
            const char* gh2 = (const char*)P.h() + (size_t)tm * 128 * D_ * 2;
            int slot = 0;
            auto issue = [&](int u) {
                const int k = (korder(u & (NK - 1), (tm + fn) & 3, NK) + 4 * (grp >> 1)) & (NK - 1);
                L.issue_m<false>(gm + k * 128, ringl + slot * SB);
                L.issue_n<false>(gn + (size_t)k * (64 * 3 * D_ * 2) + (u >= NK ? 16 * 64 : 0), ringl + slot * SB);
                slot = (slot == 2) ? 0 : slot + 1;
            };
            issue(0); issue(1);
            WAIT_VM(L.NPW); BARRIER();
            issue(2);
            int t = 0;
            WAIT_VM(L.NPW); BARRIER(); issue(3); ++t;
#pragma unroll 1
            for (int seg = 0; seg < 2; ++seg) {
                const int end = seg == 0 ? 12 : 2 * NK - 3;
#pragma unroll 1
                for (; t < end; ++t) {
                    const int u = t + 3, k = (korder(u & (NK - 1), (tm + fn) & 3, NK) + 4 * (grp >> 1)) & (NK - 1);
                    L.turn<true, false, 10>((u >= NK ? gh2 : gm) + k * 128, gn + (size_t)k * (64 * 3 * D_ * 2) + (u & NK) * 64, ringl + slot * SB);
                    slot = (slot == 2) ? 0 : slot + 1;
                }
                if (seg == 0 && lw == 0) {
                    poll_flags(P.flags(), lane < 8 ? FL_L + tm * 8 + lane : -1, tag);
                    dma16<true, LDS_QX>((const char*)P.stat() + tm * 2048, (unsigned)(lane * 16), ring0);
                    dma16<true, LDS_QX + 1024>((const char*)P.stat() + tm * 2048 + 1024, (unsigned)(lane * 16), ring0);
                }
            }
            poll_flags(P.flags(), attn_dep(hd, qb, lane), tag);
            WAIT_VM(0);
            BARRIER();
            attn_load<1>(P, ring0 + LDS_AK, hd, qb, lw, lane);
            attn_load<2>(P, ring0 + LDS_AV, hd, qb, lw, lane);
            BARRIER();
            attn_load<0>(P, ring0 + LDS_AQ, hd, qb, lw, lane);
            BARRIER();
        } else {
            const int fr = lane & 15, fq = lane >> 4;
            if (wid == 1) {
                if (TLBD) __builtin_amdgcn_s_sleep(TLBD);
                const int pgx = i16 * 64 + lane;
                const char* b; int o;
                if (pgx < 224) { b = (const char*)P.w1; o = pgx; } else if (pgx < 448) { b = (const char*)P.w2; o = pgx - 224; }
                else if (pgx < 544) { b = (const char*)P.wqkv; o = pgx - 448; } else if (pgx < 576) { b = (const char*)P.wo; o = pgx - 544; }
                else if (pgx < 640) { b = (const char*)P.x; o = pgx - 576; } else if (pgx < 704) { b = (const char*)P.out; o = pgx - 640; }
                else { b = P.ws; o = pgx - 704 < 306 ? pgx - 704 : 0; }
                asm volatile("s_mov_b32 m0, %1\n\ts_nop 0\n\tglobal_load_lds_dword %0, off sc1" ::"v"(b + (size_t)o * 65536), "s"(ring0 + LDS_SINK) : "memory");
            }
            Reader<ROW, 128, TR, 192, 4, 6> rd; rd.init(ring0, lane, wm, wn);
            rd.n.b = rd.n.a + 32 - wm * 32; rd.n.a += wm * 32;
            Regs<4, 6> R0;
            int slot = 0;
            WAIT_LGKM0();
            BARRIER();
            rd.read(R0, 0);
            const __amdgpu_buffer_rsrc_t rs = rsrc(P.qkv(), (unsigned)S_ * 3 * D_ * 2);
            const int s0 = tm * 128 + wm * 64;
            const unsigned so = (unsigned)(((s0 + fr) * 32 + ((fq & 1) ^ wm) * 16 + (fq >> 1) * 8) * 2);
            constexpr float QS = 0.17677669529663687f * 1.4426950408889634f;
            {
                f32x4 acc[5][6] = {};
                for (int t = 0; t < NK / 4; ++t) {
                    stage_step<3, SB>(slot, rd, rd, R0, acc); stage_step<3, SB>(slot, rd, rd, R0, acc);
                    stage_step<3, SB>(slot, rd, rd, R0, acc); stage_step<3, SB>(slot, rd, rd, R0, acc);
                }
                LAS char* ex = lds + LDS_QX + 2048;
                if (fr == 0) {
#pragma unroll
                    for (int jp = 0; jp < 3; ++jp) *(LAS f32x4*)(ex + wid * 256 + jp * 64 + fq * 16) = acc[4][2 * jp];
                }
                WAIT_LGKM0();
                CFENCE();
                __hip_atomic_store((LAS unsigned*)(lds + LDS_CTL + wid * 4), 1u, __ATOMIC_RELAXED, __HIP_MEMORY_SCOPE_WORKGROUP);
                f32x2 st[4];
#pragma unroll
                for (int i = 0; i < 4; ++i) st[i] = *(LAS f32x2*)(lds + LDS_QX + (wm * 64 + i * 16 + fr) * 16);
                while (__builtin_amdgcn_readfirstlane(__hip_atomic_load((LAS unsigned*)(lds + LDS_CTL + (wid ^ 2) * 4), __ATOMIC_RELAXED, __HIP_MEMORY_SCOPE_WORKGROUP)) == 0u) {}
                CFENCE();
#pragma unroll
                for (int jp = 0; jp < 3; ++jp) {
                    const f32x4 ca = acc[4][2 * jp], cb = *(LAS f32x4*)(ex + (wid ^ 2) * 256 + jp * 64 + fq * 16);
#pragma unroll
                    for (int i = 0; i < 4; ++i) {
                        const float r = st[i].y * (jp == 0 ? QS : 1.0f), mr = -st[i].x * r;
                        const f32x4 a = acc[i][2 * jp] * r + ca * mr, b = acc[i][2 * jp + 1] * r + cb * mr;
                        __builtin_amdgcn_raw_buffer_store_b128(pair16(pack2(a[0], a[1]), pack2(a[2], a[3]), pack2(b[0], b[1]), pack2(b[2], b[3])), rs,
                                                               so + (unsigned)((qkv_head(fn, jp) + wn) * (S_ * 64) + i * 1024), 0, SC1);
                    }
                }
            }
            {
                f32x4 acc[4][6] = {};
                for (int t = 0; t < NK / 4; ++t) {
                    if (__builtin_expect(t == 1 || t == 2, 0)) {
                        if (t == 1) WAIT_VM(0);
                        else if (tid == 0) set_flag(P.flags(), FL_Q + tm * 32 + fn, tag);
                    }
                    stage_step<3, SB>(slot, rd, rd, R0, acc); stage_step<3, SB>(slot, rd, rd, R0, acc);
                    stage_step<3, SB>(slot, rd, rd, R0, acc); stage_step<3, SB>(slot, rd, rd, R0, acc);
                }
#pragma unroll
                for (int i = 0; i < 4; ++i) {
                    const float sd = *(LAS float*)(lds + LDS_QX + (wm * 64 + i * 16 + fr) * 16 + 8);
#pragma unroll
                    for (int jp = 0; jp < 3; ++jp) {
                        const float r = sd * (jp == 0 ? QS : 1.0f);
                        const f32x4 a = acc[i][2 * jp] * r, b = acc[i][2 * jp + 1] * r;
                        __builtin_amdgcn_raw_buffer_store_b128(pair16(pack2(a[0], a[1]), pack2(a[2], a[3]), pack2(b[0], b[1]), pack2(b[2], b[3])), rs,
                                                               so + (unsigned)((qkv_head(fn, jp) + 16 + wn) * (S_ * 64) + i * 1024), 0, SC1);
                    }
                }
            }
        }
    }

    constexpr int SB = SB_F, NS = NS_F;
    constexpr size_t OS = 2 * (S_ / 16) * 1024, WOS = 64 * D_ * 2, W2S = 64 * S_ * 2, US = 2 * 65536;
    const int tile = fm * 8 + fn, cls = (fm + fn) & 3, nf = isM ? NF_M : NF_Q;
    const int uh = ((fm >> 1) & 1) ^ (isM ? 0 : 1);
    auto fof = [&](int i) {
        int r = korder(i - 7, cls, nf - 7) + (grp & 1) * ((nf - 7) >> 1);
        r = r >= nf - 7 ? r - (nf - 7) : r;
        const int im = (fm & 1) ? (i < 3 ? i + 4 : i - 3) : i;
        const int j = i < 7 ? 7 * uh + (isM ? im : korder(i, cls, 7)) : (isM ? 14 + NA : 14) + r, k = 14 * (grp >> 1) + j;
        return __builtin_amdgcn_readfirstlane(k >= 56 ? k - 56 : k);
    };
    const char* g2 = (const char*)P.w2 + fm * 256;
    Loader<4, 4> L;
    if (loader) L.init_m<TR>(lw, lane, S_ * 2);
    auto preload_w2 = [&]() {
        int k0 = fof(0), k1 = fof(1);
        asm volatile("" : "+s"(k0), "+s"(k1));
        L.issue_m<false>(g2 + k0 * W2S, ringl); L.issue_m<false>(g2 + k1 * W2S, ringl + SB);
    };
    if (isM) {
        if (loader) { preload_w2(); L.issue_m<false>(g2 + fof(2) * W2S, ringl + 2 * SB); WAIT_VM(0); }
        WAIT_LGKM0();
        BARRIER();
    } else {
        if (loader) { WAIT_VM(0); __builtin_amdgcn_s_setprio(1); }
        WAIT_LGKM0();
        BARRIER();
#pragma unroll 1
        for (int it = 0; it < 2; ++it) {
            f32x16 sc[8];
            int ln = lane;
            asm volatile("" : "+v"(ln));
            const int dg = (ln & 31) - 4 * (ln >> 5);
            const unsigned up = dg < 0 ? ~0u : ~1u << dg;
            int nbad = qb == 0 ? 8 - wid : 0;
            asm volatile("" : "+s"(nbad));
            SCHED0();
            const float m = attn_scores(lds + (it ? LDS_BK : LDS_AK), lds + LDS_AQ, nbad, wid, ln, up, sc);
            SCHED0();
            WAIT_VM(0);
            BARRIER();
            if (tid == 0) set_flag(P.flags(), it == 0 ? FL_Q + fm * 32 + fn + 8 : FL_O + qb * 32 + hd, tag);
            if (it == 1 && loader) preload_w2();
            f32x16 oacc = {};
            float lsum;
            attn_pv(lds + (it ? LDS_BV : LDS_AV), nbad, wid, ln, up, sc, m, oacc, lsum);
            attn_out(P, hd + 16 * it, qb * 8 + wid, ln, lsum, oacc);
            if (it == 0) {
                if (loader) {
                    __builtin_amdgcn_s_setprio(0);
                    int h2 = hd + 16, q2 = qb, w2 = lw;
                    asm volatile("" : "+s"(h2), "+s"(q2), "+s"(w2));
                    poll_flags(P.flags(), attn_dep(h2, q2, ln), tag);
                    attn_load<0>(P, ring0 + LDS_AQ, h2, q2, w2, ln);
                    attn_load<1>(P, ring0 + LDS_BK, h2, q2, w2, ln);
                    attn_load<2>(P, ring0 + LDS_BV, h2, q2, w2, ln);
                    WAIT_VM(8);
                    __builtin_amdgcn_s_setprio(1);
                }
                BARRIER();
            }
        }
        WAIT_LGKM0();
    }
    const int own = isM ? 0 : 1;
    if (loader) {
        const char* go = (const char*)P.o() + fm * 8192 + own * 4096;
        const char* gw = (const char*)P.wo + fn * 256;
        const char* gu = (const char*)P.U() + fn * 8192;
        __builtin_amdgcn_s_setprio(0);
        L.init_n<FRAG>(lw, lane, US / 2);
        auto issue_f = [&](int u) {
            const int k = fof(u);
            L.issue_m<false>(g2 + k * W2S, ringl + (u & (NS - 1)) * SB); L.issue_n<true>(gu + k * US, ringl + (u & (NS - 1)) * SB);
        };
        const int ulo = isM ? ((14 + NA) * 2) / 7 : 4, uhi = isM ? 15 : ((14 + NA) * 2 - 1) / 7;
        const int udep = (lane >= ulo && lane <= uhi) ? FL_U + fn * 32 + (((grp >> 1) * 4 + lane) & 15) : -1;
        const int odep = lane < 32 ? FL_O + (fm >> 1) * 32 + lane : lane < 48 ? FL_U + fn * 32 + lane - 32 : -1, pdep = lane == 0 ? FL_P + tile * 32 + own : -1;
        u64 pk;
        if (isM) { BARRIER(); BARRIER(); }
        poll_flags(P.flags(), lane < 2 ? FL_U + fn * 32 + (grp >> 1) * 4 + 2 * uh + lane : -1, tag);
        if (isM) { issue_f(3); issue_f(4); }
        else {
#pragma unroll
            for (int u = 0; u < 2; ++u) L.issue_n<true>(gu + fof(u) * US, ringl + u * SB);
            WAIT_VM(4); BARRIER();
            issue_f(2); issue_f(3);
            WAIT_VM(16); BARRIER(); issue_f(4);
            WAIT_VM(16); BARRIER();
        }
        peek_issue(pk, P.flags(), udep); issue_f(5);
        WAIT_VM(16); BARRIER(); issue_f(6);
        WAIT_VM(16);
        int nxt = 7;
        if (!peek_ok(pk, udep, tag)) {
            bool uready = false;
#pragma unroll 1
            for (int t = 3; t < 7; ++t) {
                if (t == 6 && !uready) { poll_flags(P.flags(), udep, tag); uready = true; for (; nxt < 10; ++nxt) issue_f(nxt); }
                if (nxt - (t + 2) >= 2) WAIT_VM(16); else WAIT_VM(0);
                BARRIER();
                if (!uready) uready = flags_set(P.flags(), udep, tag);
                if (uready) for (; nxt <= t + NS; ++nxt) issue_f(nxt);
            }
        }
#pragma unroll 1
        for (; nxt < nf - 2; ++nxt) {
            int r = korder(nxt - 7, cls, nf - 7) + (grp & 1) * ((nf - 7) >> 1);
            r = r >= nf - 7 ? r - (nf - 7) : r;
            int k = 14 * (grp >> 1) + (isM ? 14 + NA : 14) + r;
            k = k >= 56 ? k - 56 : k;
            L.turn<false, true, 16>(g2 + k * W2S, gu + k * US, ringl + (nxt & (NS - 1)) * SB);
        }
        const unsigned xo = (unsigned)(((fm * 128 + (lane >> 2)) * D_ + fn * 128 + lw * 32 + (lane & 3) * 8) * 2);
        constexpr unsigned ROWS16 = 16 * D_ * 2;
        WAIT_VM(16); BARRIER(); peek_issue(pk, P.flags(), odep);
        issue_f(nf - 2);
        WAIT_VM(16); BARRIER(); issue_f(nf - 1);
        L.init_n<TR>(lw, lane, D_ * 2);
        const unsigned oo = (unsigned)(lw * 1024 + lane * 16);
        const unsigned oo2 = oo + (unsigned)(OS / 2);
        const char *po, *pw; unsigned pd;
        auto addr_w = [&](int i) {
            const int k = (korder(i, cls, KW) + 4 * (grp >> 1)) & (KW - 1);
            po = go + k * OS; pw = gw + k * WOS; pd = ringl + ((nf + i) & (NS - 1)) * SB;
            asm volatile("" : "+s"(po), "+s"(pw), "+s"(pd));
        };
        auto issue_w = [&]() {
            asm volatile("s_add_u32 m0, %2, %3\n\ts_nop 0\n\tglobal_load_lds_dwordx4 %4, %0 sc1\n\ts_addk_i32 m0, 8448\n\ts_nop 0\n\tglobal_load_lds_dwordx4 %5, %0 sc1\n\t"
                         "s_add_u32 m0, %2, 16896\n\ts_nop 0\n\tglobal_load_lds_dwordx4 %6, %1\n\ts_addk_i32 m0, 4224\n\ts_nop 0\n\tglobal_load_lds_dwordx4 %7, %1\n\t"
                         "s_addk_i32 m0, 4224\n\ts_nop 0\n\tglobal_load_lds_dwordx4 %8, %1\n\ts_addk_i32 m0, 4224\n\ts_nop 0\n\tglobal_load_lds_dwordx4 %9, %1"
                         ::"s"(po), "s"(pw), "s"(pd), "s"(own * 4 * PIECE), "v"(oo), "v"(oo2), "v"(L.on[0]), "v"(L.on[1]), "v"(L.on[2]), "v"(L.on[3]) : "scc", "memory");
        };
#define TURN_W(i, w) do { addr_w(i); WAIT_VM(w); BARRIER(); issue_w(); } while (0)
        addr_w(0);
        WAIT_VM(16);
        if (!peek_ok(pk, odep, tag)) poll_flags(P.flags(), odep, tag);
        BARRIER(); issue_w();
        TURN_W(1, 14); TURN_W(2, 12); TURN_W(3, 12);
        TURN_W(4, 12);
        {
            u32x4 t0, t1, t2, t3;
            asm volatile("ds_read_b128 %0, %4\n\tds_read_b128 %1, %4 offset:1024\n\tds_read_b128 %2, %4 offset:2048\n\tds_read_b128 %3, %4 offset:3072\n\ts_waitcnt lgkmcnt(0)"
                         : "=&v"(t0), "=&v"(t1), "=&v"(t2), "=&v"(t3) : "v"(ring0 + LDS_EX + lw * 4096 + lane * 16) : "memory");
            const __amdgpu_buffer_rsrc_t ro = rsrc(P.out, (unsigned)S_ * D_ * 2);
            const unsigned so = xo + (1 - own) * 4 * ROWS16;
            __builtin_amdgcn_raw_buffer_store_b128(t0, ro, so, 0, SC1); __builtin_amdgcn_raw_buffer_store_b128(t1, ro, so + ROWS16, 0, SC1);
            __builtin_amdgcn_raw_buffer_store_b128(t2, ro, so + 2 * ROWS16, 0, SC1); __builtin_amdgcn_raw_buffer_store_b128(t3, ro, so + 3 * ROWS16, 0, SC1);
        }
        TURN_W(5, 12); TURN_W(6, 12);
        TURN_W(7, 0);
        if (tid == 256) set_flag(P.flags(), FL_P + tile * 32 + 1 - own, tag);
        TURN_W(8, 12); TURN_W(9, 12); TURN_W(10, 12); TURN_W(11, 12);
        TURN_W(12, 12); TURN_W(13, 12); TURN_W(14, 12);
        TURN_W(15, 12);
#undef TURN_W
        peek_issue(pk, P.flags(), pdep);
        WAIT_VM(13); BARRIER();
        WAIT_VM(7); BARRIER();
        WAIT_VM(0);
        auto load_partial = [&]() {
#pragma unroll
            for (int i = 0; i < 4; ++i) dma16<true, 0>((const char*)P.out + (own * 4 + i) * ROWS16, xo, ring0 + LDS_EX + (lw * 4 + i) * 1024);
        };
        const bool early = peek_ok(pk, pdep, tag);
        if (early) load_partial();
        BARRIER();
        BARRIER();
        if (!early) { poll_flags(P.flags(), pdep, tag); load_partial(); }
        WAIT_VM(0); BARRIER();
        if (blockIdx.x == 0 && lw == 0) {
#pragma unroll 1
            for (int i = 0; i < 4; ++i) poll_flags(P.flags(), FL_O + i * 64 + lane, tag);
            if (lane == 0) set_flag(P.flags(), FL_E, tag);
        }
    } else {
        const __amdgpu_buffer_rsrc_t ro = rsrc(P.out, (unsigned)S_ * D_ * 2);
        f32x4 acc[4][4];
        if (isM) {
            LAS char* xp = lds + LDS_X + (wn * 16 + wm * 4) * 1024 + (lane & 15) * 64 + (((lane >> 4) & 1) * 2 + (lane >> 5)) * 16;
            u32x4 xv[4][2]; unsigned bv[4];
#pragma unroll
            for (int i = 0; i < 4; ++i) {
                xv[i][0] = *(LAS u32x4*)(xp + i * 1024); xv[i][1] = *(LAS u32x4*)(xp + 8192 + i * 1024);
                bv[i] = *(LAS u16*)(lds + LDS_X + 32768 + (wm * 64 + i * 16 + (lane & 15)) * 2);
            }
            SCHED0();
#pragma unroll
            for (int i = 0; i < 4; ++i)
#pragma unroll
                for (int jp = 0; jp < 2; ++jp) {
                    auto e0 = __builtin_amdgcn_permlane16_swap(xv[i][jp][0], xv[i][jp][2], false, false);
                    auto e1 = __builtin_amdgcn_permlane16_swap(xv[i][jp][1], xv[i][jp][3], false, false);
                    acc[i][2 * jp] = (f32x4){lo16(e0[0]), hi16(e0[0]), lo16(e1[0]), hi16(e1[0])} + lo16(bv[i]);
                    acc[i][2 * jp + 1] = (f32x4){lo16(e0[1]), hi16(e0[1]), lo16(e1[1]), hi16(e1[1])} + lo16(bv[i]);
                }
            pin_acc<4, 4, 4>(acc);
        } else {
            float z = 0.f;
            asm volatile("" : "+v"(z));
#pragma unroll
            for (int i = 0; i < 4; ++i)
#pragma unroll
                for (int j = 0; j < 4; ++j) acc[i][j] = (f32x4){z, z, z, z};
        }
        pin_acc<4, 4, 4>(acc);
        Reader<TR, 128, FRAG, 128, 4, 4> rf;
        HalfReader rw;
        Regs<4, 4> R0;
        LAS char* exl = lds + LDS_EX + wn * 8192 + lane * 16;
        int slot = 0;
#define STEP(a, b) stage_step<NS, SB>(slot, a, b, R0, acc)
        rf.init(ring0, lane, wm, wn);
        WAIT_LGKM0();
        if (!isM) BARRIER();
        rf.read(R0, 0);
        for (int t = 0; t < (nf - 2) / 4; ++t) {
            if (__builtin_expect(!isM && (t == 1 || t == 2), 0)) {
                if (t == 1) WAIT_VM(0);
                else if (tid == 0) set_flag(P.flags(), FL_O + qb * 32 + hd + 16, tag);
            }
            STEP(rf, rf); STEP(rf, rf); STEP(rf, rf); STEP(rf, rf);
        }
        static_assert((NF_M & 3) == (NF_Q & 3), "NA must be odd");
#pragma unroll
        for (int t = 0; t < ((NF_M - 2) & 3); ++t) STEP(rf, rf);
        STEP(rf, rf);
        const int kq = wm != own ? 1 : 0;
        rw.init(ring0 + slot * SB, lane, own, kq, wn);
        STEP(rf, rw);
        if (kq) {
#pragma unroll
            for (int i = 0; i < 4; ++i)
#pragma unroll
                for (int jp = 0; jp < 2; ++jp) {
                    const f32x4 a = acc[i][2 * jp], b = acc[i][2 * jp + 1];
                    *(LAS u32x4*)(exl + jp * 4096 + i * 1024) = (u32x4){pack2(a[0], a[1]), pack2(a[2], a[3]), pack2(b[0], b[1]), pack2(b[2], b[3])};
                    acc[i][2 * jp] = (f32x4){0.f, 0.f, 0.f, 0.f}; acc[i][2 * jp + 1] = (f32x4){0.f, 0.f, 0.f, 0.f};
                }
        }
        pin_acc<4, 4, 4>(acc);
        Regs<4, 4> R1;
        for (int t = 0; t < 4; ++t) {
            half_step<NS, SB>(slot, rw, R0, R1, acc); half_step<NS, SB>(slot, rw, R1, R0, acc);
            half_step<NS, SB>(slot, rw, R0, R1, acc); half_step<NS, SB>(slot, rw, R1, R0, acc);
        }
#pragma unroll
        for (int i = 0; i < 2; ++i)
#pragma unroll
            for (int j = 0; j < 4; ++j) *(LAS f32x4*)(lds + (wid * 8 + i * 4 + j) * 1024 + lane * 16) = acc[2 + i][j];
        WAIT_LGKM0();
        BARRIER();
        const unsigned fo = (unsigned)(((fm * 128 + own * 64 + kq * 32 + (lane & 15)) * D_ + fn * 128 + wn * 64 + ((lane >> 4) & 1) * 16 + (lane >> 5) * 8) * 2);
#pragma unroll
        for (int i = 0; i < 2; ++i)
#pragma unroll
            for (int jp = 0; jp < 2; ++jp) {
                const u32x4 e = *(LAS u32x4*)(exl + jp * 4096 + (kq * 2 + i) * 1024);
                const f32x4 a = acc[i][2 * jp] + *(LAS f32x4*)(lds + ((wid ^ 2) * 8 + i * 4 + 2 * jp) * 1024 + lane * 16) + (f32x4){lo16(e[0]), hi16(e[0]), lo16(e[1]), hi16(e[1])};
                const f32x4 b = acc[i][2 * jp + 1] + *(LAS f32x4*)(lds + ((wid ^ 2) * 8 + i * 4 + 2 * jp + 1) * 1024 + lane * 16) + (f32x4){lo16(e[2]), hi16(e[2]), lo16(e[3]), hi16(e[3])};
                __builtin_amdgcn_raw_buffer_store_b128(pair16(pack2(a[0], a[1]), pack2(a[2], a[3]), pack2(b[0], b[1]), pack2(b[2], b[3])), ro, fo + i * (16 * D_ * 2) + jp * 64, 0, SC1);
            }
#undef STEP
    }
}

extern "C" void kernel_launch(void* const* d_in, const int* in_sizes, int n_in, void* d_out, int out_size, void* d_ws, size_t ws_size, hipStream_t stream) {
    char* ws = (char*)d_ws;
    Params P;
    P.x = (const u16*)d_in[0]; P.wqkv = (const u16*)d_in[1]; P.wo = (const u16*)d_in[2];
    P.w1 = (const u16*)d_in[4]; P.b1 = (const u16*)d_in[5]; P.w2 = (const u16*)d_in[6]; P.b2 = (const u16*)d_in[7];
    P.out = (u16*)d_out; P.ws = ws;
    (void)hipFuncSetAttribute((const void*)k_main, hipFuncAttributeMaxDynamicSharedMemorySize, LDS_BYTES);
    k_main<<<256, 512, LDS_BYTES, stream>>>(P);
}
```

```cpp
#include <hip/hip_runtime.h>
#include <hip/hip_bf16.h>
#include <stdint.h>

#define LAS __attribute__((address_space(3)))
#define GAS __attribute__((address_space(1)))
typedef __bf16 bf16x8 __attribute__((ext_vector_type(8)));
typedef __bf16 bf16x4 __attribute__((ext_vector_type(4)));
typedef __bf16 bf16x2 __attribute__((ext_vector_type(2)));
typedef float f32x2 __attribute__((ext_vector_type(2)));
typedef float f32x4 __attribute__((ext_vector_type(4)));
typedef float f32x16 __attribute__((ext_vector_type(16)));
typedef unsigned u32x4 __attribute__((ext_vector_type(4)));
typedef unsigned u32x2 __attribute__((ext_vector_type(2)));
typedef unsigned short u16;
typedef unsigned long long u64;

constexpr int S_ = 2048, D_ = 1024, F_ = 3584, NH = 32;

constexpr size_t WS_FLAGS = 0;
constexpr size_t WS_STAT  = 1 << 16;
constexpr size_t WS_QKV   = 1 << 17;
constexpr size_t WS_U     = WS_QKV + (size_t)S_ * 3 * D_ * 2;
constexpr int FL_E = 0;
constexpr int FL_L = 32;
constexpr int FL_U = 8 * 32;
constexpr int FL_Q = 16 * 32;
constexpr int FL_O = 32 * 32;
constexpr int FL_P = 40 * 32;

#define WAIT_VM(n) asm volatile("s_waitcnt vmcnt(%0)" ::"n"(n) : "memory")
#define WAIT_LGKM0() __builtin_amdgcn_s_waitcnt(0xC07F)
#define CFENCE() asm volatile("" ::: "memory")
#define BARRIER() do { CFENCE(); __builtin_amdgcn_s_barrier(); CFENCE(); } while (0)
#define SCHED0() __builtin_amdgcn_sched_barrier(0)
#define RLX_AGENT __ATOMIC_RELAXED, __HIP_MEMORY_SCOPE_AGENT
constexpr int SC1 = 16;

__device__ __forceinline__ float bf2f(u16 v) { return __uint_as_float((unsigned)v << 16); }
__device__ __forceinline__ float lo16(unsigned v) { return __uint_as_float(v << 16); }
__device__ __forceinline__ float hi16(unsigned v) { return __uint_as_float(v & 0xffff0000u); }
__device__ __forceinline__ unsigned pack2(float lo, float hi) {
    f32x2 v = {lo, hi};
    bf16x2 b = __builtin_convertvector(v, bf16x2);
    return __builtin_bit_cast(unsigned, b);
}
__device__ __forceinline__ float rbf(float x) { return __uint_as_float(pack2(x, 0.f) << 16); }
template <int CTRL, int RMASK> __device__ __forceinline__ float dpp_add(float v) {
    return v + __builtin_bit_cast(float, __builtin_amdgcn_update_dpp(0, __builtin_bit_cast(int, v), CTRL, RMASK, 0xf, false));
}
__device__ __forceinline__ float wave_sum(float v) {
    v = dpp_add<0x111, 0xf>(v); v = dpp_add<0x112, 0xf>(v); v = dpp_add<0x114, 0xf>(v); v = dpp_add<0x118, 0xf>(v);
    v = dpp_add<0x142, 0xa>(v); v = dpp_add<0x143, 0xc>(v);
    return __builtin_bit_cast(float, __builtin_amdgcn_readlane(__builtin_bit_cast(int, v), 63));
}
__device__ __forceinline__ __amdgpu_buffer_rsrc_t rsrc(const void* p, unsigned bytes) { return __builtin_amdgcn_make_buffer_rsrc((void*)p, 0, bytes, 0x00020000); }

enum { ROW = 0, TR = 1, FRAG = 2 };
constexpr int PIECE = 1056, BLK = 8 * PIECE;
template <int T, int X> struct Op {
    static constexpr int BYTES = X * 132;
    __device__ static __forceinline__ unsigned src_off(int blk, int p, int lane, unsigned ld, unsigned cb) {
        const int pos = lane >> 3;
        if (T == TR) {
            if (X == 224 && blk == 3) {
                const int ps = lane >> 2, g = (p & 1) | ((ps >> 3) << 1), r = 32 * (p >> 1) + 8 * g + 4 * ((ps >> 2) & 1) + (ps & 3);
                return (unsigned)r * ld + cb + (unsigned)((lane & 3) * 16);
            }
            const int g = (p >> 2) | (pos & 2), r = 32 * (pos >> 2) + 8 * g + 4 * (pos & 1) + (p & 3);
            return (unsigned)r * ld + cb + (unsigned)((lane & 7) * 16);
        } else if (T == ROW) {
            const int j = ((pos >> 1) & 1) | ((pos & 1) << 1) | (pos & 4);
            return (unsigned)(blk * 64 + j * 8 + p) * ld + (unsigned)((lane & 7) * 16);
        } else {
            return (unsigned)blk * ld + (unsigned)(p * 1024 + lane * 16);
        }
    }
};
template <bool SC, int IMM> __device__ __forceinline__ void dma16(const char* base, unsigned off, unsigned l) {
    if (SC) asm volatile("s_add_u32 m0, %2, %3\n\ts_nop 0\n\tglobal_load_lds_dwordx4 %0, %1 sc1" ::"v"(off), "s"(base), "s"(l), "n"(IMM) : "scc", "memory");
    else    asm volatile("s_add_u32 m0, %2, %3\n\ts_nop 0\n\tglobal_load_lds_dwordx4 %0, %1" ::"v"(off), "s"(base), "s"(l), "n"(IMM) : "scc", "memory");
}
#define DMA_FIRST(sc) "s_add_u32 m0, %1, %2\n\ts_nop 0\n\tglobal_load_lds_dwordx4 %3, %0" sc
#define DMA_NEXT(n, sc) "\n\ts_addk_i32 m0, 4224\n\ts_nop 0\n\tglobal_load_lds_dwordx4 %" #n ", %0" sc
#define DMA_G4(sc) DMA_FIRST(sc) DMA_NEXT(4, sc) DMA_NEXT(5, sc) DMA_NEXT(6, sc)
template <bool SC, int IMM, int N> __device__ __forceinline__ void dma_group(const char* base, const unsigned (&o)[N], unsigned l) {
    static_assert(4 * PIECE == 4224 && (N == 4 || ((N == 6 || N == 7) && !SC)), "dma_group");
    if constexpr (N == 4 && SC) asm volatile(DMA_G4(" sc1") ::"s"(base), "s"(l), "n"(IMM), "v"(o[0]), "v"(o[1]), "v"(o[2]), "v"(o[3]) : "scc", "memory");
    else if constexpr (N == 4) asm volatile(DMA_G4("") ::"s"(base), "s"(l), "n"(IMM), "v"(o[0]), "v"(o[1]), "v"(o[2]), "v"(o[3]) : "scc", "memory");
    else if constexpr (N == 6) asm volatile(DMA_G4("") DMA_NEXT(7, "") DMA_NEXT(8, "") ::"s"(base), "s"(l), "n"(IMM), "v"(o[0]), "v"(o[1]), "v"(o[2]), "v"(o[3]), "v"(o[4]), "v"(o[5]) : "scc", "memory");
    else asm volatile(DMA_G4("") DMA_NEXT(7, "") DMA_NEXT(8, "") DMA_NEXT(9, "") ::"s"(base), "s"(l), "n"(IMM), "v"(o[0]), "v"(o[1]), "v"(o[2]), "v"(o[3]), "v"(o[4]), "v"(o[5]), "v"(o[6]) : "scc", "memory");
}
__device__ __forceinline__ void dma4(const char* base, unsigned off, unsigned l) {
    asm volatile("s_mov_b32 m0, %2\n\ts_nop 0\n\tglobal_load_lds_dword %0, %1" ::"v"(off), "s"(base), "s"(l) : "memory");
}
__device__ __forceinline__ void dma16x4(const char* base, unsigned off, unsigned l) {
    asm volatile("s_mov_b32 m0, %2\n\ts_nop 0\n\tglobal_load_lds_dwordx4 %0, %1 sc1\n\tglobal_load_lds_dwordx4 %0, %1 offset:1024 sc1\n\t"
                 "global_load_lds_dwordx4 %0, %1 offset:2048 sc1\n\tglobal_load_lds_dwordx4 %0, %1 offset:3072 sc1" ::"v"(off), "s"(base), "s"(l) : "memory");
}

template <int T, int X> struct Addr {
    unsigned a, b;
    static constexpr int NV = (X == 224 || X == 192) ? 2 : 1;
    __device__ __forceinline__ void init(unsigned base, int lane, int w) {
        const int g = lane >> 4;
        b = 0;
        if (T == TR) {
            const int q = (lane & 15) >> 2, p = lane & 3;
            a = base + (unsigned)((q | ((g & 1) << 2)) * PIECE + (g >> 1) * 256 + p * 8 + (X == 128 ? w * BLK : w * 64));
            b = base + (unsigned)(3 * BLK + (g & 1) * PIECE + (g >> 1) * 512 + q * 64 + w * 32 + p * 8);
        } else if (T == ROW) {
            const int r = lane & 15;
            a = base + (unsigned)(w * BLK + (r & 7) * PIECE + (r >> 3) * 256 + g * 16);
        } else {
            a = base + (unsigned)(w * 4 * PIECE + lane * 16);
        }
    }
    __device__ __forceinline__ void step(unsigned d) { a += d; if (NV == 2) b += d; }
    __device__ __forceinline__ bf16x8 read(int n, int kk) const {
        if (T == TR) {
            LAS char* p; int t;
            if (X == 224 && n == 6) { p = (LAS char*)(uintptr_t)b + kk * 2 * PIECE; t = 256; }
            else if (X == 192) { p = (LAS char*)(uintptr_t)((n & 1) ? b : a) + kk * 512 + (n >> 1) * BLK; t = 128; }
            else { p = (LAS char*)(uintptr_t)a + kk * 512 + (X == 128 ? n * 32 : (n >> 1) * BLK + (n & 1) * 32); t = 128; }
            const bf16x4 lo = __builtin_amdgcn_ds_read_tr16_b64_v4bf16((LAS bf16x4*)p);
            const bf16x4 hi = __builtin_amdgcn_ds_read_tr16_b64_v4bf16((LAS bf16x4*)(p + t));
            return __builtin_shufflevector(lo, hi, 0, 1, 2, 3, 4, 5, 6, 7);
        } else if (T == ROW) {
            return *(LAS bf16x8*)((LAS char*)(uintptr_t)a + (n & 1) * 128 + (n >> 1) * 512 + kk * 64);
        } else {
            return *(LAS bf16x8*)((LAS char*)(uintptr_t)a + kk * BLK + n * PIECE);
        }
    }
};
template <int FM, int FN> struct Regs { bf16x8 m[FM], n[FN]; };
#define MFMA16(a, b, c) __builtin_amdgcn_mfma_f32_16x16x32_bf16(a, b, c, 0, 0, 0)

template <int TM, int BM, int TN, int BN, int FM, int FN> struct Reader {
    Addr<TM, BM> m; Addr<TN, BN> n;
    static constexpr int NDS = FM * (TM == TR ? 2 : 1) + FN * (TN == TR ? 2 : 1), NV = Addr<TM, BM>::NV + Addr<TN, BN>::NV;
    __device__ __forceinline__ void init(unsigned slot_base, int lane, int wm, int wn) { m.init(slot_base, lane, wm); n.init(slot_base + BM * 132, lane, wn); }
    __device__ __forceinline__ void read(Regs<FM, FN>& R, int kk) const {
#pragma unroll
        for (int i = 0; i < FM; ++i) R.m[i] = m.read(i, kk);
#pragma unroll
        for (int j = 0; j < FN; ++j) R.n[j] = n.read(j, kk);
    }
    __device__ __forceinline__ void step(unsigned d) { m.step(d); n.step(d); }
};
template <int AM, int FM, int FN> __device__ __forceinline__ void mma(f32x4 (&acc)[AM][FN], const Regs<FM, FN>& R) {
    const u32x4 one = {0x3f803f80u, 0x3f803f80u, 0x3f803f80u, 0x3f803f80u};
#pragma unroll
    for (int j = 0; j < FN; ++j) {
        if (AM > FM && !(j & 1)) acc[AM - 1][j] = MFMA16(R.n[j], __builtin_bit_cast(bf16x8, one), acc[AM - 1][j]);
#pragma unroll
        for (int ii = 0; ii < FM; ++ii) { const int i = (j & 1) ? FM - 1 - ii : ii; acc[i][j] = MFMA16(R.n[j], R.m[i], acc[i][j]); }
    }
}
template <int AM, int FM, int FN> __device__ __forceinline__ void pin_acc(f32x4 (&acc)[AM][FN]) {
#pragma unroll
    for (int j = 0; j < FN; ++j) {
        asm volatile("" : "+v"(acc[0][j]), "+v"(acc[1][j]), "+v"(acc[2][j]), "+v"(acc[3][j]));
        if (AM > FM && !(j & 1)) asm volatile("" : "+v"(acc[AM - 1][j]));
    }
}
template <int NDS, int NV, int NMF> __device__ __forceinline__ void interleave() {
#pragma unroll
    for (int i = 0; i < NMF; ++i) {
        __builtin_amdgcn_sched_group_barrier(0x8, 1, 0);
        if (i < NDS) __builtin_amdgcn_sched_group_barrier(0x80, 1, 0);
        if (i >= NMF - NV) __builtin_amdgcn_sched_group_barrier(0x2, 1, 0);
    }
}
template <int NS, int SB, class RD, class RDN, int AM, int FM, int FN>
__device__ __forceinline__ void stage_step(int& slot, RD& rd, RDN& rn, Regs<FM, FN>& R0, f32x4 (&acc)[AM][FN]) {
    constexpr int NMF = FM * FN + (AM - FM) * (FN / 2);
    Regs<FM, FN> R1;
    const unsigned d = (slot + 1 == NS) ? (unsigned)(-(NS - 1) * SB) : (unsigned)SB;
    slot = (slot + 1 == NS) ? 0 : slot + 1;
    rd.read(R1, 1);
    rn.step(d);
    mma(acc, R0);
    interleave<RD::NDS, RDN::NV, NMF>();
    SCHED0();
    pin_acc<AM, FM, FN>(acc);
    WAIT_LGKM0();
    BARRIER();
    SCHED0();
    rn.read(R0, 0);
    mma(acc, R1);
    interleave<RDN::NDS, 0, NMF>();
    SCHED0();
    pin_acc<AM, FM, FN>(acc);
    SCHED0();
}

struct HalfReader {
    unsigned m0, m1; Addr<TR, 128> n;
    static constexpr int NDS = 12, NV = 3;
    __device__ __forceinline__ void init(unsigned slot_base, int lane, int own, int kq, int wn) {
        const unsigned a = slot_base + (unsigned)(own * 4 * PIECE + kq * BLK + lane * 16);
        m0 = a + kq * 2 * PIECE; m1 = a - kq * 2 * PIECE;
        n.init(slot_base + 128 * 132 + kq * 512, lane, wn);
    }
    __device__ __forceinline__ void read(Regs<4, 4>& R, int) const {
#pragma unroll
        for (int i = 0; i < 4; ++i) R.m[i] = *(LAS bf16x8*)((LAS char*)(uintptr_t)(i < 2 ? m0 : m1) + i * PIECE);
#pragma unroll
        for (int j = 0; j < 4; ++j) R.n[j] = n.read(j, 0);
    }
    __device__ __forceinline__ void step(unsigned d) { m0 += d; m1 += d; n.step(d); }
};
template <int NS, int SB> __device__ __forceinline__ void half_step(int& slot, HalfReader& rd, Regs<4, 4>& Rc, Regs<4, 4>& Rn, f32x4 (&acc)[4][4]) {
    rd.step((slot + 1 == NS) ? (unsigned)(-(NS - 1) * SB) : (unsigned)SB);
    slot = (slot + 1 == NS) ? 0 : slot + 1;
    SCHED0();
    WAIT_LGKM0();
    BARRIER();
    SCHED0();
    rd.read(Rn, 0);
    mma(acc, Rc);
    interleave<12, 0, 16>();
    SCHED0();
    pin_acc<4, 4, 4>(acc);
    SCHED0();
}

template <int NM, int NN> struct Loader {
    static constexpr int NPW = NM + NN;
    unsigned om[NM], on[NN];
    template <int T> __device__ __forceinline__ void init_m(int lw, int lane, unsigned ld) {
#pragma unroll
        for (int i = 0; i < NM; ++i) om[i] = Op<T, NM * 32>::src_off(i >> 1, lw + 4 * (i & 1), lane, ld, (i >> 1) * 128);
    }
    template <int T> __device__ __forceinline__ void init_n(int lw, int lane, unsigned ld, unsigned c0 = 0, unsigned c1 = 128, unsigned c2 = 256, unsigned c3 = 384) {
#pragma unroll
        for (int i = 0; i < NN; ++i) on[i] = Op<T, NN * 32>::src_off(i >> 1, lw + 4 * (i & 1), lane, ld, (i >> 1) == 0 ? c0 : (i >> 1) == 1 ? c1 : (i >> 1) == 2 ? c2 : c3);
    }
    template <bool SC> __device__ __forceinline__ void issue_m(const char* gm, unsigned dst) const { dma_group<SC, 0>(gm, om, dst); }
    template <bool SC> __device__ __forceinline__ void issue_n(const char* gn, unsigned dst) const { dma_group<SC, NM * 32 * 132>(gn, on, dst); }
    template <bool SCM, bool SCN, int W> __device__ __forceinline__ void turn(const char* gm, const char* gn, unsigned dst) const {
        asm volatile("" : "+s"(gm), "+s"(gn), "+s"(dst));
        WAIT_VM(W); BARRIER();
        issue_m<SCM>(gm, dst); issue_n<SCN>(gn, dst);
    }
};
__device__ __forceinline__ int korder(int p, int c, int nk) {
    const int r = (p - c) & 3;
    const int d = (0x1003 >> (4 * r) & 15) - 1;
    return (unsigned)(p - r) < (unsigned)(nk - 2) ? p + d : p;
}
__device__ __forceinline__ f32x2 gelu2(f32x2 u) {
    const float c1 = -2.0f * 0.7978845608028654f * 1.4426950408889634f, c2 = c1 * 0.044715f;
    const f32x2 z = u * ((u * u) * c2 + c1);
    f32x2 e; e.x = __builtin_amdgcn_exp2f(z.x); e.y = __builtin_amdgcn_exp2f(z.y);
    e = e + 1.0f;
    f32x2 r; r.x = __builtin_amdgcn_rcpf(e.x); r.y = __builtin_amdgcn_rcpf(e.y);
    return u * r;
}
__device__ __forceinline__ u32x4 pair16(unsigned x0, unsigned x1, unsigned y0, unsigned y1) {
    auto a = __builtin_amdgcn_permlane16_swap(x0, y0, false, false);
    auto b = __builtin_amdgcn_permlane16_swap(x1, y1, false, false);
    return (u32x4){a[0], b[0], a[1], b[1]};
}
__device__ __forceinline__ bool flags_set(u64* flags, int idx, u64 tag) {
    const u64 v = __hip_atomic_load((GAS u64*)flags + (idx < 0 ? 0 : idx), RLX_AGENT);
    const bool ok = __builtin_amdgcn_ballot_w64(idx >= 0 && v - 1ull != tag) == 0ull;
    CFENCE();
    return ok;
}
__device__ __forceinline__ void poll_flags(u64* flags, int idx, u64 tag) {
#pragma unroll 1
    for (unsigned spins = 0; spins < (1u << 20); ++spins) {
        if (flags_set(flags, idx, tag)) break;
        __builtin_amdgcn_s_sleep(2);
    }
}
__device__ __forceinline__ void peek_issue(u64& v, u64* flags, int idx) {
    asm volatile("global_load_dwordx2 %0, %1, off sc1" : "=v"(v) : "v"((GAS u64*)flags + (idx < 0 ? 0 : idx)) : "memory");
}
__device__ __forceinline__ bool peek_ok(u64& v, int idx, u64 tag) {
    asm volatile("" : "+v"(v) :: "memory");
    return __builtin_amdgcn_ballot_w64(idx >= 0 && v - 1ull != tag) == 0ull;
}
__device__ __forceinline__ void set_flag(u64* flags, int idx, u64 tag) {
    asm volatile("" : "+v"(tag));
    __hip_atomic_store((GAS u64*)flags + idx, tag + 1ull, RLX_AGENT);
}

struct Params {
    const u16 *x, *wqkv, *wo, *w1, *b1, *w2, *b2;
    u16* out;
    char* ws;
    __device__ __forceinline__ u16* h() const { return out; }
    __device__ __forceinline__ u16* qkv() const { return (u16*)(ws + WS_QKV); }
    __device__ __forceinline__ u16* U() const { return (u16*)(ws + WS_U); }
    __device__ __forceinline__ u16* o() const { return (u16*)(ws + WS_QKV); }
    __device__ __forceinline__ float* stat() const { return (float*)(ws + WS_STAT); }
    __device__ __forceinline__ u64* flags() const { return (u64*)(ws + WS_FLAGS); }
};

constexpr int SB_M = (128 + 224) * 132, SB_Q = (128 + 192) * 132, SB_F = 256 * 132, NS_F = 4;
constexpr int LDS_QX = 3 * SB_Q;
constexpr int LDS_CTL = LDS_QX + 3072;
constexpr int LDS_AQ = 0, LDS_AK = 2 * SB_Q, LDS_AV = 128 * 1024, LDS_BYTES = 160 * 1024;
constexpr int LDS_MC = LDS_BYTES - 512;
constexpr int LDS_X = 3 * SB_F;
constexpr int LDS_BK = 16 * 1024, LDS_BV = 96 * 1024;
constexpr int LDS_EX = 136 * 1024;
constexpr int LDS_SINK = LDS_BYTES - 256;
static_assert(LDS_CTL + 32 <= LDS_AV && 3 * SB_M <= LDS_SINK && 3 * SB_F <= LDS_AV && NS_F * SB_F <= LDS_EX && LDS_EX + 16384 <= LDS_MC, "LDS map");
constexpr int NA = 19;
constexpr int KW = 16, NF_Q = 7 + NA, NF_M = 7 + 42 - NA;

__device__ __forceinline__ int qkv_head(int tn, int blk) { return blk * NH + ((tn & ~3) | ((tn + blk) & 3)) * 2; }

__device__ __forceinline__ f32x4 ln_row(const u32x4 a, const u32x4 b, u32x4& oa, u32x4& ob) {
    f32x2 v[8];
#pragma unroll
    for (int i = 0; i < 4; ++i) { v[i] = (f32x2){lo16(a[i]), hi16(a[i])}; v[4 + i] = (f32x2){lo16(b[i]), hi16(b[i])}; }
    const f32x2 s2 = ((v[0] + v[1]) + (v[2] + v[3])) + ((v[4] + v[5]) + (v[6] + v[7]));
    const float mean = wave_sum(s2.x + s2.y) * (1.0f / D_);
    f32x2 q2 = {0.f, 0.f};
#pragma unroll
    for (int i = 0; i < 8; ++i) { v[i] -= mean; q2 += v[i] * v[i]; }
    const float var = wave_sum(q2.x + q2.y) * (1.0f / D_);
    const float rs = __builtin_amdgcn_rcpf(rbf(__builtin_amdgcn_sqrtf(rbf(rbf(var) + 1e-5f))));
    const float sd2 = rbf(__builtin_amdgcn_sqrtf(rbf(rbf(var * rs * rs) + 1e-5f)));
    const float g = rs * __builtin_amdgcn_rcpf(sd2);
#pragma unroll
    for (int i = 0; i < 8; ++i) {
        const f32x2 t = v[i] * g;
        if (i < 4) oa[i] = pack2(t.x, t.y); else ob[i - 4] = pack2(t.x, t.y);
    }
    return (f32x4){mean, rs, sd2, 0.f};
}

#define MFMA32(a, b, c) __builtin_amdgcn_mfma_f32_32x32x16_bf16(a, b, c, 0, 0, 0)
__device__ __forceinline__ int attn_dep(int hd, int qb, int lane) {
    const int which = lane >> 2, st = 2 * qb - 2 + (lane & 3), p = hd >> 1;
    if (lane >= 12 || st < 0 || (which == 0 && (lane & 3) < 2)) return -1;
    return FL_Q + st * 32 + ((p & ~3) | ((p - which) & 3));
}
template <int WHICH> __device__ __forceinline__ void attn_load(const Params& P, unsigned dst, int hd, int qb, int w, int lane) {
    const int row = WHICH == 0 ? qb * 256 + w * 64 : qb * 256 - ((qb == 0 && w < 2) ? 0 : 256) + w * 128;
    const char* g = (const char*)(P.qkv() + ((size_t)(WHICH * NH + hd) * S_ + row) * 32);
    const unsigned off = (unsigned)((lane >> 2) * 64 + (WHICH == 2 ? lane & 3 : (lane & 3) ^ ((lane >> 4) & 3)) * 16);
    dma16x4(g, off, dst + w * (WHICH == 0 ? 4096 : 8192));
    if (WHICH != 0) dma16x4(g + 4096, off, dst + w * 8192 + 4096);
}
#define PIN(x) asm volatile("" : "+v"(x))
__device__ __forceinline__ float attn_scores(LAS char* buf, LAS char* qbuf, int nbad, int wid, int lane, unsigned up, f32x16 (&sc)[8]) {
    const int r32 = lane & 31, hh = lane >> 5, sw = (r32 >> 2) & 3;
    LAS char* k0p = buf + (wid * 32 + r32) * 64 + ((hh ^ sw) * 16);
    LAS char* k1p = buf + (wid * 32 + r32) * 64 + (((2 + hh) ^ sw) * 16);
    const bf16x8 qf0 = *(LAS bf16x8*)(qbuf + (wid * 32 + r32) * 64 + ((hh ^ sw) * 16));
    const bf16x8 qf1 = *(LAS bf16x8*)(qbuf + (wid * 32 + r32) * 64 + (((2 + hh) ^ sw) * 16));
    bf16x8 ka[9], kb[9];
#pragma unroll
    for (int i = 0; i < 3; ++i) { const int j = i == 0 ? 8 : i - 1; ka[i] = *(LAS bf16x8*)(k0p + j * 2048); kb[i] = *(LAS bf16x8*)(k1p + j * 2048); SCHED0(); }
    const float NEG = -INFINITY;
    const f32x16 zero = {};
    f32x16 s8;
    float m = NEG;
#pragma unroll
    for (int i = 0; i < 11; ++i) {
        if (i + 3 < 9) { ka[i + 3] = *(LAS bf16x8*)(k0p + (i + 2) * 2048); kb[i + 3] = *(LAS bf16x8*)(k1p + (i + 2) * 2048); }
        if (i < 9) {
            const f32x16 t = MFMA32(kb[i], qf1, MFMA32(ka[i], qf0, zero));
            if (i == 0) { s8 = t; PIN(s8); } else { sc[i - 1] = t; PIN(sc[i - 1]); }
        }
        SCHED0();
        const int j = i - 3;
        if (j == 0) {
            if (__builtin_expect(nbad > 0, 0)) {
#pragma unroll
                for (int r = 0; r < 16; ++r) sc[0][r] = NEG;
                PIN(sc[0]);
            }
#pragma unroll
            for (int r = 0; r < 16; ++r) {
                const unsigned mk = (unsigned)__builtin_amdgcn_sbfe((int)up, (r & 3) + 8 * (r >> 2), 1);
                sc[0][r] = __uint_as_float(__builtin_amdgcn_bitop3_b32(mk, __float_as_uint(sc[0][r]), __float_as_uint(s8[r]), 0xca));
            }
        }
        if (j >= 0) {
            float mj = m;
#pragma unroll
            for (int r = 0; r < 16; r += 2) mj = __builtin_fmaxf(__builtin_fmaxf(mj, sc[j][r]), sc[j][r + 1]);
            m = (j > 0 && j < nbad) ? m : mj;
            PIN(m);
        }
        SCHED0();
    }
    auto rr = __builtin_amdgcn_permlane32_swap(__float_as_uint(m), __float_as_uint(m), false, false);
    return __builtin_fmaxf(__uint_as_float(rr[0]), __uint_as_float(rr[1]));
}
__device__ __forceinline__ bf16x8 v_read(LAS char* vp, int j, int s) {
    const bf16x4 lo = __builtin_amdgcn_ds_read_tr16_b64_v4bf16((LAS bf16x4*)(vp + j * 2048 + s * 1024));
    const bf16x4 hi = __builtin_amdgcn_ds_read_tr16_b64_v4bf16((LAS bf16x4*)(vp + j * 2048 + s * 1024 + 512));
    return __builtin_shufflevector(lo, hi, 0, 1, 2, 3, 4, 5, 6, 7);
}
__device__ __forceinline__ void attn_pv(LAS char* buf, int nbad, int wid, int lane, unsigned up, f32x16 (&sc)[8], float m, f32x16& o, float& lsum) {
    const int hh = lane >> 5;
    LAS char* vp = buf + (wid * 32 + 4 * hh + ((lane & 15) >> 2)) * 64 + ((lane >> 4) & 1) * 32 + (lane & 3) * 8;
    u32x4 one4 = {0x3f803f80u, 0x3f803f80u, 0x3f803f80u, 0x3f803f80u};
    PIN(one4);
    const bf16x8 ones = __builtin_bit_cast(bf16x8, one4);
    const float h0 = rbf(-m), h1 = rbf(-m - h0);
    const unsigned w0 = hh ? 0u : pack2(h0, h1), w1 = hh ? 0u : pack2((-m - h0) - h1, 0.f);
    auto minus_m = [&](int j) {
        const u32x4 bm = {(j > 0 && j < nbad) ? 0xff80u : w0, w1, 0u, 0u};
        sc[j] = MFMA32(ones, __builtin_bit_cast(bf16x8, bm), sc[j]);
        PIN(sc[j]);
    };
    minus_m(0); minus_m(1);
    f32x16 l = {};
    bf16x8 vf[2], v8[2];
    u32x4 pw[2], p8[2], pl[2];
    SCHED0();
#pragma unroll
    for (int j = 0; j < 8; ++j) {
        bf16x8 vn[2] = {v_read(vp, j, 0), v_read(vp, j, 1)};
        if (j == 0) { v8[0] = v_read(vp, 8, 0); v8[1] = v_read(vp, 8, 1); }
        SCHED0();
#pragma unroll
        for (int g = 0; g < 4; ++g) {
#pragma unroll
            for (int r = 4 * g; r < 4 * g + 4; ++r) sc[j][r] = __builtin_amdgcn_exp2f(sc[j][r]);
            PIN(sc[j]);
            SCHED0();
            if (j > 0) {
                if (g & 1) { l = MFMA32(ones, __builtin_bit_cast(bf16x8, j == 1 ? pl[g >> 1] : pw[g >> 1]), l); PIN(l); }
                else { o = MFMA32(vf[g >> 1], __builtin_bit_cast(bf16x8, pw[g >> 1]), o); PIN(o); }
            }
            if (j == 1 && (g & 1)) { o = MFMA32(v8[g >> 1], __builtin_bit_cast(bf16x8, p8[g >> 1]), o); PIN(o); }
            SCHED0();
        }
#pragma unroll
        for (int i = 0; i < 8; ++i) pw[i >> 2][i & 3] = pack2(sc[j][2 * i], sc[j][2 * i + 1]);
        if (j == 0) {
            pl[0] = pw[0]; pl[1] = pw[1];
#pragma unroll
            for (int i = 0; i < 8; ++i) {
                const int kr = ((2 * i) & 3) + 8 * (i >> 1);
                const unsigned mk = __builtin_amdgcn_bitop3_b32(0xffffu, (unsigned)__builtin_amdgcn_sbfe((int)up, kr, 1), (unsigned)__builtin_amdgcn_sbfe((int)up, kr + 1, 1), 0xca);
                p8[i >> 2][i & 3] = pw[i >> 2][i & 3] & ~mk; pw[i >> 2][i & 3] &= mk;
            }
        }
        vf[0] = vn[0]; vf[1] = vn[1];
        PIN(pw[0]); PIN(pw[1]);
        SCHED0();
        if (j + 2 < 8) minus_m(j + 2);
        SCHED0();
    }
    l = MFMA32(ones, __builtin_bit_cast(bf16x8, pw[0]), l);
    l = MFMA32(ones, __builtin_bit_cast(bf16x8, pw[1]), l);
    o = MFMA32(vf[0], __builtin_bit_cast(bf16x8, pw[0]), o);
    o = MFMA32(vf[1], __builtin_bit_cast(bf16x8, pw[1]), o);
    lsum = l[0];
}
__device__ __forceinline__ void attn_out(const Params& P, int hd, int qblk, int lane, float l, const f32x16& oacc) {
    const int r32 = lane & 31, hh = lane >> 5;
    const float inv = __builtin_amdgcn_rcpf(l);
    unsigned w[8];
#pragma unroll
    for (int g = 0; g < 4; ++g) { w[2 * g] = pack2(oacc[4 * g] * inv, oacc[4 * g + 1] * inv); w[2 * g + 1] = pack2(oacc[4 * g + 2] * inv, oacc[4 * g + 3] * inv); }
    const __amdgpu_buffer_rsrc_t rs = rsrc(P.o(), (unsigned)S_ * D_ * 2);
    const unsigned off = (unsigned)((hd * (S_ / 16) + qblk * 2 + (r32 >> 4)) * 1024 + (hh * 16 + (r32 & 15)) * 16);
#pragma unroll
    for (int gp = 0; gp < 2; ++gp) {
        auto a = __builtin_amdgcn_permlane32_swap(w[4 * gp], w[4 * gp + 2], false, false);
        auto b = __builtin_amdgcn_permlane32_swap(w[4 * gp + 1], w[4 * gp + 3], false, false);
        __builtin_amdgcn_raw_buffer_store_b128((u32x4){a[0], b[0], a[1], b[1]}, rs, off + gp * 512, 0, SC1);
    }
}

extern "C" __global__ __launch_bounds__(512, 2) void k_main(const Params P) {
    extern __shared__ __attribute__((aligned(16))) char smem[];
    LAS char* lds = (LAS char*)smem;
    const int tid = threadIdx.x, lane = tid & 63, wid = __builtin_amdgcn_readfirstlane(tid >> 6);
    const int grp = blockIdx.x & 7, idx = blockIdx.x >> 3;
    const bool loader = wid >= 4;
    const int lw = wid & 3, wm = (wid >> 1) & 1, wn = wid & 1;
    const unsigned ring0 = (unsigned)(uintptr_t)lds, ringl = ring0 + lw * PIECE;
    const int i16 = idx & 15;
    const bool isM = idx < 16;
    const int fm = (grp >> 1) * 4 + (isM ? i16 & 3 : i16 >> 2), fn = (grp & 1) * 4 + (isM ? i16 >> 2 : i16 & 3);
    const int um = (grp & 1) * 4 + (i16 >> 2), un = (grp >> 1) * 4 + (i16 & 3);
    const int hd = (grp & 1) * 8 + (i16 & 7), qb = (grp >> 1) * 2 + (i16 >> 3);
    if (wid == 0) {
        u64 pc; unsigned rel, len;
        asm volatile("s_getpc_b64 %0\n1:\n\ts_mov_b32 %1, 1b-k_main\n\ts_mov_b32 %2, 2f-k_main\n\t.subsection 1\n2:\n\t.subsection 0" : "=s"(pc), "=s"(rel), "=s"(len));
        const unsigned off = (unsigned)((idx + 32 * (lane & 15)) * 128);
        dma4((const char*)(pc - rel), off < len ? off : 0u, ring0 + LDS_SINK);
    }
    u64 tag;
    asm volatile("global_load_dwordx2 %0, %1, off sc1" : "=v"(tag) : "v"((GAS u64*)P.flags() + FL_E) : "memory");

    if (tid < 8) *(LAS unsigned*)(lds + (isM ? LDS_MC : LDS_CTL) + tid * 4) = 0u;
    if (isM) {
        constexpr int NK = S_ / 64, SB = SB_M;
        const int pg = (grp >> 1) + 4 * (grp & 1);
        const int tm = um, tn = un, mi = pg * 16 + i16;
        const char* gm = (const char*)P.h() + tm * 256; const char* gn = (const char*)P.w1 + tn * 448;
        const int cls = (tm + tn) & 3;
        auto kof = [&](int u) { const int p0 = u - ((u - cls) & 3); return __builtin_amdgcn_readfirstlane((((p0 == 1 || p0 == 2) ? u : korder(u, cls, NK)) + 4 * pg) & (NK - 1)); };
        Loader<4, 7> L;
        {
            const int row = mi * 16 + wid * 2;
            const u32x4* px = (const u32x4*)(P.x + (size_t)row * D_);
            u32x4 a0 = px[lane], b0 = px[64 + lane], a1 = px[128 + lane], b1 = px[192 + lane];
            SCHED0();
            if (loader) {
                const unsigned c0 = (tn & 1) * 64;
                L.init_m<TR>(lw, lane, D_ * 2);
                L.init_n<TR>(lw, lane, F_ * 2, c0, c0 + 128, c0 + 256, (tn & 1) ? 0 : 384);
            }
            asm volatile("" : "+v"(a0), "+v"(b0), "+v"(a1), "+v"(b1));
            if (loader) L.issue_n<false>(gn + (size_t)kof(0) * (64 * F_ * 2), ringl);
            const __amdgpu_buffer_rsrc_t rh = rsrc(P.h(), (unsigned)S_ * D_ * 2);
            u32x4 oa0, ob0, oa1, ob1;
            const f32x4 s0 = ln_row(a0, b0, oa0, ob0), s1 = ln_row(a1, b1, oa1, ob1);
            __builtin_amdgcn_raw_buffer_store_b128(oa0, rh, (unsigned)(row * 2048 + lane * 16), 0, SC1);
            __builtin_amdgcn_raw_buffer_store_b128(ob0, rh, (unsigned)(row * 2048 + 1024 + lane * 16), 0, SC1);
            __builtin_amdgcn_raw_buffer_store_b128(oa1, rh, (unsigned)(row * 2048 + 2048 + lane * 16), 0, SC1);
            __builtin_amdgcn_raw_buffer_store_b128(ob1, rh, (unsigned)(row * 2048 + 3072 + lane * 16), 0, SC1);
            if (lane < 2) __builtin_amdgcn_raw_buffer_store_b128(__builtin_bit_cast(u32x4, lane ? s1 : s0), rsrc(P.stat(), S_ * 16), (unsigned)((row + lane) * 16), 0, SC1);
            SCHED0();
        }
        if (loader) {
            WAIT_VM(0);
            BARRIER();
            if (tid == 256 + 64) set_flag(P.flags(), FL_L + mi, tag);
#pragma unroll
            for (int u = 1; u < 3; ++u) L.issue_n<false>(gn + (size_t)kof(u) * (64 * F_ * 2), ringl + u * SB);
            if (lw == 0) poll_flags(P.flags(), lane < 16 ? FL_L + pg * 16 + lane : -1, tag);
            BARRIER();
#pragma unroll
            for (int u = 0; u < 3; ++u) L.issue_m<true>(gm + (size_t)kof(u) * (64 * D_ * 2), ringl + u * SB);
            WAIT_VM(8); BARRIER();
            WAIT_VM(4);
            int slot = 0, t = 0;
            auto issue = [&]() {
                const int k = kof(t + 3);
                L.issue_m<true>(gm + (size_t)k * (64 * D_ * 2), ringl + slot * SB);
                L.issue_n<false>(gn + (size_t)k * (64 * F_ * 2), ringl + slot * SB);
                slot = (slot == 2) ? 0 : slot + 1;
            };
            if (lw == 0) poll_flags(P.flags(), lane < 16 ? FL_L + ((pg + 1) & 7) * 16 + lane : -1, tag);
            BARRIER(); issue(); ++t;
            WAIT_VM(L.NPW); BARRIER(); issue(); ++t;
            WAIT_VM(L.NPW);
            if (lw == 0) { poll_flags(P.flags(), FL_L + lane, tag); poll_flags(P.flags(), FL_L + 64 + lane, tag); }
            BARRIER(); issue(); ++t;
#pragma unroll 1
            for (; t < NK - 3; ++t) {
                const int k = (korder(t + 3, cls, NK) + 4 * pg) & (NK - 1);
                L.turn<true, false, 11>(gm + (size_t)k * (64 * D_ * 2), gn + (size_t)k * (64 * F_ * 2), ringl + slot * SB);
                slot = (slot == 2) ? 0 : slot + 1;
            }
            {
                const int f = ((i16 * 4 + lw) * 7 + (lane < 56 ? lane >> 3 : 6)) * 2 + (grp & 1) + 896 * (grp >> 1);
                dma4((const char*)P.w2 + (grp >> 1) * 1024, (unsigned)(f * (S_ * 2) + (lane & 7) * 128), ring0 + LDS_SINK);
            }
            WAIT_VM(L.NPW + 1); BARRIER();
            WAIT_VM(1); BARRIER();
            BARRIER();
#pragma unroll
            for (int i = 0; i < 8; ++i)
                dma16<false, 0>((const char*)P.x + ((size_t)(fm * 128 + i * 16) * D_ + fn * 128 + lw * 32) * 2, (unsigned)((lane >> 2) * (D_ * 2) + (lane & 3) * 16), ring0 + LDS_X + (lw * 8 + i) * 1024);
            dma4((const char*)P.b2 + fm * 256, (unsigned)(lane * 4), ring0 + LDS_X + 32768);
        } else {
            const int fr = lane & 15, fq = lane >> 4;
            const int fb = tn * 224 + ((tn & 1) ? 32 : 0) + wn * 32, fh = tn * 224 + ((tn & 1) ? 0 : 192) + wn * 16;
            WAIT_VM(0);
            WAIT_LGKM0();
            BARRIER();
            f32x4 acc[4][7];
#pragma unroll
            for (int j = 0; j < 7; ++j) {
                const u32x2 bb = *(const u32x2*)(P.b1 + (j < 6 ? fb + (j >> 1) * 64 + (j & 1) * 16 : fh) + fq * 4);
                const f32x4 bv = {lo16(bb[0]), hi16(bb[0]), lo16(bb[1]), hi16(bb[1])};
#pragma unroll
                for (int i = 0; i < 4; ++i) acc[i][j] = bv;
            }
            Reader<TR, 128, TR, 224, 4, 7> rd; rd.init(ring0, lane, wm, wn);
            Regs<4, 7> R0;
            int slot = 0;
            BARRIER();
            __builtin_amdgcn_s_waitcnt(0x0070);
            BARRIER();
            rd.read(R0, 0);
            for (int t = 0; t < NK / 4; ++t) {
                stage_step<3, SB>(slot, rd, rd, R0, acc); stage_step<3, SB>(slot, rd, rd, R0, acc);
                stage_step<3, SB>(slot, rd, rd, R0, acc); stage_step<3, SB>(slot, rd, rd, R0, acc);
            }
            const __amdgpu_buffer_rsrc_t rs = rsrc(P.U(), (unsigned)D_ * F_ * 2);
            const unsigned df = (unsigned)(tm * 8 + wm * 4);
            const unsigned ob = (unsigned)((fb >> 5) * 64 + df) * 1024u + (unsigned)((((fq & 1) * 2 + (fq >> 1)) * 16 + fr) * 16);
            const unsigned oh = (unsigned)((fh >> 5) * 64 + df + (fq & 1)) * 1024u + (unsigned)(((wn * 2 + (fq >> 1)) * 16 + fr) * 16);
            LAS char* ul = lds + 128 * 132 + (wn * 8 + wm * 4) * PIECE + (((fq & 1) * 2 + (fq >> 1)) * 16 + fr) * 16;
            unsigned pk[4][7][2];
#pragma unroll
            for (int i = 0; i < 4; ++i) {
#pragma unroll
                for (int j = 0; j < 7; ++j) {
                    const f32x2 a = gelu2((f32x2){acc[i][j][0], acc[i][j][1]}), b = gelu2((f32x2){acc[i][j][2], acc[i][j][3]});
                    pk[i][j][0] = pack2(a.x, a.y); pk[i][j][1] = pack2(b.x, b.y);
                }
#pragma unroll
                for (int jp = 0; jp < 3; ++jp) {
                    const u32x4 w = pair16(pk[i][2 * jp][0], pk[i][2 * jp][1], pk[i][2 * jp + 1][0], pk[i][2 * jp + 1][1]);
                    __builtin_amdgcn_raw_buffer_store_b128(w, rs, ob + i * 1024 + jp * 2 * 65536, 0, SC1);
                    *(LAS u32x4*)(ul + jp * SB_F + i * PIECE) = w;
                }
                if (i & 1)
                    __builtin_amdgcn_raw_buffer_store_b128(pair16(pk[i - 1][6][0], pk[i - 1][6][1], pk[i][6][0], pk[i][6][1]), rs, oh + (i - 1) * 1024, 0, SC1);
                SCHED0();
            }
            WAIT_VM(0);
            if (lane == 0 && __hip_atomic_fetch_add((LAS unsigned*)(lds + LDS_MC), 1u, __ATOMIC_RELAXED, __HIP_MEMORY_SCOPE_WORKGROUP) == 3u)
                set_flag(P.flags(), FL_U + um * 32 + un, tag);
        }
    } else {
        constexpr int NK = D_ / 64, SB = SB_Q;
        const int tm = fm;
        if (loader) {
            Loader<4, 6> L; L.init_m<ROW>(lw, lane, D_ * 2);
            L.init_n<TR>(lw, lane, 3 * D_ * 2, qkv_head(fn, 0) * 64, qkv_head(fn, 1) * 64, qkv_head(fn, 2) * 64);
            const char* gm = (const char*)P.x + (size_t)tm * 128 * D_ * 2; const char* gn = (const char*)P.wqkv;
            const char* gh2 = (const char*)P.h() + (size_t)tm * 128 * D_ * 2;
            int slot = 0;
            auto issue = [&](int u) {
                const int k = (korder(u & (NK - 1), (tm + fn) & 3, NK) + 4 * (grp >> 1)) & (NK - 1);
                L.issue_m<false>(gm + k * 128, ringl + slot * SB);
                L.issue_n<false>(gn + (size_t)k * (64 * 3 * D_ * 2) + (u >= NK ? 16 * 64 : 0), ringl + slot * SB);
                slot = (slot == 2) ? 0 : slot + 1;
            };
            issue(0); issue(1);
            WAIT_VM(L.NPW); BARRIER();
            issue(2);
            int t = 0;
            WAIT_VM(L.NPW); BARRIER(); issue(3); ++t;
#pragma unroll 1
            for (int seg = 0; seg < 2; ++seg) {
                const int end = seg == 0 ? 12 : 2 * NK - 3;
#pragma unroll 1
                for (; t < end; ++t) {
                    const int u = t + 3, k = (korder(u & (NK - 1), (tm + fn) & 3, NK) + 4 * (grp >> 1)) & (NK - 1);
                    L.turn<true, false, 10>((u >= NK ? gh2 : gm) + k * 128, gn + (size_t)k * (64 * 3 * D_ * 2) + (u & NK) * 64, ringl + slot * SB);
                    slot = (slot == 2) ? 0 : slot + 1;
                }
                if (seg == 0 && lw == 0) {
                    poll_flags(P.flags(), lane < 8 ? FL_L + tm * 8 + lane : -1, tag);
                    dma16<true, LDS_QX>((const char*)P.stat() + tm * 2048, (unsigned)(lane * 16), ring0);
                    dma16<true, LDS_QX + 1024>((const char*)P.stat() + tm * 2048 + 1024, (unsigned)(lane * 16), ring0);
                }
            }
            poll_flags(P.flags(), attn_dep(hd, qb, lane), tag);
            WAIT_VM(0);
            BARRIER();
            attn_load<1>(P, ring0 + LDS_AK, hd, qb, lw, lane);
            attn_load<2>(P, ring0 + LDS_AV, hd, qb, lw, lane);
            BARRIER();
            attn_load<0>(P, ring0 + LDS_AQ, hd, qb, lw, lane);
            BARRIER();
        } else {
            const int fr = lane & 15, fq = lane >> 4;
            if (wid == 1) {
                const int mb = i16 * 4 + (lane & 3);
                const char* b; int o;
                if (mb < 14) { b = (const char*)P.w1; o = mb; } else if (mb < 28) { b = (const char*)P.w2; o = mb - 14; }
                else if (mb < 34) { b = (const char*)P.wqkv; o = mb - 28; } else if (mb < 36) { b = (const char*)P.wo; o = mb - 34; }
                else if (mb < 40) { b = (const char*)P.x; o = mb - 36; } else if (mb < 44) { b = (const char*)P.out; o = mb - 40; }
                else { b = P.ws; o = mb - 44; }
                asm volatile("s_mov_b32 m0, %1\n\ts_nop 0\n\tglobal_load_lds_dword %0, off sc1" ::"v"(b + (size_t)o * 1048576 + mb * 4352), "s"(ring0 + LDS_SINK) : "memory");
            }
            Reader<ROW, 128, TR, 192, 4, 6> rd; rd.init(ring0, lane, wm, wn);
            rd.n.b = rd.n.a + 32 - wm * 32; rd.n.a += wm * 32;
            Regs<4, 6> R0;
            int slot = 0;
            WAIT_LGKM0();
            BARRIER();
            rd.read(R0, 0);
            const __amdgpu_buffer_rsrc_t rs = rsrc(P.qkv(), (unsigned)S_ * 3 * D_ * 2);
            const int s0 = tm * 128 + wm * 64;
            const unsigned so = (unsigned)(((s0 + fr) * 32 + ((fq & 1) ^ wm) * 16 + (fq >> 1) * 8) * 2);
            constexpr float QS = 0.17677669529663687f * 1.4426950408889634f;
            {
                f32x4 acc[5][6] = {};
                for (int t = 0; t < NK / 4; ++t) {
                    stage_step<3, SB>(slot, rd, rd, R0, acc); stage_step<3, SB>(slot, rd, rd, R0, acc);
                    stage_step<3, SB>(slot, rd, rd, R0, acc); stage_step<3, SB>(slot, rd, rd, R0, acc);
                }
                LAS char* ex = lds + LDS_QX + 2048;
                if (fr == 0) {
#pragma unroll
                    for (int jp = 0; jp < 3; ++jp) *(LAS f32x4*)(ex + wid * 256 + jp * 64 + fq * 16) = acc[4][2 * jp];
                }
                WAIT_LGKM0();
                CFENCE();
                __hip_atomic_store((LAS unsigned*)(lds + LDS_CTL + wid * 4), 1u, __ATOMIC_RELAXED, __HIP_MEMORY_SCOPE_WORKGROUP);
                f32x2 st[4];
#pragma unroll
                for (int i = 0; i < 4; ++i) st[i] = *(LAS f32x2*)(lds + LDS_QX + (wm * 64 + i * 16 + fr) * 16);
                while (__builtin_amdgcn_readfirstlane(__hip_atomic_load((LAS unsigned*)(lds + LDS_CTL + (wid ^ 2) * 4), __ATOMIC_RELAXED, __HIP_MEMORY_SCOPE_WORKGROUP)) == 0u) {}
                CFENCE();
#pragma unroll
                for (int jp = 0; jp < 3; ++jp) {
                    const f32x4 ca = acc[4][2 * jp], cb = *(LAS f32x4*)(ex + (wid ^ 2) * 256 + jp * 64 + fq * 16);
#pragma unroll
                    for (int i = 0; i < 4; ++i) {
                        const float r = st[i].y * (jp == 0 ? QS : 1.0f), mr = -st[i].x * r;
                        const f32x4 a = acc[i][2 * jp] * r + ca * mr, b = acc[i][2 * jp + 1] * r + cb * mr;
                        __builtin_amdgcn_raw_buffer_store_b128(pair16(pack2(a[0], a[1]), pack2(a[2], a[3]), pack2(b[0], b[1]), pack2(b[2], b[3])), rs,
                                                               so + (unsigned)((qkv_head(fn, jp) + wn) * (S_ * 64) + i * 1024), 0, SC1);
                    }
                }
            }
            {
                f32x4 acc[4][6] = {};
                for (int t = 0; t < NK / 4; ++t) {
                    if (__builtin_expect(t == 1 || t == 2, 0)) {
                        if (t == 1) WAIT_VM(0);
                        else if (tid == 0) set_flag(P.flags(), FL_Q + tm * 32 + fn, tag);
                    }
                    stage_step<3, SB>(slot, rd, rd, R0, acc); stage_step<3, SB>(slot, rd, rd, R0, acc);
                    stage_step<3, SB>(slot, rd, rd, R0, acc); stage_step<3, SB>(slot, rd, rd, R0, acc);
                }
#pragma unroll
                for (int i = 0; i < 4; ++i) {
                    const float sd = *(LAS float*)(lds + LDS_QX + (wm * 64 + i * 16 + fr) * 16 + 8);
#pragma unroll
                    for (int jp = 0; jp < 3; ++jp) {
                        const float r = sd * (jp == 0 ? QS : 1.0f);
                        const f32x4 a = acc[i][2 * jp] * r, b = acc[i][2 * jp + 1] * r;
                        __builtin_amdgcn_raw_buffer_store_b128(pair16(pack2(a[0], a[1]), pack2(a[2], a[3]), pack2(b[0], b[1]), pack2(b[2], b[3])), rs,
                                                               so + (unsigned)((qkv_head(fn, jp) + 16 + wn) * (S_ * 64) + i * 1024), 0, SC1);
                    }
                }
            }
        }
    }

    constexpr int SB = SB_F, NS = NS_F;
    constexpr size_t OS = 2 * (S_ / 16) * 1024, WOS = 64 * D_ * 2, W2S = 64 * S_ * 2, US = 2 * 65536;
    const int tile = fm * 8 + fn, cls = (fm + fn) & 3, nf = isM ? NF_M : NF_Q;
    const int uh = ((fm >> 1) & 1) ^ (isM ? 0 : 1);
    auto fof = [&](int i) {
        int r = korder(i - 7, cls, nf - 7) + (grp & 1) * ((nf - 7) >> 1);
        r = r >= nf - 7 ? r - (nf - 7) : r;
        const int im = (fm & 1) ? (i < 3 ? i + 4 : i - 3) : i;
        const int j = i < 7 ? 7 * uh + (isM ? im : korder(i, cls, 7)) : (isM ? 14 + NA : 14) + r, k = 14 * (grp >> 1) + j;
        return __builtin_amdgcn_readfirstlane(k >= 56 ? k - 56 : k);
    };
    const char* g2 = (const char*)P.w2 + fm * 256;
    Loader<4, 4> L;
    if (loader) L.init_m<TR>(lw, lane, S_ * 2);
    auto preload_w2 = [&]() {
        int k0 = fof(0), k1 = fof(1);
        asm volatile("" : "+s"(k0), "+s"(k1));
        L.issue_m<false>(g2 + k0 * W2S, ringl); L.issue_m<false>(g2 + k1 * W2S, ringl + SB);
    };
    if (isM) {
        if (loader) { preload_w2(); L.issue_m<false>(g2 + fof(2) * W2S, ringl + 2 * SB); WAIT_VM(0); }
        WAIT_LGKM0();
        BARRIER();
    } else {
        if (loader) { WAIT_VM(0); __builtin_amdgcn_s_setprio(1); }
        WAIT_LGKM0();
        BARRIER();
#pragma unroll 1
        for (int it = 0; it < 2; ++it) {
            f32x16 sc[8];
            int ln = lane;
            asm volatile("" : "+v"(ln));
            const int dg = (ln & 31) - 4 * (ln >> 5);
            const unsigned up = dg < 0 ? ~0u : ~1u << dg;
            int nbad = qb == 0 ? 8 - wid : 0;
            asm volatile("" : "+s"(nbad));
            SCHED0();
            const float m = attn_scores(lds + (it ? LDS_BK : LDS_AK), lds + LDS_AQ, nbad, wid, ln, up, sc);
            SCHED0();
            WAIT_VM(0);
            BARRIER();
            if (tid == 0) set_flag(P.flags(), it == 0 ? FL_Q + fm * 32 + fn + 8 : FL_O + qb * 32 + hd, tag);
            if (it == 1 && loader) preload_w2();
            f32x16 oacc = {};
            float lsum;
            attn_pv(lds + (it ? LDS_BV : LDS_AV), nbad, wid, ln, up, sc, m, oacc, lsum);
            attn_out(P, hd + 16 * it, qb * 8 + wid, ln, lsum, oacc);
            if (it == 0) {
                if (loader) {
                    __builtin_amdgcn_s_setprio(0);
                    int h2 = hd + 16, q2 = qb, w2 = lw;
                    asm volatile("" : "+s"(h2), "+s"(q2), "+s"(w2));
                    poll_flags(P.flags(), attn_dep(h2, q2, ln), tag);
                    attn_load<0>(P, ring0 + LDS_AQ, h2, q2, w2, ln);
                    attn_load<1>(P, ring0 + LDS_BK, h2, q2, w2, ln);
                    attn_load<2>(P, ring0 + LDS_BV, h2, q2, w2, ln);
                    WAIT_VM(8);
                    __builtin_amdgcn_s_setprio(1);
                }
                BARRIER();
            }
        }
        WAIT_LGKM0();
    }
    const int own = isM ? 0 : 1;
    if (loader) {
        const char* go = (const char*)P.o() + fm * 8192 + own * 4096;
        const char* gw = (const char*)P.wo + fn * 256;
        const char* gu = (const char*)P.U() + fn * 8192;
        __builtin_amdgcn_s_setprio(0);
        L.init_n<FRAG>(lw, lane, US / 2);
        auto issue_f = [&](int u) {
            const int k = fof(u);
            L.issue_m<false>(g2 + k * W2S, ringl + (u & (NS - 1)) * SB); L.issue_n<true>(gu + k * US, ringl + (u & (NS - 1)) * SB);
        };
        const int ulo = isM ? ((14 + NA) * 2) / 7 : 4, uhi = isM ? 15 : ((14 + NA) * 2 - 1) / 7;
        const int udep = (lane >= ulo && lane <= uhi) ? FL_U + fn * 32 + (((grp >> 1) * 4 + lane) & 15) : -1;
        const int odep = lane < 32 ? FL_O + (fm >> 1) * 32 + lane : lane < 48 ? FL_U + fn * 32 + lane - 32 : -1, pdep = lane == 0 ? FL_P + tile * 32 + own : -1;
        u64 pk;
        if (isM) { BARRIER(); BARRIER(); }
        poll_flags(P.flags(), lane < 2 ? FL_U + fn * 32 + (grp >> 1) * 4 + 2 * uh + lane : -1, tag);
        if (isM) { issue_f(3); issue_f(4); }
        else {
#pragma unroll
            for (int u = 0; u < 2; ++u) L.issue_n<true>(gu + fof(u) * US, ringl + u * SB);
            WAIT_VM(4); BARRIER();
            issue_f(2); issue_f(3);
            WAIT_VM(16); BARRIER(); issue_f(4);
            WAIT_VM(16); BARRIER();
        }
        peek_issue(pk, P.flags(), udep); issue_f(5);
        WAIT_VM(16); BARRIER(); issue_f(6);
        WAIT_VM(16);
        int nxt = 7;
        if (!peek_ok(pk, udep, tag)) {
            bool uready = false;
#pragma unroll 1
            for (int t = 3; t < 7; ++t) {
                if (t == 6 && !uready) { poll_flags(P.flags(), udep, tag); uready = true; for (; nxt < 10; ++nxt) issue_f(nxt); }
                if (nxt - (t + 2) >= 2) WAIT_VM(16); else WAIT_VM(0);
                BARRIER();
                if (!uready) uready = flags_set(P.flags(), udep, tag);
                if (uready) for (; nxt <= t + NS; ++nxt) issue_f(nxt);
            }
        }
#pragma unroll 1
        for (; nxt < nf - 2; ++nxt) {
            int r = korder(nxt - 7, cls, nf - 7) + (grp & 1) * ((nf - 7) >> 1);
            r = r >= nf - 7 ? r - (nf - 7) : r;
            int k = 14 * (grp >> 1) + (isM ? 14 + NA : 14) + r;
            k = k >= 56 ? k - 56 : k;
            L.turn<false, true, 16>(g2 + k * W2S, gu + k * US, ringl + (nxt & (NS - 1)) * SB);
        }
        const unsigned xo = (unsigned)(((fm * 128 + (lane >> 2)) * D_ + fn * 128 + lw * 32 + (lane & 3) * 8) * 2);
        constexpr unsigned ROWS16 = 16 * D_ * 2;
        WAIT_VM(16); BARRIER(); peek_issue(pk, P.flags(), odep);
        issue_f(nf - 2);
        WAIT_VM(16); BARRIER(); issue_f(nf - 1);
        L.init_n<TR>(lw, lane, D_ * 2);
        const unsigned oo = (unsigned)(lw * 1024 + lane * 16);
        const unsigned oo2 = oo + (unsigned)(OS / 2);
        const char *po, *pw; unsigned pd;
        auto addr_w = [&](int i) {
            const int k = (korder(i, cls, KW) + 4 * (grp >> 1)) & (KW - 1);
            po = go + k * OS; pw = gw + k * WOS; pd = ringl + ((nf + i) & (NS - 1)) * SB;
            asm volatile("" : "+s"(po), "+s"(pw), "+s"(pd));
        };
        auto issue_w = [&]() {
            asm volatile("s_add_u32 m0, %2, %3\n\ts_nop 0\n\tglobal_load_lds_dwordx4 %4, %0 sc1\n\ts_addk_i32 m0, 8448\n\ts_nop 0\n\tglobal_load_lds_dwordx4 %5, %0 sc1\n\t"
                         "s_add_u32 m0, %2, 16896\n\ts_nop 0\n\tglobal_load_lds_dwordx4 %6, %1\n\ts_addk_i32 m0, 4224\n\ts_nop 0\n\tglobal_load_lds_dwordx4 %7, %1\n\t"
                         "s_addk_i32 m0, 4224\n\ts_nop 0\n\tglobal_load_lds_dwordx4 %8, %1\n\ts_addk_i32 m0, 4224\n\ts_nop 0\n\tglobal_load_lds_dwordx4 %9, %1"
                         ::"s"(po), "s"(pw), "s"(pd), "s"(own * 4 * PIECE), "v"(oo), "v"(oo2), "v"(L.on[0]), "v"(L.on[1]), "v"(L.on[2]), "v"(L.on[3]) : "scc", "memory");
        };
#define TURN_W(i, w) do { addr_w(i); WAIT_VM(w); BARRIER(); issue_w(); } while (0)
        addr_w(0);
        WAIT_VM(16);
        if (!peek_ok(pk, odep, tag)) poll_flags(P.flags(), odep, tag);
        BARRIER(); issue_w();
        TURN_W(1, 14); TURN_W(2, 12); TURN_W(3, 12);
        TURN_W(4, 12);
        {
            u32x4 t0, t1, t2, t3;
            asm volatile("ds_read_b128 %0, %4\n\tds_read_b128 %1, %4 offset:1024\n\tds_read_b128 %2, %4 offset:2048\n\tds_read_b128 %3, %4 offset:3072\n\ts_waitcnt lgkmcnt(0)"
                         : "=&v"(t0), "=&v"(t1), "=&v"(t2), "=&v"(t3) : "v"(ring0 + LDS_EX + lw * 4096 + lane * 16) : "memory");
            const __amdgpu_buffer_rsrc_t ro = rsrc(P.out, (unsigned)S_ * D_ * 2);
            const unsigned so = xo + (1 - own) * 4 * ROWS16;
            __builtin_amdgcn_raw_buffer_store_b128(t0, ro, so, 0, SC1); __builtin_amdgcn_raw_buffer_store_b128(t1, ro, so + ROWS16, 0, SC1);
            __builtin_amdgcn_raw_buffer_store_b128(t2, ro, so + 2 * ROWS16, 0, SC1); __builtin_amdgcn_raw_buffer_store_b128(t3, ro, so + 3 * ROWS16, 0, SC1);
        }
        TURN_W(5, 12); TURN_W(6, 12);
        TURN_W(7, 0);
        if (tid == 256) set_flag(P.flags(), FL_P + tile * 32 + 1 - own, tag);
        TURN_W(8, 12); TURN_W(9, 12); TURN_W(10, 12); TURN_W(11, 12);
        TURN_W(12, 12); TURN_W(13, 12); TURN_W(14, 12);
        TURN_W(15, 12);
#undef TURN_W
        peek_issue(pk, P.flags(), pdep);
        WAIT_VM(13); BARRIER();
        WAIT_VM(7); BARRIER();
        WAIT_VM(0);
        auto load_partial = [&]() {
#pragma unroll
            for (int i = 0; i < 4; ++i) dma16<true, 0>((const char*)P.out + (own * 4 + i) * ROWS16, xo, ring0 + LDS_EX + (lw * 4 + i) * 1024);
        };
        const bool early = peek_ok(pk, pdep, tag);
        if (early) load_partial();
        BARRIER();
        BARRIER();
        if (!early) { poll_flags(P.flags(), pdep, tag); load_partial(); }
        WAIT_VM(0); BARRIER();
        if (blockIdx.x == 0 && lw == 0) {
#pragma unroll 1
            for (int i = 0; i < 4; ++i) poll_flags(P.flags(), FL_O + i * 64 + lane, tag);
            if (lane == 0) set_flag(P.flags(), FL_E, tag);
        }
    } else {
        const __amdgpu_buffer_rsrc_t ro = rsrc(P.out, (unsigned)S_ * D_ * 2);
        f32x4 acc[4][4];
        if (isM) {
            LAS char* xp = lds + LDS_X + (wn * 16 + wm * 4) * 1024 + (lane & 15) * 64 + (((lane >> 4) & 1) * 2 + (lane >> 5)) * 16;
            u32x4 xv[4][2]; unsigned bv[4];
#pragma unroll
            for (int i = 0; i < 4; ++i) {
                xv[i][0] = *(LAS u32x4*)(xp + i * 1024); xv[i][1] = *(LAS u32x4*)(xp + 8192 + i * 1024);
                bv[i] = *(LAS u16*)(lds + LDS_X + 32768 + (wm * 64 + i * 16 + (lane & 15)) * 2);
            }
            SCHED0();
#pragma unroll
            for (int i = 0; i < 4; ++i)
#pragma unroll
                for (int jp = 0; jp < 2; ++jp) {
                    auto e0 = __builtin_amdgcn_permlane16_swap(xv[i][jp][0], xv[i][jp][2], false, false);
                    auto e1 = __builtin_amdgcn_permlane16_swap(xv[i][jp][1], xv[i][jp][3], false, false);
                    acc[i][2 * jp] = (f32x4){lo16(e0[0]), hi16(e0[0]), lo16(e1[0]), hi16(e1[0])} + lo16(bv[i]);
                    acc[i][2 * jp + 1] = (f32x4){lo16(e0[1]), hi16(e0[1]), lo16(e1[1]), hi16(e1[1])} + lo16(bv[i]);
                }
            pin_acc<4, 4, 4>(acc);
        } else {
            float z = 0.f;
            asm volatile("" : "+v"(z));
#pragma unroll
            for (int i = 0; i < 4; ++i)
#pragma unroll
                for (int j = 0; j < 4; ++j) acc[i][j] = (f32x4){z, z, z, z};
        }
        pin_acc<4, 4, 4>(acc);
        Reader<TR, 128, FRAG, 128, 4, 4> rf;
        HalfReader rw;
        Regs<4, 4> R0;
        LAS char* exl = lds + LDS_EX + wn * 8192 + lane * 16;
        int slot = 0;
#define STEP(a, b) stage_step<NS, SB>(slot, a, b, R0, acc)
        rf.init(ring0, lane, wm, wn);
        WAIT_LGKM0();
        if (!isM) BARRIER();
        rf.read(R0, 0);
        for (int t = 0; t < (nf - 2) / 4; ++t) {
            if (__builtin_expect(!isM && (t == 1 || t == 2), 0)) {
                if (t == 1) WAIT_VM(0);
                else if (tid == 0) set_flag(P.flags(), FL_O + qb * 32 + hd + 16, tag);
            }
            STEP(rf, rf); STEP(rf, rf); STEP(rf, rf); STEP(rf, rf);
        }
        static_assert((NF_M & 3) == (NF_Q & 3), "NA must be odd");
#pragma unroll
        for (int t = 0; t < ((NF_M - 2) & 3); ++t) STEP(rf, rf);
        STEP(rf, rf);
        const int kq = wm != own ? 1 : 0;
        rw.init(ring0 + slot * SB, lane, own, kq, wn);
        STEP(rf, rw);
        if (kq) {
#pragma unroll
            for (int i = 0; i < 4; ++i)
#pragma unroll
                for (int jp = 0; jp < 2; ++jp) {
                    const f32x4 a = acc[i][2 * jp], b = acc[i][2 * jp + 1];
                    *(LAS u32x4*)(exl + jp * 4096 + i * 1024) = (u32x4){pack2(a[0], a[1]), pack2(a[2], a[3]), pack2(b[0], b[1]), pack2(b[2], b[3])};
                    acc[i][2 * jp] = (f32x4){0.f, 0.f, 0.f, 0.f}; acc[i][2 * jp + 1] = (f32x4){0.f, 0.f, 0.f, 0.f};
                }
        }
        pin_acc<4, 4, 4>(acc);
        Regs<4, 4> R1;
        for (int t = 0; t < 4; ++t) {
            half_step<NS, SB>(slot, rw, R0, R1, acc); half_step<NS, SB>(slot, rw, R1, R0, acc);
            half_step<NS, SB>(slot, rw, R0, R1, acc); half_step<NS, SB>(slot, rw, R1, R0, acc);
        }
#pragma unroll
        for (int i = 0; i < 2; ++i)
#pragma unroll
            for (int j = 0; j < 4; ++j) *(LAS f32x4*)(lds + (wid * 8 + i * 4 + j) * 1024 + lane * 16) = acc[2 + i][j];
        WAIT_LGKM0();
        BARRIER();
        const unsigned fo = (unsigned)(((fm * 128 + own * 64 + kq * 32 + (lane & 15)) * D_ + fn * 128 + wn * 64 + ((lane >> 4) & 1) * 16 + (lane >> 5) * 8) * 2);
#pragma unroll
        for (int i = 0; i < 2; ++i)
#pragma unroll
            for (int jp = 0; jp < 2; ++jp) {
                const u32x4 e = *(LAS u32x4*)(exl + jp * 4096 + (kq * 2 + i) * 1024);
                const f32x4 a = acc[i][2 * jp] + *(LAS f32x4*)(lds + ((wid ^ 2) * 8 + i * 4 + 2 * jp) * 1024 + lane * 16) + (f32x4){lo16(e[0]), hi16(e[0]), lo16(e[1]), hi16(e[1])};
                const f32x4 b = acc[i][2 * jp + 1] + *(LAS f32x4*)(lds + ((wid ^ 2) * 8 + i * 4 + 2 * jp + 1) * 1024 + lane * 16) + (f32x4){lo16(e[2]), hi16(e[2]), lo16(e[3]), hi16(e[3])};
                __builtin_amdgcn_raw_buffer_store_b128(pair16(pack2(a[0], a[1]), pack2(a[2], a[3]), pack2(b[0], b[1]), pack2(b[2], b[3])), ro, fo + i * (16 * D_ * 2) + jp * 64, 0, SC1);
            }
#undef STEP
    }
}

extern "C" void kernel_launch(void* const* d_in, const int* in_sizes, int n_in, void* d_out, int out_size, void* d_ws, size_t ws_size, hipStream_t stream) {
    char* ws = (char*)d_ws;
    Params P;
    P.x = (const u16*)d_in[0]; P.wqkv = (const u16*)d_in[1]; P.wo = (const u16*)d_in[2];
    P.w1 = (const u16*)d_in[4]; P.b1 = (const u16*)d_in[5]; P.w2 = (const u16*)d_in[6]; P.b2 = (const u16*)d_in[7];
    P.out = (u16*)d_out; P.ws = ws;
    (void)hipFuncSetAttribute((const void*)k_main, hipFuncAttributeMaxDynamicSharedMemorySize, LDS_BYTES);
    k_main<<<256, 512, LDS_BYTES, stream>>>(P);
}
```

```cpp
#include <hip/hip_runtime.h>
#include <hip/hip_bf16.h>
#include <stdint.h>

#define LAS __attribute__((address_space(3)))
#define GAS __attribute__((address_space(1)))
typedef __bf16 bf16x8 __attribute__((ext_vector_type(8)));
typedef __bf16 bf16x4 __attribute__((ext_vector_type(4)));
typedef __bf16 bf16x2 __attribute__((ext_vector_type(2)));
typedef float f32x2 __attribute__((ext_vector_type(2)));
typedef float f32x4 __attribute__((ext_vector_type(4)));
typedef float f32x16 __attribute__((ext_vector_type(16)));
typedef unsigned u32x4 __attribute__((ext_vector_type(4)));
typedef unsigned u32x2 __attribute__((ext_vector_type(2)));
typedef unsigned short u16;
typedef unsigned long long u64;

constexpr int S_ = 2048, D_ = 1024, F_ = 3584, NH = 32;

constexpr size_t WS_FLAGS = 0;
constexpr size_t WS_STAT  = 1 << 16;
constexpr size_t WS_QKV   = 1 << 17;
constexpr size_t WS_U     = WS_QKV + (size_t)S_ * 3 * D_ * 2;
constexpr int FL_E = 0;
constexpr int FL_L = 32;
constexpr int FL_U = 8 * 32;
constexpr int FL_Q = 16 * 32;
constexpr int FL_O = 32 * 32;
constexpr int FL_P = 40 * 32;

#define WAIT_VM(n) asm volatile("s_waitcnt vmcnt(%0)" ::"n"(n) : "memory")
#define WAIT_LGKM0() __builtin_amdgcn_s_waitcnt(0xC07F)
#define CFENCE() asm volatile("" ::: "memory")
#define BARRIER() do { CFENCE(); __builtin_amdgcn_s_barrier(); CFENCE(); } while (0)
#define SCHED0() __builtin_amdgcn_sched_barrier(0)
#define RLX_AGENT __ATOMIC_RELAXED, __HIP_MEMORY_SCOPE_AGENT
constexpr int SC1 = 16;

__device__ __forceinline__ float bf2f(u16 v) { return __uint_as_float((unsigned)v << 16); }
__device__ __forceinline__ float lo16(unsigned v) { return __uint_as_float(v << 16); }
__device__ __forceinline__ float hi16(unsigned v) { return __uint_as_float(v & 0xffff0000u); }
__device__ __forceinline__ unsigned pack2(float lo, float hi) {
    f32x2 v = {lo, hi};
    bf16x2 b = __builtin_convertvector(v, bf16x2);
    return __builtin_bit_cast(unsigned, b);
}
__device__ __forceinline__ float rbf(float x) { return __uint_as_float(pack2(x, 0.f) << 16); }
template <int CTRL, int RMASK> __device__ __forceinline__ float dpp_add(float v) {
    return v + __builtin_bit_cast(float, __builtin_amdgcn_update_dpp(0, __builtin_bit_cast(int, v), CTRL, RMASK, 0xf, false));
}
__device__ __forceinline__ float wave_sum(float v) {
    v = dpp_add<0x111, 0xf>(v); v = dpp_add<0x112, 0xf>(v); v = dpp_add<0x114, 0xf>(v); v = dpp_add<0x118, 0xf>(v);
    v = dpp_add<0x142, 0xa>(v); v = dpp_add<0x143, 0xc>(v);
    return __builtin_bit_cast(float, __builtin_amdgcn_readlane(__builtin_bit_cast(int, v), 63));
}
__device__ __forceinline__ __amdgpu_buffer_rsrc_t rsrc(const void* p, unsigned bytes) { return __builtin_amdgcn_make_buffer_rsrc((void*)p, 0, bytes, 0x00020000); }

enum { ROW = 0, TR = 1, FRAG = 2 };
constexpr int PIECE = 1056, BLK = 8 * PIECE;
template <int T, int X> struct Op {
    static constexpr int BYTES = X * 132;
    __device__ static __forceinline__ unsigned src_off(int blk, int p, int lane, unsigned ld, unsigned cb) {
        const int pos = lane >> 3;
        if (T == TR) {
            if (X == 224 && blk == 3) {
                const int ps = lane >> 2, g = (p & 1) | ((ps >> 3) << 1), r = 32 * (p >> 1) + 8 * g + 4 * ((ps >> 2) & 1) + (ps & 3);
                return (unsigned)r * ld + cb + (unsigned)((lane & 3) * 16);
            }
            const int g = (p >> 2) | (pos & 2), r = 32 * (pos >> 2) + 8 * g + 4 * (pos & 1) + (p & 3);
            return (unsigned)r * ld + cb + (unsigned)((lane & 7) * 16);
        } else if (T == ROW) {
            const int j = ((pos >> 1) & 1) | ((pos & 1) << 1) | (pos & 4);
            return (unsigned)(blk * 64 + j * 8 + p) * ld + (unsigned)((lane & 7) * 16);
        } else {
            return (unsigned)blk * ld + (unsigned)(p * 1024 + lane * 16);
        }
    }
};
template <bool SC, int IMM> __device__ __forceinline__ void dma16(const char* base, unsigned off, unsigned l) {
    if (SC) asm volatile("s_add_u32 m0, %2, %3\n\ts_nop 0\n\tglobal_load_lds_dwordx4 %0, %1 sc1" ::"v"(off), "s"(base), "s"(l), "n"(IMM) : "scc", "memory");
    else    asm volatile("s_add_u32 m0, %2, %3\n\ts_nop 0\n\tglobal_load_lds_dwordx4 %0, %1" ::"v"(off), "s"(base), "s"(l), "n"(IMM) : "scc", "memory");
}
#define DMA_FIRST(sc) "s_add_u32 m0, %1, %2\n\ts_nop 0\n\tglobal_load_lds_dwordx4 %3, %0" sc
#define DMA_NEXT(n, sc) "\n\ts_addk_i32 m0, 4224\n\ts_nop 0\n\tglobal_load_lds_dwordx4 %" #n ", %0" sc
#define DMA_G4(sc) DMA_FIRST(sc) DMA_NEXT(4, sc) DMA_NEXT(5, sc) DMA_NEXT(6, sc)
template <bool SC, int IMM, int N> __device__ __forceinline__ void dma_group(const char* base, const unsigned (&o)[N], unsigned l) {
    static_assert(4 * PIECE == 4224 && (N == 4 || ((N == 6 || N == 7) && !SC)), "dma_group");
    if constexpr (N == 4 && SC) asm volatile(DMA_G4(" sc1") ::"s"(base), "s"(l), "n"(IMM), "v"(o[0]), "v"(o[1]), "v"(o[2]), "v"(o[3]) : "scc", "memory");
    else if constexpr (N == 4) asm volatile(DMA_G4("") ::"s"(base), "s"(l), "n"(IMM), "v"(o[0]), "v"(o[1]), "v"(o[2]), "v"(o[3]) : "scc", "memory");
    else if constexpr (N == 6) asm volatile(DMA_G4("") DMA_NEXT(7, "") DMA_NEXT(8, "") ::"s"(base), "s"(l), "n"(IMM), "v"(o[0]), "v"(o[1]), "v"(o[2]), "v"(o[3]), "v"(o[4]), "v"(o[5]) : "scc", "memory");
    else asm volatile(DMA_G4("") DMA_NEXT(7, "") DMA_NEXT(8, "") DMA_NEXT(9, "") ::"s"(base), "s"(l), "n"(IMM), "v"(o[0]), "v"(o[1]), "v"(o[2]), "v"(o[3]), "v"(o[4]), "v"(o[5]), "v"(o[6]) : "scc", "memory");
}
__device__ __forceinline__ void dma4(const char* base, unsigned off, unsigned l) {
    asm volatile("s_mov_b32 m0, %2\n\ts_nop 0\n\tglobal_load_lds_dword %0, %1" ::"v"(off), "s"(base), "s"(l) : "memory");
}
__device__ __forceinline__ void dma16x4(const char* base, unsigned off, unsigned l) {
    asm volatile("s_mov_b32 m0, %2\n\ts_nop 0\n\tglobal_load_lds_dwordx4 %0, %1 sc1\n\tglobal_load_lds_dwordx4 %0, %1 offset:1024 sc1\n\t"
                 "global_load_lds_dwordx4 %0, %1 offset:2048 sc1\n\tglobal_load_lds_dwordx4 %0, %1 offset:3072 sc1" ::"v"(off), "s"(base), "s"(l) : "memory");
}

template <int T, int X> struct Addr {
    unsigned a, b;
    static constexpr int NV = (X == 224 || X == 192) ? 2 : 1;
    __device__ __forceinline__ void init(unsigned base, int lane, int w) {
        const int g = lane >> 4;
        b = 0;
        if (T == TR) {
            const int q = (lane & 15) >> 2, p = lane & 3;
            a = base + (unsigned)((q | ((g & 1) << 2)) * PIECE + (g >> 1) * 256 + p * 8 + (X == 128 ? w * BLK : w * 64));
            b = base + (unsigned)(3 * BLK + (g & 1) * PIECE + (g >> 1) * 512 + q * 64 + w * 32 + p * 8);
        } else if (T == ROW) {
            const int r = lane & 15;
            a = base + (unsigned)(w * BLK + (r & 7) * PIECE + (r >> 3) * 256 + g * 16);
        } else {
            a = base + (unsigned)(w * 4 * PIECE + lane * 16);
        }
    }
    __device__ __forceinline__ void step(unsigned d) { a += d; if (NV == 2) b += d; }
    __device__ __forceinline__ bf16x8 read(int n, int kk) const {
        if (T == TR) {
            LAS char* p; int t;
            if (X == 224 && n == 6) { p = (LAS char*)(uintptr_t)b + kk * 2 * PIECE; t = 256; }
            else if (X == 192) { p = (LAS char*)(uintptr_t)((n & 1) ? b : a) + kk * 512 + (n >> 1) * BLK; t = 128; }
            else { p = (LAS char*)(uintptr_t)a + kk * 512 + (X == 128 ? n * 32 : (n >> 1) * BLK + (n & 1) * 32); t = 128; }
            const bf16x4 lo = __builtin_amdgcn_ds_read_tr16_b64_v4bf16((LAS bf16x4*)p);
            const bf16x4 hi = __builtin_amdgcn_ds_read_tr16_b64_v4bf16((LAS bf16x4*)(p + t));
            return __builtin_shufflevector(lo, hi, 0, 1, 2, 3, 4, 5, 6, 7);
        } else if (T == ROW) {
            return *(LAS bf16x8*)((LAS char*)(uintptr_t)a + (n & 1) * 128 + (n >> 1) * 512 + kk * 64);
        } else {
            return *(LAS bf16x8*)((LAS char*)(uintptr_t)a + kk * BLK + n * PIECE);
        }
    }
};
template <int FM, int FN> struct Regs { bf16x8 m[FM], n[FN]; };
#define MFMA16(a, b, c) __builtin_amdgcn_mfma_f32_16x16x32_bf16(a, b, c, 0, 0, 0)

template <int TM, int BM, int TN, int BN, int FM, int FN> struct Reader {
    Addr<TM, BM> m; Addr<TN, BN> n;
    static constexpr int NDS = FM * (TM == TR ? 2 : 1) + FN * (TN == TR ? 2 : 1), NV = Addr<TM, BM>::NV + Addr<TN, BN>::NV;
    __device__ __forceinline__ void init(unsigned slot_base, int lane, int wm, int wn) { m.init(slot_base, lane, wm); n.init(slot_base + BM * 132, lane, wn); }
    __device__ __forceinline__ void read(Regs<FM, FN>& R, int kk) const {
#pragma unroll
        for (int i = 0; i < FM; ++i) R.m[i] = m.read(i, kk);
#pragma unroll
        for (int j = 0; j < FN; ++j) R.n[j] = n.read(j, kk);
    }
    __device__ __forceinline__ void step(unsigned d) { m.step(d); n.step(d); }
};
template <int AM, int FM, int FN> __device__ __forceinline__ void mma(f32x4 (&acc)[AM][FN], const Regs<FM, FN>& R) {
    const u32x4 one = {0x3f803f80u, 0x3f803f80u, 0x3f803f80u, 0x3f803f80u};
#pragma unroll
    for (int j = 0; j < FN; ++j) {
        if (AM > FM && !(j & 1)) acc[AM - 1][j] = MFMA16(R.n[j], __builtin_bit_cast(bf16x8, one), acc[AM - 1][j]);
#pragma unroll
        for (int ii = 0; ii < FM; ++ii) { const int i = (j & 1) ? FM - 1 - ii : ii; acc[i][j] = MFMA16(R.n[j], R.m[i], acc[i][j]); }
    }
}
template <int AM, int FM, int FN> __device__ __forceinline__ void pin_acc(f32x4 (&acc)[AM][FN]) {
#pragma unroll
    for (int j = 0; j < FN; ++j) {
        asm volatile("" : "+v"(acc[0][j]), "+v"(acc[1][j]), "+v"(acc[2][j]), "+v"(acc[3][j]));
        if (AM > FM && !(j & 1)) asm volatile("" : "+v"(acc[AM - 1][j]));
    }
}
template <int NDS, int NV, int NMF> __device__ __forceinline__ void interleave() {
#pragma unroll
    for (int i = 0; i < NMF; ++i) {
        __builtin_amdgcn_sched_group_barrier(0x8, 1, 0);
        if (i < NDS) __builtin_amdgcn_sched_group_barrier(0x80, 1, 0);
        if (i >= NMF - NV) __builtin_amdgcn_sched_group_barrier(0x2, 1, 0);
    }
}
template <int NS, int SB, class RD, class RDN, int AM, int FM, int FN>
__device__ __forceinline__ void stage_step(int& slot, RD& rd, RDN& rn, Regs<FM, FN>& R0, f32x4 (&acc)[AM][FN]) {
    constexpr int NMF = FM * FN + (AM - FM) * (FN / 2);
    Regs<FM, FN> R1;
    const unsigned d = (slot + 1 == NS) ? (unsigned)(-(NS - 1) * SB) : (unsigned)SB;
    slot = (slot + 1 == NS) ? 0 : slot + 1;
    rd.read(R1, 1);
    rn.step(d);
    mma(acc, R0);
    interleave<RD::NDS, RDN::NV, NMF>();
    SCHED0();
    pin_acc<AM, FM, FN>(acc);
    WAIT_LGKM0();
    BARRIER();
    SCHED0();
    rn.read(R0, 0);
    mma(acc, R1);
    interleave<RDN::NDS, 0, NMF>();
    SCHED0();
    pin_acc<AM, FM, FN>(acc);
    SCHED0();
}

struct HalfReader {
    unsigned m0, m1; Addr<TR, 128> n;
    static constexpr int NDS = 12, NV = 3;
    __device__ __forceinline__ void init(unsigned slot_base, int lane, int own, int kq, int wn) {
        const unsigned a = slot_base + (unsigned)(own * 4 * PIECE + kq * BLK + lane * 16);
        m0 = a + kq * 2 * PIECE; m1 = a - kq * 2 * PIECE;
        n.init(slot_base + 128 * 132 + kq * 512, lane, wn);
    }
    __device__ __forceinline__ void read(Regs<4, 4>& R, int) const {
#pragma unroll
        for (int i = 0; i < 4; ++i) R.m[i] = *(LAS bf16x8*)((LAS char*)(uintptr_t)(i < 2 ? m0 : m1) + i * PIECE);
#pragma unroll
        for (int j = 0; j < 4; ++j) R.n[j] = n.read(j, 0);
    }
    __device__ __forceinline__ void step(unsigned d) { m0 += d; m1 += d; n.step(d); }
};
template <int NS, int SB> __device__ __forceinline__ void half_step(int& slot, HalfReader& rd, Regs<4, 4>& Rc, Regs<4, 4>& Rn, f32x4 (&acc)[4][4]) {
    rd.step((slot + 1 == NS) ? (unsigned)(-(NS - 1) * SB) : (unsigned)SB);
    slot = (slot + 1 == NS) ? 0 : slot + 1;
    SCHED0();
    WAIT_LGKM0();
    BARRIER();
    SCHED0();
    rd.read(Rn, 0);
    mma(acc, Rc);
    interleave<12, 0, 16>();
    SCHED0();
    pin_acc<4, 4, 4>(acc);
    SCHED0();
}

template <int NM, int NN> struct Loader {
    static constexpr int NPW = NM + NN;
    unsigned om[NM], on[NN];
    template <int T> __device__ __forceinline__ void init_m(int lw, int lane, unsigned ld) {
#pragma unroll
        for (int i = 0; i < NM; ++i) om[i] = Op<T, NM * 32>::src_off(i >> 1, lw + 4 * (i & 1), lane, ld, (i >> 1) * 128);
    }
    template <int T> __device__ __forceinline__ void init_n(int lw, int lane, unsigned ld, unsigned c0 = 0, unsigned c1 = 128, unsigned c2 = 256, unsigned c3 = 384) {
#pragma unroll
        for (int i = 0; i < NN; ++i) on[i] = Op<T, NN * 32>::src_off(i >> 1, lw + 4 * (i & 1), lane, ld, (i >> 1) == 0 ? c0 : (i >> 1) == 1 ? c1 : (i >> 1) == 2 ? c2 : c3);
    }
    template <bool SC> __device__ __forceinline__ void issue_m(const char* gm, unsigned dst) const { dma_group<SC, 0>(gm, om, dst); }
    template <bool SC> __device__ __forceinline__ void issue_n(const char* gn, unsigned dst) const { dma_group<SC, NM * 32 * 132>(gn, on, dst); }
    template <bool SCM, bool SCN, int W> __device__ __forceinline__ void turn(const char* gm, const char* gn, unsigned dst) const {
        asm volatile("" : "+s"(gm), "+s"(gn), "+s"(dst));
        WAIT_VM(W); BARRIER();
        issue_m<SCM>(gm, dst); issue_n<SCN>(gn, dst);
    }
};
__device__ __forceinline__ int korder(int p, int c, int nk) {
    const int r = (p - c) & 3;
    const int d = (0x1003 >> (4 * r) & 15) - 1;
    return (unsigned)(p - r) < (unsigned)(nk - 2) ? p + d : p;
}
__device__ __forceinline__ f32x2 gelu2(f32x2 u) {
    const float c1 = -2.0f * 0.7978845608028654f * 1.4426950408889634f, c2 = c1 * 0.044715f;
    const f32x2 z = u * ((u * u) * c2 + c1);
    f32x2 e; e.x = __builtin_amdgcn_exp2f(z.x); e.y = __builtin_amdgcn_exp2f(z.y);
    e = e + 1.0f;
    f32x2 r; r.x = __builtin_amdgcn_rcpf(e.x); r.y = __builtin_amdgcn_rcpf(e.y);
    return u * r;
}
__device__ __forceinline__ u32x4 pair16(unsigned x0, unsigned x1, unsigned y0, unsigned y1) {
    auto a = __builtin_amdgcn_permlane16_swap(x0, y0, false, false);
    auto b = __builtin_amdgcn_permlane16_swap(x1, y1, false, false);
    return (u32x4){a[0], b[0], a[1], b[1]};
}
__device__ __forceinline__ bool flags_set(u64* flags, int idx, u64 tag) {
    const u64 v = __hip_atomic_load((GAS u64*)flags + (idx < 0 ? 0 : idx), RLX_AGENT);
    const bool ok = __builtin_amdgcn_ballot_w64(idx >= 0 && v - 1ull != tag) == 0ull;
    CFENCE();
    return ok;
}
__device__ __forceinline__ void poll_flags(u64* flags, int idx, u64 tag) {
#pragma unroll 1
    for (unsigned spins = 0; spins < (1u << 20); ++spins) {
        if (flags_set(flags, idx, tag)) break;
        __builtin_amdgcn_s_sleep(2);
    }
}
__device__ __forceinline__ void peek_issue(u64& v, u64* flags, int idx) {
    asm volatile("global_load_dwordx2 %0, %1, off sc1" : "=v"(v) : "v"((GAS u64*)flags + (idx < 0 ? 0 : idx)) : "memory");
}
__device__ __forceinline__ bool peek_ok(u64& v, int idx, u64 tag) {
    asm volatile("" : "+v"(v) :: "memory");
    return __builtin_amdgcn_ballot_w64(idx >= 0 && v - 1ull != tag) == 0ull;
}
__device__ __forceinline__ void set_flag(u64* flags, int idx, u64 tag) {
    asm volatile("" : "+v"(tag));
    __hip_atomic_store((GAS u64*)flags + idx, tag + 1ull, RLX_AGENT);
}

struct Params {
    const u16 *x, *wqkv, *wo, *w1, *b1, *w2, *b2;
    u16* out;
    char* ws;
    __device__ __forceinline__ u16* h() const { return out; }
    __device__ __forceinline__ u16* qkv() const { return (u16*)(ws + WS_QKV); }
    __device__ __forceinline__ u16* U() const { return (u16*)(ws + WS_U); }
    __device__ __forceinline__ u16* o() const { return (u16*)(ws + WS_QKV); }
    __device__ __forceinline__ float* stat() const { return (float*)(ws + WS_STAT); }
    __device__ __forceinline__ u64* flags() const { return (u64*)(ws + WS_FLAGS); }
};

constexpr int SB_M = (128 + 224) * 132, SB_Q = (128 + 192) * 132, SB_F = 256 * 132, NS_F = 4;
constexpr int LDS_QX = 3 * SB_Q;
constexpr int LDS_CTL = LDS_QX + 3072;
constexpr int LDS_AQ = 0, LDS_AK = 2 * SB_Q, LDS_AV = 128 * 1024, LDS_BYTES = 160 * 1024;
constexpr int LDS_MC = LDS_BYTES - 512;
constexpr int LDS_X = 3 * SB_F;
constexpr int LDS_BK = 16 * 1024, LDS_BV = 96 * 1024;
constexpr int LDS_EX = 136 * 1024;
constexpr int LDS_SINK = LDS_BYTES - 256;
static_assert(LDS_CTL + 32 <= LDS_AV && 3 * SB_M <= LDS_SINK && 3 * SB_F <= LDS_AV && NS_F * SB_F <= LDS_EX && LDS_EX + 16384 <= LDS_MC, "LDS map");
constexpr int NA = 19;
constexpr int KW = 16, NF_Q = 7 + NA, NF_M = 7 + 42 - NA;

__device__ __forceinline__ int qkv_head(int tn, int blk) { return blk * NH + ((tn & ~3) | ((tn + blk) & 3)) * 2; }

__device__ __forceinline__ f32x4 ln_row(const u32x4 a, const u32x4 b, u32x4& oa, u32x4& ob) {
    f32x2 v[8];
#pragma unroll
    for (int i = 0; i < 4; ++i) { v[i] = (f32x2){lo16(a[i]), hi16(a[i])}; v[4 + i] = (f32x2){lo16(b[i]), hi16(b[i])}; }
    const f32x2 s2 = ((v[0] + v[1]) + (v[2] + v[3])) + ((v[4] + v[5]) + (v[6] + v[7]));
    const float mean = wave_sum(s2.x + s2.y) * (1.0f / D_);
    f32x2 q2 = {0.f, 0.f};
#pragma unroll
    for (int i = 0; i < 8; ++i) { v[i] -= mean; q2 += v[i] * v[i]; }
    const float var = wave_sum(q2.x + q2.y) * (1.0f / D_);
    const float rs = __builtin_amdgcn_rcpf(rbf(__builtin_amdgcn_sqrtf(rbf(rbf(var) + 1e-5f))));
    const float sd2 = rbf(__builtin_amdgcn_sqrtf(rbf(rbf(var * rs * rs) + 1e-5f)));
    const float g = rs * __builtin_amdgcn_rcpf(sd2);
#pragma unroll
    for (int i = 0; i < 8; ++i) {
        const f32x2 t = v[i] * g;
        if (i < 4) oa[i] = pack2(t.x, t.y); else ob[i - 4] = pack2(t.x, t.y);
    }
    return (f32x4){mean, rs, sd2, 0.f};
}

#define MFMA32(a, b, c) __builtin_amdgcn_mfma_f32_32x32x16_bf16(a, b, c, 0, 0, 0)
__device__ __forceinline__ int attn_dep(int hd, int qb, int lane) {
    const int which = lane >> 2, st = 2 * qb - 2 + (lane & 3), p = hd >> 1;
    if (lane >= 12 || st < 0 || (which == 0 && (lane & 3) < 2)) return -1;
    return FL_Q + st * 32 + ((p & ~3) | ((p - which) & 3));
}
template <int WHICH> __device__ __forceinline__ void attn_load(const Params& P, unsigned dst, int hd, int qb, int w, int lane) {
    const int row = WHICH == 0 ? qb * 256 + w * 64 : qb * 256 - ((qb == 0 && w < 2) ? 0 : 256) + w * 128;
    const char* g = (const char*)(P.qkv() + ((size_t)(WHICH * NH + hd) * S_ + row) * 32);
    const unsigned off = (unsigned)((lane >> 2) * 64 + (WHICH == 2 ? lane & 3 : (lane & 3) ^ ((lane >> 4) & 3)) * 16);
    dma16x4(g, off, dst + w * (WHICH == 0 ? 4096 : 8192));
    if (WHICH != 0) dma16x4(g + 4096, off, dst + w * 8192 + 4096);
}
#define PIN(x) asm volatile("" : "+v"(x))
__device__ __forceinline__ float attn_scores(LAS char* buf, LAS char* qbuf, int nbad, int wid, int lane, unsigned up, f32x16 (&sc)[8]) {
    const int r32 = lane & 31, hh = lane >> 5, sw = (r32 >> 2) & 3;
    LAS char* k0p = buf + (wid * 32 + r32) * 64 + ((hh ^ sw) * 16);
    LAS char* k1p = buf + (wid * 32 + r32) * 64 + (((2 + hh) ^ sw) * 16);
    const bf16x8 qf0 = *(LAS bf16x8*)(qbuf + (wid * 32 + r32) * 64 + ((hh ^ sw) * 16));
    const bf16x8 qf1 = *(LAS bf16x8*)(qbuf + (wid * 32 + r32) * 64 + (((2 + hh) ^ sw) * 16));
    bf16x8 ka[9], kb[9];
#pragma unroll
    for (int i = 0; i < 3; ++i) { const int j = i == 0 ? 8 : i - 1; ka[i] = *(LAS bf16x8*)(k0p + j * 2048); kb[i] = *(LAS bf16x8*)(k1p + j * 2048); SCHED0(); }
    const float NEG = -INFINITY;
    const f32x16 zero = {};
    f32x16 s8;
    float m = NEG;
#pragma unroll
    for (int i = 0; i < 11; ++i) {
        if (i + 3 < 9) { ka[i + 3] = *(LAS bf16x8*)(k0p + (i + 2) * 2048); kb[i + 3] = *(LAS bf16x8*)(k1p + (i + 2) * 2048); }
        if (i < 9) {
            const f32x16 t = MFMA32(kb[i], qf1, MFMA32(ka[i], qf0, zero));
            if (i == 0) { s8 = t; PIN(s8); } else { sc[i - 1] = t; PIN(sc[i - 1]); }
        }
        SCHED0();
        const int j = i - 3;
        if (j == 0) {
            if (__builtin_expect(nbad > 0, 0)) {
#pragma unroll
                for (int r = 0; r < 16; ++r) sc[0][r] = NEG;
                PIN(sc[0]);
            }
#pragma unroll
            for (int r = 0; r < 16; ++r) {
                const unsigned mk = (unsigned)__builtin_amdgcn_sbfe((int)up, (r & 3) + 8 * (r >> 2), 1);
                sc[0][r] = __uint_as_float(__builtin_amdgcn_bitop3_b32(mk, __float_as_uint(sc[0][r]), __float_as_uint(s8[r]), 0xca));
            }
        }
        if (j >= 0) {
            float mj = m;
#pragma unroll
            for (int r = 0; r < 16; r += 2) mj = __builtin_fmaxf(__builtin_fmaxf(mj, sc[j][r]), sc[j][r + 1]);
            m = (j > 0 && j < nbad) ? m : mj;
            PIN(m);
        }
        SCHED0();
    }
    auto rr = __builtin_amdgcn_permlane32_swap(__float_as_uint(m), __float_as_uint(m), false, false);
    return __builtin_fmaxf(__uint_as_float(rr[0]), __uint_as_float(rr[1]));
}
__device__ __forceinline__ bf16x8 v_read(LAS char* vp, int j, int s) {
    const bf16x4 lo = __builtin_amdgcn_ds_read_tr16_b64_v4bf16((LAS bf16x4*)(vp + j * 2048 + s * 1024));
    const bf16x4 hi = __builtin_amdgcn_ds_read_tr16_b64_v4bf16((LAS bf16x4*)(vp + j * 2048 + s * 1024 + 512));
    return __builtin_shufflevector(lo, hi, 0, 1, 2, 3, 4, 5, 6, 7);
}
__device__ __forceinline__ void attn_pv(LAS char* buf, int nbad, int wid, int lane, unsigned up, f32x16 (&sc)[8], float m, f32x16& o, float& lsum) {
    const int hh = lane >> 5;
    LAS char* vp = buf + (wid * 32 + 4 * hh + ((lane & 15) >> 2)) * 64 + ((lane >> 4) & 1) * 32 + (lane & 3) * 8;
    u32x4 one4 = {0x3f803f80u, 0x3f803f80u, 0x3f803f80u, 0x3f803f80u};
    PIN(one4);
    const bf16x8 ones = __builtin_bit_cast(bf16x8, one4);
    const float h0 = rbf(-m), h1 = rbf(-m - h0);
    const unsigned w0 = hh ? 0u : pack2(h0, h1), w1 = hh ? 0u : pack2((-m - h0) - h1, 0.f);
    auto minus_m = [&](int j) {
        const u32x4 bm = {(j > 0 && j < nbad) ? 0xff80u : w0, w1, 0u, 0u};
        sc[j] = MFMA32(ones, __builtin_bit_cast(bf16x8, bm), sc[j]);
        PIN(sc[j]);
    };
    minus_m(0); minus_m(1);
    f32x16 l = {};
    bf16x8 vf[2], v8[2];
    u32x4 pw[2], p8[2], pl[2];
    SCHED0();
#pragma unroll
    for (int j = 0; j < 8; ++j) {
        bf16x8 vn[2] = {v_read(vp, j, 0), v_read(vp, j, 1)};
        if (j == 0) { v8[0] = v_read(vp, 8, 0); v8[1] = v_read(vp, 8, 1); }
        SCHED0();
#pragma unroll
        for (int g = 0; g < 4; ++g) {
#pragma unroll
            for (int r = 4 * g; r < 4 * g + 4; ++r) sc[j][r] = __builtin_amdgcn_exp2f(sc[j][r]);
            PIN(sc[j]);
            SCHED0();
            if (j > 0) {
                if (g & 1) { l = MFMA32(ones, __builtin_bit_cast(bf16x8, j == 1 ? pl[g >> 1] : pw[g >> 1]), l); PIN(l); }
                else { o = MFMA32(vf[g >> 1], __builtin_bit_cast(bf16x8, pw[g >> 1]), o); PIN(o); }
            }
            if (j == 1 && (g & 1)) { o = MFMA32(v8[g >> 1], __builtin_bit_cast(bf16x8, p8[g >> 1]), o); PIN(o); }
            SCHED0();
        }
#pragma unroll
        for (int i = 0; i < 8; ++i) pw[i >> 2][i & 3] = pack2(sc[j][2 * i], sc[j][2 * i + 1]);
        if (j == 0) {
            pl[0] = pw[0]; pl[1] = pw[1];
#pragma unroll
            for (int i = 0; i < 8; ++i) {
                const int kr = ((2 * i) & 3) + 8 * (i >> 1);
                const unsigned mk = __builtin_amdgcn_bitop3_b32(0xffffu, (unsigned)__builtin_amdgcn_sbfe((int)up, kr, 1), (unsigned)__builtin_amdgcn_sbfe((int)up, kr + 1, 1), 0xca);
                p8[i >> 2][i & 3] = pw[i >> 2][i & 3] & ~mk; pw[i >> 2][i & 3] &= mk;
            }
        }
        vf[0] = vn[0]; vf[1] = vn[1];
        PIN(pw[0]); PIN(pw[1]);
        SCHED0();
        if (j + 2 < 8) minus_m(j + 2);
        SCHED0();
    }
    l = MFMA32(ones, __builtin_bit_cast(bf16x8, pw[0]), l);
    l = MFMA32(ones, __builtin_bit_cast(bf16x8, pw[1]), l);
    o = MFMA32(vf[0], __builtin_bit_cast(bf16x8, pw[0]), o);
    o = MFMA32(vf[1], __builtin_bit_cast(bf16x8, pw[1]), o);
    lsum = l[0];
}
__device__ __forceinline__ void attn_out(const Params& P, int hd, int qblk, int lane, float l, const f32x16& oacc) {
    const int r32 = lane & 31, hh = lane >> 5;
    const float inv = __builtin_amdgcn_rcpf(l);
    unsigned w[8];
#pragma unroll
    for (int g = 0; g < 4; ++g) { w[2 * g] = pack2(oacc[4 * g] * inv, oacc[4 * g + 1] * inv); w[2 * g + 1] = pack2(oacc[4 * g + 2] * inv, oacc[4 * g + 3] * inv); }
    const __amdgpu_buffer_rsrc_t rs = rsrc(P.o(), (unsigned)S_ * D_ * 2);
    const unsigned off = (unsigned)((hd * (S_ / 16) + qblk * 2 + (r32 >> 4)) * 1024 + (hh * 16 + (r32 & 15)) * 16);
#pragma unroll
    for (int gp = 0; gp < 2; ++gp) {
        auto a = __builtin_amdgcn_permlane32_swap(w[4 * gp], w[4 * gp + 2], false, false);
        auto b = __builtin_amdgcn_permlane32_swap(w[4 * gp + 1], w[4 * gp + 3], false, false);
        __builtin_amdgcn_raw_buffer_store_b128((u32x4){a[0], b[0], a[1], b[1]}, rs, off + gp * 512, 0, SC1);
    }
}

extern "C" __global__ __launch_bounds__(512, 2) void k_main(const Params P) {
    extern __shared__ __attribute__((aligned(16))) char smem[];
    LAS char* lds = (LAS char*)smem;
    const int tid = threadIdx.x, lane = tid & 63, wid = __builtin_amdgcn_readfirstlane(tid >> 6);
    const int grp = blockIdx.x & 7, idx = blockIdx.x >> 3;
    const bool loader = wid >= 4;
    const int lw = wid & 3, wm = (wid >> 1) & 1, wn = wid & 1;
    const unsigned ring0 = (unsigned)(uintptr_t)lds, ringl = ring0 + lw * PIECE;
    const int i16 = idx & 15;
    const bool isM = idx < 16;
    const int fm = (grp >> 1) * 4 + (isM ? i16 & 3 : i16 >> 2), fn = (grp & 1) * 4 + (isM ? i16 >> 2 : i16 & 3);
    const int um = (grp & 1) * 4 + (i16 >> 2), un = (grp >> 1) * 4 + (i16 & 3);
    const int hd = (grp & 1) * 8 + (i16 & 7), qb = (grp >> 1) * 2 + (i16 >> 3);
    if (wid == 0) {
        u64 pc; unsigned rel, len;
        asm volatile("s_getpc_b64 %0\n1:\n\ts_mov_b32 %1, 1b-k_main\n\ts_mov_b32 %2, 2f-k_main\n\t.subsection 1\n2:\n\t.subsection 0" : "=s"(pc), "=s"(rel), "=s"(len));
        const unsigned off = (unsigned)((idx + 32 * (lane & 15)) * 128);
        dma4((const char*)(pc - rel), off < len ? off : 0u, ring0 + LDS_SINK);
    }
    u64 tag;
    asm volatile("global_load_dwordx2 %0, %1, off sc1" : "=v"(tag) : "v"((GAS u64*)P.flags() + FL_E) : "memory");

    if (tid < 8) *(LAS unsigned*)(lds + (isM ? LDS_MC : LDS_CTL) + tid * 4) = 0u;
    if (isM) {
        constexpr int NK = S_ / 64, SB = SB_M;
        const int pg = (grp >> 1) + 4 * (grp & 1);
        const int tm = um, tn = un, mi = pg * 16 + i16;
        const char* gm = (const char*)P.h() + tm * 256; const char* gn = (const char*)P.w1 + tn * 448;
        const int cls = (tm + tn) & 3;
        auto kof = [&](int u) { const int p0 = u - ((u - cls) & 3); return __builtin_amdgcn_readfirstlane((((p0 == 1 || p0 == 2) ? u : korder(u, cls, NK)) + 4 * pg) & (NK - 1)); };
        Loader<4, 7> L;
        {
            const int row = mi * 16 + wid * 2;
            const u32x4* px = (const u32x4*)(P.x + (size_t)row * D_);
            u32x4 a0 = px[lane], b0 = px[64 + lane], a1 = px[128 + lane], b1 = px[192 + lane];
            SCHED0();
            if (loader) {
                const unsigned c0 = (tn & 1) * 64;
                L.init_m<TR>(lw, lane, D_ * 2);
                L.init_n<TR>(lw, lane, F_ * 2, c0, c0 + 128, c0 + 256, (tn & 1) ? 0 : 384);
            }
            asm volatile("" : "+v"(a0), "+v"(b0), "+v"(a1), "+v"(b1));
            if (loader) L.issue_n<false>(gn + (size_t)kof(0) * (64 * F_ * 2), ringl);
            const __amdgpu_buffer_rsrc_t rh = rsrc(P.h(), (unsigned)S_ * D_ * 2);
            u32x4 oa0, ob0, oa1, ob1;
            const f32x4 s0 = ln_row(a0, b0, oa0, ob0), s1 = ln_row(a1, b1, oa1, ob1);
            __builtin_amdgcn_raw_buffer_store_b128(oa0, rh, (unsigned)(row * 2048 + lane * 16), 0, SC1);
            __builtin_amdgcn_raw_buffer_store_b128(ob0, rh, (unsigned)(row * 2048 + 1024 + lane * 16), 0, SC1);
            __builtin_amdgcn_raw_buffer_store_b128(oa1, rh, (unsigned)(row * 2048 + 2048 + lane * 16), 0, SC1);
            __builtin_amdgcn_raw_buffer_store_b128(ob1, rh, (unsigned)(row * 2048 + 3072 + lane * 16), 0, SC1);
            if (lane < 2) __builtin_amdgcn_raw_buffer_store_b128(__builtin_bit_cast(u32x4, lane ? s1 : s0), rsrc(P.stat(), S_ * 16), (unsigned)((row + lane) * 16), 0, SC1);
            SCHED0();
        }
        if (loader) {
            WAIT_VM(0);
            BARRIER();
            if (tid == 256 + 64) set_flag(P.flags(), FL_L + mi, tag);
#pragma unroll
            for (int u = 1; u < 3; ++u) L.issue_n<false>(gn + (size_t)kof(u) * (64 * F_ * 2), ringl + u * SB);
            if (lw == 0) poll_flags(P.flags(), lane < 16 ? FL_L + pg * 16 + lane : -1, tag);
            BARRIER();
#pragma unroll
            for (int u = 0; u < 3; ++u) L.issue_m<true>(gm + (size_t)kof(u) * (64 * D_ * 2), ringl + u * SB);
            WAIT_VM(8); BARRIER();
            WAIT_VM(4);
            int slot = 0, t = 0;
            auto issue = [&]() {
                const int k = kof(t + 3);
                L.issue_m<true>(gm + (size_t)k * (64 * D_ * 2), ringl + slot * SB);
                L.issue_n<false>(gn + (size_t)k * (64 * F_ * 2), ringl + slot * SB);
                slot = (slot == 2) ? 0 : slot + 1;
            };
            if (lw == 0) poll_flags(P.flags(), lane < 16 ? FL_L + ((pg + 1) & 7) * 16 + lane : -1, tag);
            BARRIER(); issue(); ++t;
            WAIT_VM(L.NPW); BARRIER(); issue(); ++t;
            WAIT_VM(L.NPW);
            if (lw == 0) { poll_flags(P.flags(), FL_L + lane, tag); poll_flags(P.flags(), FL_L + 64 + lane, tag); }
            BARRIER(); issue(); ++t;
#pragma unroll 1
            for (; t < NK - 3; ++t) {
                const int k = (korder(t + 3, cls, NK) + 4 * pg) & (NK - 1);
                L.turn<true, false, 11>(gm + (size_t)k * (64 * D_ * 2), gn + (size_t)k * (64 * F_ * 2), ringl + slot * SB);
                slot = (slot == 2) ? 0 : slot + 1;
            }
            {
                const int f = ((i16 * 4 + lw) * 7 + (lane < 56 ? lane >> 3 : 6)) * 2 + (grp & 1) + 896 * (grp >> 1);
                dma4((const char*)P.w2 + (grp >> 1) * 1024, (unsigned)(f * (S_ * 2) + (lane & 7) * 128), ring0 + LDS_SINK);
            }
            WAIT_VM(L.NPW + 1); BARRIER();
            WAIT_VM(1); BARRIER();
            BARRIER();
#pragma unroll
            for (int i = 0; i < 8; ++i)
                dma16<false, 0>((const char*)P.x + ((size_t)(fm * 128 + i * 16) * D_ + fn * 128 + lw * 32) * 2, (unsigned)((lane >> 2) * (D_ * 2) + (lane & 3) * 16), ring0 + LDS_X + (lw * 8 + i) * 1024);
            dma4((const char*)P.b2 + fm * 256, (unsigned)(lane * 4), ring0 + LDS_X + 32768);
        } else {
            const int fr = lane & 15, fq = lane >> 4;
            const int fb = tn * 224 + ((tn & 1) ? 32 : 0) + wn * 32, fh = tn * 224 + ((tn & 1) ? 0 : 192) + wn * 16;
            WAIT_VM(0);
            WAIT_LGKM0();
            BARRIER();
            f32x4 acc[4][7];
#pragma unroll
            for (int j = 0; j < 7; ++j) {
                const u32x2 bb = *(const u32x2*)(P.b1 + (j < 6 ? fb + (j >> 1) * 64 + (j & 1) * 16 : fh) + fq * 4);
                const f32x4 bv = {lo16(bb[0]), hi16(bb[0]), lo16(bb[1]), hi16(bb[1])};
#pragma unroll
                for (int i = 0; i < 4; ++i) acc[i][j] = bv;
            }
            Reader<TR, 128, TR, 224, 4, 7> rd; rd.init(ring0, lane, wm, wn);
            Regs<4, 7> R0;
            int slot = 0;
            BARRIER();
            __builtin_amdgcn_s_waitcnt(0x0070);
            BARRIER();
            rd.read(R0, 0);
            for (int t = 0; t < NK / 4; ++t) {
                stage_step<3, SB>(slot, rd, rd, R0, acc); stage_step<3, SB>(slot, rd, rd, R0, acc);
                stage_step<3, SB>(slot, rd, rd, R0, acc); stage_step<3, SB>(slot, rd, rd, R0, acc);
            }
            const __amdgpu_buffer_rsrc_t rs = rsrc(P.U(), (unsigned)D_ * F_ * 2);
            const unsigned df = (unsigned)(tm * 8 + wm * 4);
            const unsigned ob = (unsigned)((fb >> 5) * 64 + df) * 1024u + (unsigned)((((fq & 1) * 2 + (fq >> 1)) * 16 + fr) * 16);
            const unsigned oh = (unsigned)((fh >> 5) * 64 + df + (fq & 1)) * 1024u + (unsigned)(((wn * 2 + (fq >> 1)) * 16 + fr) * 16);
            LAS char* ul = lds + 128 * 132 + (wn * 8 + wm * 4) * PIECE + (((fq & 1) * 2 + (fq >> 1)) * 16 + fr) * 16;
            unsigned pk[4][7][2];
#pragma unroll
            for (int i = 0; i < 4; ++i) {
#pragma unroll
                for (int j = 0; j < 7; ++j) {
                    const f32x2 a = gelu2((f32x2){acc[i][j][0], acc[i][j][1]}), b = gelu2((f32x2){acc[i][j][2], acc[i][j][3]});
                    pk[i][j][0] = pack2(a.x, a.y); pk[i][j][1] = pack2(b.x, b.y);
                }
#pragma unroll
                for (int jp = 0; jp < 3; ++jp) {
                    const u32x4 w = pair16(pk[i][2 * jp][0], pk[i][2 * jp][1], pk[i][2 * jp + 1][0], pk[i][2 * jp + 1][1]);
                    __builtin_amdgcn_raw_buffer_store_b128(w, rs, ob + i * 1024 + jp * 2 * 65536, 0, SC1);
                    *(LAS u32x4*)(ul + jp * SB_F + i * PIECE) = w;
                }
                if (i & 1)
                    __builtin_amdgcn_raw_buffer_store_b128(pair16(pk[i - 1][6][0], pk[i - 1][6][1], pk[i][6][0], pk[i][6][1]), rs, oh + (i - 1) * 1024, 0, SC1);
                SCHED0();
            }
            WAIT_VM(0);
            if (lane == 0 && __hip_atomic_fetch_add((LAS unsigned*)(lds + LDS_MC), 1u, __ATOMIC_RELAXED, __HIP_MEMORY_SCOPE_WORKGROUP) == 3u)
                set_flag(P.flags(), FL_U + um * 32 + un, tag);
        }
    } else {
        constexpr int NK = D_ / 64, SB = SB_Q;
        const int tm = fm;
        if (loader) {
            Loader<4, 6> L; L.init_m<ROW>(lw, lane, D_ * 2);
            L.init_n<TR>(lw, lane, 3 * D_ * 2, qkv_head(fn, 0) * 64, qkv_head(fn, 1) * 64, qkv_head(fn, 2) * 64);
            const char* gm = (const char*)P.x + (size_t)tm * 128 * D_ * 2; const char* gn = (const char*)P.wqkv;
            const char* gh2 = (const char*)P.h() + (size_t)tm * 128 * D_ * 2;
            int slot = 0;
            auto issue = [&](int u) {
                const int k = (korder(u & (NK - 1), (tm + fn) & 3, NK) + 4 * (grp >> 1)) & (NK - 1);
                L.issue_m<false>(gm + k * 128, ringl + slot * SB);
                L.issue_n<false>(gn + (size_t)k * (64 * 3 * D_ * 2) + (u >= NK ? 16 * 64 : 0), ringl + slot * SB);
                slot = (slot == 2) ? 0 : slot + 1;
            };
            issue(0); issue(1);
            WAIT_VM(L.NPW); BARRIER();
            issue(2);
            int t = 0;
            WAIT_VM(L.NPW); BARRIER(); issue(3); ++t;
#pragma unroll 1
            for (int seg = 0; seg < 2; ++seg) {
                const int end = seg == 0 ? 12 : 2 * NK - 3;
#pragma unroll 1
                for (; t < end; ++t) {
                    const int u = t + 3, k = (korder(u & (NK - 1), (tm + fn) & 3, NK) + 4 * (grp >> 1)) & (NK - 1);
                    L.turn<true, false, 10>((u >= NK ? gh2 : gm) + k * 128, gn + (size_t)k * (64 * 3 * D_ * 2) + (u & NK) * 64, ringl + slot * SB);
                    slot = (slot == 2) ? 0 : slot + 1;
                }
                if (seg == 0 && lw == 0) {
                    poll_flags(P.flags(), lane < 8 ? FL_L + tm * 8 + lane : -1, tag);
                    dma16<true, LDS_QX>((const char*)P.stat() + tm * 2048, (unsigned)(lane * 16), ring0);
                    dma16<true, LDS_QX + 1024>((const char*)P.stat() + tm * 2048 + 1024, (unsigned)(lane * 16), ring0);
                }
            }
            poll_flags(P.flags(), attn_dep(hd, qb, lane), tag);
            WAIT_VM(0);
            BARRIER();
            attn_load<1>(P, ring0 + LDS_AK, hd, qb, lw, lane);
            attn_load<2>(P, ring0 + LDS_AV, hd, qb, lw, lane);
            BARRIER();
            attn_load<0>(P, ring0 + LDS_AQ, hd, qb, lw, lane);
            BARRIER();
        } else {
            const int fr = lane & 15, fq = lane >> 4;
            if (wid == 1) {
                const int mb = i16 * 4 + (lane & 3);
                const char* b; int o, n;
                if (mb < 14) { b = (const char*)P.w1; o = mb; n = 14; } else if (mb < 28) { b = (const char*)P.w2; o = mb - 14; n = 14; }
                else if (mb < 34) { b = (const char*)P.wqkv; o = mb - 28; n = 6; } else if (mb < 36) { b = (const char*)P.wo; o = mb - 34; n = 2; }
                else if (mb < 40) { b = (const char*)P.x; o = mb - 36; n = 4; } else if (mb < 44) { b = (const char*)P.out; o = mb - 40; n = 4; }
                else { b = P.ws; o = mb - 44; n = 0; }
                b += (size_t)o * 1048576 + (((lane & 4) && o == n - 1) ? 1048576 - 128 : mb * 4352);
                if ((lane & 4) && mb == 63) b = (const char*)P.b2;
                asm volatile("s_mov_b32 m0, %1\n\ts_nop 0\n\tglobal_load_lds_dword %0, off sc1" ::"v"(b), "s"(ring0 + LDS_SINK) : "memory");
            }
            Reader<ROW, 128, TR, 192, 4, 6> rd; rd.init(ring0, lane, wm, wn);
            rd.n.b = rd.n.a + 32 - wm * 32; rd.n.a += wm * 32;
            Regs<4, 6> R0;
            int slot = 0;
            WAIT_LGKM0();
            BARRIER();
            rd.read(R0, 0);
            const __amdgpu_buffer_rsrc_t rs = rsrc(P.qkv(), (unsigned)S_ * 3 * D_ * 2);
            const int s0 = tm * 128 + wm * 64;
            const unsigned so = (unsigned)(((s0 + fr) * 32 + ((fq & 1) ^ wm) * 16 + (fq >> 1) * 8) * 2);
            constexpr float QS = 0.17677669529663687f * 1.4426950408889634f;
            {
                f32x4 acc[5][6] = {};
                for (int t = 0; t < NK / 4; ++t) {
                    stage_step<3, SB>(slot, rd, rd, R0, acc); stage_step<3, SB>(slot, rd, rd, R0, acc);
                    stage_step<3, SB>(slot, rd, rd, R0, acc); stage_step<3, SB>(slot, rd, rd, R0, acc);
                }
                LAS char* ex = lds + LDS_QX + 2048;
                if (fr == 0) {
#pragma unroll
                    for (int jp = 0; jp < 3; ++jp) *(LAS f32x4*)(ex + wid * 256 + jp * 64 + fq * 16) = acc[4][2 * jp];
                }
                WAIT_LGKM0();
                CFENCE();
                __hip_atomic_store((LAS unsigned*)(lds + LDS_CTL + wid * 4), 1u, __ATOMIC_RELAXED, __HIP_MEMORY_SCOPE_WORKGROUP);
                f32x2 st[4];
#pragma unroll
                for (int i = 0; i < 4; ++i) st[i] = *(LAS f32x2*)(lds + LDS_QX + (wm * 64 + i * 16 + fr) * 16);
                while (__builtin_amdgcn_readfirstlane(__hip_atomic_load((LAS unsigned*)(lds + LDS_CTL + (wid ^ 2) * 4), __ATOMIC_RELAXED, __HIP_MEMORY_SCOPE_WORKGROUP)) == 0u) {}
                CFENCE();
#pragma unroll
                for (int jp = 0; jp < 3; ++jp) {
                    const f32x4 ca = acc[4][2 * jp], cb = *(LAS f32x4*)(ex + (wid ^ 2) * 256 + jp * 64 + fq * 16);
#pragma unroll
                    for (int i = 0; i < 4; ++i) {
                        const float r = st[i].y * (jp == 0 ? QS : 1.0f), mr = -st[i].x * r;
                        const f32x4 a = acc[i][2 * jp] * r + ca * mr, b = acc[i][2 * jp + 1] * r + cb * mr;
                        __builtin_amdgcn_raw_buffer_store_b128(pair16(pack2(a[0], a[1]), pack2(a[2], a[3]), pack2(b[0], b[1]), pack2(b[2], b[3])), rs,
                                                               so + (unsigned)((qkv_head(fn, jp) + wn) * (S_ * 64) + i * 1024), 0, SC1);
                    }
                }
            }
            {
                f32x4 acc[4][6] = {};
                for (int t = 0; t < NK / 4; ++t) {
                    if (__builtin_expect(t == 1 || t == 2, 0)) {
                        if (t == 1) WAIT_VM(0);
                        else if (tid == 0) set_flag(P.flags(), FL_Q + tm * 32 + fn, tag);
                    }
                    stage_step<3, SB>(slot, rd, rd, R0, acc); stage_step<3, SB>(slot, rd, rd, R0, acc);
                    stage_step<3, SB>(slot, rd, rd, R0, acc); stage_step<3, SB>(slot, rd, rd, R0, acc);
                }
#pragma unroll
                for (int i = 0; i < 4; ++i) {
                    const float sd = *(LAS float*)(lds + LDS_QX + (wm * 64 + i * 16 + fr) * 16 + 8);
#pragma unroll
                    for (int jp = 0; jp < 3; ++jp) {
                        const float r = sd * (jp == 0 ? QS : 1.0f);
                        const f32x4 a = acc[i][2 * jp] * r, b = acc[i][2 * jp + 1] * r;
                        __builtin_amdgcn_raw_buffer_store_b128(pair16(pack2(a[0], a[1]), pack2(a[2], a[3]), pack2(b[0], b[1]), pack2(b[2], b[3])), rs,
                                                               so + (unsigned)((qkv_head(fn, jp) + 16 + wn) * (S_ * 64) + i * 1024), 0, SC1);
                    }
                }
            }
        }
    }

    constexpr int SB = SB_F, NS = NS_F;
    constexpr size_t OS = 2 * (S_ / 16) * 1024, WOS = 64 * D_ * 2, W2S = 64 * S_ * 2, US = 2 * 65536;
    const int tile = fm * 8 + fn, cls = (fm + fn) & 3, nf = isM ? NF_M : NF_Q;
    const int uh = ((fm >> 1) & 1) ^ (isM ? 0 : 1);
    auto fof = [&](int i) {
        int r = korder(i - 7, cls, nf - 7) + (grp & 1) * ((nf - 7) >> 1);
        r = r >= nf - 7 ? r - (nf - 7) : r;
        const int im = (fm & 1) ? (i < 3 ? i + 4 : i - 3) : i;
        const int j = i < 7 ? 7 * uh + (isM ? im : korder(i, cls, 7)) : (isM ? 14 + NA : 14) + r, k = 14 * (grp >> 1) + j;
        return __builtin_amdgcn_readfirstlane(k >= 56 ? k - 56 : k);
    };
    const char* g2 = (const char*)P.w2 + fm * 256;
    Loader<4, 4> L;
    if (loader) L.init_m<TR>(lw, lane, S_ * 2);
    auto preload_w2 = [&]() {
        int k0 = fof(0), k1 = fof(1);
        asm volatile("" : "+s"(k0), "+s"(k1));
        L.issue_m<false>(g2 + k0 * W2S, ringl); L.issue_m<false>(g2 + k1 * W2S, ringl + SB);
    };
    if (isM) {
        if (loader) { preload_w2(); L.issue_m<false>(g2 + fof(2) * W2S, ringl + 2 * SB); WAIT_VM(0); }
        WAIT_LGKM0();
        BARRIER();
    } else {
        if (loader) { WAIT_VM(0); __builtin_amdgcn_s_setprio(1); }
        WAIT_LGKM0();
        BARRIER();
#pragma unroll 1
        for (int it = 0; it < 2; ++it) {
            f32x16 sc[8];
            int ln = lane;
            asm volatile("" : "+v"(ln));
            const int dg = (ln & 31) - 4 * (ln >> 5);
            const unsigned up = dg < 0 ? ~0u : ~1u << dg;
            int nbad = qb == 0 ? 8 - wid : 0;
            asm volatile("" : "+s"(nbad));
            SCHED0();
            const float m = attn_scores(lds + (it ? LDS_BK : LDS_AK), lds + LDS_AQ, nbad, wid, ln, up, sc);
            SCHED0();
            WAIT_VM(0);
            BARRIER();
            if (tid == 0) set_flag(P.flags(), it == 0 ? FL_Q + fm * 32 + fn + 8 : FL_O + qb * 32 + hd, tag);
            if (it == 1 && loader) preload_w2();
            f32x16 oacc = {};
            float lsum;
            attn_pv(lds + (it ? LDS_BV : LDS_AV), nbad, wid, ln, up, sc, m, oacc, lsum);
            attn_out(P, hd + 16 * it, qb * 8 + wid, ln, lsum, oacc);
            if (it == 0) {
                if (loader) {
                    __builtin_amdgcn_s_setprio(0);
                    int h2 = hd + 16, q2 = qb, w2 = lw;
                    asm volatile("" : "+s"(h2), "+s"(q2), "+s"(w2));
                    poll_flags(P.flags(), attn_dep(h2, q2, ln), tag);
                    attn_load<0>(P, ring0 + LDS_AQ, h2, q2, w2, ln);
                    attn_load<1>(P, ring0 + LDS_BK, h2, q2, w2, ln);
                    attn_load<2>(P, ring0 + LDS_BV, h2, q2, w2, ln);
                    WAIT_VM(8);
                    __builtin_amdgcn_s_setprio(1);
                }
                BARRIER();
            }
        }
        WAIT_LGKM0();
    }
    const int own = isM ? 0 : 1;
    if (loader) {
        const char* go = (const char*)P.o() + fm * 8192 + own * 4096;
        const char* gw = (const char*)P.wo + fn * 256;
        const char* gu = (const char*)P.U() + fn * 8192;
        __builtin_amdgcn_s_setprio(0);
        L.init_n<FRAG>(lw, lane, US / 2);
        auto issue_f = [&](int u) {
            const int k = fof(u);
            L.issue_m<false>(g2 + k * W2S, ringl + (u & (NS - 1)) * SB); L.issue_n<true>(gu + k * US, ringl + (u & (NS - 1)) * SB);
        };
        const int ulo = isM ? ((14 + NA) * 2) / 7 : 4, uhi = isM ? 15 : ((14 + NA) * 2 - 1) / 7;
        const int udep = (lane >= ulo && lane <= uhi) ? FL_U + fn * 32 + (((grp >> 1) * 4 + lane) & 15) : -1;
        const int odep = lane < 32 ? FL_O + (fm >> 1) * 32 + lane : lane < 48 ? FL_U + fn * 32 + lane - 32 : -1, pdep = lane == 0 ? FL_P + tile * 32 + own : -1;
        u64 pk;
        if (isM) { BARRIER(); BARRIER(); }
        poll_flags(P.flags(), lane < 2 ? FL_U + fn * 32 + (grp >> 1) * 4 + 2 * uh + lane : -1, tag);
        if (isM) { issue_f(3); issue_f(4); }
        else {
#pragma unroll
            for (int u = 0; u < 2; ++u) L.issue_n<true>(gu + fof(u) * US, ringl + u * SB);
            WAIT_VM(4); BARRIER();
            issue_f(2); issue_f(3);
            WAIT_VM(16); BARRIER(); issue_f(4);
            WAIT_VM(16); BARRIER();
        }
        peek_issue(pk, P.flags(), udep); issue_f(5);
        WAIT_VM(16); BARRIER(); issue_f(6);
        WAIT_VM(16);
        int nxt = 7;
        if (!peek_ok(pk, udep, tag)) {
            bool uready = false;
#pragma unroll 1
            for (int t = 3; t < 7; ++t) {
                if (t == 6 && !uready) { poll_flags(P.flags(), udep, tag); uready = true; for (; nxt < 10; ++nxt) issue_f(nxt); }
                if (nxt - (t + 2) >= 2) WAIT_VM(16); else WAIT_VM(0);
                BARRIER();
                if (!uready) uready = flags_set(P.flags(), udep, tag);
                if (uready) for (; nxt <= t + NS; ++nxt) issue_f(nxt);
            }
        }
#pragma unroll 1
        for (; nxt < nf - 2; ++nxt) {
            int r = korder(nxt - 7, cls, nf - 7) + (grp & 1) * ((nf - 7) >> 1);
            r = r >= nf - 7 ? r - (nf - 7) : r;
            int k = 14 * (grp >> 1) + (isM ? 14 + NA : 14) + r;
            k = k >= 56 ? k - 56 : k;
            L.turn<false, true, 16>(g2 + k * W2S, gu + k * US, ringl + (nxt & (NS - 1)) * SB);
        }
        const unsigned xo = (unsigned)(((fm * 128 + (lane >> 2)) * D_ + fn * 128 + lw * 32 + (lane & 3) * 8) * 2);
        constexpr unsigned ROWS16 = 16 * D_ * 2;
        WAIT_VM(16); BARRIER(); peek_issue(pk, P.flags(), odep);
        issue_f(nf - 2);
        WAIT_VM(16); BARRIER(); issue_f(nf - 1);
        L.init_n<TR>(lw, lane, D_ * 2);
        const unsigned oo = (unsigned)(lw * 1024 + lane * 16);
        const unsigned oo2 = oo + (unsigned)(OS / 2);
        const char *po, *pw; unsigned pd;
        auto addr_w = [&](int i) {
            const int k = (korder(i, cls, KW) + 4 * (grp >> 1)) & (KW - 1);
            po = go + k * OS; pw = gw + k * WOS; pd = ringl + ((nf + i) & (NS - 1)) * SB;
            asm volatile("" : "+s"(po), "+s"(pw), "+s"(pd));
        };
        auto issue_w = [&]() {
            asm volatile("s_add_u32 m0, %2, %3\n\ts_nop 0\n\tglobal_load_lds_dwordx4 %4, %0 sc1\n\ts_addk_i32 m0, 8448\n\ts_nop 0\n\tglobal_load_lds_dwordx4 %5, %0 sc1\n\t"
                         "s_add_u32 m0, %2, 16896\n\ts_nop 0\n\tglobal_load_lds_dwordx4 %6, %1\n\ts_addk_i32 m0, 4224\n\ts_nop 0\n\tglobal_load_lds_dwordx4 %7, %1\n\t"
                         "s_addk_i32 m0, 4224\n\ts_nop 0\n\tglobal_load_lds_dwordx4 %8, %1\n\ts_addk_i32 m0, 4224\n\ts_nop 0\n\tglobal_load_lds_dwordx4 %9, %1"
                         ::"s"(po), "s"(pw), "s"(pd), "s"(own * 4 * PIECE), "v"(oo), "v"(oo2), "v"(L.on[0]), "v"(L.on[1]), "v"(L.on[2]), "v"(L.on[3]) : "scc", "memory");
        };
#define TURN_W(i, w) do { addr_w(i); WAIT_VM(w); BARRIER(); issue_w(); } while (0)
        addr_w(0);
        WAIT_VM(16);
        if (!peek_ok(pk, odep, tag)) poll_flags(P.flags(), odep, tag);
        BARRIER(); issue_w();
        TURN_W(1, 14); TURN_W(2, 12); TURN_W(3, 12);
        TURN_W(4, 12);
        {
            u32x4 t0, t1, t2, t3;
            asm volatile("ds_read_b128 %0, %4\n\tds_read_b128 %1, %4 offset:1024\n\tds_read_b128 %2, %4 offset:2048\n\tds_read_b128 %3, %4 offset:3072\n\ts_waitcnt lgkmcnt(0)"
                         : "=&v"(t0), "=&v"(t1), "=&v"(t2), "=&v"(t3) : "v"(ring0 + LDS_EX + lw * 4096 + lane * 16) : "memory");
            const __amdgpu_buffer_rsrc_t ro = rsrc(P.out, (unsigned)S_ * D_ * 2);
            const unsigned so = xo + (1 - own) * 4 * ROWS16;
            __builtin_amdgcn_raw_buffer_store_b128(t0, ro, so, 0, SC1); __builtin_amdgcn_raw_buffer_store_b128(t1, ro, so + ROWS16, 0, SC1);
            __builtin_amdgcn_raw_buffer_store_b128(t2, ro, so + 2 * ROWS16, 0, SC1); __builtin_amdgcn_raw_buffer_store_b128(t3, ro, so + 3 * ROWS16, 0, SC1);
        }
        TURN_W(5, 12); TURN_W(6, 12);
        TURN_W(7, 0);
        if (tid == 256) set_flag(P.flags(), FL_P + tile * 32 + 1 - own, tag);
        TURN_W(8, 12); TURN_W(9, 12); TURN_W(10, 12); TURN_W(11, 12);
        TURN_W(12, 12); TURN_W(13, 12); TURN_W(14, 12);
        TURN_W(15, 12);
#undef TURN_W
        peek_issue(pk, P.flags(), pdep);
        WAIT_VM(13); BARRIER();
        WAIT_VM(7); BARRIER();
        WAIT_VM(0);
        auto load_partial = [&]() {
#pragma unroll
            for (int i = 0; i < 4; ++i) dma16<true, 0>((const char*)P.out + (own * 4 + i) * ROWS16, xo, ring0 + LDS_EX + (lw * 4 + i) * 1024);
        };
        const bool early = peek_ok(pk, pdep, tag);
        if (early) load_partial();
        BARRIER();
        BARRIER();
        if (!early) { poll_flags(P.flags(), pdep, tag); load_partial(); }
        WAIT_VM(0); BARRIER();
        if (blockIdx.x == 0 && lw == 0) {
#pragma unroll 1
            for (int i = 0; i < 4; ++i) poll_flags(P.flags(), FL_O + i * 64 + lane, tag);
            if (lane == 0) set_flag(P.flags(), FL_E, tag);
        }
    } else {
        const __amdgpu_buffer_rsrc_t ro = rsrc(P.out, (unsigned)S_ * D_ * 2);
        f32x4 acc[4][4];
        if (isM) {
            LAS char* xp = lds + LDS_X + (wn * 16 + wm * 4) * 1024 + (lane & 15) * 64 + (((lane >> 4) & 1) * 2 + (lane >> 5)) * 16;
            u32x4 xv[4][2]; unsigned bv[4];
#pragma unroll
            for (int i = 0; i < 4; ++i) {
                xv[i][0] = *(LAS u32x4*)(xp + i * 1024); xv[i][1] = *(LAS u32x4*)(xp + 8192 + i * 1024);
                bv[i] = *(LAS u16*)(lds + LDS_X + 32768 + (wm * 64 + i * 16 + (lane & 15)) * 2);
            }
            SCHED0();
#pragma unroll
            for (int i = 0; i < 4; ++i)
#pragma unroll
                for (int jp = 0; jp < 2; ++jp) {
                    auto e0 = __builtin_amdgcn_permlane16_swap(xv[i][jp][0], xv[i][jp][2], false, false);
                    auto e1 = __builtin_amdgcn_permlane16_swap(xv[i][jp][1], xv[i][jp][3], false, false);
                    acc[i][2 * jp] = (f32x4){lo16(e0[0]), hi16(e0[0]), lo16(e1[0]), hi16(e1[0])} + lo16(bv[i]);
                    acc[i][2 * jp + 1] = (f32x4){lo16(e0[1]), hi16(e0[1]), lo16(e1[1]), hi16(e1[1])} + lo16(bv[i]);
                }
            pin_acc<4, 4, 4>(acc);
        } else {
            float z = 0.f;
            asm volatile("" : "+v"(z));
#pragma unroll
            for (int i = 0; i < 4; ++i)
#pragma unroll
                for (int j = 0; j < 4; ++j) acc[i][j] = (f32x4){z, z, z, z};
        }
        pin_acc<4, 4, 4>(acc);
        Reader<TR, 128, FRAG, 128, 4, 4> rf;
        HalfReader rw;
        Regs<4, 4> R0;
        LAS char* exl = lds + LDS_EX + wn * 8192 + lane * 16;
        int slot = 0;
#define STEP(a, b) stage_step<NS, SB>(slot, a, b, R0, acc)
        rf.init(ring0, lane, wm, wn);
        WAIT_LGKM0();
        if (!isM) BARRIER();
        rf.read(R0, 0);
        for (int t = 0; t < (nf - 2) / 4; ++t) {
            if (__builtin_expect(!isM && (t == 1 || t == 2), 0)) {
                if (t == 1) WAIT_VM(0);
                else if (tid == 0) set_flag(P.flags(), FL_O + qb * 32 + hd + 16, tag);
            }
            STEP(rf, rf); STEP(rf, rf); STEP(rf, rf); STEP(rf, rf);
        }
        static_assert((NF_M & 3) == (NF_Q & 3), "NA must be odd");
#pragma unroll
        for (int t = 0; t < ((NF_M - 2) & 3); ++t) STEP(rf, rf);
        STEP(rf, rf);
        const int kq = wm != own ? 1 : 0;
        rw.init(ring0 + slot * SB, lane, own, kq, wn);
        STEP(rf, rw);
        if (kq) {
#pragma unroll
            for (int i = 0; i < 4; ++i)
#pragma unroll
                for (int jp = 0; jp < 2; ++jp) {
                    const f32x4 a = acc[i][2 * jp], b = acc[i][2 * jp + 1];
                    *(LAS u32x4*)(exl + jp * 4096 + i * 1024) = (u32x4){pack2(a[0], a[1]), pack2(a[2], a[3]), pack2(b[0], b[1]), pack2(b[2], b[3])};
                    acc[i][2 * jp] = (f32x4){0.f, 0.f, 0.f, 0.f}; acc[i][2 * jp + 1] = (f32x4){0.f, 0.f, 0.f, 0.f};
                }
        }
        pin_acc<4, 4, 4>(acc);
        Regs<4, 4> R1;
        for (int t = 0; t < 4; ++t) {
            half_step<NS, SB>(slot, rw, R0, R1, acc); half_step<NS, SB>(slot, rw, R1, R0, acc);
            half_step<NS, SB>(slot, rw, R0, R1, acc); half_step<NS, SB>(slot, rw, R1, R0, acc);
        }
#pragma unroll
        for (int i = 0; i < 2; ++i)
#pragma unroll
            for (int j = 0; j < 4; ++j) *(LAS f32x4*)(lds + (wid * 8 + i * 4 + j) * 1024 + lane * 16) = acc[2 + i][j];
        WAIT_LGKM0();
        BARRIER();
        const unsigned fo = (unsigned)(((fm * 128 + own * 64 + kq * 32 + (lane & 15)) * D_ + fn * 128 + wn * 64 + ((lane >> 4) & 1) * 16 + (lane >> 5) * 8) * 2);
#pragma unroll
        for (int i = 0; i < 2; ++i)
#pragma unroll
            for (int jp = 0; jp < 2; ++jp) {
                const u32x4 e = *(LAS u32x4*)(exl + jp * 4096 + (kq * 2 + i) * 1024);
                const f32x4 a = acc[i][2 * jp] + *(LAS f32x4*)(lds + ((wid ^ 2) * 8 + i * 4 + 2 * jp) * 1024 + lane * 16) + (f32x4){lo16(e[0]), hi16(e[0]), lo16(e[1]), hi16(e[1])};
                const f32x4 b = acc[i][2 * jp + 1] + *(LAS f32x4*)(lds + ((wid ^ 2) * 8 + i * 4 + 2 * jp + 1) * 1024 + lane * 16) + (f32x4){lo16(e[2]), hi16(e[2]), lo16(e[3]), hi16(e[3])};
                __builtin_amdgcn_raw_buffer_store_b128(pair16(pack2(a[0], a[1]), pack2(a[2], a[3]), pack2(b[0], b[1]), pack2(b[2], b[3])), ro, fo + i * (16 * D_ * 2) + jp * 64, 0, SC1);
            }
#undef STEP
    }
}

extern "C" void kernel_launch(void* const* d_in, const int* in_sizes, int n_in, void* d_out, int out_size, void* d_ws, size_t ws_size, hipStream_t stream) {
    char* ws = (char*)d_ws;
    Params P;
    P.x = (const u16*)d_in[0]; P.wqkv = (const u16*)d_in[1]; P.wo = (const u16*)d_in[2];
    P.w1 = (const u16*)d_in[4]; P.b1 = (const u16*)d_in[5]; P.w2 = (const u16*)d_in[6]; P.b2 = (const u16*)d_in[7];
    P.out = (u16*)d_out; P.ws = ws;
    (void)hipFuncSetAttribute((const void*)k_main, hipFuncAttributeMaxDynamicSharedMemorySize, LDS_BYTES);
    k_main<<<256, 512, LDS_BYTES, stream>>>(P);
}
```

```cpp
#include <hip/hip_runtime.h>
#include <hip/hip_bf16.h>
#include <stdint.h>

#define LAS __attribute__((address_space(3)))
#define GAS __attribute__((address_space(1)))
typedef __bf16 bf16x8 __attribute__((ext_vector_type(8)));
typedef __bf16 bf16x4 __attribute__((ext_vector_type(4)));
typedef __bf16 bf16x2 __attribute__((ext_vector_type(2)));
typedef float f32x2 __attribute__((ext_vector_type(2)));
typedef float f32x4 __attribute__((ext_vector_type(4)));
typedef float f32x16 __attribute__((ext_vector_type(16)));
typedef unsigned u32x4 __attribute__((ext_vector_type(4)));
typedef unsigned u32x2 __attribute__((ext_vector_type(2)));
typedef unsigned short u16;
typedef unsigned long long u64;

constexpr int S_ = 2048, D_ = 1024, F_ = 3584, NH = 32;

constexpr size_t WS_FLAGS = 0;
constexpr size_t WS_STAT  = 1 << 16;
constexpr size_t WS_QKV   = 1 << 17;
constexpr size_t WS_U     = WS_QKV + (size_t)S_ * 3 * D_ * 2;
constexpr int FL_E = 0;
constexpr int FL_L = 32;
constexpr int FL_U = 8 * 32;
constexpr int FL_Q = 16 * 32;
constexpr int FL_O = 32 * 32;
constexpr int FL_P = 40 * 32;

#define WAIT_VM(n) asm volatile("s_waitcnt vmcnt(%0)" ::"n"(n) : "memory")
#define WAIT_LGKM0() __builtin_amdgcn_s_waitcnt(0xC07F)
#define CFENCE() asm volatile("" ::: "memory")
#define BARRIER() do { CFENCE(); __builtin_amdgcn_s_barrier(); CFENCE(); } while (0)
#define SCHED0() __builtin_amdgcn_sched_barrier(0)
#define RLX_AGENT __ATOMIC_RELAXED, __HIP_MEMORY_SCOPE_AGENT
constexpr int SC1 = 16;

__device__ __forceinline__ float bf2f(u16 v) { return __uint_as_float((unsigned)v << 16); }
__device__ __forceinline__ float lo16(unsigned v) { return __uint_as_float(v << 16); }
__device__ __forceinline__ float hi16(unsigned v) { return __uint_as_float(v & 0xffff0000u); }
__device__ __forceinline__ unsigned pack2(float lo, float hi) {
    f32x2 v = {lo, hi};
    bf16x2 b = __builtin_convertvector(v, bf16x2);
    return __builtin_bit_cast(unsigned, b);
}
__device__ __forceinline__ float rbf(float x) { return __uint_as_float(pack2(x, 0.f) << 16); }
template <int CTRL, int RMASK> __device__ __forceinline__ float dpp_add(float v) {
    return v + __builtin_bit_cast(float, __builtin_amdgcn_update_dpp(0, __builtin_bit_cast(int, v), CTRL, RMASK, 0xf, false));
}
__device__ __forceinline__ float wave_sum(float v) {
    v = dpp_add<0x111, 0xf>(v); v = dpp_add<0x112, 0xf>(v); v = dpp_add<0x114, 0xf>(v); v = dpp_add<0x118, 0xf>(v);
    v = dpp_add<0x142, 0xa>(v); v = dpp_add<0x143, 0xc>(v);
    return __builtin_bit_cast(float, __builtin_amdgcn_readlane(__builtin_bit_cast(int, v), 63));
}
__device__ __forceinline__ __amdgpu_buffer_rsrc_t rsrc(const void* p, unsigned bytes) { return __builtin_amdgcn_make_buffer_rsrc((void*)p, 0, bytes, 0x00020000); }

enum { ROW = 0, TR = 1, FRAG = 2 };
constexpr int PIECE = 1056, BLK = 8 * PIECE;
template <int T, int X> struct Op {
    static constexpr int BYTES = X * 132;
    __device__ static __forceinline__ unsigned src_off(int blk, int p, int lane, unsigned ld, unsigned cb) {
        const int pos = lane >> 3;
        if (T == TR) {
            if (X == 224 && blk == 3) {
                const int ps = lane >> 2, g = (p & 1) | ((ps >> 3) << 1), r = 32 * (p >> 1) + 8 * g + 4 * ((ps >> 2) & 1) + (ps & 3);
                return (unsigned)r * ld + cb + (unsigned)((lane & 3) * 16);
            }
            const int g = (p >> 2) | (pos & 2), r = 32 * (pos >> 2) + 8 * g + 4 * (pos & 1) + (p & 3);
            return (unsigned)r * ld + cb + (unsigned)((lane & 7) * 16);
        } else if (T == ROW) {
            const int j = ((pos >> 1) & 1) | ((pos & 1) << 1) | (pos & 4);
            return (unsigned)(blk * 64 + j * 8 + p) * ld + (unsigned)((lane & 7) * 16);
        } else {
            return (unsigned)blk * ld + (unsigned)(p * 1024 + lane * 16);
        }
    }
};
template <bool SC, int IMM> __device__ __forceinline__ void dma16(const char* base, unsigned off, unsigned l) {
    if (SC) asm volatile("s_add_u32 m0, %2, %3\n\ts_nop 0\n\tglobal_load_lds_dwordx4 %0, %1 sc1" ::"v"(off), "s"(base), "s"(l), "n"(IMM) : "scc", "memory");
    else    asm volatile("s_add_u32 m0, %2, %3\n\ts_nop 0\n\tglobal_load_lds_dwordx4 %0, %1" ::"v"(off), "s"(base), "s"(l), "n"(IMM) : "scc", "memory");
}
#define DMA_FIRST(sc) "s_add_u32 m0, %1, %2\n\ts_nop 0\n\tglobal_load_lds_dwordx4 %3, %0" sc
#define DMA_NEXT(n, sc) "\n\ts_addk_i32 m0, 4224\n\ts_nop 0\n\tglobal_load_lds_dwordx4 %" #n ", %0" sc
#define DMA_G4(sc) DMA_FIRST(sc) DMA_NEXT(4, sc) DMA_NEXT(5, sc) DMA_NEXT(6, sc)
template <bool SC, int IMM, int N> __device__ __forceinline__ void dma_group(const char* base, const unsigned (&o)[N], unsigned l) {
    static_assert(4 * PIECE == 4224 && (N == 4 || ((N == 6 || N == 7) && !SC)), "dma_group");
    if constexpr (N == 4 && SC) asm volatile(DMA_G4(" sc1") ::"s"(base), "s"(l), "n"(IMM), "v"(o[0]), "v"(o[1]), "v"(o[2]), "v"(o[3]) : "scc", "memory");
    else if constexpr (N == 4) asm volatile(DMA_G4("") ::"s"(base), "s"(l), "n"(IMM), "v"(o[0]), "v"(o[1]), "v"(o[2]), "v"(o[3]) : "scc", "memory");
    else if constexpr (N == 6) asm volatile(DMA_G4("") DMA_NEXT(7, "") DMA_NEXT(8, "") ::"s"(base), "s"(l), "n"(IMM), "v"(o[0]), "v"(o[1]), "v"(o[2]), "v"(o[3]), "v"(o[4]), "v"(o[5]) : "scc", "memory");
    else asm volatile(DMA_G4("") DMA_NEXT(7, "") DMA_NEXT(8, "") DMA_NEXT(9, "") ::"s"(base), "s"(l), "n"(IMM), "v"(o[0]), "v"(o[1]), "v"(o[2]), "v"(o[3]), "v"(o[4]), "v"(o[5]), "v"(o[6]) : "scc", "memory");
}
__device__ __forceinline__ void dma4(const char* base, unsigned off, unsigned l) {
    asm volatile("s_mov_b32 m0, %2\n\ts_nop 0\n\tglobal_load_lds_dword %0, %1" ::"v"(off), "s"(base), "s"(l) : "memory");
}
__device__ __forceinline__ void dma16x4(const char* base, unsigned off, unsigned l) {
    asm volatile("s_mov_b32 m0, %2\n\ts_nop 0\n\tglobal_load_lds_dwordx4 %0, %1 sc1\n\tglobal_load_lds_dwordx4 %0, %1 offset:1024 sc1\n\t"
                 "global_load_lds_dwordx4 %0, %1 offset:2048 sc1\n\tglobal_load_lds_dwordx4 %0, %1 offset:3072 sc1" ::"v"(off), "s"(base), "s"(l) : "memory");
}

template <int T, int X> struct Addr {
    unsigned a, b;
    static constexpr int NV = (X == 224 || X == 192) ? 2 : 1;
    __device__ __forceinline__ void init(unsigned base, int lane, int w) {
        const int g = lane >> 4;
        b = 0;
        if (T == TR) {
            const int q = (lane & 15) >> 2, p = lane & 3;
            a = base + (unsigned)((q | ((g & 1) << 2)) * PIECE + (g >> 1) * 256 + p * 8 + (X == 128 ? w * BLK : w * 64));
            b = base + (unsigned)(3 * BLK + (g & 1) * PIECE + (g >> 1) * 512 + q * 64 + w * 32 + p * 8);
        } else if (T == ROW) {
            const int r = lane & 15;
            a = base + (unsigned)(w * BLK + (r & 7) * PIECE + (r >> 3) * 256 + g * 16);
        } else {
            a = base + (unsigned)(w * 4 * PIECE + lane * 16);
        }
    }
    __device__ __forceinline__ void step(unsigned d) { a += d; if (NV == 2) b += d; }
    __device__ __forceinline__ bf16x8 read(int n, int kk) const {
        if (T == TR) {
            LAS char* p; int t;
            if (X == 224 && n == 6) { p = (LAS char*)(uintptr_t)b + kk * 2 * PIECE; t = 256; }
            else if (X == 192) { p = (LAS char*)(uintptr_t)((n & 1) ? b : a) + kk * 512 + (n >> 1) * BLK; t = 128; }
            else { p = (LAS char*)(uintptr_t)a + kk * 512 + (X == 128 ? n * 32 : (n >> 1) * BLK + (n & 1) * 32); t = 128; }
            const bf16x4 lo = __builtin_amdgcn_ds_read_tr16_b64_v4bf16((LAS bf16x4*)p);
            const bf16x4 hi = __builtin_amdgcn_ds_read_tr16_b64_v4bf16((LAS bf16x4*)(p + t));
            return __builtin_shufflevector(lo, hi, 0, 1, 2, 3, 4, 5, 6, 7);
        } else if (T == ROW) {
            return *(LAS bf16x8*)((LAS char*)(uintptr_t)a + (n & 1) * 128 + (n >> 1) * 512 + kk * 64);
        } else {
            return *(LAS bf16x8*)((LAS char*)(uintptr_t)a + kk * BLK + n * PIECE);
        }
    }
};
template <int FM, int FN> struct Regs { bf16x8 m[FM], n[FN]; };
#define MFMA16(a, b, c) __builtin_amdgcn_mfma_f32_16x16x32_bf16(a, b, c, 0, 0, 0)

template <int TM, int BM, int TN, int BN, int FM, int FN> struct Reader {
    Addr<TM, BM> m; Addr<TN, BN> n;
    static constexpr int NDS = FM * (TM == TR ? 2 : 1) + FN * (TN == TR ? 2 : 1), NV = Addr<TM, BM>::NV + Addr<TN, BN>::NV;
    __device__ __forceinline__ void init(unsigned slot_base, int lane, int wm, int wn) { m.init(slot_base, lane, wm); n.init(slot_base + BM * 132, lane, wn); }
    __device__ __forceinline__ void read(Regs<FM, FN>& R, int kk) const {
#pragma unroll
        for (int i = 0; i < FM; ++i) R.m[i] = m.read(i, kk);
#pragma unroll
        for (int j = 0; j < FN; ++j) R.n[j] = n.read(j, kk);
    }
    __device__ __forceinline__ void step(unsigned d) { m.step(d); n.step(d); }
};
template <int AM, int FM, int FN> __device__ __forceinline__ void mma(f32x4 (&acc)[AM][FN], const Regs<FM, FN>& R) {
    const u32x4 one = {0x3f803f80u, 0x3f803f80u, 0x3f803f80u, 0x3f803f80u};
#pragma unroll
    for (int j = 0; j < FN; ++j) {
        if (AM > FM && !(j & 1)) acc[AM - 1][j] = MFMA16(R.n[j], __builtin_bit_cast(bf16x8, one), acc[AM - 1][j]);
#pragma unroll
        for (int ii = 0; ii < FM; ++ii) { const int i = (j & 1) ? FM - 1 - ii : ii; acc[i][j] = MFMA16(R.n[j], R.m[i], acc[i][j]); }
    }
}
template <int AM, int FM, int FN> __device__ __forceinline__ void pin_acc(f32x4 (&acc)[AM][FN]) {
#pragma unroll
    for (int j = 0; j < FN; ++j) {
        asm volatile("" : "+v"(acc[0][j]), "+v"(acc[1][j]), "+v"(acc[2][j]), "+v"(acc[3][j]));
        if (AM > FM && !(j & 1)) asm volatile("" : "+v"(acc[AM - 1][j]));
    }
}
template <int NDS, int NV, int NMF> __device__ __forceinline__ void interleave() {
#pragma unroll
    for (int i = 0; i < NMF; ++i) {
        __builtin_amdgcn_sched_group_barrier(0x8, 1, 0);
        if (i < NDS) __builtin_amdgcn_sched_group_barrier(0x80, 1, 0);
        if (i >= NMF - NV) __builtin_amdgcn_sched_group_barrier(0x2, 1, 0);
    }
}
template <int NS, int SB, class RD, class RDN, int AM, int FM, int FN>
__device__ __forceinline__ void stage_step(int& slot, RD& rd, RDN& rn, Regs<FM, FN>& R0, f32x4 (&acc)[AM][FN]) {
    constexpr int NMF = FM * FN + (AM - FM) * (FN / 2);
    Regs<FM, FN> R1;
    const unsigned d = (slot + 1 == NS) ? (unsigned)(-(NS - 1) * SB) : (unsigned)SB;
    slot = (slot + 1 == NS) ? 0 : slot + 1;
    rd.read(R1, 1);
    rn.step(d);
    mma(acc, R0);
    interleave<RD::NDS, RDN::NV, NMF>();
    SCHED0();
    pin_acc<AM, FM, FN>(acc);
    WAIT_LGKM0();
    BARRIER();
    SCHED0();
    rn.read(R0, 0);
    mma(acc, R1);
    interleave<RDN::NDS, 0, NMF>();
    SCHED0();
    pin_acc<AM, FM, FN>(acc);
    SCHED0();
}

struct HalfReader {
    unsigned m0, m1; Addr<TR, 128> n;
    static constexpr int NDS = 12, NV = 3;
    __device__ __forceinline__ void init(unsigned slot_base, int lane, int own, int kq, int wn) {
        const unsigned a = slot_base + (unsigned)(own * 4 * PIECE + kq * BLK + lane * 16);
        m0 = a + kq * 2 * PIECE; m1 = a - kq * 2 * PIECE;
        n.init(slot_base + 128 * 132 + kq * 512, lane, wn);
    }
    __device__ __forceinline__ void read(Regs<4, 4>& R, int) const {
#pragma unroll
        for (int i = 0; i < 4; ++i) R.m[i] = *(LAS bf16x8*)((LAS char*)(uintptr_t)(i < 2 ? m0 : m1) + i * PIECE);
#pragma unroll
        for (int j = 0; j < 4; ++j) R.n[j] = n.read(j, 0);
    }
    __device__ __forceinline__ void step(unsigned d) { m0 += d; m1 += d; n.step(d); }
};
template <int NS, int SB> __device__ __forceinline__ void half_step(int& slot, HalfReader& rd, Regs<4, 4>& Rc, Regs<4, 4>& Rn, f32x4 (&acc)[4][4]) {
    rd.step((slot + 1 == NS) ? (unsigned)(-(NS - 1) * SB) : (unsigned)SB);
    slot = (slot + 1 == NS) ? 0 : slot + 1;
    SCHED0();
    WAIT_LGKM0();
    BARRIER();
    SCHED0();
    rd.read(Rn, 0);
    mma(acc, Rc);
    interleave<12, 0, 16>();
    SCHED0();
    pin_acc<4, 4, 4>(acc);
    SCHED0();
}

template <int NM, int NN> struct Loader {
    static constexpr int NPW = NM + NN;
    unsigned om[NM], on[NN];
    template <int T> __device__ __forceinline__ void init_m(int lw, int lane, unsigned ld) {
#pragma unroll
        for (int i = 0; i < NM; ++i) om[i] = Op<T, NM * 32>::src_off(i >> 1, lw + 4 * (i & 1), lane, ld, (i >> 1) * 128);
    }
    template <int T> __device__ __forceinline__ void init_n(int lw, int lane, unsigned ld, unsigned c0 = 0, unsigned c1 = 128, unsigned c2 = 256, unsigned c3 = 384) {
#pragma unroll
        for (int i = 0; i < NN; ++i) on[i] = Op<T, NN * 32>::src_off(i >> 1, lw + 4 * (i & 1), lane, ld, (i >> 1) == 0 ? c0 : (i >> 1) == 1 ? c1 : (i >> 1) == 2 ? c2 : c3);
    }
    template <bool SC> __device__ __forceinline__ void issue_m(const char* gm, unsigned dst) const { dma_group<SC, 0>(gm, om, dst); }
    template <bool SC> __device__ __forceinline__ void issue_n(const char* gn, unsigned dst) const { dma_group<SC, NM * 32 * 132>(gn, on, dst); }
    template <bool SCM, bool SCN, int W> __device__ __forceinline__ void turn(const char* gm, const char* gn, unsigned dst) const {
        asm volatile("" : "+s"(gm), "+s"(gn), "+s"(dst));
        WAIT_VM(W); BARRIER();
        issue_m<SCM>(gm, dst); issue_n<SCN>(gn, dst);
    }
};
__device__ __forceinline__ int korder(int p, int c, int nk) {
    const int r = (p - c) & 3;
    const int d = (0x1003 >> (4 * r) & 15) - 1;
    return (unsigned)(p - r) < (unsigned)(nk - 2) ? p + d : p;
}
__device__ __forceinline__ f32x2 gelu2(f32x2 u) {
    const float c1 = -2.0f * 0.7978845608028654f * 1.4426950408889634f, c2 = c1 * 0.044715f;
    const f32x2 z = u * ((u * u) * c2 + c1);
    f32x2 e; e.x = __builtin_amdgcn_exp2f(z.x); e.y = __builtin_amdgcn_exp2f(z.y);
    e = e + 1.0f;
    f32x2 r; r.x = __builtin_amdgcn_rcpf(e.x); r.y = __builtin_amdgcn_rcpf(e.y);
    return u * r;
}
__device__ __forceinline__ u32x4 pair16(unsigned x0, unsigned x1, unsigned y0, unsigned y1) {
    auto a = __builtin_amdgcn_permlane16_swap(x0, y0, false, false);
    auto b = __builtin_amdgcn_permlane16_swap(x1, y1, false, false);
    return (u32x4){a[0], b[0], a[1], b[1]};
}
__device__ __forceinline__ bool flags_set(u64* flags, int idx, u64 tag) {
    const u64 v = __hip_atomic_load((GAS u64*)flags + (idx < 0 ? 0 : idx), RLX_AGENT);
    const bool ok = __builtin_amdgcn_ballot_w64(idx >= 0 && v - 1ull != tag) == 0ull;
    CFENCE();
    return ok;
}
__device__ __forceinline__ void poll_flags(u64* flags, int idx, u64 tag) {
#pragma unroll 1
    for (unsigned spins = 0; spins < (1u << 20); ++spins) {
        if (flags_set(flags, idx, tag)) break;
        __builtin_amdgcn_s_sleep(2);
    }
}
__device__ __forceinline__ void peek_issue(u64& v, u64* flags, int idx) {
    asm volatile("global_load_dwordx2 %0, %1, off sc1" : "=v"(v) : "v"((GAS u64*)flags + (idx < 0 ? 0 : idx)) : "memory");
}
__device__ __forceinline__ bool peek_ok(u64& v, int idx, u64 tag) {
    asm volatile("" : "+v"(v) :: "memory");
    return __builtin_amdgcn_ballot_w64(idx >= 0 && v - 1ull != tag) == 0ull;
}
__device__ __forceinline__ void set_flag(u64* flags, int idx, u64 tag) {
    asm volatile("" : "+v"(tag));
    __hip_atomic_store((GAS u64*)flags + idx, tag + 1ull, RLX_AGENT);
}

struct Params {
    const u16 *x, *wqkv, *wo, *w1, *b1, *w2, *b2;
    u16* out;
    char* ws;
    __device__ __forceinline__ u16* h() const { return out; }
    __device__ __forceinline__ u16* qkv() const { return (u16*)(ws + WS_QKV); }
    __device__ __forceinline__ u16* U() const { return (u16*)(ws + WS_U); }
    __device__ __forceinline__ u16* o() const { return (u16*)(ws + WS_QKV); }
    __device__ __forceinline__ float* stat() const { return (float*)(ws + WS_STAT); }
    __device__ __forceinline__ u64* flags() const { return (u64*)(ws + WS_FLAGS); }
};

constexpr int SB_M = (128 + 224) * 132, SB_Q = (128 + 192) * 132, SB_F = 256 * 132, NS_F = 4;
constexpr int LDS_QX = 3 * SB_Q;
constexpr int LDS_CTL = LDS_QX + 3072;
constexpr int LDS_AQ = 0, LDS_AK = 2 * SB_Q, LDS_AV = 128 * 1024, LDS_BYTES = 160 * 1024;
constexpr int LDS_MC = LDS_BYTES - 512;
constexpr int LDS_X = 3 * SB_F;
constexpr int LDS_BK = 16 * 1024, LDS_BV = 96 * 1024;
constexpr int LDS_EX = 136 * 1024;
constexpr int LDS_SINK = LDS_BYTES - 256;
static_assert(LDS_CTL + 32 <= LDS_AV && 3 * SB_M <= LDS_SINK && 3 * SB_F <= LDS_AV && NS_F * SB_F <= LDS_EX && LDS_EX + 16384 <= LDS_MC, "LDS map");
constexpr int NA = 19;
constexpr int KW = 16, NF_Q = 7 + NA, NF_M = 7 + 42 - NA;

__device__ __forceinline__ int qkv_head(int tn, int blk) { return blk * NH + ((tn & ~3) | ((tn + blk) & 3)) * 2; }

__device__ __forceinline__ f32x4 ln_row(const u32x4 a, const u32x4 b, u32x4& oa, u32x4& ob) {
    f32x2 v[8];
#pragma unroll
    for (int i = 0; i < 4; ++i) { v[i] = (f32x2){lo16(a[i]), hi16(a[i])}; v[4 + i] = (f32x2){lo16(b[i]), hi16(b[i])}; }
    const f32x2 s2 = ((v[0] + v[1]) + (v[2] + v[3])) + ((v[4] + v[5]) + (v[6] + v[7]));
    const float mean = wave_sum(s2.x + s2.y) * (1.0f / D_);
    f32x2 q2 = {0.f, 0.f};
#pragma unroll
    for (int i = 0; i < 8; ++i) { v[i] -= mean; q2 += v[i] * v[i]; }
    const float var = wave_sum(q2.x + q2.y) * (1.0f / D_);
    const float rs = __builtin_amdgcn_rcpf(rbf(__builtin_amdgcn_sqrtf(rbf(rbf(var) + 1e-5f))));
    const float sd2 = rbf(__builtin_amdgcn_sqrtf(rbf(rbf(var * rs * rs) + 1e-5f)));
    const float g = rs * __builtin_amdgcn_rcpf(sd2);
#pragma unroll
    for (int i = 0; i < 8; ++i) {
        const f32x2 t = v[i] * g;
        if (i < 4) oa[i] = pack2(t.x, t.y); else ob[i - 4] = pack2(t.x, t.y);
    }
    return (f32x4){mean, rs, sd2, 0.f};
}

#define MFMA32(a, b, c) __builtin_amdgcn_mfma_f32_32x32x16_bf16(a, b, c, 0, 0, 0)
__device__ __forceinline__ int attn_dep(int hd, int qb, int lane) {
    const int which = lane >> 2, st = 2 * qb - 2 + (lane & 3), p = hd >> 1;
    if (lane >= 12 || st < 0 || (which == 0 && (lane & 3) < 2)) return -1;
    return FL_Q + st * 32 + ((p & ~3) | ((p - which) & 3));
}
template <int WHICH> __device__ __forceinline__ void attn_load(const Params& P, unsigned dst, int hd, int qb, int w, int lane) {
    const int row = WHICH == 0 ? qb * 256 + w * 64 : qb * 256 - ((qb == 0 && w < 2) ? 0 : 256) + w * 128;
    const char* g = (const char*)(P.qkv() + ((size_t)(WHICH * NH + hd) * S_ + row) * 32);
    const unsigned off = (unsigned)((lane >> 2) * 64 + (WHICH == 2 ? lane & 3 : (lane & 3) ^ ((lane >> 4) & 3)) * 16);
    dma16x4(g, off, dst + w * (WHICH == 0 ? 4096 : 8192));
    if (WHICH != 0) dma16x4(g + 4096, off, dst + w * 8192 + 4096);
}
#define PIN(x) asm volatile("" : "+v"(x))
__device__ __forceinline__ float attn_scores(LAS char* buf, LAS char* qbuf, int nbad, int wid, int lane, unsigned up, f32x16 (&sc)[8]) {
    const int r32 = lane & 31, hh = lane >> 5, sw = (r32 >> 2) & 3;
    LAS char* k0p = buf + (wid * 32 + r32) * 64 + ((hh ^ sw) * 16);
    LAS char* k1p = buf + (wid * 32 + r32) * 64 + (((2 + hh) ^ sw) * 16);
    const bf16x8 qf0 = *(LAS bf16x8*)(qbuf + (wid * 32 + r32) * 64 + ((hh ^ sw) * 16));
    const bf16x8 qf1 = *(LAS bf16x8*)(qbuf + (wid * 32 + r32) * 64 + (((2 + hh) ^ sw) * 16));
    bf16x8 ka[9], kb[9];
#pragma unroll
    for (int i = 0; i < 3; ++i) { const int j = i == 0 ? 8 : i - 1; ka[i] = *(LAS bf16x8*)(k0p + j * 2048); kb[i] = *(LAS bf16x8*)(k1p + j * 2048); SCHED0(); }
    const float NEG = -INFINITY;
    const f32x16 zero = {};
    f32x16 s8;
    float m = NEG;
#pragma unroll
    for (int i = 0; i < 11; ++i) {
        if (i + 3 < 9) { ka[i + 3] = *(LAS bf16x8*)(k0p + (i + 2) * 2048); kb[i + 3] = *(LAS bf16x8*)(k1p + (i + 2) * 2048); }
        if (i < 9) {
            const f32x16 t = MFMA32(kb[i], qf1, MFMA32(ka[i], qf0, zero));
            if (i == 0) { s8 = t; PIN(s8); } else { sc[i - 1] = t; PIN(sc[i - 1]); }
        }
        SCHED0();
        const int j = i - 3;
        if (j == 0) {
            if (__builtin_expect(nbad > 0, 0)) {
#pragma unroll
                for (int r = 0; r < 16; ++r) sc[0][r] = NEG;
                PIN(sc[0]);
            }
#pragma unroll
            for (int r = 0; r < 16; ++r) {
                const unsigned mk = (unsigned)__builtin_amdgcn_sbfe((int)up, (r & 3) + 8 * (r >> 2), 1);
                sc[0][r] = __uint_as_float(__builtin_amdgcn_bitop3_b32(mk, __float_as_uint(sc[0][r]), __float_as_uint(s8[r]), 0xca));
            }
        }
        if (j >= 0) {
            float mj = m;
#pragma unroll
            for (int r = 0; r < 16; r += 2) mj = __builtin_fmaxf(__builtin_fmaxf(mj, sc[j][r]), sc[j][r + 1]);
            m = (j > 0 && j < nbad) ? m : mj;
            PIN(m);
        }
        SCHED0();
    }
    auto rr = __builtin_amdgcn_permlane32_swap(__float_as_uint(m), __float_as_uint(m), false, false);
    return __builtin_fmaxf(__uint_as_float(rr[0]), __uint_as_float(rr[1]));
}
__device__ __forceinline__ bf16x8 v_read(LAS char* vp, int j, int s) {
    const bf16x4 lo = __builtin_amdgcn_ds_read_tr16_b64_v4bf16((LAS bf16x4*)(vp + j * 2048 + s * 1024));
    const bf16x4 hi = __builtin_amdgcn_ds_read_tr16_b64_v4bf16((LAS bf16x4*)(vp + j * 2048 + s * 1024 + 512));
    return __builtin_shufflevector(lo, hi, 0, 1, 2, 3, 4, 5, 6, 7);
}
__device__ __forceinline__ void attn_pv(LAS char* buf, int nbad, int wid, int lane, unsigned up, f32x16 (&sc)[8], float m, f32x16& o, float& lsum) {
    const int hh = lane >> 5;
    LAS char* vp = buf + (wid * 32 + 4 * hh + ((lane & 15) >> 2)) * 64 + ((lane >> 4) & 1) * 32 + (lane & 3) * 8;
    u32x4 one4 = {0x3f803f80u, 0x3f803f80u, 0x3f803f80u, 0x3f803f80u};
    PIN(one4);
    const bf16x8 ones = __builtin_bit_cast(bf16x8, one4);
    const float h0 = rbf(-m), h1 = rbf(-m - h0);
    const unsigned w0 = hh ? 0u : pack2(h0, h1), w1 = hh ? 0u : pack2((-m - h0) - h1, 0.f);
    auto minus_m = [&](int j) {
        const u32x4 bm = {(j > 0 && j < nbad) ? 0xff80u : w0, w1, 0u, 0u};
        sc[j] = MFMA32(ones, __builtin_bit_cast(bf16x8, bm), sc[j]);
        PIN(sc[j]);
    };
    minus_m(0); minus_m(1);
    f32x16 l = {};
    bf16x8 vf[2], v8[2];
    u32x4 pw[2], p8[2], pl[2];
    SCHED0();
#pragma unroll
    for (int j = 0; j < 8; ++j) {
        bf16x8 vn[2] = {v_read(vp, j, 0), v_read(vp, j, 1)};
        if (j == 0) { v8[0] = v_read(vp, 8, 0); v8[1] = v_read(vp, 8, 1); }
        SCHED0();
#pragma unroll
        for (int g = 0; g < 4; ++g) {
#pragma unroll
            for (int r = 4 * g; r < 4 * g + 4; ++r) sc[j][r] = __builtin_amdgcn_exp2f(sc[j][r]);
            PIN(sc[j]);
            SCHED0();
            if (j > 0) {
                if (g & 1) { l = MFMA32(ones, __builtin_bit_cast(bf16x8, j == 1 ? pl[g >> 1] : pw[g >> 1]), l); PIN(l); }
                else { o = MFMA32(vf[g >> 1], __builtin_bit_cast(bf16x8, pw[g >> 1]), o); PIN(o); }
            }
            if (j == 1 && (g & 1)) { o = MFMA32(v8[g >> 1], __builtin_bit_cast(bf16x8, p8[g >> 1]), o); PIN(o); }
            SCHED0();
        }
#pragma unroll
        for (int i = 0; i < 8; ++i) pw[i >> 2][i & 3] = pack2(sc[j][2 * i], sc[j][2 * i + 1]);
        if (j == 0) {
            pl[0] = pw[0]; pl[1] = pw[1];
#pragma unroll
            for (int i = 0; i < 8; ++i) {
                const int kr = ((2 * i) & 3) + 8 * (i >> 1);
                const unsigned mk = __builtin_amdgcn_bitop3_b32(0xffffu, (unsigned)__builtin_amdgcn_sbfe((int)up, kr, 1), (unsigned)__builtin_amdgcn_sbfe((int)up, kr + 1, 1), 0xca);
                p8[i >> 2][i & 3] = pw[i >> 2][i & 3] & ~mk; pw[i >> 2][i & 3] &= mk;
            }
        }
        vf[0] = vn[0]; vf[1] = vn[1];
        PIN(pw[0]); PIN(pw[1]);
        SCHED0();
        if (j + 2 < 8) minus_m(j + 2);
        SCHED0();
    }
    l = MFMA32(ones, __builtin_bit_cast(bf16x8, pw[0]), l);
    l = MFMA32(ones, __builtin_bit_cast(bf16x8, pw[1]), l);
    o = MFMA32(vf[0], __builtin_bit_cast(bf16x8, pw[0]), o);
    o = MFMA32(vf[1], __builtin_bit_cast(bf16x8, pw[1]), o);
    lsum = l[0];
}
__device__ __forceinline__ void attn_out(const Params& P, int hd, int qblk, int lane, float l, const f32x16& oacc) {
    const int r32 = lane & 31, hh = lane >> 5;
    const float inv = __builtin_amdgcn_rcpf(l);
    unsigned w[8];
#pragma unroll
    for (int g = 0; g < 4; ++g) { w[2 * g] = pack2(oacc[4 * g] * inv, oacc[4 * g + 1] * inv); w[2 * g + 1] = pack2(oacc[4 * g + 2] * inv, oacc[4 * g + 3] * inv); }
    const __amdgpu_buffer_rsrc_t rs = rsrc(P.o(), (unsigned)S_ * D_ * 2);
    const unsigned off = (unsigned)((hd * (S_ / 16) + qblk * 2 + (r32 >> 4)) * 1024 + (hh * 16 + (r32 & 15)) * 16);
#pragma unroll
    for (int gp = 0; gp < 2; ++gp) {
        auto a = __builtin_amdgcn_permlane32_swap(w[4 * gp], w[4 * gp + 2], false, false);
        auto b = __builtin_amdgcn_permlane32_swap(w[4 * gp + 1], w[4 * gp + 3], false, false);
        __builtin_amdgcn_raw_buffer_store_b128((u32x4){a[0], b[0], a[1], b[1]}, rs, off + gp * 512, 0, SC1);
    }
}

extern "C" __global__ __launch_bounds__(512, 2) void k_main(const Params P) {
    extern __shared__ __attribute__((aligned(16))) char smem[];
    LAS char* lds = (LAS char*)smem;
    const int tid = threadIdx.x, lane = tid & 63, wid = __builtin_amdgcn_readfirstlane(tid >> 6);
    const int grp = blockIdx.x & 7, idx = blockIdx.x >> 3;
    const bool loader = wid >= 4;
    const int lw = wid & 3, wm = (wid >> 1) & 1, wn = wid & 1;
    const unsigned ring0 = (unsigned)(uintptr_t)lds, ringl = ring0 + lw * PIECE;
    const int i16 = idx & 15;
    const bool isM = idx < 16;
    const int fm = (grp >> 1) * 4 + (isM ? i16 & 3 : i16 >> 2), fn = (grp & 1) * 4 + (isM ? i16 >> 2 : i16 & 3);
    const int um = (grp & 1) * 4 + (i16 >> 2), un = (grp >> 1) * 4 + (i16 & 3);
    const int hd = (grp & 1) * 8 + (i16 & 7), qb = (grp >> 1) * 2 + (i16 >> 3);
    if (wid == 0) {
        u64 pc; unsigned rel, len;
        asm volatile("s_getpc_b64 %0\n1:\n\ts_mov_b32 %1, 1b-k_main\n\ts_mov_b32 %2, 2f-k_main\n\t.subsection 1\n2:\n\t.subsection 0" : "=s"(pc), "=s"(rel), "=s"(len));
        const unsigned off = (unsigned)((idx + 32 * (lane & 15)) * 128);
        dma4((const char*)(pc - rel), off < len ? off : 0u, ring0 + LDS_SINK);
    }
    u64 tag;
    asm volatile("global_load_dwordx2 %0, %1, off sc1" : "=v"(tag) : "v"((GAS u64*)P.flags() + FL_E) : "memory");

    if (tid < 8) *(LAS unsigned*)(lds + (isM ? LDS_MC : LDS_CTL) + tid * 4) = 0u;
    if (isM) {
        constexpr int NK = S_ / 64, SB = SB_M;
        const int pg = (grp >> 1) + 4 * (grp & 1);
        const int tm = um, tn = un, mi = pg * 16 + i16;
        const char* gm = (const char*)P.h() + tm * 256; const char* gn = (const char*)P.w1 + tn * 448;
        const int cls = (tm + tn) & 3;
        auto kof = [&](int u) { const int p0 = u - ((u - cls) & 3); return __builtin_amdgcn_readfirstlane((((p0 == 1 || p0 == 2) ? u : korder(u, cls, NK)) + 4 * pg) & (NK - 1)); };
        Loader<4, 7> L;
        {
            const int row = mi * 16 + wid * 2;
            const u32x4* px = (const u32x4*)(P.x + (size_t)row * D_);
            u32x4 a0 = px[lane], b0 = px[64 + lane], a1 = px[128 + lane], b1 = px[192 + lane];
            SCHED0();
            if (loader) {
                const unsigned c0 = (tn & 1) * 64;
                L.init_m<TR>(lw, lane, D_ * 2);
                L.init_n<TR>(lw, lane, F_ * 2, c0, c0 + 128, c0 + 256, (tn & 1) ? 0 : 384);
            }
            asm volatile("" : "+v"(a0), "+v"(b0), "+v"(a1), "+v"(b1));
            if (loader) L.issue_n<false>(gn + (size_t)kof(0) * (64 * F_ * 2), ringl);
            const __amdgpu_buffer_rsrc_t rh = rsrc(P.h(), (unsigned)S_ * D_ * 2);
            u32x4 oa0, ob0, oa1, ob1;
            const f32x4 s0 = ln_row(a0, b0, oa0, ob0), s1 = ln_row(a1, b1, oa1, ob1);
            __builtin_amdgcn_raw_buffer_store_b128(oa0, rh, (unsigned)(row * 2048 + lane * 16), 0, SC1);
            __builtin_amdgcn_raw_buffer_store_b128(ob0, rh, (unsigned)(row * 2048 + 1024 + lane * 16), 0, SC1);
            __builtin_amdgcn_raw_buffer_store_b128(oa1, rh, (unsigned)(row * 2048 + 2048 + lane * 16), 0, SC1);
            __builtin_amdgcn_raw_buffer_store_b128(ob1, rh, (unsigned)(row * 2048 + 3072 + lane * 16), 0, SC1);
            if (lane < 2) __builtin_amdgcn_raw_buffer_store_b128(__builtin_bit_cast(u32x4, lane ? s1 : s0), rsrc(P.stat(), S_ * 16), (unsigned)((row + lane) * 16), 0, SC1);
            SCHED0();
        }
        if (loader) {
            WAIT_VM(0);
            BARRIER();
            if (tid == 256 + 64) set_flag(P.flags(), FL_L + mi, tag);
#pragma unroll
            for (int u = 1; u < 3; ++u) L.issue_n<false>(gn + (size_t)kof(u) * (64 * F_ * 2), ringl + u * SB);
            if (lw == 0) poll_flags(P.flags(), lane < 16 ? FL_L + pg * 16 + lane : -1, tag);
            BARRIER();
#pragma unroll
            for (int u = 0; u < 3; ++u) L.issue_m<true>(gm + (size_t)kof(u) * (64 * D_ * 2), ringl + u * SB);
            WAIT_VM(8); BARRIER();
            WAIT_VM(4);
            int slot = 0, t = 0;
            auto issue = [&]() {
                const int k = kof(t + 3);
                L.issue_m<true>(gm + (size_t)k * (64 * D_ * 2), ringl + slot * SB);
                L.issue_n<false>(gn + (size_t)k * (64 * F_ * 2), ringl + slot * SB);
                slot = (slot == 2) ? 0 : slot + 1;
            };
            if (lw == 0) poll_flags(P.flags(), lane < 16 ? FL_L + ((pg + 1) & 7) * 16 + lane : -1, tag);
            BARRIER(); issue(); ++t;
            WAIT_VM(L.NPW); BARRIER(); issue(); ++t;
            WAIT_VM(L.NPW);
            if (lw == 0) { poll_flags(P.flags(), FL_L + lane, tag); poll_flags(P.flags(), FL_L + 64 + lane, tag); }
            BARRIER(); issue(); ++t;
#pragma unroll 1
            for (; t < NK - 3; ++t) {
                const int k = (korder(t + 3, cls, NK) + 4 * pg) & (NK - 1);
                L.turn<true, false, 11>(gm + (size_t)k * (64 * D_ * 2), gn + (size_t)k * (64 * F_ * 2), ringl + slot * SB);
                slot = (slot == 2) ? 0 : slot + 1;
            }
            {
                const int f = ((i16 * 4 + lw) * 7 + (lane < 56 ? lane >> 3 : 6)) * 2 + (grp & 1) + 896 * (grp >> 1);
                dma4((const char*)P.w2 + (grp >> 1) * 1024, (unsigned)(f * (S_ * 2) + (lane & 7) * 128), ring0 + LDS_SINK);
            }
            WAIT_VM(L.NPW + 1); BARRIER();
            WAIT_VM(1); BARRIER();
            BARRIER();
#pragma unroll
            for (int i = 0; i < 8; ++i)
                dma16<false, 0>((const char*)P.x + ((size_t)(fm * 128 + i * 16) * D_ + fn * 128 + lw * 32) * 2, (unsigned)((lane >> 2) * (D_ * 2) + (lane & 3) * 16), ring0 + LDS_X + (lw * 8 + i) * 1024);
            dma4((const char*)P.b2 + fm * 256, (unsigned)(lane * 4), ring0 + LDS_X + 32768);
        } else {
            const int fr = lane & 15, fq = lane >> 4;
            const int fb = tn * 224 + ((tn & 1) ? 32 : 0) + wn * 32, fh = tn * 224 + ((tn & 1) ? 0 : 192) + wn * 16;
            WAIT_VM(0);
            WAIT_LGKM0();
            BARRIER();
            f32x4 acc[4][7];
#pragma unroll
            for (int j = 0; j < 7; ++j) {
                const u32x2 bb = *(const u32x2*)(P.b1 + (j < 6 ? fb + (j >> 1) * 64 + (j & 1) * 16 : fh) + fq * 4);
                const f32x4 bv = {lo16(bb[0]), hi16(bb[0]), lo16(bb[1]), hi16(bb[1])};
#pragma unroll
                for (int i = 0; i < 4; ++i) acc[i][j] = bv;
            }
            Reader<TR, 128, TR, 224, 4, 7> rd; rd.init(ring0, lane, wm, wn);
            Regs<4, 7> R0;
            int slot = 0;
            BARRIER();
            __builtin_amdgcn_s_waitcnt(0x0070);
            BARRIER();
            rd.read(R0, 0);
            for (int t = 0; t < NK / 4; ++t) {
                stage_step<3, SB>(slot, rd, rd, R0, acc); stage_step<3, SB>(slot, rd, rd, R0, acc);
                stage_step<3, SB>(slot, rd, rd, R0, acc); stage_step<3, SB>(slot, rd, rd, R0, acc);
            }
            const __amdgpu_buffer_rsrc_t rs = rsrc(P.U(), (unsigned)D_ * F_ * 2);
            const unsigned df = (unsigned)(tm * 8 + wm * 4);
            const unsigned ob = (unsigned)((fb >> 5) * 64 + df) * 1024u + (unsigned)((((fq & 1) * 2 + (fq >> 1)) * 16 + fr) * 16);
            const unsigned oh = (unsigned)((fh >> 5) * 64 + df + (fq & 1)) * 1024u + (unsigned)(((wn * 2 + (fq >> 1)) * 16 + fr) * 16);
            LAS char* ul = lds + 128 * 132 + (wn * 8 + wm * 4) * PIECE + (((fq & 1) * 2 + (fq >> 1)) * 16 + fr) * 16;
            unsigned pk[4][7][2];
#pragma unroll
            for (int i = 0; i < 4; ++i) {
#pragma unroll
                for (int j = 0; j < 7; ++j) {
                    const f32x2 a = gelu2((f32x2){acc[i][j][0], acc[i][j][1]}), b = gelu2((f32x2){acc[i][j][2], acc[i][j][3]});
                    pk[i][j][0] = pack2(a.x, a.y); pk[i][j][1] = pack2(b.x, b.y);
                }
#pragma unroll
                for (int jp = 0; jp < 3; ++jp) {
                    const u32x4 w = pair16(pk[i][2 * jp][0], pk[i][2 * jp][1], pk[i][2 * jp + 1][0], pk[i][2 * jp + 1][1]);
                    __builtin_amdgcn_raw_buffer_store_b128(w, rs, ob + i * 1024 + jp * 2 * 65536, 0, SC1);
                    *(LAS u32x4*)(ul + jp * SB_F + i * PIECE) = w;
                }
                if (i & 1)
                    __builtin_amdgcn_raw_buffer_store_b128(pair16(pk[i - 1][6][0], pk[i - 1][6][1], pk[i][6][0], pk[i][6][1]), rs, oh + (i - 1) * 1024, 0, SC1);
                SCHED0();
            }
            WAIT_VM(0);
            if (lane == 0 && __hip_atomic_fetch_add((LAS unsigned*)(lds + LDS_MC), 1u, __ATOMIC_RELAXED, __HIP_MEMORY_SCOPE_WORKGROUP) == 3u)
                set_flag(P.flags(), FL_U + um * 32 + un, tag);
        }
    } else {
        constexpr int NK = D_ / 64, SB = SB_Q;
        const int tm = fm;
        if (loader) {
            Loader<4, 6> L; L.init_m<ROW>(lw, lane, D_ * 2);
            L.init_n<TR>(lw, lane, 3 * D_ * 2, qkv_head(fn, 0) * 64, qkv_head(fn, 1) * 64, qkv_head(fn, 2) * 64);
            const char* gm = (const char*)P.x + (size_t)tm * 128 * D_ * 2; const char* gn = (const char*)P.wqkv;
            const char* gh2 = (const char*)P.h() + (size_t)tm * 128 * D_ * 2;
            int slot = 0;
            auto issue = [&](int u) {
                const int k = (korder(u & (NK - 1), (tm + fn) & 3, NK) + 4 * (grp >> 1)) & (NK - 1);
                L.issue_m<false>(gm + k * 128, ringl + slot * SB);
                L.issue_n<false>(gn + (size_t)k * (64 * 3 * D_ * 2) + (u >= NK ? 16 * 64 : 0), ringl + slot * SB);
                slot = (slot == 2) ? 0 : slot + 1;
            };
            issue(0); issue(1);
            WAIT_VM(L.NPW); BARRIER();
            issue(2);
            int t = 0;
            WAIT_VM(L.NPW); BARRIER(); issue(3); ++t;
#pragma unroll 1
            for (int seg = 0; seg < 2; ++seg) {
                const int end = seg == 0 ? 12 : 2 * NK - 3;
#pragma unroll 1
                for (; t < end; ++t) {
                    const int u = t + 3, k = (korder(u & (NK - 1), (tm + fn) & 3, NK) + 4 * (grp >> 1)) & (NK - 1);
                    L.turn<true, false, 10>((u >= NK ? gh2 : gm) + k * 128, gn + (size_t)k * (64 * 3 * D_ * 2) + (u & NK) * 64, ringl + slot * SB);
                    slot = (slot == 2) ? 0 : slot + 1;
                }
                if (seg == 0 && lw == 0) {
                    poll_flags(P.flags(), lane < 8 ? FL_L + tm * 8 + lane : -1, tag);
                    dma16<true, LDS_QX>((const char*)P.stat() + tm * 2048, (unsigned)(lane * 16), ring0);
                    dma16<true, LDS_QX + 1024>((const char*)P.stat() + tm * 2048 + 1024, (unsigned)(lane * 16), ring0);
                }
            }
            poll_flags(P.flags(), attn_dep(hd, qb, lane), tag);
            WAIT_VM(0);
            BARRIER();
            attn_load<1>(P, ring0 + LDS_AK, hd, qb, lw, lane);
            attn_load<2>(P, ring0 + LDS_AV, hd, qb, lw, lane);
            BARRIER();
            attn_load<0>(P, ring0 + LDS_AQ, hd, qb, lw, lane);
            BARRIER();
        } else {
            const int fr = lane & 15, fq = lane >> 4;
            Reader<ROW, 128, TR, 192, 4, 6> rd; rd.init(ring0, lane, wm, wn);
            rd.n.b = rd.n.a + 32 - wm * 32; rd.n.a += wm * 32;
            Regs<4, 6> R0;
            int slot = 0;
            __builtin_amdgcn_s_waitcnt(0x0070);
            BARRIER();
            rd.read(R0, 0);
            const __amdgpu_buffer_rsrc_t rs = rsrc(P.qkv(), (unsigned)S_ * 3 * D_ * 2);
            const int s0 = tm * 128 + wm * 64;
            const unsigned so = (unsigned)(((s0 + fr) * 32 + ((fq & 1) ^ wm) * 16 + (fq >> 1) * 8) * 2);
            constexpr float QS = 0.17677669529663687f * 1.4426950408889634f;
            {
                f32x4 acc[5][6] = {};
                for (int t = 0; t < NK / 4; ++t) {
                    stage_step<3, SB>(slot, rd, rd, R0, acc); stage_step<3, SB>(slot, rd, rd, R0, acc);
                    stage_step<3, SB>(slot, rd, rd, R0, acc); stage_step<3, SB>(slot, rd, rd, R0, acc);
                }
                LAS char* ex = lds + LDS_QX + 2048;
                if (fr == 0) {
#pragma unroll
                    for (int jp = 0; jp < 3; ++jp) *(LAS f32x4*)(ex + wid * 256 + jp * 64 + fq * 16) = acc[4][2 * jp];
                }
                WAIT_LGKM0();
                CFENCE();
                __hip_atomic_store((LAS unsigned*)(lds + LDS_CTL + wid * 4), 1u, __ATOMIC_RELAXED, __HIP_MEMORY_SCOPE_WORKGROUP);
                f32x2 st[4];
#pragma unroll
                for (int i = 0; i < 4; ++i) st[i] = *(LAS f32x2*)(lds + LDS_QX + (wm * 64 + i * 16 + fr) * 16);
                while (__builtin_amdgcn_readfirstlane(__hip_atomic_load((LAS unsigned*)(lds + LDS_CTL + (wid ^ 2) * 4), __ATOMIC_RELAXED, __HIP_MEMORY_SCOPE_WORKGROUP)) == 0u) {}
                CFENCE();
#pragma unroll
                for (int jp = 0; jp < 3; ++jp) {
                    const f32x4 ca = acc[4][2 * jp], cb = *(LAS f32x4*)(ex + (wid ^ 2) * 256 + jp * 64 + fq * 16);
#pragma unroll
                    for (int i = 0; i < 4; ++i) {
                        const float r = st[i].y * (jp == 0 ? QS : 1.0f), mr = -st[i].x * r;
                        const f32x4 a = acc[i][2 * jp] * r + ca * mr, b = acc[i][2 * jp + 1] * r + cb * mr;
                        __builtin_amdgcn_raw_buffer_store_b128(pair16(pack2(a[0], a[1]), pack2(a[2], a[3]), pack2(b[0], b[1]), pack2(b[2], b[3])), rs,
                                                               so + (unsigned)((qkv_head(fn, jp) + wn) * (S_ * 64) + i * 1024), 0, SC1);
                    }
                }
            }
            {
                f32x4 acc[4][6] = {};
                for (int t = 0; t < NK / 4; ++t) {
                    if (__builtin_expect(t == 1 || t == 2, 0)) {
                        if (t == 1) WAIT_VM(0);
                        else if (tid == 0) set_flag(P.flags(), FL_Q + tm * 32 + fn, tag);
                    }
                    stage_step<3, SB>(slot, rd, rd, R0, acc); stage_step<3, SB>(slot, rd, rd, R0, acc);
                    stage_step<3, SB>(slot, rd, rd, R0, acc); stage_step<3, SB>(slot, rd, rd, R0, acc);
                }
#pragma unroll
                for (int i = 0; i < 4; ++i) {
                    const float sd = *(LAS float*)(lds + LDS_QX + (wm * 64 + i * 16 + fr) * 16 + 8);
#pragma unroll
                    for (int jp = 0; jp < 3; ++jp) {
                        const float r = sd * (jp == 0 ? QS : 1.0f);
                        const f32x4 a = acc[i][2 * jp] * r, b = acc[i][2 * jp + 1] * r;
                        __builtin_amdgcn_raw_buffer_store_b128(pair16(pack2(a[0], a[1]), pack2(a[2], a[3]), pack2(b[0], b[1]), pack2(b[2], b[3])), rs,
                                                               so + (unsigned)((qkv_head(fn, jp) + 16 + wn) * (S_ * 64) + i * 1024), 0, SC1);
                    }
                }
            }
        }
    }

    constexpr int SB = SB_F, NS = NS_F;
    constexpr size_t OS = 2 * (S_ / 16) * 1024, WOS = 64 * D_ * 2, W2S = 64 * S_ * 2, US = 2 * 65536;
    const int tile = fm * 8 + fn, cls = (fm + fn) & 3, nf = isM ? NF_M : NF_Q;
    const int uh = ((fm >> 1) & 1) ^ (isM ? 0 : 1);
    auto fof = [&](int i) {
        int r = korder(i - 7, cls, nf - 7) + (grp & 1) * ((nf - 7) >> 1);
        r = r >= nf - 7 ? r - (nf - 7) : r;
        const int im = (fm & 1) ? (i < 3 ? i + 4 : i - 3) : i;
        const int j = i < 7 ? 7 * uh + (isM ? im : korder(i, cls, 7)) : (isM ? 14 + NA : 14) + r, k = 14 * (grp >> 1) + j;
        return __builtin_amdgcn_readfirstlane(k >= 56 ? k - 56 : k);
    };
    const char* g2 = (const char*)P.w2 + fm * 256;
    Loader<4, 4> L;
    if (loader) L.init_m<TR>(lw, lane, S_ * 2);
    auto preload_w2 = [&]() {
        int k0 = fof(0), k1 = fof(1);
        asm volatile("" : "+s"(k0), "+s"(k1));
        L.issue_m<false>(g2 + k0 * W2S, ringl); L.issue_m<false>(g2 + k1 * W2S, ringl + SB);
    };
    if (isM) {
        if (loader) { preload_w2(); L.issue_m<false>(g2 + fof(2) * W2S, ringl + 2 * SB); WAIT_VM(0); }
        WAIT_LGKM0();
        BARRIER();
    } else {
        if (loader) { WAIT_VM(0); __builtin_amdgcn_s_setprio(1); }
        WAIT_LGKM0();
        BARRIER();
#pragma unroll 1
        for (int it = 0; it < 2; ++it) {
            f32x16 sc[8];
            int ln = lane;
            asm volatile("" : "+v"(ln));
            const int dg = (ln & 31) - 4 * (ln >> 5);
            const unsigned up = dg < 0 ? ~0u : ~1u << dg;
            int nbad = qb == 0 ? 8 - wid : 0;
            asm volatile("" : "+s"(nbad));
            SCHED0();
            const float m = attn_scores(lds + (it ? LDS_BK : LDS_AK), lds + LDS_AQ, nbad, wid, ln, up, sc);
            SCHED0();
            WAIT_VM(0);
            BARRIER();
            if (tid == 0) set_flag(P.flags(), it == 0 ? FL_Q + fm * 32 + fn + 8 : FL_O + qb * 32 + hd, tag);
            if (it == 1 && loader) preload_w2();
            f32x16 oacc = {};
            float lsum;
            attn_pv(lds + (it ? LDS_BV : LDS_AV), nbad, wid, ln, up, sc, m, oacc, lsum);
            attn_out(P, hd + 16 * it, qb * 8 + wid, ln, lsum, oacc);
            if (it == 0) {
                if (loader) {
                    __builtin_amdgcn_s_setprio(0);
                    int h2 = hd + 16, q2 = qb, w2 = lw;
                    asm volatile("" : "+s"(h2), "+s"(q2), "+s"(w2));
                    poll_flags(P.flags(), attn_dep(h2, q2, ln), tag);
                    attn_load<0>(P, ring0 + LDS_AQ, h2, q2, w2, ln);
                    attn_load<1>(P, ring0 + LDS_BK, h2, q2, w2, ln);
                    attn_load<2>(P, ring0 + LDS_BV, h2, q2, w2, ln);
                    WAIT_VM(8);
                    __builtin_amdgcn_s_setprio(1);
                }
                BARRIER();
            }
        }
        WAIT_LGKM0();
    }
    const int own = isM ? 0 : 1;
    if (loader) {
        const char* go = (const char*)P.o() + fm * 8192 + own * 4096;
        const char* gw = (const char*)P.wo + fn * 256;
        const char* gu = (const char*)P.U() + fn * 8192;
        __builtin_amdgcn_s_setprio(0);
        L.init_n<FRAG>(lw, lane, US / 2);
        auto issue_f = [&](int u) {
            const int k = fof(u);
            L.issue_m<false>(g2 + k * W2S, ringl + (u & (NS - 1)) * SB); L.issue_n<true>(gu + k * US, ringl + (u & (NS - 1)) * SB);
        };
        const int ulo = isM ? ((14 + NA) * 2) / 7 : 4, uhi = isM ? 15 : ((14 + NA) * 2 - 1) / 7;
        const int udep = (lane >= ulo && lane <= uhi) ? FL_U + fn * 32 + (((grp >> 1) * 4 + lane) & 15) : -1;
        const int odep = lane < 32 ? FL_O + (fm >> 1) * 32 + lane : lane < 48 ? FL_U + fn * 32 + lane - 32 : -1, pdep = lane == 0 ? FL_P + tile * 32 + own : -1;
        u64 pk;
        if (isM) { BARRIER(); BARRIER(); }
        poll_flags(P.flags(), lane < 2 ? FL_U + fn * 32 + (grp >> 1) * 4 + 2 * uh + lane : -1, tag);
        if (isM) { issue_f(3); issue_f(4); }
        else {
#pragma unroll
            for (int u = 0; u < 2; ++u) L.issue_n<true>(gu + fof(u) * US, ringl + u * SB);
            WAIT_VM(4); BARRIER();
            issue_f(2); issue_f(3);
            WAIT_VM(16); BARRIER(); issue_f(4);
            WAIT_VM(16); BARRIER();
        }
        peek_issue(pk, P.flags(), udep); issue_f(5);
        WAIT_VM(16); BARRIER(); issue_f(6);
        WAIT_VM(16);
        int nxt = 7;
        if (!peek_ok(pk, udep, tag)) {
            bool uready = false;
#pragma unroll 1
            for (int t = 3; t < 7; ++t) {
                if (t == 6 && !uready) { poll_flags(P.flags(), udep, tag); uready = true; for (; nxt < 10; ++nxt) issue_f(nxt); }
                if (nxt - (t + 2) >= 2) WAIT_VM(16); else WAIT_VM(0);
                BARRIER();
                if (!uready) uready = flags_set(P.flags(), udep, tag);
                if (uready) for (; nxt <= t + NS; ++nxt) issue_f(nxt);
            }
        }
#pragma unroll 1
        for (; nxt < nf - 2; ++nxt) {
            int r = korder(nxt - 7, cls, nf - 7) + (grp & 1) * ((nf - 7) >> 1);
            r = r >= nf - 7 ? r - (nf - 7) : r;
            int k = 14 * (grp >> 1) + (isM ? 14 + NA : 14) + r;
            k = k >= 56 ? k - 56 : k;
            L.turn<false, true, 16>(g2 + k * W2S, gu + k * US, ringl + (nxt & (NS - 1)) * SB);
        }
        const unsigned xo = (unsigned)(((fm * 128 + (lane >> 2)) * D_ + fn * 128 + lw * 32 + (lane & 3) * 8) * 2);
        constexpr unsigned ROWS16 = 16 * D_ * 2;
        WAIT_VM(16); BARRIER(); peek_issue(pk, P.flags(), odep);
        issue_f(nf - 2);
        WAIT_VM(16); BARRIER(); issue_f(nf - 1);
        L.init_n<TR>(lw, lane, D_ * 2);
        const unsigned oo = (unsigned)(lw * 1024 + lane * 16);
        const unsigned oo2 = oo + (unsigned)(OS / 2);
        const char *po, *pw; unsigned pd;
        auto addr_w = [&](int i) {
            const int k = (korder(i, cls, KW) + 4 * (grp >> 1)) & (KW - 1);
            po = go + k * OS; pw = gw + k * WOS; pd = ringl + ((nf + i) & (NS - 1)) * SB;
            asm volatile("" : "+s"(po), "+s"(pw), "+s"(pd));
        };
        auto issue_w = [&]() {
            asm volatile("s_add_u32 m0, %2, %3\n\ts_nop 0\n\tglobal_load_lds_dwordx4 %4, %0 sc1\n\ts_addk_i32 m0, 8448\n\ts_nop 0\n\tglobal_load_lds_dwordx4 %5, %0 sc1\n\t"
                         "s_add_u32 m0, %2, 16896\n\ts_nop 0\n\tglobal_load_lds_dwordx4 %6, %1\n\ts_addk_i32 m0, 4224\n\ts_nop 0\n\tglobal_load_lds_dwordx4 %7, %1\n\t"
                         "s_addk_i32 m0, 4224\n\ts_nop 0\n\tglobal_load_lds_dwordx4 %8, %1\n\ts_addk_i32 m0, 4224\n\ts_nop 0\n\tglobal_load_lds_dwordx4 %9, %1"
                         ::"s"(po), "s"(pw), "s"(pd), "s"(own * 4 * PIECE), "v"(oo), "v"(oo2), "v"(L.on[0]), "v"(L.on[1]), "v"(L.on[2]), "v"(L.on[3]) : "scc", "memory");
        };
#define TURN_W(i, w) do { addr_w(i); WAIT_VM(w); BARRIER(); issue_w(); } while (0)
        addr_w(0);
        WAIT_VM(16);
        if (!peek_ok(pk, odep, tag)) poll_flags(P.flags(), odep, tag);
        BARRIER(); issue_w();
        TURN_W(1, 14); TURN_W(2, 12); TURN_W(3, 12);
        TURN_W(4, 12);
        {
            u32x4 t0, t1, t2, t3;
            asm volatile("ds_read_b128 %0, %4\n\tds_read_b128 %1, %4 offset:1024\n\tds_read_b128 %2, %4 offset:2048\n\tds_read_b128 %3, %4 offset:3072\n\ts_waitcnt lgkmcnt(0)"
                         : "=&v"(t0), "=&v"(t1), "=&v"(t2), "=&v"(t3) : "v"(ring0 + LDS_EX + lw * 4096 + lane * 16) : "memory");
            const __amdgpu_buffer_rsrc_t ro = rsrc(P.out, (unsigned)S_ * D_ * 2);
            const unsigned so = xo + (1 - own) * 4 * ROWS16;
            __builtin_amdgcn_raw_buffer_store_b128(t0, ro, so, 0, SC1); __builtin_amdgcn_raw_buffer_store_b128(t1, ro, so + ROWS16, 0, SC1);
            __builtin_amdgcn_raw_buffer_store_b128(t2, ro, so + 2 * ROWS16, 0, SC1); __builtin_amdgcn_raw_buffer_store_b128(t3, ro, so + 3 * ROWS16, 0, SC1);
        }
        TURN_W(5, 12); TURN_W(6, 12);
        TURN_W(7, 0);
        if (tid == 256) set_flag(P.flags(), FL_P + tile * 32 + 1 - own, tag);
        TURN_W(8, 12); TURN_W(9, 12); TURN_W(10, 12); TURN_W(11, 12);
        TURN_W(12, 12); TURN_W(13, 12); TURN_W(14, 12);
        TURN_W(15, 12);
#undef TURN_W
        peek_issue(pk, P.flags(), pdep);
        WAIT_VM(13); BARRIER();
        WAIT_VM(7); BARRIER();
        WAIT_VM(0);
        auto load_partial = [&]() {
#pragma unroll
            for (int i = 0; i < 4; ++i) dma16<true, 0>((const char*)P.out + (own * 4 + i) * ROWS16, xo, ring0 + LDS_EX + (lw * 4 + i) * 1024);
        };
        const bool early = peek_ok(pk, pdep, tag);
        if (early) load_partial();
        BARRIER();
        BARRIER();
        if (!early) { poll_flags(P.flags(), pdep, tag); load_partial(); }
        WAIT_VM(0); BARRIER();
        if (blockIdx.x == 0 && lw == 0) {
#pragma unroll 1
            for (int i = 0; i < 4; ++i) poll_flags(P.flags(), FL_O + i * 64 + lane, tag);
            if (lane == 0) set_flag(P.flags(), FL_E, tag);
        }
    } else {
        const __amdgpu_buffer_rsrc_t ro = rsrc(P.out, (unsigned)S_ * D_ * 2);
        f32x4 acc[4][4];
        if (isM) {
            LAS char* xp = lds + LDS_X + (wn * 16 + wm * 4) * 1024 + (lane & 15) * 64 + (((lane >> 4) & 1) * 2 + (lane >> 5)) * 16;
            u32x4 xv[4][2]; unsigned bv[4];
#pragma unroll
            for (int i = 0; i < 4; ++i) {
                xv[i][0] = *(LAS u32x4*)(xp + i * 1024); xv[i][1] = *(LAS u32x4*)(xp + 8192 + i * 1024);
                bv[i] = *(LAS u16*)(lds + LDS_X + 32768 + (wm * 64 + i * 16 + (lane & 15)) * 2);
            }
            SCHED0();
#pragma unroll
            for (int i = 0; i < 4; ++i)
#pragma unroll
                for (int jp = 0; jp < 2; ++jp) {
                    auto e0 = __builtin_amdgcn_permlane16_swap(xv[i][jp][0], xv[i][jp][2], false, false);
                    auto e1 = __builtin_amdgcn_permlane16_swap(xv[i][jp][1], xv[i][jp][3], false, false);
                    acc[i][2 * jp] = (f32x4){lo16(e0[0]), hi16(e0[0]), lo16(e1[0]), hi16(e1[0])} + lo16(bv[i]);
                    acc[i][2 * jp + 1] = (f32x4){lo16(e0[1]), hi16(e0[1]), lo16(e1[1]), hi16(e1[1])} + lo16(bv[i]);
                }
            pin_acc<4, 4, 4>(acc);
        } else {
            float z = 0.f;
            asm volatile("" : "+v"(z));
#pragma unroll
            for (int i = 0; i < 4; ++i)
#pragma unroll
                for (int j = 0; j < 4; ++j) acc[i][j] = (f32x4){z, z, z, z};
        }
        pin_acc<4, 4, 4>(acc);
        Reader<TR, 128, FRAG, 128, 4, 4> rf;
        HalfReader rw;
        Regs<4, 4> R0;
        LAS char* exl = lds + LDS_EX + wn * 8192 + lane * 16;
        int slot = 0;
#define STEP(a, b) stage_step<NS, SB>(slot, a, b, R0, acc)
        rf.init(ring0, lane, wm, wn);
        WAIT_LGKM0();
        if (!isM) BARRIER();
        rf.read(R0, 0);
        for (int t = 0; t < (nf - 2) / 4; ++t) {
            if (__builtin_expect(!isM && (t == 1 || t == 2), 0)) {
                if (t == 1) WAIT_VM(0);
                else if (tid == 0) set_flag(P.flags(), FL_O + qb * 32 + hd + 16, tag);
            }
            STEP(rf, rf); STEP(rf, rf); STEP(rf, rf); STEP(rf, rf);
        }
        static_assert((NF_M & 3) == (NF_Q & 3), "NA must be odd");
#pragma unroll
        for (int t = 0; t < ((NF_M - 2) & 3); ++t) STEP(rf, rf);
        STEP(rf, rf);
        const int kq = wm != own ? 1 : 0;
        rw.init(ring0 + slot * SB, lane, own, kq, wn);
        STEP(rf, rw);
        if (kq) {
#pragma unroll
            for (int i = 0; i < 4; ++i)
#pragma unroll
                for (int jp = 0; jp < 2; ++jp) {
                    const f32x4 a = acc[i][2 * jp], b = acc[i][2 * jp + 1];
                    *(LAS u32x4*)(exl + jp * 4096 + i * 1024) = (u32x4){pack2(a[0], a[1]), pack2(a[2], a[3]), pack2(b[0], b[1]), pack2(b[2], b[3])};
                    acc[i][2 * jp] = (f32x4){0.f, 0.f, 0.f, 0.f}; acc[i][2 * jp + 1] = (f32x4){0.f, 0.f, 0.f, 0.f};
                }
        }
        pin_acc<4, 4, 4>(acc);
        Regs<4, 4> R1;
        for (int t = 0; t < 4; ++t) {
            half_step<NS, SB>(slot, rw, R0, R1, acc); half_step<NS, SB>(slot, rw, R1, R0, acc);
            half_step<NS, SB>(slot, rw, R0, R1, acc); half_step<NS, SB>(slot, rw, R1, R0, acc);
        }
#pragma unroll
        for (int i = 0; i < 2; ++i)
#pragma unroll
            for (int j = 0; j < 4; ++j) *(LAS f32x4*)(lds + (wid * 8 + i * 4 + j) * 1024 + lane * 16) = acc[2 + i][j];
        WAIT_LGKM0();
        BARRIER();
        const unsigned fo = (unsigned)(((fm * 128 + own * 64 + kq * 32 + (lane & 15)) * D_ + fn * 128 + wn * 64 + ((lane >> 4) & 1) * 16 + (lane >> 5) * 8) * 2);
#pragma unroll
        for (int i = 0; i < 2; ++i)
#pragma unroll
            for (int jp = 0; jp < 2; ++jp) {
                const u32x4 e = *(LAS u32x4*)(exl + jp * 4096 + (kq * 2 + i) * 1024);
                const f32x4 a = acc[i][2 * jp] + *(LAS f32x4*)(lds + ((wid ^ 2) * 8 + i * 4 + 2 * jp) * 1024 + lane * 16) + (f32x4){lo16(e[0]), hi16(e[0]), lo16(e[1]), hi16(e[1])};
                const f32x4 b = acc[i][2 * jp + 1] + *(LAS f32x4*)(lds + ((wid ^ 2) * 8 + i * 4 + 2 * jp + 1) * 1024 + lane * 16) + (f32x4){lo16(e[2]), hi16(e[2]), lo16(e[3]), hi16(e[3])};
                __builtin_amdgcn_raw_buffer_store_b128(pair16(pack2(a[0], a[1]), pack2(a[2], a[3]), pack2(b[0], b[1]), pack2(b[2], b[3])), ro, fo + i * (16 * D_ * 2) + jp * 64, 0, SC1);
            }
#undef STEP
    }
}

extern "C" void kernel_launch(void* const* d_in, const int* in_sizes, int n_in, void* d_out, int out_size, void* d_ws, size_t ws_size, hipStream_t stream) {
    char* ws = (char*)d_ws;
    Params P;
    P.x = (const u16*)d_in[0]; P.wqkv = (const u16*)d_in[1]; P.wo = (const u16*)d_in[2];
    P.w1 = (const u16*)d_in[4]; P.b1 = (const u16*)d_in[5]; P.w2 = (const u16*)d_in[6]; P.b2 = (const u16*)d_in[7];
    P.out = (u16*)d_out; P.ws = ws;
    (void)hipFuncSetAttribute((const void*)k_main, hipFuncAttributeMaxDynamicSharedMemorySize, LDS_BYTES);
    k_main<<<256, 512, LDS_BYTES, stream>>>(P);
}
```
